```python
import torch

def _softmax(x, axis=-1):
    return torch.softmax(x, dim=axis)
def _gelu(x):
    return torch.nn.functional.gelu(x, approximate="tanh")
def _silu(x):
    return torch.nn.functional.silu(x)
def _sigmoid(x):
    return torch.sigmoid(x)
def _layernorm(x, axis=-1, eps=1e-5):
    m = x.mean(dim=axis, keepdim=True)
    v = x.var(dim=axis, keepdim=True, unbiased=False)
    return (x - m) / torch.sqrt(v + eps)
def _scatter_add(tgt, idx, src, axis=0):
    return tgt.clone().index_add_(axis, idx, src)
def _segment_sum(src, seg, n):
    out = torch.zeros((n,) + src.shape[1:], dtype=src.dtype)
    return out.index_add_(0, seg, src)
def _pad(x, pw):
    flat = tuple(v for pair in reversed(pw) for v in pair)
    return torch.nn.functional.pad(x, flat)

kernel_name = 'standalone__detection_seg_dino__vision'

def setup_inputs():

    _w = (lambda a: (lambda t: t.to(torch.bfloat16) if t.is_floating_point() else t)(torch.as_tensor(a)))
    H = W = (512 // 2) * 2
    C = 192
    Nq = 16
    sc = C ** -0.5
    return {
        "q": _w((lambda *s: torch.randn(*s, dtype=torch.bfloat16) * 0.2)(1, Nq, C)),
        "f0": _w((lambda *s: torch.randn(*s, dtype=torch.bfloat16) * 3.2)(1, H, W, C)),
        "f1": _w((lambda *s: torch.randn(*s, dtype=torch.bfloat16) * 3.2)(1, H // 2, W // 2, C)),
        "wq": _w((lambda *s: torch.randn(*s, dtype=torch.bfloat16) * 1.6)(C, C) * sc),
        "wk": _w((lambda *s: torch.randn(*s, dtype=torch.bfloat16) * 1.6)(C, C) * sc),
        "wv": _w((lambda *s: torch.randn(*s, dtype=torch.bfloat16) * 3.2)(C, C) * sc),
        "wo": _w((lambda *s: torch.randn(*s, dtype=torch.bfloat16) * 3.2)(C, C) * sc),
    }

def reference(q, f0, f1, wq, wk, wv, wo):
    B, H, W, C = f0.shape
    f1u = (lambda x, n, axis: torch.repeat_interleave(x, n, dim=axis))((lambda x, n, axis: torch.repeat_interleave(x, n, dim=axis))(f1, 2, axis=1), 2, axis=2)
    fused = f0 + f1u
    feats = _layernorm(fused.reshape(B, H * W, C), axis=-1)
    qn = _layernorm(q, axis=-1)
    Q = torch.einsum("bnc,cd->bnd", qn, wq)
    K = torch.einsum("bmc,cd->bmd", feats, wk)
    V = torch.einsum("bmc,cd->bmd", feats, wv)
    scale = C ** -0.5
    logits = torch.einsum("bnd,bmd->bnm", Q, K) * scale
    attn = _softmax(logits, axis=-1)
    ctx = torch.einsum("bnm,bmd->bnd", attn, V)
    return q + torch.einsum("bnd,dc->bnc", ctx, wo)
```

```cpp
#include <hip/hip_runtime.h>
#include <hip/hip_bf16.h>
#include <stdint.h>

#define TNOW(v) do {} while (0)
#define TPUT(id, v) do {} while (0)

using bf16x8  = __attribute__((ext_vector_type(8))) short;
using f32x4   = __attribute__((ext_vector_type(4))) float;
using f32x2   = __attribute__((ext_vector_type(2))) float;
using u32x4   = __attribute__((ext_vector_type(4))) unsigned;
using u32x2   = __attribute__((ext_vector_type(2))) unsigned;
using v4i16_t = __attribute__((ext_vector_type(4))) short;
typedef __attribute__((address_space(3))) char* lds_ptr;
#define LDS_AS(T) __attribute__((address_space(3))) T

constexpr int CC = 192, IMW = 512;
constexpr int NWG = 256, NS = 7, NE = 4;
constexpr int TPW = 33;
constexpr int ROWB = 384;
constexpr int SLOT = 40 * ROWB;
constexpr int NSLOT = 10;
constexpr int BOX_BYTES = 64 * NWG * 128, EPOCH_OFF = BOX_BYTES, QK_OFF = EPOCH_OFF + 4096, W2F_OFF = QK_OFF + 12288, W2_OFF = W2F_OFF + 1024;
constexpr int GSPIN_CAP = 1 << 16;
#define PACE16 786
#define QKWAIT 290
#define NEARLY 4
#define T0BACK 8
#define W2MID 3
constexpr int W2K0 = TPW - 7 - 2 * W2MID;
constexpr int NH1 = 3, NITEM = TPW + NH1;
constexpr int NPK = 6;
constexpr float EPS = 1e-5f;
constexpr float SQRT_C = 13.856406460551018f;
constexpr float LOG2E = 1.4426950408889634f;
constexpr float THR = 20.0f;
constexpr float TOFF = 192.0f;
constexpr int SPIN_CAP = 1 << 22;

constexpr int IMG_A = NSLOT * SLOT;
constexpr int CT_OFF = IMG_A + 6144;
constexpr int PKM = CT_OFF + 512, PKU = PKM + NPK * 128;
constexpr int ONES_OFF = PKU + NPK * 256;
constexpr int JUNK = ONES_OFF + ROWB;
constexpr int LDS_BYTES = JUNK + 256;
constexpr int H_R = 192 * ROWB, H_Q = H_R + 32 * ROWB;
#define PWIN 30
static_assert(LDS_BYTES <= 163840 && (PKU & 15) == 0 && (ONES_OFF & 127) == 0, "lds");
constexpr int C_FULL = 0, C_FREE = 64, C_EP = 128, C_RED = 144, C_OUT = 148, C_ISS = 152, C_QI = 160, C_PFL = 192, C_CONS = 256,
              C_MREF = 320, C_LW = 384;

typedef __bf16 bf16x2_t __attribute__((ext_vector_type(2)));
__device__ __forceinline__ unsigned cvtpk(float lo, float hi) {
  return __builtin_bit_cast(unsigned, __builtin_convertvector(f32x2{lo, hi}, bf16x2_t));
}
__device__ __forceinline__ float rbf(float x) { return __uint_as_float(cvtpk(x, x) << 16); }
__device__ __forceinline__ float bflo(unsigned p) { return __uint_as_float(p << 16); }
__device__ __forceinline__ float bfhi(unsigned p) { return __uint_as_float(p & 0xffff0000u); }
__device__ __forceinline__ bf16x8 pack8(const f32x4& a, const f32x4& b) {
  return __builtin_bit_cast(bf16x8, u32x4{cvtpk(a[0], a[1]), cvtpk(a[2], a[3]), cvtpk(b[0], b[1]), cvtpk(b[2], b[3])});
}
__device__ __forceinline__ float xrow16_max(float x) {
  auto s = __builtin_amdgcn_permlane16_swap(__float_as_uint(x), __float_as_uint(x), false, false);
  x = fmaxf(__uint_as_float(s[0]), __uint_as_float(s[1]));
  auto t = __builtin_amdgcn_permlane32_swap(__float_as_uint(x), __float_as_uint(x), false, false);
  return fmaxf(__uint_as_float(t[0]), __uint_as_float(t[1]));
}
__device__ __forceinline__ float xrow16_sum(float x) {
  auto s = __builtin_amdgcn_permlane16_swap(__float_as_uint(x), __float_as_uint(x), false, false);
  x = __uint_as_float(s[0]) + __uint_as_float(s[1]);
  auto t = __builtin_amdgcn_permlane32_swap(__float_as_uint(x), __float_as_uint(x), false, false);
  return __uint_as_float(t[0]) + __uint_as_float(t[1]);
}
__device__ __forceinline__ v4i16_t tr_read(lds_ptr p) {
  return __builtin_amdgcn_ds_read_tr16_b64_v4i16((LDS_AS(v4i16_t)*)p);
}
__device__ __forceinline__ f32x4 mfma(bf16x8 a, bf16x8 b, f32x4 c) {
  return __builtin_amdgcn_mfma_f32_16x16x32_bf16(a, b, c, 0, 0, 0);
}
__device__ __forceinline__ int swz(int p) { return (p & 6) | ((p >> 2) & 1); }
constexpr unsigned swz_tbl(int i) {
  unsigned t = 0;
  for (int j = 0; j < 8; ++j) { const int p = (8 * i + j) / 3; t |= (unsigned)((p & 6) | ((p >> 2) & 1)) << (4 * j); }
  return t;
}
__device__ __forceinline__ bf16x8 mk8(v4i16_t a, v4i16_t b) { return bf16x8{a[0], a[1], a[2], a[3], b[0], b[1], b[2], b[3]}; }

__device__ __forceinline__ void lds_st32(unsigned addr, unsigned v) {
  asm volatile("ds_write_b32 %0, %1" : : "v"(addr), "v"(v) : "memory");
}
__device__ __forceinline__ unsigned lds_ld32(unsigned addr) {
  unsigned v;
  asm volatile("ds_read_b32 %0, %1\n\ts_waitcnt lgkmcnt(0)" : "=v"(v) : "v"(addr) : "memory");
  return __builtin_amdgcn_readfirstlane(v);
}
__device__ __forceinline__ void lds_add(unsigned addr, unsigned n) {
  asm volatile("s_mov_b64 exec, 1\n\tds_add_u32 %0, %1\n\ts_mov_b64 exec, -1\n\ts_nop 3" : : "v"(addr), "v"(n) : "memory");
}
__device__ __forceinline__ void poll_ge(unsigned addr, unsigned need) {
  unsigned v, s, n2, cnt = SPIN_CAP;
  asm volatile("1:\n\tds_read_b32 %0, %4\n\ts_sub_u32 %3, %3, 1\n\ts_cselect_b32 %2, 0, %5\n\ts_waitcnt lgkmcnt(0)\n\t"
               "v_readfirstlane_b32 %1, %0\n\ts_cmp_lt_u32 %1, %2\n\ts_cbranch_scc1 1b"
               : "=&v"(v), "=&s"(s), "=&s"(n2), "+s"(cnt) : "v"(addr), "s"(need) : "memory", "scc");
}
__device__ __forceinline__ void poll_ge_sleep(unsigned addr, unsigned need) {
  need = __builtin_amdgcn_readfirstlane(need);
  unsigned v, s, n2, cnt = SPIN_CAP;
  asm volatile("s_branch 2f\n1:\n\ts_sleep 1\n2:\n\tds_read_b32 %0, %4\n\ts_sub_u32 %3, %3, 1\n\ts_cselect_b32 %2, 0, %5\n\ts_waitcnt lgkmcnt(0)\n\t"
               "v_readfirstlane_b32 %1, %0\n\ts_cmp_lt_u32 %1, %2\n\ts_cbranch_scc1 1b"
               : "=&v"(v), "=&s"(s), "=&s"(n2), "+s"(cnt) : "v"(addr), "s"(need) : "memory", "scc");
}
__device__ __forceinline__ bf16x8 img_ld(lds_ptr L0, int off, int kk, int lane) { return *(LDS_AS(bf16x8)*)(L0 + off + kk * 1024 + lane * 16); }
__device__ __forceinline__ void img_st(lds_ptr L0, int off, int kk, int lane, bf16x8 v) { *(LDS_AS(bf16x8)*)(L0 + off + kk * 1024 + lane * 16) = v; }

template <int N> __device__ __forceinline__ void wait_vm() { asm volatile("s_waitcnt vmcnt(%0)" :: "i"(N) : "memory"); }
constexpr int A_IMG = ((TPW + 1 - NPK) % NSLOT) * SLOT, RED_OFF = ((TPW - NPK) % NSLOT) * SLOT;
__device__ __forceinline__ unsigned line_tag(unsigned epoch, unsigned i) {
  unsigned x = (epoch + 1u + i * 0x9E3779B9u) * 0x85EBCA6Bu;
  x ^= x >> 15; x *= 0xC2B2AE35u;
  return x ^ (x >> 13);
}
__device__ __forceinline__ unsigned epoch_load(const char* box) {
  unsigned e;
  asm volatile("global_load_dword %0, %1, %2 sc1\n\ts_waitcnt vmcnt(0)" : "=v"(e) : "v"(EPOCH_OFF), "s"(box) : "memory");
  return __builtin_amdgcn_readfirstlane(e);
}
__device__ __forceinline__ int tr_off(int row, int col4) {
  const int ch = col4 >> 1;
  return row * ROWB + (((ch & ~7) | ((ch & 7) ^ swz(row & 15))) << 4) + 8 * (col4 & 1);
}
#define PIN3(a, i) "+v"(a[i]), "+v"(a[i + 1]), "+v"(a[i + 2])

#define DMA1(d, b, off, pol) "global_load_lds_dwordx4 %[" #d "], %[" #b "] offset:" #off pol "\n\t"
__device__ __forceinline__ void issue_rows16(const char* a0, unsigned dst, const unsigned (&d)[3]) {
  unsigned keep;
  asm volatile("s_mov_b32 %[keep], m0\n\ts_mov_b32 m0, %[dst]\n\ts_nop 0\n\t"
               DMA1(d0, a, 0, "") DMA1(d1, a, 1024, "") DMA1(d2, a, 2048, "") DMA1(d0, a, 3072, "")
               "s_add_u32 m0, %[dst], 0x1000\n\ts_nop 0\n\t" DMA1(d1, b, 0, "") DMA1(d2, b, 1024, "") "s_mov_b32 m0, %[keep]"
               : [keep] "=&s"(keep) : [dst] "s"(dst), [a] "s"(a0), [b] "s"(a0 + 4096), [d0] "v"(d[0]), [d1] "v"(d[1]), [d2] "v"(d[2]) : "memory", "scc");
}
#define LD_GATE(n) \
  #n "0:\n\ts_getreg_b32 s42, hwreg(HW_REG_IB_STS)\n\ts_bfe_u32 s43, s42, 0x20016\n\ts_and_b32 s42, s42, 15\n\ts_lshl4_add_u32 s42, s43, s42\n\t" \
  "s_cmp_le_i32 s42, s46\n\ts_cbranch_scc1 " #n "2f\n" \
  #n "1:\n\ts_waitcnt lgkmcnt(0)\n\ts_sub_u32 s45, s40, %[t0]\n\ts_lshl_b32 s45, s45, 4\n\ts_cmp_gt_u32 s42, %[win]\n\ts_cselect_b32 s43, 3200, 0\n\t" \
  "s_memrealtime s[40:41]\n\ts_add_u32 s43, s43, s44\n\ts_cmp_lt_u32 s45, s43\n\ts_cbranch_scc1 " #n "0b\n\t" \
  "s_waitcnt vmcnt(%[win])\n\ts_add_u32 s44, s44, %[p3]\n\ts_add_u32 s46, s46, 5\n\t"
#define LD_PUB(n, a, b, c) \
  #n "2:\n\tv_add_u32 %[pub], 2, %[pub]\n\ts_cmp_ge_u32 s46, 30\n\ts_cbranch_scc1 " #n "4f\n\ts_cmp_ge_u32 s46, 15\n\ts_cbranch_scc1 " #n "3f\n\t" \
  "s_waitcnt vmcnt(" #a ")\n" #n "3:\n\ts_waitcnt vmcnt(" #b ")\n" #n "4:\n\ts_waitcnt vmcnt(" #c ")\n\tds_write_b32 %[af], %[pub]\n\t" \
  "s_sub_u32 s46, s46, 15\n\ts_branch " #n "1b\n"
#define LD1(d, b, off) "global_load_lds_dwordx4 %[" #d "], s[" #b "] offset:" #off " nt\n\t"

using u32x32 = __attribute__((ext_vector_type(32))) unsigned;
using u32x8 = __attribute__((ext_vector_type(8))) unsigned;
using f32x16 = __attribute__((ext_vector_type(16))) float;
#define PV_REQ(P, U, M, C, CA) \
  "v_lshl_add_u32 v150, s44, 3, %[apk]\n\tv_lshl_add_u32 v151, s44, 1, %[apu]\n\tv_add_u32 v152, s44, %[apm]\n\t" \
  "ds_read_b32 v" #C ", v" #CA "\n\tds_read_b128 v[" P "], v150\n\tds_read_b64 v[" M "], v152\n\tds_read_b128 v[" U "], v151\n\t"
#define PV_TR(d0, d1, a, o) "ds_read_b64_tr_b16 v[" #d0 ":" #d1 "], v" #a " offset:" #o "\n\t"
#define PV_FETCH(y0, y0b, a0, a0b, b0, b0b, y1, y1b, a1, a1b, b1, b1b, y2, y2b, a2, a2b, b2, b2b) \
  "v_add_u32 v153, s45, %[ox]\n\tv_add_u32 v154, s45, %[oy]\n\t" \
  PV_TR(a0, a0b, 153, 0) PV_TR(b0, b0b, 153, 6144) PV_TR(a1, a1b, 153, 128) PV_TR(b1, b1b, 153, 6272) PV_TR(a2, a2b, 153, 256) PV_TR(b2, b2b, 153, 6400) \
  PV_TR(y0, y0b, 154, 0) PV_TR(y1, y1b, 154, 128) PV_TR(y2, y2b, 154, 256) \
  "s_add_u32 s45, s45, %[slot]\n\ts_cmp_eq_u32 s45, %[ring]\n\ts_cselect_b32 s45, 0, s45\n\t"
#define PV_FETCH_A PV_FETCH(90, 91, 92, 93, 94, 95, 96, 97, 98, 99, 100, 101, 102, 103, 104, 105, 106, 107)
#define PV_FETCH_B PV_FETCH(108, 109, 110, 111, 112, 113, 114, 115, 116, 117, 118, 119, 120, 121, 122, 123, 124, 125)
#define PV_MF(d, a, p) "v_mfma_f32_16x16x32_bf16 v[" d "], v[" a "], v[" p "], v[" d "]\n\t"
#define PV_MOV(d, s) "v_mov_b32 v" #d ", v" #s "\n\t"
#define PV_WAITF(n, CA)   \
  #n ":\n\tds_read_b32 v155, v" #CA "\n\ts_sub_i32 s47, s47, 1\n\ts_cmp_lt_i32 s47, 0\n\ts_cbranch_scc1 " #n "9f\n\ts_waitcnt lgkmcnt(0)\n\t" \
  "v_cmp_lt_u32 vcc, v155, %[need]\n\ts_and_b64 vcc, vcc, 2\n\ts_cbranch_scc1 " #n "b\n" #n "9:\n\ts_waitcnt lgkmcnt(0)\n\t"
#define PV_STEP(n, P, U, M, m0, m1, C, CA, P2, U2, M2, C2, CA2, X0, X1, X2, Y0, Y1, Y2, MV0, MV1, MV2, HOOK) \
  "s_waitcnt lgkmcnt(10)\n\t" \
  "v_cmp_lt_u32 vcc, v" #C ", %[need]\n\tv_cmp_neq_f32 s[48:49], v" #m0 ", v88\n\tv_cmp_neq_f32 s[50:51], v" #m1 ", v88\n\t" \
  "v_add_u32 v" #CA2 ", 4, v" #CA "\n\ts_or_b64 s[48:49], s[48:49], vcc\n\tv_cmp_eq_u32 s[52:53], v" #CA2 ", %[lim]\n\t" \
  "s_or_b64 s[48:49], s[48:49], s[50:51]\n\ts_cbranch_scc1 " #n "1f\n" \
  #n "0:\n\ts_add_u32 s44, s44, 128\n\ts_cmp_eq_u32 s44, %[pring]\n\ts_cselect_b32 s44, 0, s44\n\t" \
  "v_cndmask_b32 v" #CA2 ", v" #CA2 ", %[base], s[52:53]\n\t" \
  PV_MF("84:87", U, P) PV_MF("72:75", X0, P) PV_MF("76:79", X1, P) MV0 PV_MF("80:83", X2, P) MV1 \
  PV_MF("72:75", Y0, P) MV2 HOOK PV_REQ(P2, U2, M2, C2, CA2) PV_MF("76:79", Y1, P) PV_MF("80:83", Y2, P)
#define PV_REL(CA) \
  "s_mov_b64 exec, 3\n\tds_add_u32 v" #CA ", %[one] offset:64\n\ts_mov_b64 exec, -1\n\tv_add_u32 %[need], 2, %[need]\n\ts_add_u32 s46, s46, 1\n\t"
#define PV_SC1(p) "v_lshlrev_b32 v151, 16, v" #p "\n\tv_and_b32 v152, 0xffff0000, v" #p "\n\tv_mul_f32 v151, v151, v150\n\tv_mul_f32 v152, v152, v150\n\t" \
                  "v_cvt_pk_bf16_f32 v" #p ", v151, v152\n\t"
#define PV_MUL4(a, b, c, d) "v_mul_f32 v" #a ", v" #a ", v150\n\tv_mul_f32 v" #b ", v" #b ", v150\n\tv_mul_f32 v" #c ", v" #c ", v150\n\tv_mul_f32 v" #d ", v" #d ", v150\n\t"
#define PV_SLOW(n, P, U, M, m0, m1, C, CA, p0, p1, p2, p3) \
  #n "1:\n\ts_bitcmp0_b32 vcc_lo, 0\n\ts_cbranch_scc1 " #n "4f\n\ts_cmp_gt_u32 s46, %[tight]\n\ts_cbranch_scc1 " #n "3f\n" \
  #n "2:\n\ts_sleep 1\n\tds_read_b32 v" #C ", v" #CA "\n\ts_sub_i32 s47, s47, 1\n\ts_cmp_lt_i32 s47, 0\n\ts_cbranch_scc1 " #n "3f\n\ts_waitcnt lgkmcnt(0)\n\t" \
  "v_cmp_lt_u32 vcc, v" #C ", %[need]\n\ts_and_b64 vcc, vcc, 1\n\ts_cbranch_scc1 " #n "2b\n" \
  #n "3:\n\t" PV_REQ(P, U, M, C, CA) "s_sub_i32 s47, s47, 1\n\ts_cmp_lt_i32 s47, 0\n\ts_cselect_b32 s54, 0, 1\n\ts_waitcnt lgkmcnt(0)\n\t" \
  "v_cmp_lt_u32 vcc, v" #C ", %[need]\n\tv_cmp_neq_f32 s[48:49], v" #m0 ", v88\n\tv_cmp_neq_f32 s[50:51], v" #m1 ", v88\n\t" \
  "s_and_b32 s54, s54, vcc_lo\n\ts_cbranch_scc1 " #n "3b\n\ts_or_b64 s[48:49], s[48:49], s[50:51]\n\ts_cbranch_scc0 " #n "0b\n\ts_branch " #n "5f\n" \
  #n "4:\n\tv_cmp_neq_f32 s[48:49], v" #m0 ", v88\n\ts_or_b64 s[48:49], s[48:49], s[50:51]\n\ts_cbranch_scc0 " #n "0b\n" \
  #n "5:\n\tv_mov_b32 v150, v88\n\tv_max3_f32 v88, v88, v" #m0 ", v" #m1 "\n\tv_sub_f32 v150, v150, v88\n\tv_exp_f32 v150, v150\n\ts_nop 1\n\t" \
  PV_MUL4(72, 73, 74, 75) PV_MUL4(76, 77, 78, 79) PV_MUL4(80, 81, 82, 83) PV_MUL4(84, 85, 86, 87) \
  "v_sub_f32 v150, v" #m0 ", v88\n\tv_exp_f32 v150, v150\n\ts_nop 1\n\t" PV_SC1(p0) PV_SC1(p1) \
  "v_sub_f32 v150, v" #m1 ", v88\n\tv_exp_f32 v150, v150\n\ts_nop 1\n\t" PV_SC1(p2) PV_SC1(p3) \
  "s_branch " #n "0b\n"
#define PV_W2(a0, a1, b0, b1, c0, c1) \
  "global_load_dwordx4 v[" #a0 ":" #a1 "], v150, %[w2] sc1\n\tglobal_load_dwordx4 v[" #b0 ":" #b1 "], v150, %[w2] offset:1024 sc1\n\t" \
  "global_load_dwordx4 v[" #c0 ":" #c1 "], v150, %[w2] offset:2048 sc1\n\tv_add_u32 v150, 0xc00, v150\n\ts_cbranch_scc1 30b\n\t"

struct SCtx {
  lds_ptr L0;
  unsigned ct, apk, apm, apu, api, amr;
  bool r1, r2;
  int o_rb, o_yb, ev;
  float coef, lov;
  bf16x8 qf[6];
  unsigned tq;
  unsigned fpre;
};
__device__ __forceinline__ void s_logits(const f32x4& T, const f32x4& S, const float* U, const float* rs, float* lg) {
  const float nsq = T[1] * (-1.f / CC);
#pragma unroll
  for (int i = 0; i < 4; ++i) lg[i] = __builtin_fmaf(S[i] + __builtin_fmaf(U[i], nsq, T[i & 2]), rs[i], TOFF);
}
__device__ __forceinline__ void s_get_qk(SCtx& c) {
  poll_ge_sleep(c.ct + C_QI, (unsigned)NE);
#pragma unroll
  for (int j = 0; j < 6; ++j) c.qf[j] = *(LDS_AS(bf16x8)*)(size_t)(c.apk + j * 1024);
  asm volatile("s_mov_b64 exec, 63\n\tds_add_u32 %0, %1\n\ts_mov_b64 exec, -1\n\ts_nop 3" : : "v"(c.amr + (C_CONS - C_MREF)), "v"(1u) : "memory");
}
__device__ __forceinline__ int item_tile(int it) { return it < TPW - NH1 ? it : TPW - NH1 + ((it - (TPW - NH1)) >> 1); }
template <int NB> __device__ __forceinline__ void s_item(SCtx& c, int k, int h, int nx) {
  const int s = k % NSLOT, e = k % NPK;
  const int sb_ = s * SLOT, xb_ = sb_ + h * 6144;
  const unsigned needf = (unsigned)(2 * k + 2);
  unsigned afv = c.ct + C_FULL + (nx % NSLOT) * 4;
  int yba = c.ev * sb_ + c.o_yb, xa = xb_ + c.o_rb;
  unsigned acn = c.ct + C_CONS + e * 4, afr = c.ct + C_FREE + s * 4;
  f32x4 g0 = {EPS * CC, EPS * CC, EPS * CC, EPS * CC};
  asm volatile("" : "+v"(afv), "+v"(yba), "+v"(xa), "+v"(acn), "+v"(afr), "+v"(g0));
  if constexpr (NB == 1) { if (c.fpre < needf) poll_ge(c.ct + C_FULL + s * 4, __builtin_amdgcn_readfirstlane(needf)); __builtin_amdgcn_s_setprio(1); }
  else if (c.fpre < needf) poll_ge_sleep(c.ct + C_FULL + s * 4, needf);
  unsigned cons; float mref;
  asm volatile("ds_read_b32 %1, %3\n\tds_read_b32 %0, %2" : "=&v"(cons), "=&v"(mref) : "v"(acn), "v"(c.amr) : "memory");
#define SB __builtin_amdgcn_sched_barrier(0)
  bf16x8 x[NB][6], yb[6];
  auto rd = [&](int j) {
    yb[j] = *(LDS_AS(bf16x8)*)(c.L0 + ((j & 1) ? (yba ^ 64) : yba) + (j >> 1) * 128); SB;
    x[0][j] = *(LDS_AS(bf16x8)*)(c.L0 + ((j & 1) ? (xa ^ 64) : xa) + (j >> 1) * 128); SB;
  };
  auto rd1 = [&](int j) { x[NB - 1][j] = *(LDS_AS(bf16x8)*)(c.L0 + ((j & 1) ? (xa ^ 64) : xa) + (j >> 1) * 128 + 6144); SB; };
  const f32x4 z = {0.f, 0.f, 0.f, 0.f};
  f32x4 Hy = z, T = z;
  f32x4 S[NB], G[NB], Hx[NB];
  float U[NB][4];
  float lg[4 * NB], rs[4 * NB];
  unsigned pu[2 * NB], pi[NB];
  G[0] = g0; Hx[0] = z;
  SB; rd(0); rd(1); rd(2); rd(3);
#pragma unroll
  for (int j = 0; j < 6; ++j) {
    Hx[0] = mfma(x[0][j], yb[j], Hx[0]); SB;
    G[0] = mfma(x[0][j], x[0][j], G[0]); SB;
    if (j < 2) rd(4 + j);
    else if (NB == 2 && j < 4) { rd1(3 * j - 6); rd1(3 * j - 5); rd1(3 * j - 4); }
    if (j == (NB == 2 ? 3 : 1)) lds_add(afr, 2u * NB);
    Hy = mfma(yb[j], yb[j], Hy); SB;
    if (j == 2) __builtin_amdgcn_s_setprio(1);
  }
#define DPPF(x, ctl) __int_as_float(__builtin_amdgcn_update_dpp(0, __float_as_int(x), ctl, 0xf, 0xf, true))
#define PIN2(a, b) asm volatile("" : "+v"(a), "+v"(b))
  auto stat = [&](int b, auto&& mm) {
    float t[4], a[4];
    SB; mm(0); mm(1); mm(2); SB;
#pragma unroll
    for (int i = 0; i < 4; ++i) { t[i] = __builtin_fmaf(c.coef, Hx[b][i], Hy[i & 2]); asm volatile("" : "+v"(t[i])); }
    SB; mm(3); SB;
#pragma unroll
    for (int i = 0; i < 4; ++i) { U[b][i] = DPPF(t[i], 0xF5); asm volatile("" : "+v"(U[b][i])); }
    SB; mm(4); SB;
    a[0] = G[b][0] + t[0]; a[2] = G[b][2] + t[2];
    PIN2(a[0], a[2]);
    a[1] = G[b][1] + DPPF(t[1], 0xA0); a[3] = G[b][3] + DPPF(t[3], 0xA0);
    PIN2(a[1], a[3]);
    SB; mm(5); SB;
    float u0 = c.r1 ? U[b][1] : U[b][0], u1 = c.r1 ? U[b][3] : U[b][2], a0 = c.r1 ? a[1] : a[0], a1 = c.r1 ? a[3] : a[2];
    PIN2(u0, u1); PIN2(a0, a1);
    SB; mm(6); SB;
    u0 = c.r2 ? u1 : u0; a0 = c.r2 ? a1 : a0;
    PIN2(u0, a0);
    a0 = __builtin_fmaf(u0 * (-1.f / CC), u0, a0);
    SB; mm(7); SB;
    a0 = __builtin_amdgcn_fmed3f(a0, EPS * CC, INFINITY);
    int d = __float_as_int(__builtin_amdgcn_rsqf(a0));
    unsigned h0 = cvtpk(U[b][0], U[b][1]), h1 = cvtpk(U[b][2], U[b][3]);
    SB; mm(8); SB;
    a0 *= __int_as_float(d);
    asm volatile("" : "+v"(a0));
    d = __builtin_amdgcn_update_dpp(d, d, 0x12C, 0x2, 0xf, false);
    float l0 = bflo(h0), l1 = bfhi(h0);
    SB; mm(9); SB;
    d = __builtin_amdgcn_update_dpp(d, d, 0x128, 0x4, 0xf, false);
    float l2 = bflo(h1), l3 = bfhi(h1);
    SB; mm(10); SB;
    d = __builtin_amdgcn_update_dpp(d, d, 0x124, 0x8, 0xf, false);
    l0 = __builtin_fmaf(l0, c.lov, U[b][0]);
    asm volatile("" : "+v"(l0));
    l1 = __builtin_fmaf(l1, c.lov, U[b][1]);
    SB; mm(11); SB;
    rs[4 * b + 0] = DPPF(__int_as_float(d), 0x150);
    rs[4 * b + 1] = DPPF(__int_as_float(d), 0x151);
    rs[4 * b + 2] = DPPF(__int_as_float(d), 0x152);
    rs[4 * b + 3] = DPPF(__int_as_float(d), 0x153);
    l2 = __builtin_fmaf(l2, c.lov, U[b][2]);
    asm volatile("" : "+v"(l2));
    l3 = __builtin_fmaf(l3, c.lov, U[b][3]);
    pu[2 * b] = cvtpk(l0, l1); pu[2 * b + 1] = cvtpk(l2, l3);
    PIN2(pu[2 * b], pu[2 * b + 1]);
    pi[b] = cvtpk(a0, a0 - rbf(a0));
    asm volatile("" : "+v"(pi[b]));
    SB;
  };
  asm volatile("" : "+v"(cons), "+v"(mref));
  S[0] = z; S[NB - 1] = z;
  if constexpr (NB == 2) {
    G[1] = g0; Hx[1] = z;
    stat(0, [&](int j) { if (j & 1) G[1] = mfma(x[1][j >> 1], x[1][j >> 1], G[1]); else Hx[1] = mfma(x[1][j >> 1], yb[j >> 1], Hx[1]); });
    if (__builtin_expect(k < NEARLY, 0)) {
      s_get_qk(c);
      asm volatile("ds_read_b32 %1, %3\n\tds_read_b32 %0, %2\n\ts_waitcnt lgkmcnt(0)" : "=&v"(cons), "=&v"(mref) : "v"(acn), "v"(c.amr) : "memory");
    }
    auto ms = [&](int j) {
      if (j < 6) T = mfma(yb[j], c.qf[j], T);
      else S[j / 12] = mfma(x[j / 12][j % 6], c.qf[j % 6], S[j / 12]);
    };
    stat(1, [&](int j) { if (j < 6) { ms(2 * j); ms(2 * j + 1); } else ms(j + 6); });
    s_logits(T, S[0], U[0], rs, lg);
  } else {
    stat(0, [&](int j) { if (j < 6) T = mfma(yb[j], c.qf[j], T); else S[0] = mfma(x[0][j % 6], c.qf[j % 6], S[0]); });
  }
  s_logits(T, S[NB - 1], U[NB - 1], rs + 4 * (NB - 1), lg + 4 * (NB - 1));
  unsigned fv;
  asm volatile("ds_read_b32 %0, %1" : "=v"(fv) : "v"(afv) : "memory");
  float mt = fmaxf(fmaxf(lg[0], lg[1]), fmaxf(lg[2], lg[3]));
  if constexpr (NB == 2) mt = fmaxf(mt, fmaxf(fmaxf(lg[4], lg[5]), fmaxf(lg[6], lg[7])));
  if (__builtin_expect(__any(mt > mref + THR), 0)) {
    mref = fmaxf(mref, 32.f * __builtin_rintf(xrow16_max(mt) * (1.f / 32.f)));
    asm volatile("ds_max_f32 %0, %1" : : "v"(c.amr), "v"(mref) : "memory");
  }
  u32x2 pk[NB];
#pragma unroll
  for (int b = 0; b < NB; ++b) {
    float pp[4];
#pragma unroll
    for (int i = 0; i < 4; ++i) {
      pp[i] = __builtin_amdgcn_exp2f(lg[4 * b + i] - mref) * rs[4 * b + i];
    }
    pk[b] = u32x2{cvtpk(pp[0], pp[1]), cvtpk(pp[2], pp[3])};
  }
  const unsigned needc = (unsigned)(NE * (k / NPK) + NS);
  if (__builtin_expect((unsigned)__builtin_amdgcn_readfirstlane(cons) < needc, 0)) poll_ge_sleep(c.ct + C_CONS + e * 4, needc);
  if constexpr (NB == 2) {
    asm volatile("ds_write_b128 %0, %1\n\tds_write2_b32 %2, %3, %3 offset0:0 offset1:1\n\ts_mov_b64 exec, %8\n\tds_write_b128 %6, %7\n\t"
                 "s_mov_b64 exec, %9\n\tds_write_b16 %10, %11 offset:32\n\tds_write_b16_d16_hi %10, %11 offset:48\n\t"
                 "ds_write_b16 %10, %12 offset:40\n\tds_write_b16_d16_hi %10, %12 offset:56\n\t"
                 "s_mov_b64 exec, 1\n\tds_add_u32 %4, %5\n\ts_mov_b64 exec, -1"
                 : : "v"(c.apk + e * 1024), "v"(u32x4{pk[0][0], pk[0][1], pk[NB - 1][0], pk[NB - 1][1]}), "v"(c.apm + e * 128), "v"(mref),
                     "v"(c.ct + C_PFL + e * 4), "v"(2u * NPK), "v"(c.apu + e * 256), "v"(u32x4{pu[0], pu[1], pu[2 * NB - 2], pu[2 * NB - 1]}),
                     "s"(0x0003000300030003ull), "s"(0xF0000F0000F0000Full), "v"(c.api + e * 256), "v"(pi[0]), "v"(pi[NB - 1]) : "memory");
  } else {
    asm volatile("ds_write_b64 %0, %1\n\tds_write_b32 %2, %3\n\ts_mov_b64 exec, %8\n\tds_write_b64 %6, %7\n\t"
                 "s_mov_b64 exec, %9\n\tds_write_b16 %10, %11 offset:32\n\tds_write_b16_d16_hi %10, %11 offset:48\n\t"
                 "s_mov_b64 exec, 1\n\tds_add_u32 %4, %5\n\ts_mov_b64 exec, -1"
                 : : "v"(c.apk + e * 1024 + h * 8), "v"(pk[0]), "v"(c.apm + e * 128 + h * 4), "v"(mref),
                     "v"(c.ct + C_PFL + e * 4), "v"((unsigned)NPK), "v"(c.apu + e * 256 + h * 8), "v"(u32x2{pu[0], pu[1]}),
                     "s"(0x0003000300030003ull), "s"(0xF0000F0000F0000Full), "v"(c.api + e * 256 + h * 8), "v"(pi[0]) : "memory");
  }
  asm volatile("s_waitcnt lgkmcnt(0)" : "+v"(fv) :: "memory");
  c.fpre = __builtin_amdgcn_readfirstlane(fv);
  __builtin_amdgcn_s_setprio(0);
}

struct W2 { u32x32 a, b; u32x8 c; };
__device__ __forceinline__ bf16x8 w2_frag(const W2& w, int i) {
  return __builtin_bit_cast(bf16x8, i < 8 ? u32x4{w.a[4 * i], w.a[4 * i + 1], w.a[4 * i + 2], w.a[4 * i + 3]}
                                          : (i < 16 ? u32x4{w.b[4 * i - 32], w.b[4 * i - 31], w.b[4 * i - 30], w.b[4 * i - 29]}
                                                    : u32x4{w.c[4 * i - 64], w.c[4 * i - 63], w.c[4 * i - 62], w.c[4 * i - 61]}));
}

using u32x16 = __attribute__((ext_vector_type(16))) unsigned;
typedef __attribute__((address_space(1))) unsigned short gus;
extern "C" __global__ __launch_bounds__(768, 3) void dino_main(const void*, const void*, const void*, const void*, const void*,
                                                               const void*, const void*, void*, void*) {
  u32x16 ka; u32x2 kb; unsigned long long tent64;
  __shared__ __attribute__((aligned(16))) char lds[LDS_BYTES];
  const int wave = __builtin_amdgcn_readfirstlane((int)threadIdx.x >> 6);
  const bool helper = blockIdx.x < 8;
  lds_ptr L0 = (lds_ptr)lds;
  const unsigned lbase = (unsigned)(size_t)L0;
  const unsigned ct = lbase + CT_OFF;
#define KARG(v, i) ((char*)(((unsigned long long)v[2 * (i) + 1] << 32) | v[2 * (i)]))
#define q KARG(ka, 0)
#define f0 KARG(ka, 1)
#define f1 KARG(ka, 2)
#define wq KARG(ka, 3)
#define wk KARG(ka, 4)
#define wv KARG(ka, 5)
#define wo KARG(ka, 6)
#define box KARG(ka, 7)
#define tent ((unsigned)tent64)
#define LANE_VARS int lane; asm volatile("v_mbcnt_lo_u32_b32 %0, -1, 0\n\tv_mbcnt_hi_u32_b32 %0, -1, %0" : "=v"(lane)); \
  const int r = lane & 15, g = lane >> 4; (void)r; (void)g;
#define DOFF_VARS unsigned doff[3];   \
  _Pragma("unroll") for (int i = 0; i < 3; ++i) doff[i] = (unsigned)(lane ^ ((swz_tbl(i) >> ((lane >> 3) * 4)) & 7)) << 4;
#define BARRIER() do { asm volatile("s_waitcnt lgkmcnt(0)" ::: "memory"); __builtin_amdgcn_s_barrier(); asm volatile("" ::: "memory"); } while (0)

  {
    unsigned doff[3];
    static_assert(swz_tbl(0) == swz_tbl(3) && swz_tbl(1) == swz_tbl(4) && swz_tbl(2) == swz_tbl(5), "");
    unsigned l16, db;
#define PD(d, b, off, pol) "global_load_lds_dwordx4 %[" #d "], s[" #b "] offset:" #off pol "\n\t"
    asm volatile("s_load_dwordx16 s[64:79], %[kp], 0x0\n\ts_load_dwordx2 s[80:81], %[kp], 0x40\n\ts_memrealtime s[82:83]\n\t"
                 "v_mbcnt_lo_u32_b32 %[l16], -1, 0\n\tv_mbcnt_hi_u32_b32 %[l16], -1, %[l16]\n\t"
                 "s_lshr_b32 s84, %[b], 3\n\ts_and_b32 s85, %[b], 7\n\t"
                 "v_lshrrev_b32 %[db], 1, %[l16]\n\tv_and_b32 %[db], -4, %[db]\n\t"
                 "s_mov_b32 s86, %[t0]\n\ts_mov_b32 s87, %[t1]\n\ts_mov_b32 s88, %[t2]\n\t"
                 "v_bfe_u32 %[d0], s86, %[db], 3\n\tv_bfe_u32 %[d1], s87, %[db], 3\n\tv_bfe_u32 %[d2], s88, %[db], 3\n\t"
                 "v_xor_b32 %[d0], %[d0], %[l16]\n\tv_xor_b32 %[d1], %[d1], %[l16]\n\tv_xor_b32 %[d2], %[d2], %[l16]\n\t"
                 "v_lshlrev_b32 %[d0], 4, %[d0]\n\tv_lshlrev_b32 %[d1], 4, %[d1]\n\tv_lshlrev_b32 %[d2], 4, %[d2]\n\t"
                 "v_lshlrev_b32 %[l16], 4, %[l16]\n\t"
                 "s_cmp_eq_u32 s84, 0\n\ts_cbranch_scc1 4f\n\t"
                 "s_cmp_lg_u32 %[w], 0\n\ts_cbranch_scc1 7f\n\t"
                 "s_setprio 3\n\tv_lshlrev_b32 %[db], 3, %[l16]\n\ts_mov_b32 m0, %[junk]\n\ts_lshl_b32 s89, s84, 13\n\t"
                 "s_cmp_gt_u32 s84, 3\n\ts_cbranch_scc1 2f\n\t"
                 "s_getpc_b64 s[90:91]\n1:\n\ts_sub_u32 s86, s89, 8192\n\ts_min_u32 s86, s86, dino_end-1b-8192\n\t"
                 "s_add_u32 s90, s90, s86\n\ts_addc_u32 s91, s91, 0\n\tglobal_load_lds_dword %[db], s[90:91]\n"
                 "2:\n\ts_mul_i32 s86, s85, 98304\n\ts_mul_i32 s87, s84, 3072\n\ts_sub_u32 s87, s87, 3072\n\t"
                 "s_add_u32 s88, s86, s87\n\ts_lshl1_add_u32 s86, s86, s87\n\ts_lshl_b32 s86, s86, 1\n\t"
                 "s_mul_i32 s87, s85, 12288\n\ts_lshr_b32 s90, s89, 1\n\ts_add_u32 s87, s87, s90\n\ts_sub_u32 s87, s87, 13 * 4096\n\t"
                 "s_sub_u32 s89, s89, 4 * 8192\n\ts_cmp_gt_u32 s85, 5\n\ts_cselect_b32 s89, -1, s89\n\ts_cmp_ge_u32 s89, 11 * 8192\n\t"
                 "s_waitcnt lgkmcnt(0)\n\ts_cbranch_scc1 3f\n\t"
                 "s_cmp_lt_u32 s89, 9 * 8192\n\ts_cselect_b64 s[90:91], s[70:71], s[72:73]\n\ts_cselect_b32 s89, s89, s87\n\t"
                 "s_add_u32 s90, s90, s89\n\ts_addc_u32 s91, s91, 0\n\tglobal_load_lds_dword %[db], s[90:91]\n"
                 "3:\n\ts_mov_b32 m0, 0x3000\n\t"
                 "s_add_u32 s90, s68, s88\n\ts_addc_u32 s91, s69, 0\n\t"
                 "s_add_u32 s92, s66, s86\n\ts_addc_u32 s93, s67, 0\n\t"
                 PD(d0, 90:91, 0, " nt") PD(d1, 90:91, 1024, " nt") PD(d2, 90:91, 2048, " nt")
                 "s_mov_b32 m0, 0\n\ts_add_u32 s94, s92, 0x1000\n\ts_addc_u32 s95, s93, 0\n\t"
                 PD(d0, 92:93, 0, " nt") PD(d1, 92:93, 1024, " nt") PD(d2, 92:93, 2048, " nt") PD(d0, 92:93, 3072, " nt")
                 "s_movk_i32 m0, 0x1000\n\ts_add_u32 s96, s92, 0x2f800\n\ts_addc_u32 s97, s93, 0\n\t"
                 PD(d1, 94:95, 0, " nt") PD(d2, 94:95, 1024, " nt") PD(d0, 96:97, 2048, " nt") PD(d1, 96:97, 3072, " nt")
                 "s_movk_i32 m0, 0x2000\n\ts_add_u32 s98, s92, 0x30800\n\ts_addc_u32 s99, s93, 0\n\t"
                 PD(d2, 98:99, 0, " nt") PD(d0, 98:99, 1024, " nt") PD(d1, 98:99, 2048, " nt") PD(d2, 98:99, 3072, " nt")
                 "s_sub_u32 s89, s84, 15\n\ts_cmp_gt_u32 s85, 5\n\ts_cselect_b32 s89, -1, s89\n\ts_cmp_ge_u32 s89, 11\n\ts_cbranch_scc1 9f\n\t"
                 "s_mov_b32 m0, %[junk]\n\ts_sub_u32 s87, s87, 11 * 4096\n\ts_lshl_b32 s89, s89, 13\n\ts_cmp_lt_u32 s89, 9 * 8192\n\t"
                 "s_cselect_b64 s[90:91], s[76:77], s[74:75]\n\ts_cselect_b32 s89, s89, s87\n\t"
                 "s_add_u32 s90, s90, s89\n\ts_addc_u32 s91, s91, 0\n\tglobal_load_lds_dword %[db], s[90:91]\n\t"
                 "s_branch 9f\n"
                 "7:\n\ts_barrier\n\ts_branch 8f\n"
                 "4:\n\ts_cmp_gt_u32 %[b], 5\n\ts_cbranch_scc1 8f\n\ts_cmp_gt_u32 %[w], 9\n\ts_cbranch_scc1 8f\n\t"
                 "s_mov_b32 m0, %[img]\n\ts_mul_i32 s86, %[w], 9216\n\ts_and_b32 s88, %[w], 1\n\ts_lshl1_add_u32 s89, %[b], s88\n\t"
                 "s_mul_i32 s88, s88, 6144\n\ts_mul_i32 s89, s89, 6144\n\ts_add_u32 s88, s88, %[hr]\n\t"
                 "s_cmp_lg_u32 %[w], 0\n\ts_waitcnt lgkmcnt(0)\n\ts_cbranch_scc1 5f\n\t"
                 PD(l16, 64:65, 0, "") PD(l16, 64:65, 1024, "") PD(l16, 64:65, 2048, "") PD(l16, 64:65, 3072, "")
                 "s_mov_b32 m0, %[img] + 4096\n\ts_add_u32 s90, s64, 0x1000\n\ts_addc_u32 s91, s65, 0\n\t"
                 PD(l16, 90:91, 0, "") PD(l16, 90:91, 1024, "")
                 "\n5:\n\ts_cmp_gt_u32 %[w], 7\n\ts_cselect_b64 s[92:93], s[72:73], s[70:71]\n\ts_cselect_b32 m0, s88, s86\n\ts_cselect_b32 s86, s89, s86\n\t"
                 "s_cselect_b32 s87, 2, 3\n\ts_add_u32 s92, s92, s86\n\ts_addc_u32 s93, s93, 0\n"
                 "6:\n\ts_sub_u32 s87, s87, 1\n\t"
                 PD(d0, 92:93, 0, "") PD(d1, 92:93, 1024, "") PD(d2, 92:93, 2048, "")
                 "s_add_u32 m0, m0, 3072\n\ts_add_u32 s92, s92, 3072\n\ts_addc_u32 s93, s93, 0\n\ts_cmp_lg_u32 s87, 0\n\ts_cbranch_scc1 6b"
                 "\n8:\n\ts_waitcnt lgkmcnt(0)\n9:"
                 : "={s[64:79]}"(ka), "={s[80:81]}"(kb), "={s[82:83]}"(tent64),
                   [d0] "=&v"(doff[0]), [d1] "=&v"(doff[1]), [d2] "=&v"(doff[2]), [l16] "=&v"(l16), [db] "=&v"(db)
                 : [kp] "s"(__builtin_amdgcn_kernarg_segment_ptr()), [b] "s"(blockIdx.x), [w] "s"(wave),
                   [t0] "i"(swz_tbl(0)), [t1] "i"(swz_tbl(1)), [t2] "i"(swz_tbl(2)), [junk] "i"(JUNK), [img] "i"(IMG_A), [hr] "i"(H_R)
                 : "memory", "scc", "vcc", "s84", "s85", "s86", "s87", "s88", "s89", "s90", "s91", "s92", "s93", "s94", "s95", "s96", "s97", "s98", "s99");
  }
  const unsigned t0 = (unsigned)__builtin_amdgcn_s_memrealtime() - T0BACK;

  if (__builtin_expect(blockIdx.x < 6, 0)) {
    LANE_VARS
    DOFF_VARS
    const int j = blockIdx.x;
    TNOW(ta0);
    TNOW(ta1);
    const int qq = (lane >> 2) & 3, p4 = lane & 3, e4 = wave & 3;
    int ra = H_R + r * ROWB + ((g ^ swz(r)) << 4);
    bf16x8 a[6], b[6], w[18];
    auto rows = [&](int t, bf16x8* d) {
#pragma unroll
      for (int kk = 0; kk < 6; ++kk) d[kk] = *(LDS_AS(bf16x8)*)(L0 + t * 6144 + ((kk & 1) ? (ra ^ 64) : ra) + (kk >> 1) * 128);
    };
#define TSET(v)
    if (wave < 4) {
      int o[3];
#pragma unroll
      for (int t = 0; t < 3; ++t) o[t] = tr_off(4 * g + qq, 4 * (3 * e4 + t) + p4);
      unsigned qo = lbase + H_Q + 3 * e4 * 512 + ((g >> 1) * 16 + r) * 16 + (g & 1) * 8;
      asm volatile("" : PIN3(o, 0), "+v"(qo));
      if (wave == 0) {
        wait_vm<9>();
        u32x2 qv[12];
#pragma unroll
        for (int i = 0; i < 12; ++i) qv[i] = *(LDS_AS(u32x2)*)(L0 + IMG_A + r * ROWB + i * 32 + g * 8);
        const bf16x8 ones = __builtin_bit_cast(bf16x8, u32x4{0x3f803f80u, 0x3f803f80u, 0x3f803f80u, 0x3f803f80u});
        f32x4 sm = {0.f, 0.f, 0.f, 0.f};
#pragma unroll
        for (int i = 0; i < 6; ++i) sm = mfma(ones, __builtin_bit_cast(bf16x8, u32x4{qv[2 * i][0], qv[2 * i][1], qv[2 * i + 1][0], qv[2 * i + 1][1]}), sm);
        f32x2 x[24];
#pragma unroll
        for (int i = 0; i < 24; ++i) x[i] = f32x2{bflo(qv[i >> 1][i & 1]), bfhi(qv[i >> 1][i & 1])};
        const float mean = sm[0] * (1.f / CC);
#pragma unroll
        for (int i = 0; i < 6; ++i) {
          u32x4 t;
#pragma unroll
          for (int d = 0; d < 4; ++d) { x[4 * i + d] -= mean; t[d] = cvtpk(x[4 * i + d][0], x[4 * i + d][1]); }
          img_st(L0, IMG_A, i, lane, __builtin_bit_cast(bf16x8, t));
        }
        TSET(ta2);
        f32x2 v2 = x[0] * x[0], v3 = x[1] * x[1];
#pragma unroll
        for (int i = 2; i < 24; i += 2) { v2 += x[i] * x[i]; v3 += x[i + 1] * x[i + 1]; }
        v2 += v3;
        *(LDS_AS(float)*)(L0 + CT_OFF + r * 4) = LOG2E * __builtin_amdgcn_rsqf(xrow16_sum(v2[0] + v2[1]) * (1.f / CC) + EPS);
      }
      wait_vm<0>();
      TSET(ta3);
      BARRIER();
      TSET(th1);
#pragma unroll
      for (int kk = 0; kk < 6; ++kk) {
        b[kk] = img_ld(L0, IMG_A, kk, lane);
        w[kk] = mk8(tr_read(L0 + kk * 12288 + o[0]), tr_read(L0 + kk * 12288 + 6144 + o[0]));
        __builtin_amdgcn_sched_barrier(0);
      }
#pragma unroll
      for (int i = 6; i < 18; ++i) {
        w[i] = mk8(tr_read(L0 + (i % 6) * 12288 + o[i / 6]), tr_read(L0 + (i % 6) * 12288 + 6144 + o[i / 6]));
        __builtin_amdgcn_sched_barrier(0);
      }
#pragma unroll
      for (int t = 0; t < 3; ++t) {
        f32x4 D = {0.f, 0.f, 0.f, 0.f};
#pragma unroll
        for (int kk = 0; kk < 6; ++kk) D = mfma(w[6 * t + kk], b[kk], D);
        *(LDS_AS(u32x2)*)(size_t)(qo + t * 512) = u32x2{cvtpk(D[0], D[1]), cvtpk(D[2], D[3])};
      }
      TSET(ta4);
      BARRIER();
    } else if (wave == 8 || wave == 9) {
      unsigned so = lane * 16 + (2 * j + (wave & 1)) * 1024;
      __builtin_amdgcn_s_barrier();
      const unsigned epoch = epoch_load(box);
      TSET(ta3);
      rows(wave & 1, a);
      float sc = *(LDS_AS(float)*)(L0 + CT_OFF + r * 4);
      unsigned ta = line_tag(epoch, 8), tb = line_tag(epoch, 9);
      asm volatile("" : PIN3(a, 0), PIN3(a, 3), "+v"(ta), "+v"(tb), "+v"(so), "+v"(sc));
      TSET(ta4);
      BARRIER();
      TSET(ta5);
#pragma unroll
      for (int kk = 0; kk < 6; ++kk) b[kk] = img_ld(L0, H_Q, kk, lane);
      __builtin_amdgcn_sched_barrier(0);
      f32x4 D = {0.f, 0.f, 0.f, 0.f};
#pragma unroll
      for (int kk = 0; kk < 6; ++kk) D = mfma(a[kk], b[kk], D);
      D *= sc;
      const unsigned p0 = cvtpk(D[0], D[1]), p1 = cvtpk(D[2], D[3]);
      asm volatile("global_store_dwordx4 %0, %1, %2 sc1\n\ts_nop 1" : : "v"(so), "v"(u32x4{p0, p1, p0 ^ p1 ^ ta, tb}), "s"(box + QK_OFF) : "memory");
      TSET(th2);
    } else {
      if (wave == 10) epoch_load(box);
      asm volatile("s_waitcnt vmcnt(0)\n\ts_barrier\n\ts_barrier" ::: "memory");
    }
    BARRIER();
    issue_rows16(wo + wave * 6144, lbase + wave * 6144, doff);
    if (wave < 2) issue_rows16(wv + (2 * j + wave) * 6144, lbase + H_R + wave * 6144, doff);
    const unsigned epoch = epoch_load(box);
    BARRIER();
    TSET(ta3);
    {
      const int o0 = tr_off(8 * g + qq, 4 * wave + p4), o1 = tr_off(8 * g + 4 + qq, 4 * wave + p4);
#pragma unroll
      for (int kk = 0; kk < 6; ++kk) b[kk] = mk8(tr_read(L0 + kk * 12288 + o0), tr_read(L0 + kk * 12288 + o1));
      f32x4 D[2];
#pragma unroll
      for (int h = 0; h < 2; ++h) {
        rows(h, a);
        D[h] = f32x4{0.f, 0.f, 0.f, 0.f};
#pragma unroll
        for (int kk = 0; kk < 6; ++kk) D[h] = mfma(a[kk], b[kk], D[h]);
      }
      const int e = wave / 3;
      asm volatile("global_store_dwordx4 %0, %1, %2 sc1\n\ts_waitcnt vmcnt(0)"
                   : : "v"(lane * 16 + (e * 15 + 3 * j + wave) * 1024), "v"(pack8(D[0] * SQRT_C, D[1] * SQRT_C)), "s"(box + W2_OFF) : "memory");
    }
    BARRIER();
    if (wave == 11)
      asm volatile("s_mov_b64 exec, 1\n\tglobal_store_dwordx2 %0, %1, %2 sc1\n\ts_mov_b64 exec, -1\n\ts_nop 1"
                   : : "v"(j * 128), "v"(u32x2{line_tag(epoch, 10), line_tag(epoch, 11)}), "s"(box + W2F_OFF) : "memory");
    TPUT(60 + wave * 5, ta0); TPUT(61 + wave * 5, ta1); TPUT(62 + wave * 5, ta2); TPUT(63 + wave * 5, ta3); TPUT(64 + wave * 5, ta4);
  }

  int rslot = -1;
  if (__builtin_expect(wave == 0, 1)) {
    LANE_VARS
    DOFF_VARS
    {
      const unsigned o = lane * 8;
      unsigned z = (o >= C_MREF && o < C_MREF + 64) ? 0xff800000u : 0u;
      if (helper && o >= C_CONS && o < C_MREF) z = NE * ((TPW - 1) / NPK);
      u32x2 zz = {z, z};
      if (o >= C_PFL && o < C_CONS) zz = u32x2{(o - C_PFL) / 2 + 2, (o - C_PFL) / 2 + 4} + (helper ? 2u * NPK * ((TPW - 1) / NPK) : 0u);
      *(LDS_AS(u32x2)*)(L0 + CT_OFF + o) = zz;
      *(LDS_AS(u32x2)*)(L0 + ONES_OFF + (lane < 48 ? o : 0)) = u32x2{0x3f803f80u, 0x3f803f80u};
    }
    if (helper) BARRIER();
    const unsigned afull = ct + C_FULL + r * 4;
    unsigned ts = 0;
    {
      unsigned pub = helper ? 2 * (TPW - 1) : 0, pre = 0, afr = ct + C_FREE + (helper ? ((TPW - 1) % NSLOT) * 4 : 4);
      asm volatile("s_mov_b32 s44, %[due0]\n\ts_mov_b32 s46, %[y0]\n\ts_mov_b32 s47, %[cap]\n\ts_mov_b32 s48, %[i0]\n\ts_mov_b32 s49, %[s0]\n\t"
                   "s_mov_b32 s50, %[n0]\n\ts_mov_b32 s51, 0\n\ts_memrealtime s[40:41]\n"
                   "1:\n\ts_lshr_b32 s52, s48, 5\n\ts_add_u32 s53, s52, %[b]\n\ts_and_b32 s53, s53, 7\n\ts_lshl3_add_u32 s53, s52, s53\n\t"
                   "s_mul_i32 s52, s52, 5\n\ts_add_u32 s52, s52, s48\n\ts_and_b32 s52, s52, 31\n\ts_mul_i32 s53, s53, 98304\n\ts_mul_i32 s52, s52, 3072\n\t"
                   "s_add_u32 s54, s53, s52\n\ts_lshl1_add_u32 s53, s53, s52\n\ts_lshl_b32 s53, s53, 1\n\t"
                   "s_add_u32 s54, s54, s68\n\ts_addc_u32 s55, s69, 0\n\ts_add_u32 s52, s53, s66\n\ts_addc_u32 s53, s67, 0\n\t"
                   "s_add_u32 s56, s52, 0x800\n\ts_addc_u32 s57, s53, 0\n\ts_add_u32 s58, s52, 0x30000\n\ts_addc_u32 s59, s53, 0\n\t"
                   "s_add_u32 s60, s52, 0x31000\n\ts_addc_u32 s61, s53, 0\n\ts_add_u32 m0, s49, 0x3000\n\t"
                   "s_waitcnt lgkmcnt(0)\n\tv_cmp_gt_u32 vcc, s51, %[pre]\n\ts_add_u32 s48, s48, 31\n\ts_cbranch_vccnz 5f\n"
                   LD_GATE(1)
                   LD1(d0, 54:55, 0) LD1(d1, 54:55, 1024) LD1(d2, 54:55, 2048) "s_mov_b32 m0, s49\n\ts_cmp_eq_u32 s50, %[nb]\n\t" LD1(d0, 52:53, 0) LD1(d1, 52:53, 1024)
                   "s_cbranch_scc1 8f\n"
                   "2:\n\ts_add_u32 m0, s49, 0x800\n"
                   LD_GATE(2)
                   LD1(d2, 56:57, 0) LD1(d0, 56:57, 1024) LD1(d1, 56:57, 2048) LD1(d2, 56:57, 3072) "s_add_u32 m0, s49, 0x1800\n\tv_add_u32 %[afr], 4, %[afr]\n\t" LD1(d0, 58:59, 0)
                   LD_GATE(3)
                   LD1(d1, 58:59, 1024) LD1(d2, 58:59, 2048) LD1(d0, 58:59, 3072) "s_add_u32 m0, s49, 0x2800\n\ts_add_u32 s49, s49, %[slot]\n\t" LD1(d1, 60:61, 0) LD1(d2, 60:61, 1024)
                   "s_cmp_eq_u32 s49, %[ring]\n\ts_cbranch_scc1 7f\n"
                   "3:\n\tds_read_b32 %[pre], %[afr]\n\ts_cmp_gt_i32 s46, 30\n\ts_cbranch_scc1 6f\n"
                   "4:\n\ts_sub_u32 s50, s50, 1\n\ts_cmp_lg_u32 s50, 0\n\ts_cbranch_scc1 1b\n\ts_branch 9f\n"
                   LD_PUB(1, 0, 15, 30) LD_PUB(2, 5, 20, 35) LD_PUB(3, 10, 25, 40)
                   "5:\n\tds_read_b32 %[pre], %[afr]\n\ts_waitcnt lgkmcnt(0)\n\tv_cmp_gt_u32 vcc, s51, %[pre]\n\ts_cbranch_vccz 10b\n\t"
                   "s_waitcnt vmcnt(0)\n\ts_sub_u32 s42, %[tpw], s50\n\ts_lshl_b32 s42, s42, 1\n\tv_mov_b32 %[pub], s42\n\tds_write_b32 %[af], %[pub]\n\ts_mov_b32 s46, -15\n"
                   "50:\n\tds_read_b32 %[pre], %[afr]\n\ts_sub_i32 s47, s47, 1\n\ts_cmp_lt_i32 s47, 0\n\ts_cbranch_scc1 10b\n\ts_waitcnt lgkmcnt(0)\n\t"
                   "v_cmp_gt_u32 vcc, s51, %[pre]\n\ts_cbranch_vccnz 50b\n\ts_branch 10b\n"
                   "6:\n\ts_waitcnt vmcnt(45)\n\tv_add_u32 %[pub], 2, %[pub]\n\tds_write_b32 %[af], %[pub]\n\ts_sub_u32 s46, s46, 15\n\ts_branch 4b\n"
                   "7:\n\ts_mov_b32 s49, 0\n\ts_add_u32 s51, s51, 8\n\tv_subrev_u32 %[afr], %[frb], %[afr]\n\ts_branch 3b\n"
                   "8:\n\ts_barrier\n\ts_branch 2b\n"
                   "9:\n\ts_cmp_lt_i32 s46, 30\n\ts_cbranch_scc1 91f\n\ts_waitcnt vmcnt(30)\n\tv_add_u32 %[pub], 2, %[pub]\n\tds_write_b32 %[af], %[pub]\n"
                   "91:\n\ts_waitcnt lgkmcnt(0)"
                   : [pub] "+v"(pub), [pre] "+v"(pre), [afr] "+v"(afr), [ts] "+v"(ts)
                   : "{s[64:79]}"(ka), [t0] "s"(t0), [b] "s"(blockIdx.x), [af] "v"(afull), [d0] "v"(doff[0]), [d1] "v"(doff[1]), [d2] "v"(doff[2]),
                     [due0] "s"(helper ? 0 : 3 * (PACE16 / 3)), [y0] "s"(helper ? -15 : 0), [i0] "s"(helper ? 1023 : (blockIdx.x >> 3) + 30),
                     [s0] "s"(helper ? ((TPW - 1) % NSLOT) * SLOT : SLOT), [n0] "s"(helper ? 1 : TPW - 1),
                     [cap] "i"(SPIN_CAP), [win] "i"(PWIN), [p3] "i"(PACE16 / 3), [slot] "i"(SLOT), [ring] "i"(NSLOT * SLOT), [frb] "i"(NSLOT * 4),
                     [tpw] "i"(TPW), [nb] "i"(TPW - 1)
                   : "memory", "scc", "vcc", "s40", "s41", "s42", "s43", "s44", "s45", "s46", "s47", "s48", "s49", "s50", "s51", "s52", "s53", "s54", "s55",
                     "s56", "s57", "s58", "s59", "s60", "s61");
    }
    lds_st32(ct + C_ISS, 1u);
#define CLR(b) "global_store_dwordx4 %0, %1, %" #b " sc1\n\t"
    asm volatile(CLR(2) CLR(3) CLR(4) CLR(5) CLR(6) CLR(7) CLR(8) CLR(9) "s_nop 1"
                 : : "v"(((lane >> 3) * NWG + blockIdx.x) * 128 + (lane & 7) * 16), "v"(u32x4{0u, 0u, 0u, 0u}), "s"(box), "s"(box + 1 * 262144), "s"(box + 2 * 262144),
                     "s"(box + 3 * 262144), "s"(box + 4 * 262144), "s"(box + 5 * 262144), "s"(box + 6 * 262144), "s"(box + 7 * 262144) : "memory");
    wait_vm<23>();
    lds_st32(afull, (unsigned)(2 * TPW - 2));
    wait_vm<8>();
    lds_st32(afull, ~0u);
    TNOW(tl);
    __builtin_amdgcn_s_setprio(0);
    wait_vm<0>();
    TNOW(tlx);
    BARRIER();
    rslot = 7;
  } else {
    if (helper) __builtin_amdgcn_s_barrier();
    if (wave <= NE) {
      LANE_VARS
      const int ej = wave - 1;
      const int qq = (lane >> 2) & 3, p4 = lane & 3;
      const unsigned apk = lbase + IMG_A + lane * 16;
      const unsigned epoch = epoch_load(box);
      lds_st32(ct + C_EP, epoch);
      __builtin_amdgcn_s_setprio(2);
      {
        const unsigned ta = line_tag(epoch, 8), tb = line_tag(epoch, 9);
        while ((unsigned)__builtin_amdgcn_s_memrealtime() - tent < QKWAIT) __builtin_amdgcn_s_sleep(2);
        u32x4 v[3];
#pragma unroll 1
        for (int sp = 0; sp < GSPIN_CAP; ++sp) {
          asm volatile("global_load_dwordx4 %0, %3, %4 sc1\n\tglobal_load_dwordx4 %1, %3, %4 offset:1024 sc1\n\tglobal_load_dwordx4 %2, %3, %4 offset:2048 sc1\n\t"
                       "s_waitcnt vmcnt(0)" : "=&v"(v[0]), "=&v"(v[1]), "=&v"(v[2]) : "v"(lane * 16), "s"(box + QK_OFF + ej * 3072) : "memory");
          unsigned bad = 0;
#pragma unroll
          for (int t = 0; t < 3; ++t) bad |= (v[t][0] ^ v[t][1] ^ v[t][2] ^ ta) | (v[t][3] ^ tb);
          if (!__any(bad != 0u)) break;
        }
        TSET(tqa);
#pragma unroll
        for (int t = 0; t < 3; ++t)
          *(LDS_AS(u32x2)*)(L0 + IMG_A + (3 * ej + t) * 512 + ((g >> 1) * 16 + r) * 16 + (g & 1) * 8) = u32x2{v[t][0], v[t][1]};
        asm volatile("" ::: "memory");
        lds_add(ct + C_QI, 1u);
      }
      W2 w;
      f32x16 acc = {0.f, 0.f, 0.f, 0.f, 0.f, 0.f, 0.f, 0.f, 0.f, 0.f, 0.f, 0.f, 0.f, 0.f, 0.f, 0.f};
      float macc = -INFINITY;
      {
        static_assert((TPW & 1) == 1 && (W2K0 & 1) == 0 && C_CONS == C_PFL + 64 && C_FREE == C_FULL + 64, "");
        const int k0 = helper ? TPW - 1 : 0;
        unsigned need = 2 * k0 + 2;
        asm volatile("s_mov_b32 s46, %[k0]\n\ts_mov_b32 s45, %[s0]\n\ts_mov_b32 s44, %[e0]\n\ts_mov_b32 s47, %[cap]\n\t"
                     "v_mov_b32 v148, %[ca]\n\t" PV_WAITF(4, 148) PV_FETCH_A
                     "s_mov_b64 exec, 2\n\tds_add_u32 v148, %[one] offset:64\n\ts_mov_b64 exec, -2\n\t"
                     "v_add_u32 %[need], 2, %[need]\n\tv_add_u32 v148, 4, v148\n\ts_mov_b64 exec, -1\n\t"
                     PV_WAITF(5, 148)
                     "v_mov_b32 v149, v148\n\ts_mov_b64 exec, -2\n\tv_add_u32 %[need], 2, %[need]\n\tv_add_u32 v148, 4, v148\n\t"
                     "s_mov_b64 exec, 1\n\tv_add_u32 %[need], %[npk2], %[need]\n\ts_mov_b64 exec, -1\n\t"
                     PV_REQ("126:129", "130:133", "134:135", 136, 148) PV_FETCH_B
                     "s_mov_b64 exec, 2\n\tds_add_u32 v149, %[one] offset:64\n\ts_mov_b64 exec, -1\n\t"
                     "s_cmp_lg_u32 s46, 0\n\ts_cbranch_scc1 32f\n"
                     "6:\n\t"
                     PV_STEP(1, "126:129", "130:133", "134:135", 134, 135, 136, 148, "138:141", "142:145", "146:147", 137, 149,
                             "92:95", "98:101", "104:107", "90:93", "96:99", "102:105",
                             PV_MOV(92, 90) PV_MOV(93, 91), PV_MOV(98, 96) PV_MOV(99, 97), PV_MOV(104, 102) PV_MOV(105, 103),
                             "s_cmp_eq_u32 s46, %[last]\n\ts_cbranch_scc1 99f\n\t")
                     "s_cbranch_vccnz 7f\n"
                     "16:\n\t" PV_FETCH_A PV_REL(148)
                     "s_sub_u32 s54, s46, %[w0]\n\ts_cmp_le_u32 s54, %[wl]\n\ts_cbranch_scc1 31f\n"
                     "30:\n\t"
                     PV_STEP(2, "138:141", "142:145", "146:147", 146, 147, 137, 149, "126:129", "130:133", "134:135", 136, 148,
                             "110:113", "116:119", "122:125", "108:111", "114:117", "120:123",
                             PV_MOV(110, 108) PV_MOV(111, 109), PV_MOV(116, 114) PV_MOV(117, 115), PV_MOV(122, 120) PV_MOV(123, 121), "")
                     "s_cbranch_vccnz 8f\n"
                     "26:\n\t" PV_FETCH_B PV_REL(149)
                     "s_cmp_lt_u32 s46, %[last]\n\ts_cbranch_scc1 6b\n\ts_setprio 3\n\ts_branch 6b\n"
                     PV_WAITF(7, 148) "s_branch 16b\n"
                     PV_WAITF(8, 149) "s_branch 26b\n"
                     PV_SLOW(1, "126:129", "130:133", "134:135", 134, 135, 136, 148, 126, 127, 128, 129)
                     PV_SLOW(2, "138:141", "142:145", "146:147", 146, 147, 137, 149, 138, 139, 140, 141)
                     "31:\n\ts_cmp_lg_u32 s54, 0\n\ts_cbranch_scc1 34f\n"
                     "32:\n\tv_mbcnt_lo_u32_b32 v152, 31, 0\n\tv_lshlrev_b32 v152, 7, v152\n\ts_mov_b32 s55, %[gcap]\n"
                     "33:\n\tglobal_load_dwordx2 v[150:151], v152, %[fl] sc1\n\ts_sub_u32 s55, s55, 1\n\ts_cselect_b32 s56, 0, -1\n\ts_waitcnt vmcnt(0) lgkmcnt(0)\n\t"
                     "v_cmp_ne_u64 s[48:49], %[tag], v[150:151]\n\ts_sleep 2\n\ts_and_b32 s48, s48, s56\n\ts_cbranch_scc1 33b\n\t"
                     "v_mov_b32 v150, %[apk]\n\ts_cmp_lg_u32 s46, %[last]\n\ts_cbranch_scc1 30b\n\ts_branch 38f\n"
                     "34:\n\ts_cmp_lt_u32 s54, %[wl]\n\ts_cbranch_scc1 36f\n\tv_mov_b32 v151, %[iss]\n"
                     "35:\n\tds_read_b32 v150, v151\n\ts_sub_i32 s47, s47, 1\n\ts_cmp_lt_i32 s47, 0\n\ts_cselect_b32 s55, 0, 1\n\ts_waitcnt lgkmcnt(0)\n\t"
                     "v_readfirstlane_b32 s56, v150\n\ts_cmp_lt_u32 s56, s55\n\ts_cbranch_scc1 35b\n"
                     "36:\n\ts_mul_i32 s55, s54, 1536\n\ts_sub_u32 s55, s55, 3072\n\tv_add_u32 v150, s55, %[apk]\n\ts_getpc_b64 s[48:49]\n"
                     "37:\n\ts_mul_i32 s55, s54, 18\n\ts_add_u32 s55, s55, 38f-37b-36\n\ts_add_u32 s48, s48, s55\n\ts_addc_u32 s49, s49, 0\n\t"
                     "s_cmp_lt_u32 s54, %[wl]\n\ts_setpc_b64 s[48:49]\n"
                     "38:\n\t" PV_W2(0, 3, 4, 7, 8, 11) PV_W2(12, 15, 16, 19, 20, 23) PV_W2(24, 27, 28, 31, 32, 35) PV_W2(36, 39, 40, 43, 44, 47)
                     PV_W2(48, 51, 52, 55, 56, 59) PV_W2(60, 63, 64, 67, 68, 71)
                     "s_cmp_lg_u32 s46, %[last]\n\ts_cbranch_scc1 30b\n\ts_waitcnt lgkmcnt(0)\n\ts_branch 6b\n"
                     "99:\n\t" PV_MF("76:79", "96:99", "126:129") PV_MF("80:83", "102:105", "126:129") "s_waitcnt lgkmcnt(0)\n\ts_nop 6"
                     : "=&{v[0:31]}"(w.a), "=&{v[32:63]}"(w.b), "=&{v[64:71]}"(w.c), "+{v[72:87]}"(acc), "+{v88}"(macc), [need] "+v"(need)
                     : [apk] "v"(apk), [apm] "v"(lbase + PKM + r * 8), [apu] "v"(lbase + PKU + g * 64 + (r & 3) * 16),
                       [ox] "v"(lbase + tr_off(4 * g + qq, 4 * ej + p4)), [oy] "v"(lbase + 32 * ROWB + tr_off(2 * g + (qq >> 1), 4 * ej + p4)),
                       [ca] "v"(ct + (lane == 0 ? C_PFL + (k0 % NPK) * 4 : C_FULL + (k0 % NSLOT) * 4)),
                       [lim] "v"(ct + (lane == 0 ? C_PFL + NPK * 4 : C_FULL + NSLOT * 4)), [base] "v"(ct + (lane == 0 ? C_PFL : C_FULL)), [one] "v"(1u),
                       [k0] "s"(k0), [s0] "s"(helper ? ((TPW - 1) % NSLOT) * SLOT : 0), [e0] "s"(helper ? ((TPW - 1) % NPK) * 128 : 0),
                       [w2] "s"(box + W2_OFF + ej * 18432 - (lbase + IMG_A)), [fl] "s"(box + W2F_OFF),
                       [tag] "s"(u32x2{line_tag(epoch, 10), line_tag(epoch, 11)}), [gcap] "i"(GSPIN_CAP), [iss] "i"(CT_OFF + C_ISS), [cap] "i"(SPIN_CAP), [slot] "i"(SLOT), [ring] "i"(NSLOT * SLOT), [pring] "i"(NPK * 128),
                       [npk2] "i"(2 * NPK), [last] "i"(TPW - 1), [tight] "i"(TPW - 5), [w0] "i"(W2K0 + 1), [wl] "i"(2 * W2MID + 2)
                     : "memory", "scc", "vcc", "s44", "s45", "s46", "s47", "s48", "s49", "s50", "s51", "s52", "s53", "s54", "s55", "s56", "s57",
                       "v90", "v91", "v92", "v93", "v94", "v95", "v96", "v97", "v98", "v99", "v100", "v101", "v102", "v103", "v104", "v105", "v106", "v107",
                       "v108", "v109", "v110", "v111", "v112", "v113", "v114", "v115", "v116", "v117", "v118", "v119", "v120", "v121", "v122", "v123",
                       "v124", "v125", "v126", "v127", "v128", "v129", "v130", "v131", "v132", "v133", "v134", "v135", "v136", "v137", "v138", "v139",
                       "v140", "v141", "v142", "v143", "v144", "v145", "v146", "v147", "v148", "v149", "v150", "v151", "v152", "v153", "v154", "v155");
      }
      {
        TNOW(tpv);
        const float L = acc[14] + acc[15], inv = __builtin_amdgcn_rcpf(L);
        const float nm = (acc[12] + acc[13]) * (-1.f / CC) * inv;
        const unsigned ai = apk + (A_IMG - IMG_A);
#pragma unroll
        for (int t = 0; t < 3; ++t)
          *(LDS_AS(u32x2)*)(size_t)(ai + (ej >> 1) * 1024 + (ej & 1) * 8 + t * 2048) =
              u32x2{cvtpk(__builtin_fmaf(acc[4 * t], inv, nm), __builtin_fmaf(acc[4 * t + 1], inv, nm)),
                    cvtpk(__builtin_fmaf(acc[4 * t + 2], inv, nm), __builtin_fmaf(acc[4 * t + 3], inv, nm))};
        TNOW(tp7);
        BARRIER();
        TNOW(tx1);
        LANE_VARS
        bf16x8 b[6];
#pragma unroll
        for (int kk = 0; kk < 6; ++kk) b[kk] = *(LDS_AS(bf16x8)*)(size_t)(ai + kk * 1024);
        __builtin_amdgcn_sched_barrier(0);
        *(LDS_AS(float)*)(L0 + CT_OFF + C_LW + r * 4) = (macc - TOFF) + __builtin_amdgcn_logf(L);
        asm volatile("" ::: "memory");
        const unsigned al = ct + C_LW + (4 * g + (r & 1)) * 4;
        unsigned ck0 = *(LDS_AS(unsigned)*)(size_t)al, ck1 = *(LDS_AS(unsigned)*)(size_t)(al + 8);
        asm volatile("s_waitcnt vmcnt(0)" : "+{v[0:31]}"(w.a), "+{v[32:63]}"(w.b), "+{v[64:71]}"(w.c) :: "memory");
        TNOW(tw2);
        __builtin_amdgcn_sched_barrier(0);
        f32x4 D[3];
#pragma unroll
        for (int t = 0; t < 3; ++t) D[t] = f32x4{0.f, 0.f, 0.f, 0.f};
#pragma unroll
        for (int kk = 0; kk < 6; ++kk)
#pragma unroll
          for (int t = 0; t < 3; ++t) D[t] = mfma(b[kk], w2_frag(w, 3 * kk + t), D[t]);
        const unsigned tag = line_tag(epoch, lane & 7);
        const unsigned soff = (unsigned)(((ej * 16 + (lane >> 3)) * NWG + blockIdx.x) * 128 + (lane & 7) * 16);
        ck0 ^= tag; ck1 ^= tag;
        u32x4 l0 = {cvtpk(D[0][0], D[0][1]), cvtpk(D[1][0], D[1][1]), cvtpk(D[2][0], D[2][1]), 0u};
        u32x4 l1 = {cvtpk(D[0][2], D[0][3]), cvtpk(D[1][2], D[1][3]), cvtpk(D[2][2], D[2][3]), 0u};
        l0[3] = __builtin_amdgcn_bitop3_b32(l0[0], l0[1], l0[2] ^ ck0, 0x96);
        l1[3] = __builtin_amdgcn_bitop3_b32(l1[0], l1[1], l1[2] ^ ck1, 0x96);
        asm volatile("global_store_dwordx4 %0, %1, %3 sc1\n\tglobal_store_dwordx4 %0, %2, %4 sc1\n\ts_nop 1"
                     : : "v"(soff), "v"(l0), "v"(l1), "s"(box), "s"(box + 8 * NWG * 128) : "memory");
        lds_add(ct + C_OUT, 1u);
      }
    } else {
      LANE_VARS
      const int c = wave - 1 - NE;
      SCtx sc;
      sc.L0 = L0; sc.ct = ct; sc.apk = lbase + IMG_A + lane * 16; sc.apm = lbase + PKM + r * 8;
      sc.apu = lbase + PKU + g * 64 + (r & 1) * 16; sc.api = lbase + PKU + g * 64 + (r & 3) * 2; sc.amr = ct + C_MREF + r * 4;
      sc.o_rb = r * ROWB + ((g ^ swz(r)) << 4);
      sc.r1 = r & 1; sc.r2 = r & 2;
      sc.ev = (r & 1) ^ 1;
      sc.o_yb = (r & 1) ? ONES_OFF + (g << 4) : (32 + (r >> 1)) * ROWB + ((g ^ swz(r >> 1)) << 4);
      sc.coef = (r & 1) ? 1.f : 2.f;
      sc.lov = (r & 1) ? -1.f : 0.f;
      sc.fpre = 0;
      TNOW(tq0);
      asm volatile("" : "=v"(sc.qf[0]), "=v"(sc.qf[1]), "=v"(sc.qf[2]), "=v"(sc.qf[3]), "=v"(sc.qf[4]), "=v"(sc.qf[5]));
      if (helper || c >= NEARLY) s_get_qk(sc);
#pragma unroll 1
      for (int it = helper ? NITEM - 2 + c : c; it < NITEM; it += NS) {
        if (it >= TPW - NH1) s_item<1>(sc, item_tile(it), (it - (TPW - NH1)) & 1, 0);
        else s_item<2>(sc, it, 0, item_tile(it + NS));
      }
      TNOW(tse);
      BARRIER();
      rslot = c;
    }
  }

  if (rslot >= 0 && ((blockIdx.x >> 3) & 3) == 1) {
    LANE_VARS
    __builtin_amdgcn_s_setprio(0);
    const int rid = ((blockIdx.x >> 5) << 3) | (blockIdx.x & 7);
    const int qa = ((rid >> 1) & 3) * 4 + ((rid >> 3) & 1) * 2;
    const char* src = box + (size_t)(rid * NWG + rslot * 32) * 128;
    const unsigned voff = lane * 16;
    u32x4 v[4];
    const int e = lane & 7;
    const int oidx = (qa + (e & 1)) * CC + 48 * (rid >> 4) + 16 * (e < 6 ? e >> 1 : 0) + 8 * (rid & 1) + (lane >> 3);
    const unsigned short qv = ((const gus*)q)[oidx];
    u32x4 am;
#pragma unroll
    for (int d = 0; d < 4; ++d) am[d] = (e >> 1) == d ? ((e & 1) ? 0xffff0000u : 0x0000ffffu) : 0u;
    const unsigned m4 = (lane & 4) ? 0xffffffffu : 0u;
    const unsigned epoch = *(LDS_AS(unsigned)*)(L0 + CT_OFF + C_EP);
    const unsigned tag = line_tag(epoch, e);
    float ref;
    unsigned arf = ct + C_LW + (qa + (lane & 1)) * 4;
    unsigned long long mk[4] = {~0ull, ~0ull, ~0ull, ~0ull};
    unsigned lw[4];
    f32x4 pa[4];
    float dm;
    unsigned awr = lbase + RED_OFF + (rslot * 2 + (r >> 3)) * 256 + (r & 7) * 32 + (g & 1) * 16, ard = lbase + RED_OFF + lane * 4;
    float qf = __uint_as_float(((unsigned)qv) << 16);
    asm volatile("" : "=v"(v[0]), "=v"(v[1]), "=v"(v[2]), "=v"(v[3]), "+v"(am[0]), "+v"(am[1]), "+v"(am[2]), "+v"(am[3]), "+v"(arf),
                 "=v"(lw[0]), "=v"(lw[1]), "=v"(lw[2]), "=v"(lw[3]), "=v"(pa[0]), "=v"(pa[1]), "=v"(pa[2]), "=v"(pa[3]), "+v"(awr), "+v"(ard), "+v"(qf));
    auto part = [&](int i) {
      const float w = __builtin_amdgcn_exp2f(__uint_as_float(lw[i]) - ref);
      const unsigned wb = cvtpk(w, w);
      pa[i] = mfma(__builtin_bit_cast(bf16x8, u32x4{wb & am[0], wb & am[1], wb & am[2], wb & am[3]}),
                   __builtin_bit_cast(bf16x8, u32x4{v[i][0], v[i][1], v[i][2], 0x3f803f80u}), f32x4{0.f, 0.f, 0.f, 0.f});
    };
    poll_ge_sleep(ct + C_OUT, 4u);
    ref = *(LDS_AS(float)*)(size_t)arf;
    __builtin_amdgcn_s_sleep(8);
#pragma unroll 1
    for (int sp = 0; sp < GSPIN_CAP; ++sp) {
      asm volatile("s_mov_b64 exec, %6\n\tglobal_load_dwordx4 %0, %4, %5 sc1\n\t"
                   "s_mov_b64 exec, %7\n\tglobal_load_dwordx4 %1, %4, %5 offset:1024 sc1\n\t"
                   "s_mov_b64 exec, %8\n\tglobal_load_dwordx4 %2, %4, %5 offset:2048 sc1\n\t"
                   "s_mov_b64 exec, %9\n\tglobal_load_dwordx4 %3, %4, %5 offset:3072 sc1\n\t"
                   "s_mov_b64 exec, -1\n\ts_waitcnt vmcnt(0)"
                   : "+v"(v[0]), "+v"(v[1]), "+v"(v[2]), "+v"(v[3])
                   : "v"(voff), "s"(src), "s"(mk[0]), "s"(mk[1]), "s"(mk[2]), "s"(mk[3]) : "memory");
#pragma unroll
      for (int i = 0; i < 4; ++i) {
        if (mk[i] != 0ull) {
          lw[i] = __builtin_amdgcn_bitop3_b32(__builtin_amdgcn_bitop3_b32(v[i][0], v[i][1], v[i][2], 0x96), v[i][3], tag, 0x96);
          const unsigned x2 = lw[i] ^ (unsigned)__builtin_amdgcn_update_dpp(0, (int)lw[i], 0x4E, 0xf, 0xf, false);
          const unsigned x4 = lw[i] ^ (unsigned)__builtin_amdgcn_update_dpp(0, (int)lw[i], 0x114, 0xf, 0xf, false);
          mk[i] = __builtin_amdgcn_ballot_w64((x2 | (x4 & m4)) != 0u);
          part(i);
        }
      }
      if ((mk[0] | mk[1] | mk[2] | mk[3]) == 0ull) break;
    }
    dm = fmaxf(fmaxf(__uint_as_float(lw[0]), __uint_as_float(lw[1])), fmaxf(__uint_as_float(lw[2]), __uint_as_float(lw[3])));
    unsigned arrive = 1u;
    if (__builtin_expect(__any(dm > ref + 64.f), 0)) {
      float m = fmaxf(dm, ref);
      m = fmaxf(m, __int_as_float(__builtin_amdgcn_update_dpp(0, __float_as_int(m), 0x128, 0xf, 0xf, false)));
      ref = xrow16_max(m);
      arrive = 0x101u;
#pragma unroll
      for (int i = 0; i < 4; ++i) part(i);
    }
    if (lane < 2) *(LDS_AS(float)*)(L0 + RED_OFF + 4096 + rslot * 8 + lane * 4) = ref;
    const f32x4 acc = (pa[0] + pa[1]) + (pa[2] + pa[3]);
    unsigned arr;
    f32x2 x[8];
    asm volatile("s_mov_b64 exec, %13\n\tds_write_b128 %9, %10\n\ts_mov_b64 exec, 1\n\tds_add_rtn_u32 %8, %11, %12\n\ts_mov_b64 exec, -1\n\t"
                 "ds_read2st64_b32 %0, %14 offset1:1\n\tds_read2st64_b32 %1, %14 offset0:2 offset1:3\n\t"
                 "ds_read2st64_b32 %2, %14 offset0:4 offset1:5\n\tds_read2st64_b32 %3, %14 offset0:6 offset1:7\n\t"
                 "ds_read2st64_b32 %4, %14 offset0:8 offset1:9\n\tds_read2st64_b32 %5, %14 offset0:10 offset1:11\n\t"
                 "ds_read2st64_b32 %6, %14 offset0:12 offset1:13\n\tds_read2st64_b32 %7, %14 offset0:14 offset1:15\n\t"
                 "s_waitcnt lgkmcnt(0)"
                 : "=&v"(x[0]), "=&v"(x[1]), "=&v"(x[2]), "=&v"(x[3]), "=&v"(x[4]), "=&v"(x[5]), "=&v"(x[6]), "=&v"(x[7]), "=&v"(arr)
                 : "v"(awr), "v"(acc), "v"(ct + C_RED), "v"(arrive), "s"(0xff00ff0000ff00ffull), "v"(ard) : "memory");
    arr = __builtin_amdgcn_readfirstlane(arr) + arrive;
    if (__builtin_expect(arr > 8u, 0)) {
      float rf[8], M = -INFINITY;
#pragma unroll
      for (int i = 0; i < 8; ++i) { rf[i] = *(LDS_AS(float)*)(L0 + RED_OFF + 4096 + i * 8 + (lane & 1) * 4); M = fmaxf(M, rf[i]); }
#pragma unroll
      for (int i = 0; i < 8; ++i) x[i] *= __builtin_amdgcn_exp2f(rf[i] - M);
    }
    const f32x2 t2 = ((x[0] + x[1]) + (x[2] + x[3])) + ((x[4] + x[5]) + (x[6] + x[7]));
    const float t = t2[0] + t2[1];
    const float da = __int_as_float(__builtin_amdgcn_readlane(__float_as_int(t), 6)), db = __int_as_float(__builtin_amdgcn_readlane(__float_as_int(t), 7));
    const float res = qf + rbf(t * __builtin_amdgcn_rcpf((lane & 1) ? db : da));
    unsigned o16 = cvtpk(res, res);
    asm volatile("" : "+v"(o16));
    if ((arr & 0xffu) == 8u) {
      if (e < 6) ((gus*)KARG(kb, 0))[oidx] = (unsigned short)o16;
      TPUT(7, NOW);
      if (rid == 0 && lane == 0) __hip_atomic_store((__attribute__((address_space(1))) unsigned*)(box + EPOCH_OFF), epoch + 1u, __ATOMIC_RELAXED, __HIP_MEMORY_SCOPE_AGENT);
    }
  }
}

#undef q
#undef f0
#undef f1
#undef wq
#undef wk
#undef wv
#undef wo
#undef box
extern "C" __global__ void dino_end() {}

extern "C" void kernel_launch(void* const* d_in, const int* in_sizes, int n_in,
                              void* d_out, int out_size, void* d_ws, size_t ws_size,
                              hipStream_t stream) {
  dino_main<<<NWG, 768, 0, stream>>>((const unsigned short*)d_in[0], (const char*)d_in[1], (const char*)d_in[2], (const char*)d_in[3],
                                     (const char*)d_in[4], (const char*)d_in[5], (const char*)d_in[6], (char*)d_ws,
                                     (unsigned short*)d_out);
}
```

```cpp
#include <hip/hip_runtime.h>
#include <hip/hip_bf16.h>
#include <stdint.h>

#define PROFOFF (380ll << 20)
#define TNOW(v) do {} while (0)
#define TPUT(id, v) do {} while (0)

using bf16x8  = __attribute__((ext_vector_type(8))) short;
using f32x4   = __attribute__((ext_vector_type(4))) float;
using f32x2   = __attribute__((ext_vector_type(2))) float;
using u32x4   = __attribute__((ext_vector_type(4))) unsigned;
using u32x2   = __attribute__((ext_vector_type(2))) unsigned;
using v4i16_t = __attribute__((ext_vector_type(4))) short;
typedef __attribute__((address_space(3))) char* lds_ptr;
#define LDS_AS(T) __attribute__((address_space(3))) T

constexpr int CC = 192, IMW = 512;
constexpr int NWG = 256, NS = 7, NE = 4;
constexpr int TPW = 33;
constexpr int ROWB = 384;
constexpr int SLOT = 40 * ROWB;
constexpr int NSLOT = 10;
constexpr int BOX_BYTES = 64 * NWG * 128, EPOCH_OFF = BOX_BYTES, QK_OFF = EPOCH_OFF + 4096, W2F_OFF = QK_OFF + 12288, W2_OFF = W2F_OFF + 1024;
constexpr int GSPIN_CAP = 1 << 16;
#define PACE16 777
#define QKWAIT 300
#define NEARLY 4
#define PRECLR 4
#define RSLEEP 24
#define HDUE 93
#define PACES 810
#define PK1 14
#define AG 100
#define AHI 22
#define ADL 60
#define AMG 200
#define NORELAY
#define HMAP 0x4321085
#define HB 1
#define WSOFF ((ws_size - (4u << 20)) & ~(size_t)0x1fffff)
#define EPM 0
#define RSTART 1880
#define RMODE 1
#define CLRK 17
#define X1N (PRECLR == 5 ? 11u : 5u)
#define RPOL " sc1"
#define SPOL " sc1"
#define RGAP 0
#define RDRY 0
#define RWAIT 76
#define RMIN 30
#define PROBE 0xffffffffffffffffull
#define T0BACK 4
#define W2MID 6
#define W2K (TPW - 7 - 2 * W2MID)
constexpr int W2K0 = W2K;
#define NH1 3
constexpr int NPK = 6;
constexpr float EPS = 1e-5f;
constexpr float SQRT_C = 13.856406460551018f;
constexpr float LOG2E = 1.4426950408889634f;
constexpr float THR = 20.0f;
constexpr float TOFF = 192.0f;
constexpr int SPIN_CAP = 1 << 22;

constexpr int IMG_A = NSLOT * SLOT;
constexpr int CT_OFF = IMG_A + 6144;
constexpr int PKM = CT_OFF + 512, PKU = PKM + NPK * 128;
constexpr int ONES_OFF = PKU + NPK * 256;
constexpr int JUNK = ONES_OFF + ROWB;
constexpr int LDS_BYTES = JUNK + 256;
constexpr int H_R = 192 * ROWB, H_Q = H_R + 32 * ROWB, H_C = H_Q + 6144;
#define PWIN 35
static_assert(LDS_BYTES <= 163840 && (PKU & 15) == 0 && (ONES_OFF & 127) == 0, "lds");
constexpr int C_FULL = 0, C_FREE = 64, C_EP = 128, C_RED = 144, C_ISS = 152, C_X1 = 156, C_QI = 160, C_PFL = 192, C_CONS = 256,
              C_MREF = 320, C_LW = 384;

typedef __bf16 bf16x2_t __attribute__((ext_vector_type(2)));
__device__ __forceinline__ unsigned cvtpk(float lo, float hi) {
  return __builtin_bit_cast(unsigned, __builtin_convertvector(f32x2{lo, hi}, bf16x2_t));
}
__device__ __forceinline__ float rbf(float x) { return __uint_as_float(cvtpk(x, x) << 16); }
__device__ __forceinline__ float bflo(unsigned p) { return __uint_as_float(p << 16); }
__device__ __forceinline__ float bfhi(unsigned p) { return __uint_as_float(p & 0xffff0000u); }
__device__ __forceinline__ bf16x8 pack8(const f32x4& a, const f32x4& b) {
  return __builtin_bit_cast(bf16x8, u32x4{cvtpk(a[0], a[1]), cvtpk(a[2], a[3]), cvtpk(b[0], b[1]), cvtpk(b[2], b[3])});
}
__device__ __forceinline__ float xrow16_max(float x) {
  auto s = __builtin_amdgcn_permlane16_swap(__float_as_uint(x), __float_as_uint(x), false, false);
  x = fmaxf(__uint_as_float(s[0]), __uint_as_float(s[1]));
  auto t = __builtin_amdgcn_permlane32_swap(__float_as_uint(x), __float_as_uint(x), false, false);
  return fmaxf(__uint_as_float(t[0]), __uint_as_float(t[1]));
}
__device__ __forceinline__ float xrow16_sum(float x) {
  auto s = __builtin_amdgcn_permlane16_swap(__float_as_uint(x), __float_as_uint(x), false, false);
  x = __uint_as_float(s[0]) + __uint_as_float(s[1]);
  auto t = __builtin_amdgcn_permlane32_swap(__float_as_uint(x), __float_as_uint(x), false, false);
  return __uint_as_float(t[0]) + __uint_as_float(t[1]);
}
__device__ __forceinline__ v4i16_t tr_read(lds_ptr p) {
  return __builtin_amdgcn_ds_read_tr16_b64_v4i16((LDS_AS(v4i16_t)*)p);
}
__device__ __forceinline__ f32x4 mfma(bf16x8 a, bf16x8 b, f32x4 c) {
  return __builtin_amdgcn_mfma_f32_16x16x32_bf16(a, b, c, 0, 0, 0);
}
__device__ __forceinline__ int swz(int p) { return (p & 6) | ((p >> 2) & 1); }
constexpr unsigned swz_tbl(int i) {
  unsigned t = 0;
  for (int j = 0; j < 8; ++j) { const int p = (8 * i + j) / 3; t |= (unsigned)((p & 6) | ((p >> 2) & 1)) << (4 * j); }
  return t;
}
__device__ __forceinline__ bf16x8 mk8(v4i16_t a, v4i16_t b) { return bf16x8{a[0], a[1], a[2], a[3], b[0], b[1], b[2], b[3]}; }

__device__ __forceinline__ void lds_st32(unsigned addr, unsigned v) {
  asm volatile("ds_write_b32 %0, %1" : : "v"(addr), "v"(v) : "memory");
}
__device__ __forceinline__ unsigned lds_ld32(unsigned addr) {
  unsigned v;
  asm volatile("ds_read_b32 %0, %1\n\ts_waitcnt lgkmcnt(0)" : "=v"(v) : "v"(addr) : "memory");
  return __builtin_amdgcn_readfirstlane(v);
}
__device__ __forceinline__ void lds_add(unsigned addr, unsigned n) {
  asm volatile("s_mov_b64 exec, 1\n\tds_add_u32 %0, %1\n\ts_mov_b64 exec, -1\n\ts_nop 3" : : "v"(addr), "v"(n) : "memory");
}
__device__ __forceinline__ void poll_ge(unsigned addr, unsigned need) {
  unsigned v, s, n2, cnt = SPIN_CAP;
  asm volatile("1:\n\tds_read_b32 %0, %4\n\ts_sub_u32 %3, %3, 1\n\ts_cselect_b32 %2, 0, %5\n\ts_waitcnt lgkmcnt(0)\n\t"
               "v_readfirstlane_b32 %1, %0\n\ts_cmp_lt_u32 %1, %2\n\ts_cbranch_scc1 1b"
               : "=&v"(v), "=&s"(s), "=&s"(n2), "+s"(cnt) : "v"(addr), "s"(need) : "memory", "scc");
}
__device__ __forceinline__ void poll_ge_sleep(unsigned addr, unsigned need) {
  need = __builtin_amdgcn_readfirstlane(need);
  unsigned v, s, n2, cnt = SPIN_CAP;
  asm volatile("s_branch 2f\n1:\n\ts_sleep 1\n2:\n\tds_read_b32 %0, %4\n\ts_sub_u32 %3, %3, 1\n\ts_cselect_b32 %2, 0, %5\n\ts_waitcnt lgkmcnt(0)\n\t"
               "v_readfirstlane_b32 %1, %0\n\ts_cmp_lt_u32 %1, %2\n\ts_cbranch_scc1 1b"
               : "=&v"(v), "=&s"(s), "=&s"(n2), "+s"(cnt) : "v"(addr), "s"(need) : "memory", "scc");
}
__device__ __forceinline__ bf16x8 img_ld(lds_ptr L0, int off, int kk, int lane) { return *(LDS_AS(bf16x8)*)(L0 + off + kk * 1024 + lane * 16); }
__device__ __forceinline__ void img_st(lds_ptr L0, int off, int kk, int lane, bf16x8 v) { *(LDS_AS(bf16x8)*)(L0 + off + kk * 1024 + lane * 16) = v; }

template <int N> __device__ __forceinline__ void wait_vm() { asm volatile("s_waitcnt vmcnt(%0)" :: "i"(N) : "memory"); }
constexpr int A_IMG = ((TPW + 1 - NPK) % NSLOT) * SLOT, RED_OFF = ((TPW - NPK) % NSLOT) * SLOT;
__device__ __forceinline__ unsigned line_tag(unsigned epoch, unsigned i) {
  unsigned x = (epoch + 1u + i * 0x9E3779B9u) * 0x85EBCA6Bu;
  x ^= x >> 15; x *= 0xC2B2AE35u;
  return x ^ (x >> 13);
}
__device__ __forceinline__ unsigned epoch_load(const char* box) {
  unsigned e;
  asm volatile("global_load_dword %0, %1, %2 sc1\n\ts_waitcnt vmcnt(0)" : "=v"(e) : "v"(EPOCH_OFF), "s"(box) : "memory");
  return __builtin_amdgcn_readfirstlane(e);
}
__device__ __forceinline__ int tr_off(int row, int col4) {
  const int ch = col4 >> 1;
  return row * ROWB + (((ch & ~7) | ((ch & 7) ^ swz(row & 15))) << 4) + 8 * (col4 & 1);
}
#define PIN3(a, i) "+v"(a[i]), "+v"(a[i + 1]), "+v"(a[i + 2])

#define DMA1(d, b, off, pol) "global_load_lds_dwordx4 %[" #d "], %[" #b "] offset:" #off pol "\n\t"
__device__ __forceinline__ void issue_rows16(const char* a0, unsigned dst, const unsigned (&d)[3]) {
  unsigned keep;
  asm volatile("s_mov_b32 %[keep], m0\n\ts_mov_b32 m0, %[dst]\n\ts_nop 0\n\t"
               DMA1(d0, a, 0, "") DMA1(d1, a, 1024, "") DMA1(d2, a, 2048, "") DMA1(d0, a, 3072, "")
               "s_add_u32 m0, %[dst], 0x1000\n\ts_nop 0\n\t" DMA1(d1, b, 0, "") DMA1(d2, b, 1024, "") "s_mov_b32 m0, %[keep]"
               : [keep] "=&s"(keep) : [dst] "s"(dst), [a] "s"(a0), [b] "s"(a0 + 4096), [d0] "v"(d[0]), [d1] "v"(d[1]), [d2] "v"(d[2]) : "memory", "scc");
}
#define LD_VM "s_getreg_b32 s43, hwreg(HW_REG_IB_STS, 22, 2)\n\ts_getreg_b32 s42, hwreg(HW_REG_IB_STS, 0, 4)\n\ts_lshl4_add_u32 s42, s43, s42\n\t"
#define LD_GATE(n, X) \
  #n "0:\n\t" LD_VM "s_cmp_le_i32 s42, s46\n\ts_cbranch_scc1 " #n "2f\n" \
  #n "1:\n\t" X "s_cmp_gt_u32 s42, %[win]\n\ts_cbranch_scc1 " #n "5f\n\ts_cmp_ge_u32 s45, s44\n\ts_cbranch_scc1 " #n "7f\n" \
  #n "6:\n\ts_memrealtime s[40:41]\n\ts_waitcnt lgkmcnt(0)\n\ts_sub_u32 s45, s40, %[t0]\n\ts_lshl_b32 s45, s45, 4\n\ts_cmp_lt_u32 s45, s44\n\ts_cbranch_scc1 " #n "6b\n" \
  #n "7:\n\t" LD_VM
#define LD_NEXT \
  "s_cmp_gt_u32 s42, %[ahi]\n\ts_cselect_b32 s42, %[amg], 0\n\ts_sub_u32 s43, s45, s44\n\ts_cmp_lt_u32 s43, s42\n\ts_cselect_b32 s42, %[p3] + %[adl], s39\n\t" \
  "s_add_u32 s44, s44, s42\n\ts_add_u32 s46, s46, 5\n\t"
#define LD_PUB0(n, a, b, c) \
  #n "2:\n\tv_add_u32 %[pub], 2, %[pub]\n\ts_cmp_ge_u32 s46, 30\n\ts_cbranch_scc1 " #n "4f\n\ts_cmp_ge_u32 s46, 15\n\ts_cbranch_scc1 " #n "3f\n\t" \
  "s_waitcnt vmcnt(" #a ")\n" #n "3:\n\ts_waitcnt vmcnt(" #b ")\n" #n "4:\n\ts_waitcnt vmcnt(" #c ")\n\tds_write_b32 %[af], %[pub] ; NOTRACK\n\t" \
  "s_sub_u32 s46, s46, 15\n\ts_branch " #n "1b\n"
#define LD_PUB(n, a, b, c) LD_PUB0(n, a, b, c) \
  #n "5:\n\ts_sub_i32 s47, s47, 1\n\ts_cmp_gt_i32 s47, 0\n\ts_cbranch_scc1 " #n "0b\n\ts_waitcnt vmcnt(%[win])\n\ts_branch " #n "6b\n"
#define LD1(d, b, off) "global_load_lds_dwordx4 %[" #d "], s[" #b "] offset:" #off " nt\n\t"

#define PV_REQ(P, U, M, C, CA) \
  "v_lshl_add_u32 v150, s44, 3, %[apk]\n\tv_lshl_add_u32 v151, s44, 1, %[apu]\n\tv_add_u32 v152, s44, %[apm]\n\t" \
  "ds_read_b32 v" #C ", v" #CA "\n\tds_read_b128 v[" P "], v150\n\tds_read_b64 v[" M "], v152\n\tds_read_b128 v[" U "], v151\n\t"
#define PV_TR(d0, d1, a, o) "ds_read_b64_tr_b16 v[" #d0 ":" #d1 "], v" #a " offset:" #o "\n\t"
#define PV_FETCH(y0, y0b, a0, a0b, b0, b0b, y1, y1b, a1, a1b, b1, b1b, y2, y2b, a2, a2b, b2, b2b) \
  "v_add_u32 v153, s45, %[ox]\n\tv_add_u32 v154, s45, %[oy]\n\t" \
  PV_TR(a0, a0b, 153, 0) PV_TR(b0, b0b, 153, 6144) PV_TR(a1, a1b, 153, 128) PV_TR(b1, b1b, 153, 6272) PV_TR(a2, a2b, 153, 256) PV_TR(b2, b2b, 153, 6400) \
  PV_TR(y0, y0b, 154, 0) PV_TR(y1, y1b, 154, 128) PV_TR(y2, y2b, 154, 256) \
  "s_add_u32 s45, s45, %[slot]\n\ts_cmp_eq_u32 s45, %[ring]\n\ts_cselect_b32 s45, 0, s45\n\t"
#define PV_FETCH_A PV_FETCH(90, 91, 92, 93, 94, 95, 96, 97, 98, 99, 100, 101, 102, 103, 104, 105, 106, 107)
#define PV_FETCH_B PV_FETCH(108, 109, 110, 111, 112, 113, 114, 115, 116, 117, 118, 119, 120, 121, 122, 123, 124, 125)
#define PV_MF(d, a, p) "v_mfma_f32_16x16x32_bf16 v[" d "], v[" a "], v[" p "], v[" d "]\n\t"
#define PV_MOV(d, s) "v_mov_b32 v" #d ", v" #s "\n\t"
#define PV_WAITF(n, CA)   \
  #n ":\n\tds_read_b32 v155, v" #CA "\n\ts_sub_i32 s47, s47, 1\n\ts_cmp_lt_i32 s47, 0\n\ts_cbranch_scc1 " #n "9f\n\ts_waitcnt lgkmcnt(0)\n\t" \
  "v_cmp_lt_u32 vcc, v155, %[need]\n\ts_and_b64 vcc, vcc, 2\n\ts_cbranch_scc1 " #n "b\n" #n "9:\n\ts_waitcnt lgkmcnt(0)\n\t"
#define PV_STEP(n, P, U, M, m0, m1, C, CA, P2, U2, M2, C2, CA2, X0, X1, X2, Y0, Y1, Y2, MV0, MV1, MV2, HOOK) \
  "s_waitcnt lgkmcnt(10)\n\t" \
  "v_cmp_lt_u32 vcc, v" #C ", %[need]\n\tv_cmp_neq_f32 s[48:49], v" #m0 ", v88\n\tv_cmp_neq_f32 s[50:51], v" #m1 ", v88\n\t" \
  "v_add_u32 v" #CA2 ", 4, v" #CA "\n\ts_or_b64 s[48:49], s[48:49], vcc\n\tv_cmp_eq_u32 s[52:53], v" #CA2 ", %[lim]\n\t" \
  "s_or_b64 s[48:49], s[48:49], s[50:51]\n\ts_cbranch_scc1 " #n "1f\n" \
  #n "0:\n\ts_add_u32 s44, s44, 128\n\ts_cmp_eq_u32 s44, %[pring]\n\ts_cselect_b32 s44, 0, s44\n\t" \
  "v_cndmask_b32 v" #CA2 ", v" #CA2 ", %[base], s[52:53]\n\t" \
  PV_MF("84:87", U, P) PV_MF("72:75", X0, P) PV_MF("76:79", X1, P) MV0 PV_MF("80:83", X2, P) MV1 \
  PV_MF("72:75", Y0, P) MV2 HOOK PV_REQ(P2, U2, M2, C2, CA2) PV_MF("76:79", Y1, P) PV_MF("80:83", Y2, P)
#define PV_REL(CA) \
  "s_mov_b64 exec, 3\n\tds_add_u32 v" #CA ", %[one] offset:64\n\ts_mov_b64 exec, -1\n\tv_add_u32 %[need], 2, %[need]\n\ts_add_u32 s46, s46, 1\n\t"
#define PV_SC1(p) "v_lshlrev_b32 v151, 16, v" #p "\n\tv_and_b32 v152, 0xffff0000, v" #p "\n\tv_mul_f32 v151, v151, v150\n\tv_mul_f32 v152, v152, v150\n\t" \
                  "v_cvt_pk_bf16_f32 v" #p ", v151, v152\n\t"
#define PV_MUL4(a, b, c, d) "v_mul_f32 v" #a ", v" #a ", v150\n\tv_mul_f32 v" #b ", v" #b ", v150\n\tv_mul_f32 v" #c ", v" #c ", v150\n\tv_mul_f32 v" #d ", v" #d ", v150\n\t"
#define PV_SLOW(n, P, U, M, m0, m1, C, CA, p0, p1, p2, p3) \
  #n "1:\n\ts_bitcmp0_b32 vcc_lo, 0\n\ts_cbranch_scc1 " #n "4f\n\ts_cmp_gt_u32 s46, %[tight]\n\ts_cbranch_scc1 " #n "3f\n" \
  #n "2:\n\ts_sleep 1\n\tds_read_b32 v" #C ", v" #CA "\n\ts_sub_i32 s47, s47, 1\n\ts_cmp_lt_i32 s47, 0\n\ts_cbranch_scc1 " #n "3f\n\ts_waitcnt lgkmcnt(0)\n\t" \
  "v_cmp_lt_u32 vcc, v" #C ", %[need]\n\ts_and_b64 vcc, vcc, 1\n\ts_cbranch_scc1 " #n "2b\n" \
  #n "3:\n\t" PV_REQ(P, U, M, C, CA) "s_sub_i32 s47, s47, 1\n\ts_cmp_lt_i32 s47, 0\n\ts_cselect_b32 s54, 0, 1\n\ts_waitcnt lgkmcnt(0)\n\t" \
  "v_cmp_lt_u32 vcc, v" #C ", %[need]\n\tv_cmp_neq_f32 s[48:49], v" #m0 ", v88\n\tv_cmp_neq_f32 s[50:51], v" #m1 ", v88\n\t" \
  "s_and_b32 s54, s54, vcc_lo\n\ts_cbranch_scc1 " #n "3b\n\ts_or_b64 s[48:49], s[48:49], s[50:51]\n\ts_cbranch_scc0 " #n "0b\n\ts_branch " #n "5f\n" \
  #n "4:\n\tv_cmp_neq_f32 s[48:49], v" #m0 ", v88\n\ts_or_b64 s[48:49], s[48:49], s[50:51]\n\ts_cbranch_scc0 " #n "0b\n" \
  #n "5:\n\tv_mov_b32 v150, v88\n\tv_max3_f32 v88, v88, v" #m0 ", v" #m1 "\n\tv_sub_f32 v150, v150, v88\n\tv_exp_f32 v150, v150\n\ts_nop 1\n\t" \
  PV_MUL4(72, 73, 74, 75) PV_MUL4(76, 77, 78, 79) PV_MUL4(80, 81, 82, 83) PV_MUL4(84, 85, 86, 87) \
  "v_sub_f32 v150, v" #m0 ", v88\n\tv_exp_f32 v150, v150\n\ts_nop 1\n\t" PV_SC1(p0) PV_SC1(p1) \
  "v_sub_f32 v150, v" #m1 ", v88\n\tv_exp_f32 v150, v150\n\ts_nop 1\n\t" PV_SC1(p2) PV_SC1(p3) \
  "s_branch " #n "0b\n"
#define PV_W2(a0, a1, b0, b1, c0, c1) \
  "global_load_dwordx4 v[" #a0 ":" #a1 "], v150, %[w2] sc1\n\tglobal_load_dwordx4 v[" #b0 ":" #b1 "], v150, %[w2] offset:1024 sc1\n\t" \
  "global_load_dwordx4 v[" #c0 ":" #c1 "], v150, %[w2] offset:2048 sc1\n\tv_add_u32 v150, 0xc00, v150\n\ts_cbranch_scc1 30b\n\t"

#define PV_NRM(a, b, c, d, o)   \
  "v_pk_fma_f32 v[" #a ":" #b "], v[" #a ":" #b "], v[150:151], v[150:151] op_sel:[0,0,1] op_sel_hi:[1,0,1]\n\t" \
  "v_pk_fma_f32 v[" #c ":" #d "], v[" #c ":" #d "], v[150:151], v[150:151] op_sel:[0,0,1] op_sel_hi:[1,0,1]\n\t" \
  "v_cvt_pk_bf16_f32 v" #a ", v" #a ", v" #b "\n\tv_cvt_pk_bf16_f32 v" #b ", v" #c ", v" #d "\n\tds_write_b64 v152, v[" #a ":" #b "] offset:" #o "\n\t"
#define PV_G(n, a, w0, w1, w2, c0, c1, c2) "s_waitcnt lgkmcnt(" #n ")\n\tv_mfma_f32_16x16x32_bf16 v[72:75], v[" a "], v[" w0 "], " c0 "\n\t" \
  "v_mfma_f32_16x16x32_bf16 v[76:79], v[" a "], v[" w1 "], " c1 "\n\tv_mfma_f32_16x16x32_bf16 v[80:83], v[" a "], v[" w2 "], " c2 "\n\t"
#define PV_Z4(a, b, c, d) "v_mov_b32 v" #a ", 0\n\tv_mov_b32 v" #b ", 0\n\tv_mov_b32 v" #c ", 0\n\tv_mov_b32 v" #d ", 0\n\t"
#define PV_T(n)

using u32x8 = __attribute__((ext_vector_type(8))) unsigned;
struct SCtx {
  lds_ptr L0;
  unsigned ct, apk, apm, api, amr;
  int o_rb, o_yb, ev;
  u32x8 q[3];
  unsigned tq;
};
__device__ __forceinline__ bf16x8 s_qf(const SCtx& c, int j) {
  const u32x8& v = c.q[j >> 1];
  const int o = 4 * (j & 1);
  return __builtin_bit_cast(bf16x8, u32x4{v[o], v[o + 1], v[o + 2], v[o + 3]});
}
#define S_M(d, a, b) "v_mfma_f32_16x16x32_bf16 v[" d "], v[" a "], v[" b "], v[" d "]\n\t"
#define S_M0(d, a, b) "v_mfma_f32_16x16x32_bf16 v[" d "], v[" a "], v[" b "], 0\n\t"
#define S_R(d, a, o) "ds_read_b128 v[" d "], v" #a " offset:" #o "\n\t"
#define S_W(n) "s_waitcnt lgkmcnt(" #n ")\n\t"
#define S_D " row_mask:0xf bank_mask:0xf\n\t"
#define S_3(x, y) S_M("100:103", x, y) S_M("104:107", x, x) S_M("96:99", y, y)
#define S_SQ(g, h) "v_fmac_f32 v" #g ", 2.0, v" #h "\n\t"
#define S_SQD(g, h) "v_fmac_f32_dpp v" #g ", v" #h ", %[two] quad_perm:[0,0,2,2]" S_D
#define S_U(u, h, y) "v_add_f32_dpp v" #u ", v" #h ", v" #y " quad_perm:[1,1,3,3]" S_D
#define S_C(d, a, b, m) "v_cndmask_b32 v" #d ", v" #a ", v" #b ", %[" #m "]\n\t"
#define S_B(d, n) "v_mov_b32_dpp v" #d ", v157 row_newbcast:" #n S_D
#define S_BASE(d, u, t) "v_pk_fma_f32 v[" d "], v[" u "], v[116:117], v[" t "] op_sel:[0,1,0] op_sel_hi:[1,1,0]\n\t"
#define S_LG(d, r) "v_pk_fma_f32 v[" d "], v[" d "], v[" r "], %[toff] op_sel_hi:[1,1,0]\n\t"
#define S_SUB(p, d) "v_pk_add_f32 v[" p "], v[" d "], v[150:151] op_sel_hi:[1,0] neg_lo:[0,1] neg_hi:[0,1]\n\t"
#define S_E(p) "v_exp_f32 v" #p ", v" #p "\n\t"
#define S_MUL(p, r) "v_pk_mul_f32 v[" p "], v[" p "], v[" r "]\n\t"
#define S_POS   \
  "s_mul_i32 s50, s42, 26\n\ts_lshr_b32 s50, s50, 8\n\ts_mul_i32 s50, s50, -10\n\ts_add_u32 s50, s50, s42\n\ts_mul_i32 s45, s50, %[slot]\n\ts_mov_b32 s43, s45\n\t"
#define S_POLL(n, a, o, need, back) \
  #n ":\n\tds_read_b32 v152, v" #a " offset:" #o "\n\ts_sub_i32 s47, s47, 1\n\ts_cmp_lt_i32 s47, 0\n\ts_waitcnt lgkmcnt(0)\n\ts_cbranch_scc1 " back "\n\t" \
  "v_readfirstlane_b32 s40, v152\n\ts_cmp_ge_u32 s40, " need "\n\ts_cbranch_scc1 " back "\n\ts_sleep 1\n\ts_branch " #n "b\n"
__device__ __forceinline__ void s_items(SCtx& c, int k0, int late) {
  unsigned long long tq;
  asm volatile("s_mov_b32 s47, %[cap]\n\ts_mov_b32 s49, 0\n\ts_mov_b32 s42, %[k0]\n\ts_cmp_lg_u32 %[late], 0\n\ts_cbranch_scc1 7f\n"
               "1:\n\ts_cmp_ge_u32 s42, %[end]\n\ts_cbranch_scc1 9f\n\t"
               S_POS
               "s_mul_i32 s48, s42, 43\n\ts_lshr_b32 s48, s48, 8\n\ts_mul_i32 s46, s48, -6\n\ts_add_u32 s46, s46, s42\n\ts_lshl2_add_u32 s48, s48, %[ns]\n\t"
               "v_add_u32 v144, s45, %[orb]\n\tv_mad_u32_u24 v145, s43, %[ev], %[oyb]\n\ts_lshl2_add_u32 s40, s46, %[pfl]\n\ts_lshl2_add_u32 s41, s50, %[full]\n\t"
               "v_xor_b32 v146, 64, v144\n\tv_xor_b32 v147, 64, v145\n\tv_mov_b32 v148, s40\n\tv_mov_b32 v149, s41\n\t"
               "s_lshl1_add_u32 s44, s42, 2\n\ts_lshl_b32 s46, s46, 7\n\ts_mov_b64 vcc, %[dg]\n\ts_cmp_lt_u32 s49, s44\n\ts_cbranch_scc1 5f\n"
               "2:\n\tds_read_b32 v150, %[amr]\n\tds_read_b32 v151, v148 offset:64\n\t"
               S_R("24:27", 145, 0) S_R("48:51", 144, 0) S_R("28:31", 147, 0) S_R("52:55", 146, 0) S_R("32:35", 145, 128) S_R("56:59", 144, 128)
               S_R("36:39", 147, 128) S_R("60:63", 146, 128) S_R("40:43", 145, 256) S_R("64:67", 144, 256) S_R("44:47", 147, 256) S_R("68:71", 146, 256)
               S_W(10) S_M0("100:103", "48:51", "24:27") S_M0("104:107", "48:51", "48:51") S_R("72:75", 144, 6144) S_R("76:79", 146, 6144) S_M0("96:99", "24:27", "24:27")
               S_W(10) S_M("100:103", "52:55", "28:31") S_M("104:107", "52:55", "52:55") S_R("80:83", 144, 6272) S_R("84:87", 146, 6272) S_M("96:99", "28:31", "28:31")
               S_W(10) S_M("100:103", "56:59", "32:35") S_M("104:107", "56:59", "56:59") S_R("88:91", 144, 6400) S_R("92:95", 146, 6400) S_M("96:99", "32:35", "32:35")
               "s_mov_b64 exec, 1\n\tds_add_u32 v149, %[four] offset:64\n\ts_mov_b64 exec, -1\n\tv_readfirstlane_b32 s51, v151\n\ts_setprio 1\n\ts_nop 0\n\t"
               S_W(11) S_3("60:63", "36:39") S_W(9) S_3("64:67", "40:43") S_W(7) S_3("68:71", "44:47")
               S_W(6) S_M0("108:111", "72:75", "24:27") S_M0("112:115", "72:75", "72:75")
               S_W(5) S_M("108:111", "76:79", "28:31") S_M("112:115", "76:79", "76:79")
               S_W(4) S_M("108:111", "80:83", "32:35")
               "v_mov_b32_dpp v152, v96 quad_perm:[1,1,3,3]" S_D "v_mov_b32_dpp v153, v98 quad_perm:[1,1,3,3]" S_D
               S_M("112:115", "80:83", "80:83") S_C(154, 96, 98, r2) S_SQ(104, 100)
               S_W(3) S_M("108:111", "84:87", "36:39") S_SQD(105, 101) S_SQ(106, 102)
               S_M("112:115", "84:87", "84:87") S_SQD(107, 103) "v_add_f32 v154, %[eps], v154\n\t"
               S_W(2) S_M("108:111", "88:91", "40:43") S_U(128, 100, 152) S_U(129, 101, 152)
               S_M("112:115", "88:91", "88:91") S_U(130, 102, 153) S_U(131, 103, 153)
               S_W(1) S_M("108:111", "92:95", "44:47") S_C(104, 104, 105, r1) S_C(106, 106, 107, r1)
               S_M("112:115", "92:95", "92:95") S_C(155, 128, 129, r1) S_C(156, 130, 131, r1)
               "s_cmp_lt_u32 s42, %[early]\n\ts_cbranch_scc1 7f\n"
               "3:\n\t" S_M0("116:119", "24:27", "0:3") S_C(104, 104, 106, r2) S_C(155, 155, 156, r2)
               S_M("116:119", "28:31", "4:7") "v_add_f32_dpp v104, v154, v104 quad_perm:[0,0,2,2]" S_D S_U(132, 108, 152)
               S_M("116:119", "32:35", "8:11") S_U(133, 109, 152) S_U(134, 110, 153)
               S_M("116:119", "36:39", "12:15") S_U(135, 111, 153) S_SQ(112, 108)
               S_M("116:119", "40:43", "16:19") S_SQD(113, 109) S_SQ(114, 110)
               S_M("116:119", "44:47", "20:23") S_SQD(115, 111) S_C(156, 132, 133, r1) S_C(157, 134, 135, r1)
               S_C(112, 112, 113, r1) S_C(114, 114, 115, r1) S_C(156, 156, 157, r2) S_C(112, 112, 114, r2)
               "v_add_f32_dpp v112, v154, v112 quad_perm:[0,0,2,2]" S_D
               "v_mul_f32 v117, %[nic], v117\n\tv_cndmask_b32_dpp v155, v156, v155, vcc row_ror:4" S_D
               S_BASE("120:121", "128:129", "116:117") S_BASE("122:123", "130:131", "118:119")
               "v_cndmask_b32_dpp v104, v112, v104, vcc row_ror:4" S_D "v_mul_f32 v156, %[nic], v155\n\t"
               S_M("120:123", "48:51", "0:3") S_BASE("124:125", "132:133", "116:117") S_BASE("126:127", "134:135", "118:119")
               S_M("120:123", "52:55", "4:7") "v_fmac_f32 v104, v156, v155\n\tv_max_f32 v104, %[eps], v104\n\t"
               S_M("120:123", "56:59", "8:11") "v_rsq_f32 v157, v104\n\t"
               S_M("120:123", "60:63", "12:15") "v_mul_f32 v104, v104, v157\n\tv_mov_b32_dpp v157, v157 row_ror:12 row_mask:0x2 bank_mask:0xf\n\t"
               S_M("120:123", "64:67", "16:19") "v_cvt_pk_bf16_f32 v105, v155, v104\n\tv_mov_b32_dpp v157, v157 row_ror:8 row_mask:0x4 bank_mask:0xf\n\t"
               S_M("120:123", "68:71", "20:23") "v_lshlrev_b32 v106, 16, v105\n\tv_mov_b32_dpp v157, v157 row_ror:4 row_mask:0x8 bank_mask:0xf\n\t"
               S_M("124:127", "72:75", "0:3") "v_and_b32 v107, 0xffff0000, v105\n\t" S_B(136, 0)
               S_M("124:127", "76:79", "4:7") S_B(137, 1) S_B(138, 2)
               S_M("124:127", "80:83", "8:11") S_B(139, 3) S_LG("120:121", "136:137")
               S_M("124:127", "84:87", "12:15") S_LG("122:123", "138:139") S_SUB("128:129", "120:121")
               S_M("124:127", "88:91", "16:19") S_SUB("130:131", "122:123") S_E(128)
               S_M("124:127", "92:95", "20:23") S_E(129) S_E(130) S_E(131)
               S_B(140, 4) S_B(141, 5) S_B(142, 6) S_B(143, 7) "v_sub_f32 v106, v155, v106\n\t"
               S_LG("124:125", "140:141") S_LG("126:127", "142:143") "v_sub_f32 v107, v104, v107\n\tv_max3_f32 v152, v120, v121, v122\n\t"
               S_SUB("132:133", "124:125") S_SUB("134:135", "126:127") "v_max3_f32 v153, v123, v124, v125\n\t"
               S_E(132) S_E(133) S_E(134) S_E(135)
               "v_max3_f32 v152, v152, v126, v127\n\tv_add_f32 v154, %[thr], v150\n\tv_max_f32 v152, v152, v153\n\tv_cvt_pk_bf16_f32 v106, v106, v107\n\t"
               "v_lshl_add_u32 v144, s46, 3, %[apk]\n\tv_cmp_gt_f32 vcc, v152, v154\n\tv_add_u32 v145, s46, %[apm]\n\tv_lshl_add_u32 v146, s46, 1, %[api]\n\t"
               "s_cbranch_vccnz 8f\n"
               "4:\n\t" S_MUL("128:129", "136:137") S_MUL("130:131", "138:139") S_MUL("132:133", "140:141") S_MUL("134:135", "142:143")
               "v_cvt_pk_bf16_f32 v128, v128, v129\n\tv_cvt_pk_bf16_f32 v129, v130, v131\n\tv_cvt_pk_bf16_f32 v130, v132, v133\n\tv_cvt_pk_bf16_f32 v131, v134, v135\n\t"
               "s_cmp_lt_u32 s51, s48\n\ts_cbranch_scc1 6f\n"
               "10:\n\tds_read_b32 v151, v149\n\tds_write_b128 v144, v[128:131]\n\tds_write2_b32 v145, v150, v150 offset0:0 offset1:1\n\ts_mov_b64 exec, %[dg2]\n\t"
               "ds_write_b16 v146, v105\n\tds_write_b16 v146, v106 offset:16\n\tds_write_b16_d16_hi v146, v105 offset:32\n\tds_write_b16_d16_hi v146, v106 offset:48\n\t"
               "s_mov_b64 exec, 1\n\tds_add_u32 v148, %[inc]\n\ts_mov_b64 exec, -1\n\ts_setprio 0\n\ts_add_u32 s42, s42, %[ns]\n\t"
               S_W(7) "v_readfirstlane_b32 s49, v151\n\ts_branch 1b\n"
               S_POLL(5, 149, 0, "s44", "2b")
               S_POLL(6, 148, 64, "s48", "10b")
               "7:\n\tv_mov_b32 v144, %[qi]\n\tv_mov_b32 v145, v152\n\tv_mov_b32 v146, 1\n"
               S_POLL(70, 144, 0, "%[nq]", "71f")
               "71:\n\tv_mov_b32 v152, v145\n\tds_read_b128 v[0:3], %[apk]\n\tds_read_b128 v[4:7], %[apk] offset:1024\n\tds_read_b128 v[8:11], %[apk] offset:2048\n\t"
               "ds_read_b128 v[12:15], %[apk] offset:3072\n\tds_read_b128 v[16:19], %[apk] offset:4096\n\tds_read_b128 v[20:23], %[apk] offset:5120\n\t"
               "v_subrev_u32 v144, %[mc], %[amr]\n\tds_read_b32 v150, %[amr]\n\ts_mov_b64 exec, 63\n\tds_add_u32 v144, v146\n\ts_mov_b64 exec, -1\n\t"
               "s_cmp_lg_u32 %[late], 0\n\ts_waitcnt lgkmcnt(0)\n\ts_nop 1\n\ts_cbranch_scc1 1b\n\ts_branch 3b\n"
               "8:\n\tv_mov_b32 v153, v152\n\ts_nop 1\n\tv_permlane16_swap_b32 v152, v153\n\tv_max_f32 v152, v152, v153\n\tv_mov_b32 v153, v152\n\ts_nop 1\n\t"
               "v_permlane32_swap_b32 v152, v153\n\tv_max_f32 v152, v152, v153\n\tv_mul_f32 v152, 0x3d000000, v152\n\tv_rndne_f32 v152, v152\n\t"
               "v_mul_f32 v152, 0x42000000, v152\n\tv_max_f32 v150, v150, v152\n\tds_max_f32 %[amr], v150\n\t"
               S_SUB("128:129", "120:121") S_SUB("130:131", "122:123") S_SUB("132:133", "124:125") S_SUB("134:135", "126:127")
               S_E(128) S_E(129) S_E(130) S_E(131) S_E(132) S_E(133) S_E(134) S_E(135) "s_branch 4b\n"
               "9:"
               : "=&{v[0:7]}"(c.q[0]), "=&{v[8:15]}"(c.q[1]), "=&{v[16:23]}"(c.q[2]), "=&{s[52:53]}"(tq)
               : [orb] "v"(c.o_rb), [oyb] "v"(c.o_yb), [ev] "v"(c.ev), [apk] "v"(c.apk), [apm] "v"(c.apm), [api] "v"(c.api), [amr] "v"(c.amr),
                 [two] "v"(2.f), [four] "v"(4u), [inc] "v"(2u * NPK), [k0] "s"(k0), [late] "s"(__builtin_amdgcn_readfirstlane(late)), [toff] "s"(f32x2{TOFF, TOFF}),
                 [r1] "s"(0xAAAAAAAAAAAAAAAAull), [r2] "s"(0xCCCCCCCCCCCCCCCCull), [dg] "s"(0xF0000F0000F0000Full), [dg2] "s"(0xF00FFF000FF000FFull),
                 [cap] "i"(SPIN_CAP), [end] "i"(TPW - NH1), [ns] "i"(NS), [pfl] "i"(CT_OFF + C_PFL), [full] "i"(CT_OFF + C_FULL), [early] "i"(NEARLY),
                 [qi] "i"(CT_OFF + C_QI), [nq] "i"(NS - NEARLY), [mc] "i"(C_MREF - C_CONS), [slot] "i"(SLOT),
                 [thr] "i"(__builtin_bit_cast(unsigned, THR)), [eps] "i"(__builtin_bit_cast(unsigned, EPS * CC)), [nic] "i"(__builtin_bit_cast(unsigned, -1.f / CC))
               : "memory", "scc", "vcc", "s40", "s41", "s42", "s43", "s44", "s45", "s46", "s47", "s48", "s49", "s50", "s51",
                 "v24", "v25", "v26", "v27", "v28", "v29", "v30", "v31", "v32", "v33", "v34", "v35", "v36", "v37", "v38", "v39", "v40", "v41", "v42", "v43", "v44", "v45", "v46", "v47", "v48", "v49", "v50", "v51", "v52", "v53", "v54", "v55", "v56", "v57", "v58", "v59", "v60", "v61", "v62", "v63", "v64", "v65", "v66", "v67", "v68", "v69", "v70", "v71", "v72", "v73", "v74", "v75", "v76", "v77", "v78", "v79", "v80", "v81", "v82", "v83", "v84", "v85", "v86", "v87", "v88", "v89", "v90", "v91", "v92", "v93", "v94", "v95", "v96", "v97", "v98", "v99", "v100", "v101", "v102", "v103", "v104", "v105", "v106", "v107", "v108", "v109", "v110", "v111", "v112", "v113", "v114", "v115", "v116", "v117", "v118", "v119", "v120", "v121", "v122", "v123", "v124", "v125", "v126", "v127", "v128", "v129", "v130", "v131", "v132", "v133", "v134", "v135", "v136", "v137", "v138", "v139", "v140", "v141", "v142", "v143", "v144", "v145", "v146", "v147", "v148", "v149", "v150", "v151", "v152", "v153", "v154", "v155", "v156", "v157");
  c.tq = (unsigned)tq;
}

__device__ __forceinline__ void s_half(SCtx& c, int k, int h) {
  const int s = k % NSLOT, e = k % NPK;
  const int sb_ = s * SLOT;
  int yba = c.ev * sb_ + c.o_yb;
  float hc = 1.f - 0.5f * c.ev;
  unsigned xa = (unsigned)(size_t)c.L0 + sb_ + h * 6144 + c.o_rb, xb = xa ^ 64, ai = c.api + e * 256 + h * 8;
  unsigned ap = c.apk + e * 1024 + h * 8, am = c.apm + e * 128 + h * 4, af = c.ct + C_PFL + e * 4, inc = NPK;
  float two = 2.f;
  f32x4 G = {EPS * CC, EPS * CC, EPS * CC, EPS * CC};
  poll_ge_sleep(c.ct + C_CONS + e * 4, (unsigned)(NE * (k / NPK) + NS));
  asm volatile("" : "+v"(yba), "+v"(hc), "+v"(xa), "+v"(xb), "+v"(ai), "+v"(ap), "+v"(am), "+v"(af), "+v"(inc), "+v"(two), "+{v[104:107]}"(G));
  poll_ge_sleep(c.ct + C_FULL, (unsigned)(2 * k + 1));
  __builtin_amdgcn_s_setprio(1);
  bf16x8 yb[6];
#pragma unroll
  for (int j = 0; j < 6; ++j) yb[j] = *(LDS_AS(bf16x8)*)(c.L0 + ((j & 1) ? (yba ^ 64) : yba) + (j >> 1) * 128);
  f32x4 Hx = {0.f, 0.f, 0.f, 0.f}, S = Hx;
#pragma unroll
  for (int j = 0; j < 6; ++j) { Hx = mfma(yb[j], yb[j], Hx); S = mfma(yb[j], s_qf(c, j), S); }
  const float nsq = S[1] * (-1.f / CC);
  Hx = f32x4{Hx[0] * hc, Hx[0] * hc, Hx[2] * hc, Hx[2] * hc};
  S = f32x4{S[0], S[0], S[2], S[2]};
  float mref, mt;
  f32x4 rs;
  unsigned long long raise;
#define SH_M(d, a, b) "v_mfma_f32_16x16x32_bf16 v[" d "], v[" a "], " b ", v[" d "]\n\t"
#define SH_X(n, a, y) "s_waitcnt lgkmcnt(" #n ")\n\t" SH_M("100:103", a, "%[" #y "]") SH_M("104:107", a, "v[" a "]")
#define SH_D " row_mask:0xf bank_mask:0xf\n\t"
  __builtin_amdgcn_s_setprio(0);
  if (HB == 0 || (HB == 1 && k < TPW - 1)) poll_ge_sleep(c.ct + C_FULL, (unsigned)(2 * k + 2));
  if (HB == 2) { if (k < TPW - 1) poll_ge_sleep(c.ct + C_FULL, (unsigned)(2 * k + 2)); else poll_ge(c.ct + C_FULL, (unsigned)(2 * TPW)); }
  asm volatile("s_mov_b32 s42, %[cap]\n"
               "1:\n\tds_read_b32 v136, %[afl]\n\tds_read_b128 v[112:115], %[xa]\n\tds_read_b128 v[116:119], %[xb]\n\tds_read_b128 v[120:123], %[xa] offset:128\n\t"
               "ds_read_b128 v[124:127], %[xb] offset:128\n\tds_read_b128 v[128:131], %[xa] offset:256\n\tds_read_b128 v[132:135], %[xb] offset:256\n\t"
               "ds_read_b32 %[mr], %[amr]\n\ts_sub_u32 s42, s42, 1\n\ts_cselect_b32 s41, 0, %[need]\n\ts_waitcnt lgkmcnt(7)\n\t"
               "v_readfirstlane_b32 s40, v136\n\ts_cmp_lt_u32 s40, s41\n\ts_cbranch_scc1 1b\n\ts_setprio 3\n\t"
               SH_X(6, "112:115", y0) SH_X(5, "116:119", y1) SH_X(4, "120:123", y2) SH_X(3, "124:127", y3) SH_X(2, "128:131", y4) SH_X(1, "132:135", y5)
               SH_M("108:111", "112:115", "%[q0]") SH_M("108:111", "116:119", "%[q1]")
               "s_waitcnt lgkmcnt(0)\n\tv_add_f32 v137, %[thr], %[mr]\n\tds_write_b32 %[am], %[mr]\n\ts_nop 1\n\t"
               "v_mov_b32_dpp v140, v100 quad_perm:[1,1,3,3]" SH_D "v_mov_b32_dpp v141, v101 quad_perm:[1,1,3,3]" SH_D
               "v_mov_b32_dpp v142, v102 quad_perm:[1,1,3,3]" SH_D "v_mov_b32_dpp v143, v103 quad_perm:[1,1,3,3]" SH_D
               SH_M("108:111", "120:123", "%[q2]")
               "v_fmac_f32 v104, 2.0, v100\n\tv_fmac_f32_dpp v105, v101, %[two] quad_perm:[0,0,2,2]" SH_D
               "v_fmac_f32 v106, 2.0, v102\n\tv_fmac_f32_dpp v107, v103, %[two] quad_perm:[0,0,2,2]" SH_D
               "v_cndmask_b32 v138, v140, v141, %[r1]\n\tv_cndmask_b32 v139, v142, v143, %[r1]\n\t"
               SH_M("108:111", "124:127", "%[q3]")
               "v_cndmask_b32 v104, v104, v105, %[r1]\n\tv_cndmask_b32 v106, v106, v107, %[r1]\n\tv_cndmask_b32 v138, v138, v139, %[r2]\n\t"
               "v_cndmask_b32 v104, v104, v106, %[r2]\n\tv_mul_f32 v139, %[nic], v138\n\t"
               SH_M("108:111", "128:131", "%[q4]")
               "v_fmac_f32 v104, v139, v138\n\tv_max_f32 v104, %[eps], v104\n\tv_rsq_f32 v105, v104\n\t"
               SH_M("108:111", "132:135", "%[q5]")
               "v_mul_f32 v104, v104, v105\n\t"
               "v_mov_b32_dpp v105, v105 row_ror:12 row_mask:0x2 bank_mask:0xf\n\tv_cvt_pk_bf16_f32 v106, v138, v104\n\ts_nop 0\n\t"
               "v_mov_b32_dpp v105, v105 row_ror:8 row_mask:0x4 bank_mask:0xf\n\tv_lshlrev_b32 v107, 16, v106\n\tv_and_b32 v139, 0xffff0000, v106\n\t"
               "v_mov_b32_dpp v105, v105 row_ror:4 row_mask:0x8 bank_mask:0xf\n\t"
               "v_fmac_f32 v108, v140, %[nsq]\n\tv_fmac_f32 v109, v141, %[nsq]\n\tv_fmac_f32 v110, v142, %[nsq]\n\tv_fmac_f32 v111, v143, %[nsq]\n\t"
               "v_mov_b32_dpp v100, v105 row_newbcast:0" SH_D "v_mov_b32_dpp v101, v105 row_newbcast:1" SH_D
               "v_mov_b32_dpp v102, v105 row_newbcast:2" SH_D "v_mov_b32_dpp v103, v105 row_newbcast:3" SH_D
               "v_fmaak_f32 v108, v108, v100, %[toff]\n\tv_fmaak_f32 v109, v109, v101, %[toff]\n\tv_fmaak_f32 v110, v110, v102, %[toff]\n\tv_fmaak_f32 v111, v111, v103, %[toff]\n\t"
               "v_sub_f32 v140, v108, %[mr]\n\tv_sub_f32 v141, v109, %[mr]\n\tv_sub_f32 v142, v110, %[mr]\n\tv_sub_f32 v143, v111, %[mr]\n\t"
               "v_exp_f32 v140, v140\n\tv_exp_f32 v141, v141\n\tv_exp_f32 v142, v142\n\tv_exp_f32 v143, v143\n\t"
               "v_max3_f32 %[mt], v108, v109, v110\n\tv_sub_f32 v138, v138, v107\n\tv_sub_f32 v104, v104, v139\n\tv_max_f32 %[mt], %[mt], v111\n\t"
               "v_pk_mul_f32 v[140:141], v[140:141], v[100:101]\n\tv_pk_mul_f32 v[142:143], v[142:143], v[102:103]\n\t"
               "v_cmp_gt_f32 %[ra], %[mt], v137\n\tv_cvt_pk_bf16_f32 v107, v138, v104\n\t"
               "v_cvt_pk_bf16_f32 v140, v140, v141\n\tv_cvt_pk_bf16_f32 v141, v142, v143\n\t"
               "ds_write_b64 %[ap], v[140:141]\n\ts_mov_b64 exec, %[dg]\n\t"
               "ds_write_b16 %[ai], v106\n\tds_write_b16 %[ai], v107 offset:16\n\tds_write_b16_d16_hi %[ai], v106 offset:32\n\tds_write_b16_d16_hi %[ai], v107 offset:48\n\t"
               "s_cmp_eq_u64 %[ra], 0\n\ts_cselect_b64 exec, 1, 0\n\tds_add_u32 %[af], %[inc]\n\ts_mov_b64 exec, -1\n\ts_setprio 0"
               : "+{v[100:103]}"(Hx), "+{v[104:107]}"(G), "+{v[108:111]}"(S), [mr] "=&v"(mref), [mt] "=&v"(mt), [ra] "=&s"(raise)
               : [y0] "v"(yb[0]), [y1] "v"(yb[1]), [y2] "v"(yb[2]), [y3] "v"(yb[3]), [y4] "v"(yb[4]), [y5] "v"(yb[5]),
                 [q0] "v"(s_qf(c, 0)), [q1] "v"(s_qf(c, 1)), [q2] "v"(s_qf(c, 2)), [q3] "v"(s_qf(c, 3)), [q4] "v"(s_qf(c, 4)), [q5] "v"(s_qf(c, 5)),
                 [afl] "v"(c.ct + C_FULL), [xa] "v"(xa), [xb] "v"(xb), [amr] "v"(c.amr), [am] "v"(am), [ap] "v"(ap), [ai] "v"(ai),
                 [af] "v"(af), [inc] "v"(inc), [nsq] "v"(nsq), [two] "v"(two),
                 [need] "s"(__builtin_amdgcn_readfirstlane(2 * k + 2)), [r1] "s"(0xAAAAAAAAAAAAAAAAull), [r2] "s"(0xCCCCCCCCCCCCCCCCull),
                 [dg] "s"(0xF0000F0000F0000Full), [cap] "i"(SPIN_CAP), [thr] "i"(__builtin_bit_cast(unsigned, THR)), [toff] "i"(__builtin_bit_cast(unsigned, TOFF)),
                 [eps] "i"(__builtin_bit_cast(unsigned, EPS * CC)), [nic] "i"(__builtin_bit_cast(unsigned, -1.f / CC))
               : "memory", "scc", "s40", "s41", "s42", "v112", "v113", "v114", "v115", "v116", "v117", "v118", "v119", "v120", "v121", "v122", "v123", "v124", "v125",
                 "v126", "v127", "v128", "v129", "v130", "v131", "v132", "v133", "v134", "v135", "v136", "v137", "v138", "v139", "v140", "v141", "v142", "v143");
  if (__builtin_expect(raise != 0ull, 0)) {
    rs = Hx;
    mref = fmaxf(mref, 32.f * __builtin_rintf(xrow16_max(mt) * (1.f / 32.f)));
    float pp[4];
#pragma unroll
    for (int i = 0; i < 4; ++i) pp[i] = __builtin_amdgcn_exp2f(S[i] - mref) * rs[i];
    asm volatile("ds_max_f32 %6, %3\n\tds_write_b32 %2, %3\n\tds_write_b64 %0, %1\n\ts_mov_b64 exec, 1\n\tds_add_u32 %4, %5\n\ts_mov_b64 exec, -1"
                 : : "v"(ap), "v"(u32x2{cvtpk(pp[0], pp[1]), cvtpk(pp[2], pp[3])}), "v"(am), "v"(mref), "v"(af), "v"((unsigned)NPK), "v"(c.amr) : "memory");
  }
}

using u32x16 = __attribute__((ext_vector_type(16))) unsigned;
typedef __attribute__((address_space(1))) unsigned short gus;
extern "C" __global__ __launch_bounds__(768, 3) void dino_main(const void*, const void*, const void*, const void*, const void*,
                                                               const void*, const void*, void*, void*) {
  u32x16 ka; u32x2 kb; unsigned long long tent64;
  __shared__ __attribute__((aligned(16))) char lds[LDS_BYTES];
  const int wave = __builtin_amdgcn_readfirstlane((int)threadIdx.x >> 6);
  const bool helper = blockIdx.x < 8;
  lds_ptr L0 = (lds_ptr)lds;
  const unsigned lbase = (unsigned)(size_t)L0;
  const unsigned ct = lbase + CT_OFF;
#define KARG(v, i) ((char*)(((unsigned long long)v[2 * (i) + 1] << 32) | v[2 * (i)]))
#define q KARG(ka, 0)
#define f0 KARG(ka, 1)
#define f1 KARG(ka, 2)
#define wq KARG(ka, 3)
#define wk KARG(ka, 4)
#define wv KARG(ka, 5)
#define wo KARG(ka, 6)
#define box KARG(ka, 7)
#define tent ((unsigned)tent64)
#define LANE_VARS int lane; asm volatile("v_mbcnt_lo_u32_b32 %0, -1, 0\n\tv_mbcnt_hi_u32_b32 %0, -1, %0" : "=v"(lane)); \
  const int r = lane & 15, g = lane >> 4; (void)r; (void)g;
#define DOFF_VARS unsigned doff[3];   \
  _Pragma("unroll") for (int i = 0; i < 3; ++i) doff[i] = (unsigned)(lane ^ ((swz_tbl(i) >> ((lane >> 3) * 4)) & 7)) << 4;
#define BARRIER() do { asm volatile("s_waitcnt lgkmcnt(0)" ::: "memory"); __builtin_amdgcn_s_barrier(); asm volatile("" ::: "memory"); } while (0)

  {
    unsigned doff[3];
    static_assert(swz_tbl(0) == swz_tbl(3) && swz_tbl(1) == swz_tbl(4) && swz_tbl(2) == swz_tbl(5), "");
    unsigned l16, db;
#define PD(d, b, off, pol) "global_load_lds_dwordx4 %[" #d "], s[" #b "] offset:" #off pol "\n\t"
    asm volatile("s_load_dwordx16 s[64:79], %[kp], 0x0\n\ts_load_dwordx2 s[80:81], %[kp], 0x40\n\ts_memrealtime s[82:83]\n\t"
                 "v_mbcnt_lo_u32_b32 %[l16], -1, 0\n\tv_mbcnt_hi_u32_b32 %[l16], -1, %[l16]\n\t"
                 "s_lshr_b32 s84, %[b], 3\n\ts_and_b32 s85, %[b], 7\n\t"
                 "v_lshrrev_b32 %[db], 1, %[l16]\n\tv_and_b32 %[db], -4, %[db]\n\t"
                 "s_mov_b32 s86, %[t0]\n\ts_mov_b32 s87, %[t1]\n\ts_mov_b32 s88, %[t2]\n\t"
                 "v_bfe_u32 %[d0], s86, %[db], 3\n\tv_bfe_u32 %[d1], s87, %[db], 3\n\tv_bfe_u32 %[d2], s88, %[db], 3\n\t"
                 "v_xor_b32 %[d0], %[d0], %[l16]\n\tv_xor_b32 %[d1], %[d1], %[l16]\n\tv_xor_b32 %[d2], %[d2], %[l16]\n\t"
                 "v_lshlrev_b32 %[d0], 4, %[d0]\n\tv_lshlrev_b32 %[d1], 4, %[d1]\n\tv_lshlrev_b32 %[d2], 4, %[d2]\n\t"
                 "v_lshlrev_b32 %[l16], 4, %[l16]\n\t"
                 "s_cmp_eq_u32 s84, 0\n\ts_cbranch_scc1 4f\n\t"
                 "s_cmp_lg_u32 %[w], 0\n\ts_cbranch_scc1 7f\n\t"
                 "s_setprio 3\n\tv_lshlrev_b32 %[db], 3, %[l16]\n\ts_mov_b32 m0, %[junk]\n\ts_lshl_b32 s89, s84, 13\n\t"
                 "s_cmp_gt_u32 s84, 3\n\ts_cbranch_scc1 2f\n\t"
                 "s_getpc_b64 s[90:91]\n1:\n\ts_sub_u32 s86, s89, 8192\n\ts_min_u32 s86, s86, dino_end-1b-8192\n\t"
                 "s_add_u32 s90, s90, s86\n\ts_addc_u32 s91, s91, 0\n\tglobal_load_lds_dword %[db], s[90:91]\n"
                 "2:\n\ts_mul_i32 s86, s85, 98304\n\ts_mul_i32 s87, s84, 3072\n\ts_sub_u32 s87, s87, 3072\n\t"
                 "s_add_u32 s88, s86, s87\n\ts_lshl1_add_u32 s86, s86, s87\n\ts_lshl_b32 s86, s86, 1\n\t"
                 "s_mul_i32 s87, s85, 12288\n\ts_lshr_b32 s90, s89, 1\n\ts_add_u32 s87, s87, s90\n\ts_sub_u32 s87, s87, 13 * 4096\n\t"
                 "s_sub_u32 s89, s89, 4 * 8192\n\ts_cmp_gt_u32 s85, 5\n\ts_cselect_b32 s89, -1, s89\n\ts_cmp_ge_u32 s89, 11 * 8192\n\t"
                 "s_waitcnt lgkmcnt(0)\n\ts_cbranch_scc1 3f\n\t"
                 "s_cmp_lt_u32 s89, 9 * 8192\n\ts_cselect_b64 s[90:91], s[70:71], s[72:73]\n\ts_cselect_b32 s89, s89, s87\n\t"
                 "s_add_u32 s90, s90, s89\n\ts_addc_u32 s91, s91, 0\n\tglobal_load_lds_dword %[db], s[90:91]\n"
                 "3:\n\ts_mov_b32 m0, 0x3000\n\t"
                 "s_add_u32 s90, s68, s88\n\ts_addc_u32 s91, s69, 0\n\t"
                 "s_add_u32 s92, s66, s86\n\ts_addc_u32 s93, s67, 0\n\t"
                 PD(d0, 90:91, 0, " nt") PD(d1, 90:91, 1024, " nt") PD(d2, 90:91, 2048, " nt")
                 "s_mov_b32 m0, 0\n\ts_nop 0\n\t"
                 PD(d0, 92:93, 0, " nt") PD(d1, 92:93, 1024, " nt")
                 "s_sub_u32 s89, s84, 15\n\ts_cmp_gt_u32 s85, 5\n\ts_cselect_b32 s89, -1, s89\n\ts_cmp_ge_u32 s89, 11\n\ts_cbranch_scc1 9f\n\t"
                 "s_mov_b32 m0, %[junk]\n\ts_sub_u32 s87, s87, 11 * 4096\n\ts_lshl_b32 s89, s89, 13\n\ts_cmp_lt_u32 s89, 9 * 8192\n\t"
                 "s_cselect_b64 s[90:91], s[76:77], s[74:75]\n\ts_cselect_b32 s89, s89, s87\n\t"
                 "s_add_u32 s90, s90, s89\n\ts_addc_u32 s91, s91, 0\n\tglobal_load_lds_dword %[db], s[90:91]\n\t"
                 "s_branch 9f\n"
                 "7:\n\ts_cmp_eq_u32 %[w], 1\n\ts_cbranch_scc1 8f\n\ts_barrier\n\ts_branch 8f\n"
                 "4:\n\t"
                 "s_cmp_gt_u32 %[b], 5\n\ts_cbranch_scc1 8f\n\ts_cmp_gt_u32 %[w], 9\n\ts_cbranch_scc1 8f\n\t"
                 "s_mov_b32 m0, %[img]\n\ts_mul_i32 s86, %[w], 9216\n\ts_and_b32 s88, %[w], 1\n\ts_lshl1_add_u32 s89, %[b], s88\n\t"
                 "s_mul_i32 s88, s88, 6144\n\ts_mul_i32 s89, s89, 6144\n\ts_add_u32 s88, s88, %[hr]\n\t"
                 "s_cmp_lg_u32 %[w], 0\n\ts_waitcnt lgkmcnt(0)\n\ts_cbranch_scc1 5f\n\t"
                 PD(d0, 64:65, 0, "") PD(d1, 64:65, 1024, "") PD(d2, 64:65, 2048, "") PD(d0, 64:65, 3072, "")
                 "s_mov_b32 m0, %[img] + 4096\n\ts_add_u32 s90, s64, 0x1000\n\ts_addc_u32 s91, s65, 0\n\t"
                 PD(d1, 90:91, 0, "") PD(d2, 90:91, 1024, "")
                 "\n5:\n\ts_cmp_gt_u32 %[w], 7\n\ts_cselect_b64 s[92:93], s[72:73], s[70:71]\n\ts_cselect_b32 m0, s88, s86\n\ts_cselect_b32 s86, s89, s86\n\t"
                 "s_cselect_b32 s87, 2, 3\n\ts_add_u32 s92, s92, s86\n\ts_addc_u32 s93, s93, 0\n"
                 "6:\n\ts_sub_u32 s87, s87, 1\n\t"
                 PD(d0, 92:93, 0, "") PD(d1, 92:93, 1024, "") PD(d2, 92:93, 2048, "")
                 "s_add_u32 m0, m0, 3072\n\ts_add_u32 s92, s92, 3072\n\ts_addc_u32 s93, s93, 0\n\ts_cmp_lg_u32 s87, 0\n\ts_cbranch_scc1 6b"
                 "\n8:\n\ts_waitcnt lgkmcnt(0)\n9:"
                 : "={s[64:79]}"(ka), "={s[80:81]}"(kb), "={s[82:83]}"(tent64),
                   [d0] "=&v"(doff[0]), [d1] "=&v"(doff[1]), [d2] "=&v"(doff[2]), [l16] "=&v"(l16), [db] "=&v"(db)
                 : [kp] "s"(__builtin_amdgcn_kernarg_segment_ptr()), [b] "s"(blockIdx.x), [w] "s"(wave),
                   [t0] "i"(swz_tbl(0)), [t1] "i"(swz_tbl(1)), [t2] "i"(swz_tbl(2)), [junk] "i"(JUNK), [img] "i"(IMG_A), [hr] "i"(H_R)
                 : "memory", "scc", "vcc", "s84", "s85", "s86", "s87", "s88", "s89", "s90", "s91", "s92", "s93", "s94", "s95", "s96", "s97", "s98", "s99");
  }
  const unsigned t0 = (unsigned)__builtin_amdgcn_s_memrealtime() - T0BACK;

  if (__builtin_expect(blockIdx.x < 6, 0)) {
    LANE_VARS
    DOFF_VARS
    const int j = blockIdx.x;
    TNOW(ta0);
    TNOW(ta1);
    const int qq = (lane >> 2) & 3, p4 = lane & 3, e4 = wave & 3;
    int ra = H_R + r * ROWB + ((g ^ swz(r)) << 4);
    bf16x8 a[6], b[6], w[18];
    auto rows = [&](int t, bf16x8* d) {
#pragma unroll
      for (int kk = 0; kk < 6; ++kk) d[kk] = *(LDS_AS(bf16x8)*)(L0 + t * 6144 + ((kk & 1) ? (ra ^ 64) : ra) + (kk >> 1) * 128);
    };
#define TSET(v)
    int o[3];
#pragma unroll
    for (int t = 0; t < 3; ++t) o[t] = tr_off(4 * g + qq, 4 * (3 * e4 + t) + p4);
    unsigned qo = lbase + H_Q + 3 * e4 * 512 + ((g >> 1) * 16 + r) * 16 + (g & 1) * 8, qa = lbase + IMG_A + tr_off(r, 8 * wave + g);
    if (wave == 11) {
      unsigned d;
      asm volatile("s_load_dword %0, %1, 0x40\n\ts_barrier\n\ts_barrier\n\ts_barrier\n\ts_waitcnt lgkmcnt(0)" : "=&s"(d) : "s"(box + EPOCH_OFF) : "memory");
    } else
    asm volatile("s_waitcnt vmcnt(9)\n\ts_barrier" : PIN3(o, 0), "+v"(qo), "+v"(qa) :: "memory");
    if (wave < 6 || wave == 10) {
      rows((IMG_A - H_R) / 6144, a);
      const u32x2 m0 = *(LDS_AS(u32x2)*)(size_t)qa, m1 = *(LDS_AS(u32x2)*)(size_t)(qa ^ 32);
      static_assert((IMG_A - H_R) % 6144 == 0, "");
      const bf16x8 ones = __builtin_bit_cast(bf16x8, u32x4{0x3f803f80u, 0x3f803f80u, 0x3f803f80u, 0x3f803f80u});
      f32x4 sm = {0.f, 0.f, 0.f, 0.f};
#pragma unroll
      for (int i = 0; i < 6; ++i) sm = mfma(ones, a[i], sm);
      const float mean = sm[0] * (1.f / CC);
      if (__builtin_expect(wave < 6, 1)) {
        img_st(L0, H_C, wave, lane, __builtin_bit_cast(bf16x8, u32x4{cvtpk(bflo(m0[0]) - mean, bfhi(m0[0]) - mean), cvtpk(bflo(m0[1]) - mean, bfhi(m0[1]) - mean),
                                                                   cvtpk(bflo(m1[0]) - mean, bfhi(m1[0]) - mean), cvtpk(bflo(m1[1]) - mean, bfhi(m1[1]) - mean)}));
      } else {
        asm volatile("s_barrier" : PIN3(a, 0), PIN3(a, 3) :: "memory");
        f32x2 v2 = {0.f, 0.f}, v3 = v2;
#pragma unroll
        for (int i = 0; i < 12; ++i) {
          const u32x4 d = __builtin_bit_cast(u32x4, a[i >> 1]);
          const unsigned d0 = d[2 * (i & 1)], d1 = d[2 * (i & 1) + 1];
          const f32x2 x0 = f32x2{bflo(d0), bfhi(d0)} - mean, x1 = f32x2{bflo(d1), bfhi(d1)} - mean;
          v2 += x0 * x0; v3 += x1 * x1;
        }
        v2 += v3;
        *(LDS_AS(float)*)(L0 + CT_OFF + r * 4) = LOG2E * __builtin_amdgcn_rsqf(xrow16_sum(v2[0] + v2[1]) * (1.f / CC) + EPS);
      }
      TSET(ta2);
    }
    if (wave < 4) {
      wait_vm<0>();
      TSET(ta3);
      BARRIER();
      TSET(th1);
#pragma unroll
      for (int kk = 0; kk < 6; ++kk) {
        b[kk] = img_ld(L0, H_C, kk, lane);
        w[kk] = mk8(tr_read(L0 + kk * 12288 + o[0]), tr_read(L0 + kk * 12288 + 6144 + o[0]));
        __builtin_amdgcn_sched_barrier(0);
      }
#pragma unroll
      for (int i = 6; i < 18; ++i) {
        w[i] = mk8(tr_read(L0 + (i % 6) * 12288 + o[i / 6]), tr_read(L0 + (i % 6) * 12288 + 6144 + o[i / 6]));
        __builtin_amdgcn_sched_barrier(0);
      }
#pragma unroll
      for (int t = 0; t < 3; ++t) {
        f32x4 D = {0.f, 0.f, 0.f, 0.f};
#pragma unroll
        for (int kk = 0; kk < 6; ++kk) D = mfma(w[6 * t + kk], b[kk], D);
        *(LDS_AS(u32x2)*)(size_t)(qo + t * 512) = u32x2{cvtpk(D[0], D[1]), cvtpk(D[2], D[3])};
      }
      TSET(ta4);
      BARRIER();
    } else if (wave == 8 || wave == 9) {
      unsigned so = lane * 16 + (2 * j + (wave & 1)) * 1024;
      unsigned epoch;
      asm volatile("global_load_dword %0, %1, %2 sc1\n\ts_barrier\n\ts_waitcnt vmcnt(0)" : "=v"(epoch) : "v"(EPOCH_OFF), "s"(box) : "memory");
      TSET(ta3);
      rows(wave & 1, a);
      unsigned ta = line_tag(epoch, 8), tb = line_tag(epoch, 9);
      asm volatile("" : PIN3(a, 0), PIN3(a, 3), "+v"(ta), "+v"(tb), "+v"(so));
      TSET(ta4);
      BARRIER();
      TSET(ta5);
#pragma unroll
      for (int kk = 0; kk < 6; ++kk) b[kk] = img_ld(L0, H_Q, kk, lane);
      const float sc = *(LDS_AS(float)*)(L0 + CT_OFF + r * 4);
      __builtin_amdgcn_sched_barrier(0);
      f32x4 D = {0.f, 0.f, 0.f, 0.f};
#pragma unroll
      for (int kk = 0; kk < 6; ++kk) D = mfma(a[kk], b[kk], D);
      D *= sc;
      const unsigned p0 = cvtpk(D[0], D[1]), p1 = cvtpk(D[2], D[3]);
      asm volatile("global_store_dwordx4 %0, %1, %2 sc1\n\ts_nop 1" : : "v"(so), "v"(u32x4{p0, p1, p0 ^ p1 ^ ta, tb}), "s"(box + QK_OFF) : "memory");
      TSET(th2);
    } else if (wave != 11) {
      if (wave != 10) asm volatile("s_waitcnt vmcnt(0) lgkmcnt(0)\n\ts_barrier" ::: "memory");
      BARRIER();
    }
    BARRIER();
    issue_rows16(wo + wave * 6144, lbase + wave * 6144, doff);
    if (wave < 2) issue_rows16(wv + (2 * j + wave) * 6144, lbase + H_R + wave * 6144, doff);
    const unsigned epoch = epoch_load(box);
    BARRIER();
    TSET(ta3);
    {
      const int o0 = tr_off(8 * g + qq, 4 * wave + p4), o1 = tr_off(8 * g + 4 + qq, 4 * wave + p4);
#pragma unroll
      for (int kk = 0; kk < 6; ++kk) b[kk] = mk8(tr_read(L0 + kk * 12288 + o0), tr_read(L0 + kk * 12288 + o1));
      f32x4 D[2];
#pragma unroll
      for (int h = 0; h < 2; ++h) {
        rows(h, a);
        D[h] = f32x4{0.f, 0.f, 0.f, 0.f};
#pragma unroll
        for (int kk = 0; kk < 6; ++kk) D[h] = mfma(a[kk], b[kk], D[h]);
      }
      const int e = wave / 3;
      asm volatile("global_store_dwordx4 %0, %1, %2 sc1\n\ts_waitcnt vmcnt(0)"
                   : : "v"(lane * 16 + (e * 15 + 3 * j + wave) * 1024), "v"(pack8(D[0] * SQRT_C, D[1] * SQRT_C)), "s"(box + W2_OFF) : "memory");
    }
    BARRIER();
    if (wave == 11)
      asm volatile("s_mov_b64 exec, 1\n\tglobal_store_dwordx2 %0, %1, %2 sc1\n\ts_mov_b64 exec, -1\n\ts_nop 1"
                   : : "v"(j * 128), "v"(u32x2{line_tag(epoch, 10), line_tag(epoch, 11)}), "s"(box + W2F_OFF) : "memory");
    TPUT(60 + wave * 5, ta0); TPUT(61 + wave * 5, ta1); TPUT(62 + wave * 5, ta2); TPUT(63 + wave * 5, ta3); TPUT(64 + wave * 5, ta4);
  }

  int rslot = -1;
  if (__builtin_expect(wave == 0, 1)) {
    LANE_VARS
    DOFF_VARS
    if (helper) BARRIER();
    const unsigned afull = ct + C_FULL + r * 4;
    unsigned ts = 0;
    {
      const int ti = helper ? 1023 : (lane * 31 + (int)(blockIdx.x >> 3) - 1) & 1023;
      const int sp = ti >> 5, yy = sp * 8 + ((blockIdx.x + sp) & 7), xx = (ti + 5 * sp) & 31;
      unsigned tmp, pub = helper ? 2 * (TPW - 1) : 0, pre = 0, afr = ct + C_FREE + (helper ? ((TPW - 1) % NSLOT) * 4 : 0), e0, e1, e2, g0, g1, g2;
      asm volatile("s_mov_b32 s44, %[due0]\n\ts_mov_b32 s46, %[y0]\n\ts_mov_b32 s47, %[cap]\n\ts_mov_b32 s48, %[i0]\n\ts_mov_b32 s49, %[s0]\n\t"
                   "s_mov_b32 s51, 0\n\ts_mov_b32 s45, 0\n\ts_movk_i32 s39, %[p3s]\n"
                   "1:\n\tv_readlane_b32 s53, %[ob], s48\n\tv_readlane_b32 s52, %[oa], s48\n\ts_add_u32 m0, s49, 0x3000\n\tv_cmp_gt_u32 vcc, s51, %[pre]\n\t"
                   "v_add_u32 %[g0], s53, %[d0]\n\tv_add_u32 %[g1], s53, %[d1]\n\tv_add_u32 %[g2], s53, %[d2]\n\t"
                   "v_add_u32 %[e0], s52, %[d0]\n\tv_add_u32 %[e1], s52, %[d1]\n\tv_add_u32 %[e2], s52, %[d2]\n\t"
                   "s_cbranch_vccnz 5f\n\t"
                   "s_cmp_lt_u32 s48, 2\n\ts_cbranch_scc1 8f\n"
                   LD_GATE(1, "")
                   LD1(g0, 68:69, 0) LD1(g1, 68:69, 1024) LD1(g2, 68:69, 2048) "s_mov_b32 m0, s49\n\ts_nop 0\n\t" LD1(e0, 66:67, 0) LD1(e1, 66:67, 1024)
                   LD_NEXT
                   "\n2:\n\tv_add_u32 %[afr], 4, %[afr]\n\ts_cmp_eq_u32 s49, %[lasts]\n\ts_cbranch_scc1 7f\n"
                   "3:\n\tv_mov_b32 %[pre], 0\n\ts_add_u32 m0, s49, 0x800\n\tds_read_b32 %[pre], %[afr] ; NOTRACK\n"
                   LD_GATE(2, "")
                   LD1(e2, 56:57, 0) LD1(e0, 56:57, 1024) LD1(e1, 56:57, 2048) LD1(e2, 56:57, 3072) "s_add_u32 m0, s49, 0x1800\n\ts_add_u32 s48, s48, 1\n\t" LD1(e0, 58:59, 0)
                   LD_NEXT
                   LD_GATE(3, "s_cmp_gt_u32 s48, %[tpw] - 3\n\ts_cbranch_scc1 38f\n39:\n\t")
                   LD1(e1, 58:59, 1024) LD1(e2, 58:59, 2048) LD1(e0, 58:59, 3072) "s_add_u32 m0, s49, 0x2800\n\ts_add_u32 s49, s49, %[slot]\n\t" LD1(e1, 60:61, 0) LD1(e2, 60:61, 1024)
                   LD_NEXT
                   "s_cmp_eq_u32 s49, %[ring]\n\ts_cselect_b32 s49, 0, s49\n\ts_cmp_gt_i32 s46, 30\n\ts_cbranch_scc1 6f\n"
                   "4:\n\ts_cmp_eq_u32 s48, %[pk]\n\ts_cmovk_i32 s39, %[p3]\n\ts_cmp_lg_u32 s48, %[tpw]\n\ts_cbranch_scc1 1b\n\ts_branch 9f\n"
                   LD_PUB(1, 0, 15, 30) LD_PUB(2, 5, 20, 35) LD_PUB(3, 10, 25, 40)
                   "38:\n\ts_cmp_eq_u32 s46, 10\n\ts_cselect_b32 s43, 23, 0\n\ts_cmp_lt_u32 s42, s43\n\ts_cbranch_scc0 39b\n\tv_add_u32 %[tmp], 1, %[pub]\n\ts_waitcnt vmcnt(22)\n\tds_write_b32 %[af], %[tmp] ; NOTRACK\n\ts_branch 39b\n"
                   "5:\n\ts_movk_i32 s40, 0x100\n"
                   "40:\n\tds_read_b32 %[pre], %[afr]\n\t" LD_VM
                   "s_sub_u32 s40, s40, 1\n\ts_cselect_b32 s42, 0, s42\n\ts_cselect_b32 s40, 0, s40\n\t"
                   "s_cmp_le_i32 s42, s46\n\ts_cbranch_scc1 42f\n"
                   "41:\n\ts_sub_i32 s47, s47, 1\n\ts_cmp_lt_i32 s47, 0\n\ts_waitcnt lgkmcnt(0)\n\ts_cbranch_scc1 10b\n\t"
                   "v_cmp_gt_u32 vcc, s51, %[pre]\n\ts_cbranch_vccnz 40b\n\ts_branch 10b\n"
                   LD_PUB0(4, 0, 15, 30)
                   "6:\n\ts_waitcnt vmcnt(45)\n\tv_add_u32 %[pub], 2, %[pub]\n\tds_write_b32 %[af], %[pub]\n\ts_sub_u32 s46, s46, 15\n\ts_branch 4b\n"
                   "7:\n\ts_waitcnt lgkmcnt(0)\n\ts_add_u32 s51, s51, 8\n\tv_subrev_u32 %[afr], %[frb], %[afr]\n\ts_branch 3b\n"
                   "8:\n\ts_cmp_eq_u32 s48, 0\n\ts_cbranch_scc1 2b\n\ts_barrier\n\ts_branch 10b\n"
                   "9:\n\ts_cmp_lt_i32 s46, 30\n\ts_cbranch_scc1 91f\n\ts_waitcnt vmcnt(30)\n\tv_add_u32 %[pub], 2, %[pub]\n\tds_write_b32 %[af], %[pub]\n"
                   "91:\n\ts_cmp_lt_i32 s46, 15\n\ts_cbranch_scc1 92f\n\tv_add_u32 %[tmp], 1, %[pub]\n\ts_waitcnt vmcnt(27)\n\tds_write_b32 %[af], %[tmp]\n"
                   "92:\n\ts_waitcnt lgkmcnt(0)"
                   : [pub] "+v"(pub), [pre] "+v"(pre), [afr] "+v"(afr), [ts] "+v"(ts), [tmp] "=&v"(tmp),
                     [e0] "=&v"(e0), [e1] "=&v"(e1), [e2] "=&v"(e2), [g0] "=&v"(g0), [g1] "=&v"(g1), [g2] "=&v"(g2)
                   : "{s[64:79]}"(ka), "{s[56:57]}"(f0 + 0x800), "{s[58:59]}"(f0 + 0x30000), "{s[60:61]}"(f0 + 0x31000), [t0] "s"(t0), [af] "v"(afull),
                     [d0] "v"(doff[0]), [d1] "v"(doff[1]), [d2] "v"(doff[2]), [oa] "v"(yy * 393216 + xx * 6144), [ob] "v"(yy * 98304 + xx * 3072),
                     [due0] "s"(helper ? HDUE * (PACE16 / 3) : PACE16 / 3), [y0] "s"(helper ? -15 : -10), [i0] "s"(helper ? TPW - 1 : 0),
                     [s0] "s"(helper ? ((TPW - 1) % NSLOT) * SLOT : 0),
                     [cap] "i"(SPIN_CAP), [win] "i"(PWIN), [ahi] "i"(AHI), [adl] "i"(ADL), [amg] "i"(AMG), [p3] "i"(PACE16 / 3), [p3s] "i"(PACES / 3), [pk] "i"(PK1), [slot] "i"(SLOT),
                     [ring] "i"(NSLOT * SLOT), [frb] "i"(NSLOT * 4), [lasts] "i"((NSLOT - 1) * SLOT), [tpw] "i"(TPW)
                   : "memory", "scc", "vcc", "s39", "s40", "s41", "s42", "s43", "s44", "s45", "s46", "s47", "s48", "s49", "s51", "s52", "s53", "s62", "s63");
    }
    lds_st32(ct + C_ISS, 1u);
#define CLR(b) "global_store_dwordx4 %0, %1, %" #b " sc1\n\t"
    auto clear = [&]() {
      asm volatile(CLR(2) CLR(3) CLR(4) CLR(5) CLR(6) CLR(7) CLR(8) CLR(9) "s_nop 1"
                 : : "v"(((lane >> 3) * NWG + blockIdx.x) * 128 + (lane & 7) * 16), "v"(u32x4{0u, 0u, 0u, 0u}), "s"(box), "s"(box + 1 * 262144), "s"(box + 2 * 262144),
                     "s"(box + 3 * 262144), "s"(box + 4 * 262144), "s"(box + 5 * 262144), "s"(box + 6 * 262144), "s"(box + 7 * 262144) : "memory");
    };
    if (PRECLR == 1) clear();
    wait_vm<PRECLR == 1 ? 23 : 15>();
    lds_st32(afull, (unsigned)(2 * TPW - 2));
    if (PRECLR == 3) clear();
    wait_vm<(PRECLR == 1 || PRECLR == 3) ? 20 : 12>();
    lds_st32(afull, (unsigned)(2 * TPW - 1));
    wait_vm<(PRECLR == 1 || PRECLR == 3) ? 8 : 0>();
    lds_st32(afull, ~0u);
    TNOW(tl);
    if (PRECLR == 2) clear();
    if (PRECLR == 4) { unsigned d; asm volatile("global_load_dword %0, %1, %2 sc1\n\ts_waitcnt vmcnt(0)" : "=v"(d) : "v"(blockIdx.x * 128), "s"(box) : "memory"); }
    __builtin_amdgcn_s_setprio(0);
    wait_vm<0>();
    TNOW(tlx);
    if (PRECLR != 5) lds_add(ct + C_X1, 1u);
    rslot = 7;
  } else {
    if (wave == 1) {
      LANE_VARS
    {
      const unsigned o = lane * 8;
      unsigned z = (o >= C_MREF && o < C_MREF + 64) ? 0xff800000u : 0u;
      if (helper && o >= C_CONS && o < C_MREF) z = NE * ((TPW - 1) / NPK);
      u32x2 zz = {z, z};
      if (o >= C_PFL && o < C_CONS) zz = u32x2{(o - C_PFL) / 2 + 2, (o - C_PFL) / 2 + 4} + (helper ? 2u * NPK * ((TPW - 1) / NPK) : 0u);
      *(LDS_AS(u32x2)*)(L0 + CT_OFF + o) = zz;
      *(LDS_AS(u32x2)*)(L0 + ONES_OFF + (lane < 48 ? o : 0)) = u32x2{0x3f803f80u, 0x3f803f80u};
    }
    }
    if (helper || wave == 1) BARRIER();
    if (wave <= NE) {
      LANE_VARS
      const int ej = wave - 1;
      const int qq = (lane >> 2) & 3, p4 = lane & 3;
      const unsigned apk = lbase + IMG_A + lane * 16;
      const unsigned epoch = epoch_load(box);
      __builtin_amdgcn_s_setprio(2);
      if (helper) { while ((unsigned)__builtin_amdgcn_s_memrealtime() - tent < 1300) __builtin_amdgcn_s_sleep(8); }
      unsigned long long tpv, tp7, tx1;
      {
        static_assert((TPW & 1) == 1 && (W2K0 & 1) == 0 && C_CONS == C_PFL + 64 && C_FREE == C_FULL + 64, "");
        const int k0 = helper ? TPW - 1 : 0;
        unsigned need = 2 * k0 + 2;
        asm volatile("s_mov_b32 s46, 0\n\ts_mov_b64 exec, 0\n\ts_nop 3\n\ts_branch 99f\n"
                     "2:\n\ts_mov_b64 exec, -1\n\ts_nop 7\n\t" PV_Z4(72, 73, 74, 75) PV_Z4(76, 77, 78, 79) PV_Z4(80, 81, 82, 83) PV_Z4(84, 85, 86, 87)
                     "v_mov_b32 v88, 0xff800000\n\t"
                     "s_mov_b32 s46, %[k0]\n\ts_mov_b32 s45, %[s0]\n\ts_mov_b32 s44, %[e0]\n\ts_mov_b32 s47, %[cap]\n\t"
                     "s_cmp_lg_u32 s46, 0\n\ts_cbranch_scc1 32f\n"
                     "3:\n\tv_mov_b32 v148, %[ca]\n\t" PV_WAITF(4, 148) PV_FETCH_A
                     "s_mov_b64 exec, 2\n\tds_add_u32 v148, %[one] offset:64\n\ts_mov_b64 exec, -2\n\t"
                     "v_add_u32 %[need], 2, %[need]\n\tv_add_u32 v148, 4, v148\n\ts_mov_b64 exec, -1\n\t"
                     PV_WAITF(5, 148)
                     "v_mov_b32 v149, v148\n\ts_mov_b64 exec, -2\n\tv_add_u32 %[need], 2, %[need]\n\tv_add_u32 v148, 4, v148\n\t"
                     "s_mov_b64 exec, 1\n\tv_add_u32 %[need], %[npk2], %[need]\n\ts_mov_b64 exec, -1\n\t"
                     PV_REQ("126:129", "130:133", "134:135", 136, 148) PV_FETCH_B
                     "s_mov_b64 exec, 2\n\tds_add_u32 v149, %[one] offset:64\n\ts_mov_b64 exec, -1\n"
                     "6:\n\t"
                     PV_STEP(1, "126:129", "130:133", "134:135", 134, 135, 136, 148, "138:141", "142:145", "146:147", 137, 149,
                             "92:95", "98:101", "104:107", "90:93", "96:99", "102:105",
                             PV_MOV(92, 90) PV_MOV(93, 91), PV_MOV(98, 96) PV_MOV(99, 97), PV_MOV(104, 102) PV_MOV(105, 103),
                             "s_cmp_eq_u32 s46, %[last]\n\ts_cbranch_scc1 99f\n\t")
                     "s_cbranch_vccnz 7f\n"
                     "16:\n\t" PV_FETCH_A PV_REL(148)
                     "s_sub_u32 s54, s46, %[w0]\n\ts_cmp_le_u32 s54, %[wh]\n\ts_cbranch_scc1 31f\n"
                     "30:\n\t"
                     PV_STEP(2, "138:141", "142:145", "146:147", 146, 147, 137, 149, "126:129", "130:133", "134:135", 136, 148,
                             "110:113", "116:119", "122:125", "108:111", "114:117", "120:123",
                             PV_MOV(110, 108) PV_MOV(111, 109), PV_MOV(116, 114) PV_MOV(117, 115), PV_MOV(122, 120) PV_MOV(123, 121),
                             "s_cmp_eq_u32 s46, %[last] - 1\n\ts_cbranch_scc1 98f\n\t")
                     "s_cbranch_vccnz 8f\n"
                     "26:\n\t" PV_FETCH_B PV_REL(149)
                     "s_branch 6b\n"
                     PV_WAITF(7, 148) "s_branch 16b\n"
                     PV_WAITF(8, 149) "s_branch 26b\n"
                     "98:\n\t" PV_MF("76:79", "114:117", "138:141") PV_MF("80:83", "120:123", "138:141")
                     "v_add_u32 %[need], 2, %[need]\n\ts_add_u32 s46, s46, 1\n\ts_branch 13f\n"
                     PV_SLOW(1, "126:129", "130:133", "134:135", 134, 135, 136, 148, 126, 127, 128, 129)
                     PV_SLOW(2, "138:141", "142:145", "146:147", 146, 147, 137, 149, 138, 139, 140, 141)
                     "31:\n\ts_cmp_lg_u32 s54, 0\n\ts_cbranch_scc1 34f\n"
                     "32:\n\tv_mbcnt_lo_u32_b32 v152, 31, 0\n\tv_lshlrev_b32 v152, 7, v152\n\ts_mov_b32 s55, %[gcap]\n"
                     "33:\n\tglobal_load_dwordx2 v[150:151], v152, %[fl] sc1\n\ts_sub_u32 s55, s55, 1\n\ts_cselect_b32 s56, 0, -1\n\ts_waitcnt vmcnt(0) lgkmcnt(0)\n\t"
                     "v_cmp_ne_u64 s[48:49], %[tag], v[150:151]\n\ts_sleep 2\n\ts_and_b32 s48, s48, s56\n\ts_cbranch_scc1 33b\n\t"
                     "v_mov_b32 v150, %[apk]\n\ts_cmp_lg_u32 s46, %[last]\n\ts_cbranch_scc1 30b\n\ts_branch 38f\n"
                     "34:\n\ts_cmp_lt_u32 s54, %[wl]\n\ts_cbranch_scc1 36f\n\tv_mov_b32 v151, %[iss]\n"
                     "35:\n\tds_read_b32 v150, v151\n\ts_sub_i32 s47, s47, 1\n\ts_cmp_lt_i32 s47, 0\n\ts_cselect_b32 s55, 0, 1\n\ts_waitcnt lgkmcnt(0)\n\t"
                     "v_readfirstlane_b32 s56, v150\n\ts_cmp_lt_u32 s56, s55\n\ts_cbranch_scc1 35b\n"
                     "36:\n\ts_mul_i32 s55, s54, 1536\n\ts_sub_u32 s55, s55, 3072\n\tv_add_u32 v150, s55, %[apk]\n\ts_getpc_b64 s[48:49]\n"
                     "37:\n\ts_mul_i32 s55, s54, 18\n\ts_add_u32 s55, s55, 38f-37b-36\n\ts_add_u32 s48, s48, s55\n\ts_addc_u32 s49, s49, 0\n\t"
                     "s_cmp_lt_u32 s54, %[wl]\n\ts_setpc_b64 s[48:49]\n"
                     "38:\n\t" PV_W2(0, 3, 4, 7, 8, 11) PV_W2(12, 15, 16, 19, 20, 23) PV_W2(24, 27, 28, 31, 32, 35) PV_W2(36, 39, 40, 43, 44, 47)
                     PV_W2(48, 51, 52, 55, 56, 59) PV_W2(60, 63, 64, 67, 68, 71)
                     "s_cmp_lg_u32 s46, %[last]\n\ts_cbranch_scc1 30b\n\ts_waitcnt lgkmcnt(0)\n\ts_branch 3b\n"
                     "99:\n\t" PV_MF("76:79", "96:99", "126:129") PV_MF("80:83", "102:105", "126:129") PV_T(58)
                     "v_add_f32 v153, v86, v87\n\tv_add_f32 v151, v84, v85\n\tv_rcp_f32 v150, v153\n\tv_mul_f32 v151, %[nic], v151\n\t"
                     "v_add_u32 v152, %[aw], %[apk]\n\tv_mul_f32 v151, v151, v150\n\t"
                     PV_NRM(72, 73, 74, 75, 0) PV_NRM(76, 77, 78, 79, 2048) PV_NRM(80, 81, 82, 83, 4096)
                     "v_mov_b32 v154, %[x1]\n\ts_and_b64 exec, exec, 1\n\tds_add_u32 v154, %[one]\n\ts_cselect_b64 exec, -1, 0\n\ts_cselect_b32 s55, %[x1n], 0\n\t" PV_T(60)
                     "v_log_f32 v153, v153\n\tv_subrev_f32 v88, %[toff], v88\n\tv_lshrrev_b32 v151, 1, %[apm]\n\tv_add_f32 v88, v88, v153\n\tv_add_u32 v151, %[lwo], v151\n\t"
                     "v_mbcnt_lo_u32_b32 v155, -1, 0\n\tv_mbcnt_hi_u32_b32 v155, -1, v155\n\tds_write_b32 v151, v88\n\t"
                     "v_and_b32 v151, 1, v155\n\tv_and_b32 v155, 48, v155\n\tv_lshl_add_u32 v155, v151, 2, v155\n\tv_add_u32 v155, %[lw], v155\n\t"
                     "ds_read2_b32 v[114:115], v155 offset1:2\n\tv_add_u32 v152, %[ar], %[apk]\n"
                     "97:\n\tds_read_b32 v150, v154\n\tds_read_b128 v[90:93], v152\n\tds_read_b128 v[94:97], v152 offset:1024\n\tds_read_b128 v[98:101], v152 offset:2048\n\t"
                     "ds_read_b128 v[102:105], v152 offset:3072\n\tds_read_b128 v[106:109], v152 offset:4096\n\tds_read_b128 v[110:113], v152 offset:5120\n\t"
                     "s_sub_i32 s47, s47, 1\n\ts_cmp_lt_i32 s47, 0\n\ts_cselect_b32 s55, 0, s55\n\ts_waitcnt vmcnt(0) lgkmcnt(6)\n\t"
                     "v_readfirstlane_b32 s56, v150\n\ts_cmp_lt_u32 s56, s55\n\ts_cbranch_scc1 97b\n\ts_cmp_eq_u32 s46, 0\n\t"
                     PV_G(5, "90:93", "0:3", "4:7", "8:11", "0", "0", "0")
                     PV_G(4, "94:97", "12:15", "16:19", "20:23", "v[72:75]", "v[76:79]", "v[80:83]")
                     PV_G(3, "98:101", "24:27", "28:31", "32:35", "v[72:75]", "v[76:79]", "v[80:83]")
                     PV_G(2, "102:105", "36:39", "40:43", "44:47", "v[72:75]", "v[76:79]", "v[80:83]")
                     PV_G(1, "106:109", "48:51", "52:55", "56:59", "v[72:75]", "v[76:79]", "v[80:83]")
                     PV_G(0, "110:113", "60:63", "64:67", "68:71", "v[72:75]", "v[76:79]", "v[80:83]")
                     "v_xor_b32 v114, v114, %[ltag]\n\tv_xor_b32 v115, v115, %[ltag]\n\ts_nop 3\n\t"
                     "v_cvt_pk_bf16_f32 v116, v72, v73\n\tv_cvt_pk_bf16_f32 v120, v74, v75\n\tv_cvt_pk_bf16_f32 v117, v76, v77\n\tv_cvt_pk_bf16_f32 v121, v78, v79\n\t"
                     "v_cvt_pk_bf16_f32 v118, v80, v81\n\tv_bitop3_b32 v119, v116, v117, v114 bitop3:0x96\n\tv_cvt_pk_bf16_f32 v122, v82, v83\n\t"
                     "v_bitop3_b32 v123, v120, v121, v115 bitop3:0x96\n\tv_xor_b32 v119, v119, v118\n\tv_xor_b32 v123, v123, v122\n\t"
                     "s_cbranch_scc1 2b\n\t"
                     "global_store_dwordx4 %[so], v[116:119], %[bx]" SPOL "\n\tglobal_store_dwordx4 %[so], v[120:123], %[bx2]" SPOL "\n\t"
                     PV_T(62) "s_waitcnt lgkmcnt(0)\n\ts_nop 0"
                     : [need] "+v"(need), "={s[58:59]}"(tpv), "={s[60:61]}"(tp7), "={s[62:63]}"(tx1)
                     : [so] "v"(((ej * 16 + (lane >> 3)) * NWG + blockIdx.x) * 128 + (lane & 7) * 16), [ltag] "v"(line_tag(epoch, lane & 7)),
                       [bx] "s"(box), [bx2] "s"(box + 8 * NWG * 128), [aw] "s"(A_IMG - IMG_A + (ej >> 1) * 1024 + (ej & 1) * 8), [ar] "i"(A_IMG - IMG_A),
                       [nic] "i"(__builtin_bit_cast(unsigned, -1.f / CC)), [toff] "i"(__builtin_bit_cast(unsigned, TOFF)),
                       [lwo] "i"(CT_OFF + C_LW - PKM / 2), [lw] "i"(CT_OFF + C_LW), [x1] "i"(CT_OFF + C_X1), [x1n] "i"(X1N),
                       [apk] "v"(apk), [apm] "v"(lbase + PKM + r * 8), [apu] "v"(lbase + PKU + g * 64 + (r & 3) * 16),
                       [ox] "v"(lbase + tr_off(4 * g + qq, 4 * ej + p4)), [oy] "v"(lbase + 32 * ROWB + tr_off(2 * g + (qq >> 1), 4 * ej + p4)),
                       [ca] "v"(ct + (lane == 0 ? C_PFL + (k0 % NPK) * 4 : C_FULL + (k0 % NSLOT) * 4)),
                       [lim] "v"(ct + (lane == 0 ? C_PFL + NPK * 4 : C_FULL + NSLOT * 4)), [base] "v"(ct + (lane == 0 ? C_PFL : C_FULL)), [one] "v"(1u),
                       [k0] "s"(k0), [s0] "s"(helper ? ((TPW - 1) % NSLOT) * SLOT : 0), [e0] "s"(helper ? ((TPW - 1) % NPK) * 128 : 0),
                       [w2] "s"(box + W2_OFF + ej * 18432 - (lbase + IMG_A)), [fl] "s"(box + W2F_OFF),
                       [tag] "s"(u32x2{line_tag(epoch, 10), line_tag(epoch, 11)}), [gcap] "i"(GSPIN_CAP), [iss] "i"(CT_OFF + C_ISS), [cap] "i"(SPIN_CAP), [slot] "i"(SLOT), [ring] "i"(NSLOT * SLOT), [pring] "i"(NPK * 128),
                       [npk2] "i"(2 * NPK), [last] "i"(TPW - 1), [tight] "i"(TPW - 5),
                       [w0] "i"(W2K0 + 1)
, [wl] "i"(2 * W2MID + 2), [wh] "i"(W2MID == 6 ? 12 : 2 * W2MID + 2)
                     : "memory", "scc", "vcc", "s44", "s45", "s46", "s47", "s48", "s49", "s50", "s51", "s52", "s53", "s54", "s55", "s56", "s57",
                       "v0", "v1", "v2", "v3", "v4", "v5", "v6", "v7", "v8", "v9", "v10", "v11", "v12", "v13", "v14", "v15", "v16", "v17", "v18", "v19", "v20", "v21", "v22", "v23", "v24", "v25", "v26", "v27", "v28", "v29", "v30", "v31", "v32", "v33", "v34", "v35", "v36", "v37", "v38", "v39", "v40", "v41", "v42", "v43", "v44", "v45", "v46", "v47", "v48", "v49", "v50", "v51", "v52", "v53", "v54", "v55", "v56", "v57", "v58", "v59", "v60", "v61", "v62", "v63", "v64", "v65", "v66", "v67", "v68", "v69", "v70", "v71", "v72", "v73", "v74", "v75", "v76", "v77", "v78", "v79", "v80", "v81", "v82", "v83", "v84", "v85", "v86", "v87", "v88",
                       "v90", "v91", "v92", "v93", "v94", "v95", "v96", "v97", "v98", "v99", "v100", "v101", "v102", "v103", "v104", "v105", "v106", "v107",
                       "v108", "v109", "v110", "v111", "v112", "v113", "v114", "v115", "v116", "v117", "v118", "v119", "v120", "v121", "v122", "v123",
                       "v124", "v125", "v126", "v127", "v128", "v129", "v130", "v131", "v132", "v133", "v134", "v135", "v136", "v137", "v138", "v139",
                       "v140", "v141", "v142", "v143", "v144", "v145", "v146", "v147", "v148", "v149", "v150", "v151", "v152", "v153", "v154", "v155");
      }
    } else {
      LANE_VARS
      const int c = wave - 1 - NE;
      SCtx sc;
      sc.L0 = L0; sc.ct = ct; sc.apk = lbase + IMG_A + lane * 16; sc.apm = lbase + PKM + r * 8;
      sc.api = lbase + PKU + g * 64 + (r & 3) * 2 + ((r >> 2) == g ? 0 : 8); sc.amr = ct + C_MREF + r * 4;
      sc.o_rb = r * ROWB + ((g ^ swz(r)) << 4);
      sc.ev = (r & 1) ^ 1;
      sc.o_yb = (r & 1) ? ONES_OFF + (g << 4) : (32 + (r >> 1)) * ROWB + ((g ^ swz(r >> 1)) << 4);
      TNOW(tq0);
      if (c >= NEARLY) {
        static_assert(NS - NEARLY == 3, "");
        const int u = c - NEARLY;
        const unsigned epoch = epoch_load(box);
        const unsigned ta = line_tag(epoch, 8), tb = line_tag(epoch, 9);
        while ((unsigned)__builtin_amdgcn_s_memrealtime() - tent < QKWAIT) __builtin_amdgcn_s_sleep(2);
        u32x4 v[4];
#pragma unroll 1
        for (int sp = 0; sp < GSPIN_CAP; ++sp) {
          asm volatile("global_load_dwordx4 %0, %4, %5 sc1\n\tglobal_load_dwordx4 %1, %4, %5 offset:1024 sc1\n\tglobal_load_dwordx4 %2, %4, %5 offset:2048 sc1\n\t"
                       "global_load_dwordx4 %3, %4, %5 offset:3072 sc1\n\ts_waitcnt vmcnt(0)"
                       : "=&v"(v[0]), "=&v"(v[1]), "=&v"(v[2]), "=&v"(v[3]) : "v"(lane * 16), "s"(box + QK_OFF + u * 4096) : "memory");
          unsigned bad = 0;
#pragma unroll
          for (int t = 0; t < 4; ++t) bad |= (v[t][0] ^ v[t][1] ^ v[t][2] ^ ta) | (v[t][3] ^ tb);
          if (!__any(bad != 0u)) break;
        }
        TSET(tqa);
#pragma unroll
        for (int t = 0; t < 4; ++t)
          *(LDS_AS(u32x2)*)(L0 + IMG_A + (4 * u + t) * 512 + ((g >> 1) * 16 + r) * 16 + (g & 1) * 8) = u32x2{v[t][0], v[t][1]};
        asm volatile("" ::: "memory");
        lds_add(ct + C_QI, 1u);
        lds_st32(ct + C_EP, epoch);
      }
      static_assert(NE == 4 && NPK == 6 && NSLOT == 10, "");
      s_items(sc, helper ? TPW : c, helper || c >= NEARLY);
      const int hi = (HMAP >> (4 * c)) & 15;
      if (hi < 6 && (!helper || hi >= 4)) s_half(sc, TPW - NH1 + (hi >> 1), hi & 1);
      TNOW(tse);
      rslot = c;
    }
  }

  if (rslot >= 0 && ((blockIdx.x >> 3) & 3) == 1) {
    LANE_VARS
    __builtin_amdgcn_s_setprio(0);
    const int rid = ((blockIdx.x >> 5) << 3) | (blockIdx.x & 7);
    const int qa = ((rid >> 1) & 3) * 4 + ((rid >> 3) & 1) * 2;
    const char* src = box + (size_t)(rid * NWG + rslot * 32) * 128;
    const int e = lane & 7;
    const int oidx = (qa + (e & 1)) * CC + 48 * (rid >> 4) + 16 * (e < 6 ? e >> 1 : 0) + 8 * (rid & 1) + (lane >> 3);
    const unsigned short qv = ((const gus*)q)[oidx];
    u32x4 am;
#pragma unroll
    for (int d = 0; d < 4; ++d) am[d] = (e >> 1) == d ? ((e & 1) ? 0xffff0000u : 0x0000ffffu) : 0u;
    const unsigned epoch = *(LDS_AS(unsigned)*)(L0 + CT_OFF + C_EP);
    const unsigned arf = lbase + RED_OFF + 4096 + (lane & 1) * 4;
    poll_ge_sleep(ct + C_FREE + ((TPW - NPK) % NSLOT) * 4, 8u * ((TPW - NPK) / NSLOT + 1));
    lds_st32(arf + rslot * 8, 0u);
    unsigned long long tr;
#define R_LD(d, m, o) "s_mov_b64 exec, s[" m "]\n\tglobal_load_dwordx4 v[" d "], %[vo], %[src] offset:" #o " sc1\n\t"
#define R_W(l, a0, a1, a2, a3) "v_cvt_pk_bf16_f32 v" #a3 ", v" #a3 ", v" #a3 "\n\tv_and_b32 v" #a0 ", v60, v" #a3 "\n\tv_and_b32 v" #a1 ", v61, v" #a3 "\n\t" \
                               "v_and_b32 v" #a2 ", v62, v" #a3 "\n\tv_and_b32 v" #a3 ", v63, v" #a3 "\n\t"
#define R_G(n, w, m, l, d0, d1, d2, d3, a0, a1, a2, a3, c) \
  "s_mov_b64 exec, s[" m "]\n\ts_cbranch_execz " #n "f\n\ts_waitcnt vmcnt(" #w ")\n\tv_bitop3_b32 v" #l ", v" #d0 ", v" #d1 ", v" #d2 " bitop3:0x96\n\t" \
  "v_bitop3_b32 v" #l ", v" #l ", v" #d3 ", %[tag] bitop3:0x96\n\tv_mov_b32 v" #d3 ", s49\n\ts_mov_b64 exec, -1\n\tv_exp_f32 v" #a3 ", v" #l "\n\t" \
  "v_xor_b32_dpp v59, v" #l ", v" #l " quad_perm:[2,3,0,1] row_mask:0xf bank_mask:0x5\n\tv_xor_b32_dpp v59, v" #l ", v" #l " row_shr:4 row_mask:0xf bank_mask:0xa\n\t" \
  R_W(l, a0, a1, a2, a3) "v_cmp_ne_u32 s[" m "], 0, v59\n\ts_nop 0\n" \
  "1" #n ":\n\tv_mfma_f32_16x16x32_bf16 v[36:39], v[" #a0 ":" #a3 "], v[" #d0 ":" #d3 "], " c "\n\t"
#define R_X(n) #n ":\n\ts_mov_b64 exec, -1\n\ts_nop 2\n\ts_branch 1" #n "b\n"
#define R_F(l, d0, d3, a0, a1, a2, a3, c) "v_sub_f32 v" #a3 ", v" #l ", v56\n\tv_exp_f32 v" #a3 ", v" #a3 "\n\ts_nop 0\n\t" R_W(l, a0, a1, a2, a3) \
  "s_nop 1\n\tv_mfma_f32_16x16x32_bf16 v[36:39], v[" #a0 ":" #a3 "], v[" #d0 ":" #d3 "], " c "\n\t"
#define R_RD(d, a, b) "ds_read2st64_b32 v[" d "], %[ard] offset0:" #a " offset1:" #b "\n\t"
#define R_A(d, a) "v_pk_add_f32 v[" d "], v[" d "], v[" a "]\n\t"
#define R_S(x, f) "v_sub_f32 v" #f ", v" #f ", v56\n\tv_exp_f32 v" #f ", v" #f "\n\ts_nop 0\n\tv_mul_f32 v[" #x "], v[" #x "], v" #f "\n\tv_mul_f32 v[" #x " + 1], v[" #x " + 1], v" #f "\n\t"
    asm volatile("s_mov_b64 s[52:53], 0\n\ts_mov_b64 s[40:41], -1\n\ts_mov_b64 s[42:43], -1\n\ts_mov_b64 s[44:45], -1\n\ts_mov_b64 s[46:47], -1\n\ts_mov_b32 s48, %[cap]\n\ts_mov_b32 s49, 0x3f803f80\n\t"
                 "v_mov_b32 v57, 1\n\tv_mov_b32 v58, %[x1]\n"
                 "20:\n\ts_sleep 1\n\tds_read_b32 v56, v58\n\ts_sub_u32 s48, s48, 1\n\ts_cselect_b32 s50, 0, %[x1n]\n\ts_waitcnt lgkmcnt(0)\n\t"
                 "v_readfirstlane_b32 s51, v56\n\ts_cmp_lt_u32 s51, s50\n\ts_cbranch_scc1 20b\n\tv_mov_b32 v58, %[red]\n\ts_mov_b32 s48, %[gcap]\n\ts_sleep %[rs]\n"
                 "21:\n\t" R_LD("0:3", "40:41", 0) R_LD("4:7", "42:43", 1024) R_LD("8:11", "44:45", 2048) R_LD("12:15", "46:47", 3072)
                 "s_cmp_eq_u32 s48, %[gcap]\n\ts_cbranch_scc1 24f\n\ts_waitcnt vmcnt(0)\n24:\n\t"
                 R_G(1, 3, "40:41", 32, 0, 1, 2, 3, 16, 17, 18, 19, "0") R_G(2, 2, "42:43", 33, 4, 5, 6, 7, 20, 21, 22, 23, "v[36:39]")
                 R_G(3, 1, "44:45", 34, 8, 9, 10, 11, 24, 25, 26, 27, "v[36:39]") R_G(4, 0, "46:47", 35, 12, 13, 14, 15, 28, 29, 30, 31, "v[36:39]")
                 "v_max3_f32 v56, v32, v33, v34\n\ts_sub_u32 s48, s48, 1\n\ts_cbranch_scc1 22f\n\ts_or_b64 s[50:51], s[40:41], s[42:43]\n\tv_max_f32 v56, v56, v35\n\t"
                 "s_or_b64 s[50:51], s[50:51], s[44:45]\n\ts_or_b64 s[50:51], s[50:51], s[46:47]\n\tv_cmp_nlt_f32 vcc, |v56|, %[lim]\n\ts_cbranch_scc1 21b\n\t"
                 "s_cbranch_vccnz 25f\n"
                 "22:\n\ts_mov_b64 exec, %[wm]\n\tds_write_b128 %[awr], v[36:39]\n\ts_mov_b64 exec, 1\n\tds_add_rtn_u32 v59, v58, v57\n\ts_mov_b64 exec, -1\n\t"
                 R_RD("40:41", 0, 1) R_RD("42:43", 2, 3) R_RD("44:45", 4, 5) R_RD("46:47", 6, 7) R_RD("48:49", 8, 9) R_RD("50:51", 10, 11) R_RD("52:53", 12, 13) R_RD("54:55", 14, 15)
                 "s_waitcnt lgkmcnt(8)\n\tv_add_u32 v59, v59, v57\n\ts_nop 0\n\tv_readfirstlane_b32 s50, v59\n\ts_cmp_lg_u32 s50, 8\n\ts_cbranch_scc1 26f\n"
                 "23:\n\ts_waitcnt lgkmcnt(6)\n\t" R_A("40:41", "42:43") "s_waitcnt lgkmcnt(4)\n\t" R_A("44:45", "46:47") "s_waitcnt lgkmcnt(2)\n\t" R_A("48:49", "50:51")
                 R_A("40:41", "44:45") "s_waitcnt lgkmcnt(0)\n\t" R_A("52:53", "54:55") R_A("48:49", "52:53") R_A("40:41", "48:49")
                 "v_add_f32 v40, v40, v41\n\ts_nop 1\n\tv_mov_b32_dpp v41, v40 quad_perm:[2,3,2,3] row_mask:0xf bank_mask:0xf\n\ts_nop 1\n\t"
                 "v_mov_b32_dpp v41, v41 row_shl:4 row_mask:0xf bank_mask:0x5\n\tv_rcp_f32 v41, v41\n\ts_mov_b64 exec, %[om]\n\tv_mul_f32 v40, v40, v41\n\t"
                 "v_cvt_pk_bf16_f32 v40, v40, v40\n\tv_lshlrev_b32 v40, 16, v40\n\tv_add_f32 v40, v40, %[qf]\n\tv_cvt_pk_bf16_f32 v40, v40, v40\n\t"
                 "global_store_short %[oo], v40, %[out]\n\t"
                 "s_cmp_lg_u32 %[rid], 0\n\ts_cbranch_scc1 29f\n\ts_mov_b64 exec, 1\n\tv_mov_b32 v41, %[eo]\n\tglobal_store_dword v41, %[ep], %[bx] sc1\n\ts_branch 29f\n"
                 R_X(1) R_X(2) R_X(3) R_X(4)
                 "25:\n\tv_max_f32_dpp v56, v56, v56 row_ror:8 row_mask:0xf bank_mask:0xf\n\tv_mov_b32 v59, v56\n\ts_nop 1\n\tv_permlane16_swap_b32 v56, v59\n\tv_max_f32 v56, v56, v59\n\t"
                 "v_mov_b32 v59, v56\n\ts_nop 1\n\tv_permlane32_swap_b32 v56, v59\n\tv_max_f32 v56, v56, v59\n\tv_mov_b32 v57, 0x101\n\tv_add_u32 v59, %[rs8], %[arf]\n\tds_write_b32 v59, v56\n\t"
                 R_F(32, 0, 3, 16, 17, 18, 19, "0") R_F(33, 4, 7, 20, 21, 22, 23, "v[36:39]") R_F(34, 8, 11, 24, 25, 26, 27, "v[36:39]") R_F(35, 12, 15, 28, 29, 30, 31, "v[36:39]")
                 "s_nop 7\n\ts_branch 22b\n"
                 "26:\n\ts_waitcnt vmcnt(0)\n\ts_and_b32 s50, s50, 0xff\n\ts_cmp_lg_u32 s50, 8\n\ts_cbranch_scc1 29f\n\t"
                 "ds_read2_b32 v[0:1], %[arf] offset1:2\n\tds_read2_b32 v[2:3], %[arf] offset0:4 offset1:6\n\tds_read2_b32 v[4:5], %[arf] offset0:8 offset1:10\n\t"
                 "ds_read2_b32 v[6:7], %[arf] offset0:12 offset1:14\n\ts_waitcnt lgkmcnt(0)\n\tv_max3_f32 v56, v0, v1, v2\n\tv_max3_f32 v56, v56, v3, v4\n\t"
                 "v_max3_f32 v56, v56, v5, v6\n\tv_max_f32 v56, v56, v7\n\t"
                 R_S(40, 0) R_S(42, 1) R_S(44, 2) R_S(46, 3) R_S(48, 4) R_S(50, 5) R_S(52, 6) R_S(54, 7) "s_branch 23b\n"
                 "29:\n\ts_mov_b64 exec, -1\n\ts_waitcnt lgkmcnt(0)"
                 : "=&{s[52:53]}"(tr)
                 : "{v[60:63]}"(am), [vo] "v"(lane * 16), [src] "s"(src), [tag] "v"(line_tag(epoch, e)),
                   [awr] "v"(lbase + RED_OFF + (rslot * 2 + (r >> 3)) * 256 + (r & 7) * 32 + (g & 1) * 16), [ard] "v"(lbase + RED_OFF + lane * 4), [arf] "v"(arf),
                   [qf] "v"((unsigned)qv << 16), [oo] "v"(oidx * 2), [out] "s"(KARG(kb, 0)), [ep] "v"(epoch + 1u), [bx] "s"(box), [rid] "s"(rid), [rs8] "s"(rslot * 8),
                   [wm] "s"(0xff00ff0000ff00ffull), [om] "s"(0x3f3f3f3f3f3f3f3full), [lim] "s"(64.f),
                   [cap] "i"(SPIN_CAP), [gcap] "i"(GSPIN_CAP), [x1] "i"(CT_OFF + C_X1), [x1n] "i"(X1N), [red] "i"(CT_OFF + C_RED), [rs] "i"(RSLEEP), [eo] "i"(EPOCH_OFF)
                 : "memory", "scc", "vcc", "s40", "s41", "s42", "s43", "s44", "s45", "s46", "s47", "s48", "s49", "s50", "s51", "v0", "v1", "v2", "v3", "v4", "v5", "v6", "v7", "v8", "v9", "v10", "v11", "v12", "v13", "v14", "v15", "v16", "v17", "v18", "v19", "v20", "v21", "v22", "v23", "v24", "v25", "v26", "v27", "v28", "v29", "v30", "v31", "v32", "v33", "v34", "v35", "v36", "v37", "v38", "v39", "v40", "v41", "v42", "v43", "v44", "v45", "v46", "v47", "v48", "v49", "v50", "v51", "v52", "v53", "v54", "v55", "v56", "v57", "v58", "v59");
  }
}

#undef q
#undef f0
#undef f1
#undef wq
#undef wk
#undef wv
#undef wo
#undef box
extern "C" __global__ void dino_end() {}

extern "C" void kernel_launch(void* const* d_in, const int* in_sizes, int n_in,
                              void* d_out, int out_size, void* d_ws, size_t ws_size,
                              hipStream_t stream) {
  dino_main<<<NWG, 768, 0, stream>>>((const unsigned short*)d_in[0], (const char*)d_in[1], (const char*)d_in[2], (const char*)d_in[3],
                                     (const char*)d_in[4], (const char*)d_in[5], (const char*)d_in[6], (char*)d_ws + WSOFF,
                                     (unsigned short*)d_out);
}
```

```cpp
#include <hip/hip_runtime.h>
#include <hip/hip_bf16.h>
#include <stdint.h>

#define PROFOFF (380ll << 20)
#define TNOW(v) do {} while (0)
#define TPUT(id, v) do {} while (0)

using bf16x8  = __attribute__((ext_vector_type(8))) short;
using f32x4   = __attribute__((ext_vector_type(4))) float;
using f32x2   = __attribute__((ext_vector_type(2))) float;
using u32x4   = __attribute__((ext_vector_type(4))) unsigned;
using u32x2   = __attribute__((ext_vector_type(2))) unsigned;
using v4i16_t = __attribute__((ext_vector_type(4))) short;
typedef __attribute__((address_space(3))) char* lds_ptr;
#define LDS_AS(T) __attribute__((address_space(3))) T

constexpr int CC = 192, IMW = 512;
constexpr int NWG = 256, NS = 7, NE = 4;
constexpr int TPW = 33;
constexpr int ROWB = 384;
constexpr int SLOT = 40 * ROWB;
constexpr int NSLOT = 10;
constexpr int BOX_BYTES = 64 * NWG * 128, EPOCH_OFF = BOX_BYTES, QK_OFF = EPOCH_OFF + 4096, W2F_OFF = QK_OFF + 12288, W2_OFF = W2F_OFF + 1024;
constexpr int GSPIN_CAP = 1 << 16;
#define PACE16 777
#define QKWAIT 300
#define NEARLY 4
#define PRECLR 4
#define RSLEEP 24
#define HDUE 93
#define PACES 810
#define PK1 14
#define AG 100
#define AHI 22
#define ADL 60
#define AMG 200
#define NORELAY
#define HMAP 0x4321085
#define HB 1
#define WSOFF ((ws_size - (4u << 20)) & ~(size_t)0x1fffff)
#define EPM 0
#define RSTART 1880
#define RMODE 1
#define CLRK 17
#define X1N (PRECLR == 5 ? 11u : 5u)
#define RPOL " sc1"
#define SPOL " sc1"
#define RGAP 0
#define RDRY 0
#define RWAIT 76
#define RMIN 30
#define PROBE 0xffffffffffffffffull
#define RELAYT 0
#define PRE5 1
#define SL_S 2
#define SL_FAR 12
#define SL_P 2
#define SL_L 1
#define STR_(x) #x
#define STR(x) STR_(x)
#define AHIR AHI
#define T0BACK 7
#define W2SCH 0x0311180000000000ull
#define NH1 3
constexpr int NPK = 6;
constexpr float EPS = 1e-5f;
constexpr float SQRT_C = 13.856406460551018f;
constexpr float LOG2E = 1.4426950408889634f;
constexpr float THR = 20.0f;
constexpr float TOFF = 192.0f;
constexpr int SPIN_CAP = 1 << 22;

constexpr int IMG_A = NSLOT * SLOT;
constexpr int CT_OFF = IMG_A + 6144;
constexpr int PKM = CT_OFF + 512, PKU = PKM + NPK * 128;
constexpr int ONES_OFF = PKU + NPK * 256;
constexpr int JUNK = ONES_OFF + ROWB;
constexpr int LDS_BYTES = JUNK + 256;
constexpr int H_R = 192 * ROWB, H_Q = H_R + 32 * ROWB, H_C = H_Q + 6144;
#define PWIN 35
static_assert(LDS_BYTES <= 163840 && (PKU & 15) == 0 && (ONES_OFF & 127) == 0, "lds");
constexpr int C_FULL = 0, C_FREE = 64, C_EP = 128, C_RED = 144, C_ISS = 152, C_X1 = 156, C_QI = 160, C_PFL = 192, C_CONS = 256,
              C_MREF = 320, C_LW = 384;

typedef __bf16 bf16x2_t __attribute__((ext_vector_type(2)));
__device__ __forceinline__ unsigned cvtpk(float lo, float hi) {
  return __builtin_bit_cast(unsigned, __builtin_convertvector(f32x2{lo, hi}, bf16x2_t));
}
__device__ __forceinline__ float rbf(float x) { return __uint_as_float(cvtpk(x, x) << 16); }
__device__ __forceinline__ float bflo(unsigned p) { return __uint_as_float(p << 16); }
__device__ __forceinline__ float bfhi(unsigned p) { return __uint_as_float(p & 0xffff0000u); }
__device__ __forceinline__ bf16x8 pack8(const f32x4& a, const f32x4& b) {
  return __builtin_bit_cast(bf16x8, u32x4{cvtpk(a[0], a[1]), cvtpk(a[2], a[3]), cvtpk(b[0], b[1]), cvtpk(b[2], b[3])});
}
__device__ __forceinline__ float xrow16_max(float x) {
  auto s = __builtin_amdgcn_permlane16_swap(__float_as_uint(x), __float_as_uint(x), false, false);
  x = fmaxf(__uint_as_float(s[0]), __uint_as_float(s[1]));
  auto t = __builtin_amdgcn_permlane32_swap(__float_as_uint(x), __float_as_uint(x), false, false);
  return fmaxf(__uint_as_float(t[0]), __uint_as_float(t[1]));
}
__device__ __forceinline__ float xrow16_sum(float x) {
  auto s = __builtin_amdgcn_permlane16_swap(__float_as_uint(x), __float_as_uint(x), false, false);
  x = __uint_as_float(s[0]) + __uint_as_float(s[1]);
  auto t = __builtin_amdgcn_permlane32_swap(__float_as_uint(x), __float_as_uint(x), false, false);
  return __uint_as_float(t[0]) + __uint_as_float(t[1]);
}
__device__ __forceinline__ v4i16_t tr_read(lds_ptr p) {
  return __builtin_amdgcn_ds_read_tr16_b64_v4i16((LDS_AS(v4i16_t)*)p);
}
__device__ __forceinline__ f32x4 mfma(bf16x8 a, bf16x8 b, f32x4 c) {
  return __builtin_amdgcn_mfma_f32_16x16x32_bf16(a, b, c, 0, 0, 0);
}
__device__ __forceinline__ int swz(int p) { return (p & 6) | ((p >> 2) & 1); }
constexpr unsigned swz_tbl(int i) {
  unsigned t = 0;
  for (int j = 0; j < 8; ++j) { const int p = (8 * i + j) / 3; t |= (unsigned)((p & 6) | ((p >> 2) & 1)) << (4 * j); }
  return t;
}
__device__ __forceinline__ bf16x8 mk8(v4i16_t a, v4i16_t b) { return bf16x8{a[0], a[1], a[2], a[3], b[0], b[1], b[2], b[3]}; }

__device__ __forceinline__ void lds_st32(unsigned addr, unsigned v) {
  asm volatile("ds_write_b32 %0, %1" : : "v"(addr), "v"(v) : "memory");
}
__device__ __forceinline__ unsigned lds_ld32(unsigned addr) {
  unsigned v;
  asm volatile("ds_read_b32 %0, %1\n\ts_waitcnt lgkmcnt(0)" : "=v"(v) : "v"(addr) : "memory");
  return __builtin_amdgcn_readfirstlane(v);
}
__device__ __forceinline__ void lds_add(unsigned addr, unsigned n) {
  asm volatile("s_mov_b64 exec, 1\n\tds_add_u32 %0, %1\n\ts_mov_b64 exec, -1\n\ts_nop 3" : : "v"(addr), "v"(n) : "memory");
}
__device__ __forceinline__ void poll_ge(unsigned addr, unsigned need) {
  unsigned v, s, n2, cnt = SPIN_CAP;
  asm volatile("1:\n\tds_read_b32 %0, %4\n\ts_sub_u32 %3, %3, 1\n\ts_cselect_b32 %2, 0, %5\n\ts_waitcnt lgkmcnt(0)\n\t"
               "v_readfirstlane_b32 %1, %0\n\ts_cmp_lt_u32 %1, %2\n\ts_cbranch_scc1 1b"
               : "=&v"(v), "=&s"(s), "=&s"(n2), "+s"(cnt) : "v"(addr), "s"(need) : "memory", "scc");
}
__device__ __forceinline__ void poll_ge_sleep(unsigned addr, unsigned need) {
  need = __builtin_amdgcn_readfirstlane(need);
  unsigned v, s, n2, cnt = SPIN_CAP;
  asm volatile("s_branch 2f\n1:\n\ts_sleep " STR(SL_S) "\n2:\n\tds_read_b32 %0, %4\n\ts_sub_u32 %3, %3, 1\n\ts_cselect_b32 %2, 0, %5\n\ts_waitcnt lgkmcnt(0)\n\t"
               "v_readfirstlane_b32 %1, %0\n\ts_cmp_lt_u32 %1, %2\n\ts_cbranch_scc1 1b"
               : "=&v"(v), "=&s"(s), "=&s"(n2), "+s"(cnt) : "v"(addr), "s"(need) : "memory", "scc");
}
__device__ __forceinline__ bf16x8 img_ld(lds_ptr L0, int off, int kk, int lane) { return *(LDS_AS(bf16x8)*)(L0 + off + kk * 1024 + lane * 16); }
__device__ __forceinline__ void img_st(lds_ptr L0, int off, int kk, int lane, bf16x8 v) { *(LDS_AS(bf16x8)*)(L0 + off + kk * 1024 + lane * 16) = v; }

template <int N> __device__ __forceinline__ void wait_vm() { asm volatile("s_waitcnt vmcnt(%0)" :: "i"(N) : "memory"); }
constexpr int A_IMG = ((TPW + 1 - NPK) % NSLOT) * SLOT, RED_OFF = ((TPW - NPK) % NSLOT) * SLOT;
__device__ __forceinline__ unsigned line_tag(unsigned epoch, unsigned i) {
  unsigned x = (epoch + 1u + i * 0x9E3779B9u) * 0x85EBCA6Bu;
  x ^= x >> 15; x *= 0xC2B2AE35u;
  return x ^ (x >> 13);
}
__device__ __forceinline__ unsigned epoch_load(const char* box) {
  unsigned e;
  asm volatile("global_load_dword %0, %1, %2 sc1\n\ts_waitcnt vmcnt(0)" : "=v"(e) : "v"(EPOCH_OFF), "s"(box) : "memory");
  return __builtin_amdgcn_readfirstlane(e);
}
__device__ __forceinline__ int tr_off(int row, int col4) {
  const int ch = col4 >> 1;
  return row * ROWB + (((ch & ~7) | ((ch & 7) ^ swz(row & 15))) << 4) + 8 * (col4 & 1);
}
#define PIN3(a, i) "+v"(a[i]), "+v"(a[i + 1]), "+v"(a[i + 2])

#define DMA1(d, b, off, pol) "global_load_lds_dwordx4 %[" #d "], %[" #b "] offset:" #off pol "\n\t"
__device__ __forceinline__ void issue_rows16(const char* a0, unsigned dst, const unsigned (&d)[3]) {
  unsigned keep;
  asm volatile("s_mov_b32 %[keep], m0\n\ts_mov_b32 m0, %[dst]\n\ts_nop 0\n\t"
               DMA1(d0, a, 0, "") DMA1(d1, a, 1024, "") DMA1(d2, a, 2048, "") DMA1(d0, a, 3072, "")
               "s_add_u32 m0, %[dst], 0x1000\n\ts_nop 0\n\t" DMA1(d1, b, 0, "") DMA1(d2, b, 1024, "") "s_mov_b32 m0, %[keep]"
               : [keep] "=&s"(keep) : [dst] "s"(dst), [a] "s"(a0), [b] "s"(a0 + 4096), [d0] "v"(d[0]), [d1] "v"(d[1]), [d2] "v"(d[2]) : "memory", "scc");
}
#define LD_VM "s_getreg_b32 s43, hwreg(HW_REG_IB_STS, 22, 2)\n\ts_getreg_b32 s42, hwreg(HW_REG_IB_STS, 0, 4)\n\ts_lshl4_add_u32 s42, s43, s42\n\t"
#define LD_GATE(n, X) \
  #n "0:\n\t" LD_VM "s_cmp_le_i32 s42, s46\n\ts_cbranch_scc1 " #n "2f\n" \
  #n "1:\n\t" X "s_cmp_gt_u32 s42, %[win]\n\ts_cbranch_scc1 " #n "5f\n\ts_cmp_ge_u32 s45, s44\n\ts_cbranch_scc1 " #n "7f\n" \
  #n "6:\n\ts_memrealtime s[40:41]\n\ts_sleep " STR(SL_L) "\n\ts_waitcnt lgkmcnt(0)\n\ts_sub_u32 s45, s40, %[t0]\n\ts_lshl_b32 s45, s45, 4\n\ts_cmp_lt_u32 s45, s44\n\ts_cbranch_scc1 " #n "6b\n" \
  #n "7:\n\t" LD_VM
#define LD_NEXT \
  "s_cmp_gt_u32 s42, %[ahi]\n\ts_cselect_b32 s42, %[amg], 0\n\ts_sub_u32 s43, s45, s44\n\ts_cmp_lt_u32 s43, s42\n\ts_cselect_b32 s42, %[p3] + %[adl], s39\n\t" \
  "s_add_u32 s44, s44, s42\n\ts_add_u32 s46, s46, 5\n\t"
#define LD_PUB0(n, a, b, c) \
  #n "2:\n\tv_add_u32 %[pub], 2, %[pub]\n\ts_cmp_ge_u32 s46, 30\n\ts_cbranch_scc1 " #n "4f\n\ts_cmp_ge_u32 s46, 15\n\ts_cbranch_scc1 " #n "3f\n\t" \
  "s_waitcnt vmcnt(" #a ")\n" #n "3:\n\ts_waitcnt vmcnt(" #b ")\n" #n "4:\n\ts_waitcnt vmcnt(" #c ")\n\tds_write_b32 %[af], %[pub] ; NOTRACK\n\t" \
  "s_sub_u32 s46, s46, 15\n\ts_branch " #n "1b\n"
#define LD_PUB(n, a, b, c) LD_PUB0(n, a, b, c) \
  #n "5:\n\ts_sleep 1\n\ts_sub_i32 s47, s47, 1\n\ts_cmp_gt_i32 s47, 0\n\ts_cbranch_scc1 " #n "0b\n\ts_waitcnt vmcnt(%[win])\n\ts_branch " #n "6b\n"
#define LD1(d, b, off) "global_load_lds_dwordx4 %[" #d "], s[" #b "] offset:" #off " nt\n\t"

#define PV_REQ(P, U, M, C, CA) \
  "v_lshl_add_u32 v150, s44, 3, %[apk]\n\tv_lshl_add_u32 v151, s44, 1, %[apu]\n\tv_add_u32 v152, s44, %[apm]\n\t" \
  "ds_read_b32 v" #C ", v" #CA "\n\tds_read_b128 v[" P "], v150\n\tds_read_b64 v[" M "], v152\n\tds_read_b128 v[" U "], v151\n\t"
#define PV_TR(d0, d1, a, o) "ds_read_b64_tr_b16 v[" #d0 ":" #d1 "], v" #a " offset:" #o "\n\t"
#define PV_FETCH(y0, y0b, a0, a0b, b0, b0b, y1, y1b, a1, a1b, b1, b1b, y2, y2b, a2, a2b, b2, b2b) \
  "v_add_u32 v153, s45, %[ox]\n\tv_add_u32 v154, s45, %[oy]\n\t" \
  PV_TR(a0, a0b, 153, 0) PV_TR(b0, b0b, 153, 6144) PV_TR(a1, a1b, 153, 128) PV_TR(b1, b1b, 153, 6272) PV_TR(a2, a2b, 153, 256) PV_TR(b2, b2b, 153, 6400) \
  PV_TR(y0, y0b, 154, 0) PV_TR(y1, y1b, 154, 128) PV_TR(y2, y2b, 154, 256) \
  "s_add_u32 s45, s45, %[slot]\n\ts_cmp_eq_u32 s45, %[ring]\n\ts_cselect_b32 s45, 0, s45\n\t"
#define PV_FETCH_A PV_FETCH(90, 91, 92, 93, 94, 95, 96, 97, 98, 99, 100, 101, 102, 103, 104, 105, 106, 107)
#define PV_FETCH_B PV_FETCH(108, 109, 110, 111, 112, 113, 114, 115, 116, 117, 118, 119, 120, 121, 122, 123, 124, 125)
#define PV_MF(d, a, p) "v_mfma_f32_16x16x32_bf16 v[" d "], v[" a "], v[" p "], v[" d "]\n\t"
#define PV_MOV(d, s) "v_mov_b32 v" #d ", v" #s "\n\t"
#define PV_WAITF(n, CA)   \
  #n ":\n\tds_read_b32 v155, v" #CA "\n\ts_sub_i32 s47, s47, 1\n\ts_cmp_lt_i32 s47, 0\n\ts_cbranch_scc1 " #n "9f\n\ts_waitcnt lgkmcnt(0)\n\t" \
  "v_cmp_lt_u32 vcc, v155, %[need]\n\ts_and_b64 vcc, vcc, 2\n\ts_cbranch_scc0 " #n "9f\n\ts_sleep " STR(SL_P) "\n\ts_branch " #n "b\n" #n "9:\n\ts_waitcnt lgkmcnt(0)\n\t"
#define PV_STEP(n, P, U, M, m0, m1, C, CA, P2, U2, M2, C2, CA2, X0, X1, X2, Y0, Y1, Y2, MV0, MV1, MV2, HOOK) \
  "s_waitcnt lgkmcnt(10)\n\t" \
  "v_cmp_lt_u32 vcc, v" #C ", %[need]\n\tv_cmp_neq_f32 s[48:49], v" #m0 ", v88\n\tv_cmp_neq_f32 s[50:51], v" #m1 ", v88\n\t" \
  "v_add_u32 v" #CA2 ", 4, v" #CA "\n\ts_or_b64 s[48:49], s[48:49], vcc\n\tv_cmp_eq_u32 s[52:53], v" #CA2 ", %[lim]\n\t" \
  "s_or_b64 s[48:49], s[48:49], s[50:51]\n\ts_cbranch_scc1 " #n "1f\n" \
  #n "0:\n\ts_add_u32 s44, s44, 128\n\ts_cmp_eq_u32 s44, %[pring]\n\ts_cselect_b32 s44, 0, s44\n\t" \
  "v_cndmask_b32 v" #CA2 ", v" #CA2 ", %[base], s[52:53]\n\t" \
  PV_MF("84:87", U, P) PV_MF("72:75", X0, P) PV_MF("76:79", X1, P) MV0 PV_MF("80:83", X2, P) MV1 \
  PV_MF("72:75", Y0, P) MV2 HOOK PV_REQ(P2, U2, M2, C2, CA2) PV_MF("76:79", Y1, P) PV_MF("80:83", Y2, P)
#define PV_REL(CA) \
  "s_mov_b64 exec, 3\n\tds_add_u32 v" #CA ", %[one] offset:64\n\ts_mov_b64 exec, -1\n\tv_add_u32 %[need], 2, %[need]\n\ts_add_u32 s46, s46, 1\n\t"
#define PV_SC1(p) "v_lshlrev_b32 v151, 16, v" #p "\n\tv_and_b32 v152, 0xffff0000, v" #p "\n\tv_mul_f32 v151, v151, v150\n\tv_mul_f32 v152, v152, v150\n\t" \
                  "v_cvt_pk_bf16_f32 v" #p ", v151, v152\n\t"
#define PV_MUL4(a, b, c, d) "v_mul_f32 v" #a ", v" #a ", v150\n\tv_mul_f32 v" #b ", v" #b ", v150\n\tv_mul_f32 v" #c ", v" #c ", v150\n\tv_mul_f32 v" #d ", v" #d ", v150\n\t"
#define PV_SLOW(n, P, U, M, m0, m1, C, CA, p0, p1, p2, p3) \
  #n "1:\n\ts_bitcmp0_b32 vcc_lo, 0\n\ts_cbranch_scc1 " #n "4f\n\ts_cmp_gt_u32 s46, %[tight]\n\ts_cbranch_scc1 " #n "3f\n" \
  #n "2:\n\ts_sleep " STR(SL_P) "\n\tds_read_b32 v" #C ", v" #CA "\n\ts_sub_i32 s47, s47, 1\n\ts_cmp_lt_i32 s47, 0\n\ts_cbranch_scc1 " #n "3f\n\ts_waitcnt lgkmcnt(0)\n\t" \
  "v_cmp_lt_u32 vcc, v" #C ", %[need]\n\ts_and_b64 vcc, vcc, 1\n\ts_cbranch_scc1 " #n "2b\n" \
  #n "3:\n\t" PV_REQ(P, U, M, C, CA) "s_sub_i32 s47, s47, 1\n\ts_cmp_lt_i32 s47, 0\n\ts_cselect_b32 s54, 0, 1\n\ts_waitcnt lgkmcnt(0)\n\t" \
  "v_cmp_lt_u32 vcc, v" #C ", %[need]\n\tv_cmp_neq_f32 s[48:49], v" #m0 ", v88\n\tv_cmp_neq_f32 s[50:51], v" #m1 ", v88\n\t" \
  "s_and_b32 s54, s54, vcc_lo\n\ts_cbranch_scc1 " #n "3b\n\ts_or_b64 s[48:49], s[48:49], s[50:51]\n\ts_cbranch_scc0 " #n "0b\n\ts_branch " #n "5f\n" \
  #n "4:\n\tv_cmp_neq_f32 s[48:49], v" #m0 ", v88\n\ts_or_b64 s[48:49], s[48:49], s[50:51]\n\ts_cbranch_scc0 " #n "0b\n" \
  #n "5:\n\tv_mov_b32 v150, v88\n\tv_max3_f32 v88, v88, v" #m0 ", v" #m1 "\n\tv_sub_f32 v150, v150, v88\n\tv_exp_f32 v150, v150\n\ts_nop 1\n\t" \
  PV_MUL4(72, 73, 74, 75) PV_MUL4(76, 77, 78, 79) PV_MUL4(80, 81, 82, 83) PV_MUL4(84, 85, 86, 87) \
  "v_sub_f32 v150, v" #m0 ", v88\n\tv_exp_f32 v150, v150\n\ts_nop 1\n\t" PV_SC1(p0) PV_SC1(p1) \
  "v_sub_f32 v150, v" #m1 ", v88\n\tv_exp_f32 v150, v150\n\ts_nop 1\n\t" PV_SC1(p2) PV_SC1(p3) \
  "s_branch " #n "0b\n"
#define PV_W2(a0, a1, b0, b1, c0, c1) \
  "global_load_dwordx4 v[" #a0 ":" #a1 "], v150, %[w2] sc1\n\tglobal_load_dwordx4 v[" #b0 ":" #b1 "], v150, %[w2] offset:1024 sc1\n\t" \
  "global_load_dwordx4 v[" #c0 ":" #c1 "], v150, %[w2] offset:2048 sc1\n\tv_add_u32 v150, 0xc00, v150\n\ts_sub_u32 s54, s54, 1\n\ts_cbranch_scc1 30b\n\t"

#define PV_NRM(a, b, c, d, o)   \
  "v_pk_fma_f32 v[" #a ":" #b "], v[" #a ":" #b "], v[150:151], v[150:151] op_sel:[0,0,1] op_sel_hi:[1,0,1]\n\t" \
  "v_pk_fma_f32 v[" #c ":" #d "], v[" #c ":" #d "], v[150:151], v[150:151] op_sel:[0,0,1] op_sel_hi:[1,0,1]\n\t" \
  "v_cvt_pk_bf16_f32 v" #a ", v" #a ", v" #b "\n\tv_cvt_pk_bf16_f32 v" #b ", v" #c ", v" #d "\n\tds_write_b64 v152, v[" #a ":" #b "] offset:" #o "\n\t"
#define PV_G(n, a, w0, w1, w2, c0, c1, c2) "s_waitcnt lgkmcnt(" #n ")\n\tv_mfma_f32_16x16x32_bf16 v[72:75], v[" a "], v[" w0 "], " c0 "\n\t" \
  "v_mfma_f32_16x16x32_bf16 v[76:79], v[" a "], v[" w1 "], " c1 "\n\tv_mfma_f32_16x16x32_bf16 v[80:83], v[" a "], v[" w2 "], " c2 "\n\t"
#define PV_Z4(a, b, c, d) "v_mov_b32 v" #a ", 0\n\tv_mov_b32 v" #b ", 0\n\tv_mov_b32 v" #c ", 0\n\tv_mov_b32 v" #d ", 0\n\t"
#define PV_T(n)

using u32x8 = __attribute__((ext_vector_type(8))) unsigned;
struct SCtx {
  lds_ptr L0;
  unsigned ct, apk, apm, api, amr;
  int o_rb, o_yb, ev;
  u32x8 q[3];
  unsigned tq;
};
__device__ __forceinline__ bf16x8 s_qf(const SCtx& c, int j) {
  const u32x8& v = c.q[j >> 1];
  const int o = 4 * (j & 1);
  return __builtin_bit_cast(bf16x8, u32x4{v[o], v[o + 1], v[o + 2], v[o + 3]});
}
#define S_M(d, a, b) "v_mfma_f32_16x16x32_bf16 v[" d "], v[" a "], v[" b "], v[" d "]\n\t"
#define S_M0(d, a, b) "v_mfma_f32_16x16x32_bf16 v[" d "], v[" a "], v[" b "], 0\n\t"
#define S_R(d, a, o) "ds_read_b128 v[" d "], v" #a " offset:" #o "\n\t"
#define S_W(n) "s_waitcnt lgkmcnt(" #n ")\n\t"
#define S_D " row_mask:0xf bank_mask:0xf\n\t"
#define S_3(x, y) S_M("100:103", x, y) S_M("104:107", x, x) S_M("96:99", y, y)
#define S_SQ(g, h) "v_fmac_f32 v" #g ", 2.0, v" #h "\n\t"
#define S_SQD(g, h) "v_fmac_f32_dpp v" #g ", v" #h ", %[two] quad_perm:[0,0,2,2]" S_D
#define S_U(u, h, y) "v_add_f32_dpp v" #u ", v" #h ", v" #y " quad_perm:[1,1,3,3]" S_D
#define S_C(d, a, b, m) "v_cndmask_b32 v" #d ", v" #a ", v" #b ", %[" #m "]\n\t"
#define S_B(d, n) "v_mov_b32_dpp v" #d ", v157 row_newbcast:" #n S_D
#define S_BASE(d, u, t) "v_pk_fma_f32 v[" d "], v[" u "], v[116:117], v[" t "] op_sel:[0,1,0] op_sel_hi:[1,1,0]\n\t"
#define S_LG(d, r) "v_pk_fma_f32 v[" d "], v[" d "], v[" r "], %[toff] op_sel_hi:[1,1,0]\n\t"
#define S_SUB(p, d) "v_pk_add_f32 v[" p "], v[" d "], v[150:151] op_sel_hi:[1,0] neg_lo:[0,1] neg_hi:[0,1]\n\t"
#define S_E(p) "v_exp_f32 v" #p ", v" #p "\n\t"
#define S_MUL(p, r) "v_pk_mul_f32 v[" p "], v[" p "], v[" r "]\n\t"
#define S_POS   \
  "s_mul_i32 s50, s42, 26\n\ts_lshr_b32 s50, s50, 8\n\ts_mul_i32 s50, s50, -10\n\ts_add_u32 s50, s50, s42\n\ts_mul_i32 s45, s50, %[slot]\n\ts_mov_b32 s43, s45\n\t"
#define S_POLL(n, a, o, need, back, far) \
  #n ":\n\tds_read_b32 v152, v" #a " offset:" #o "\n\ts_sub_i32 s47, s47, 1\n\ts_cmp_lt_i32 s47, 0\n\ts_waitcnt lgkmcnt(0)\n\ts_cbranch_scc1 " back "\n\t" \
  "v_readfirstlane_b32 s40, v152\n\ts_cmp_ge_u32 s40, " need "\n\ts_cbranch_scc1 " back "\n\ts_sleep " STR(SL_S) "\n\t" \
  "s_add_u32 s40, s40, " far "\n\ts_cmp_ge_u32 s40, " need "\n\ts_cbranch_scc1 " #n "b\n\ts_sleep " STR(SL_FAR) "\n\ts_branch " #n "b\n"
__device__ __forceinline__ void s_items(SCtx& c, int k0, int late) {
  unsigned long long tq;
  asm volatile("s_mov_b32 s47, %[cap]\n\ts_mov_b32 s49, 0\n\ts_mov_b32 s42, %[k0]\n\ts_cmp_lg_u32 %[late], 0\n\ts_cbranch_scc1 7f\n"
               "1:\n\ts_cmp_ge_u32 s42, %[end]\n\ts_cbranch_scc1 9f\n\t"
               S_POS
               "s_mul_i32 s48, s42, 43\n\ts_lshr_b32 s48, s48, 8\n\ts_mul_i32 s46, s48, -6\n\ts_add_u32 s46, s46, s42\n\ts_lshl2_add_u32 s48, s48, %[ns]\n\t"
               "v_add_u32 v144, s45, %[orb]\n\tv_mad_u32_u24 v145, s43, %[ev], %[oyb]\n\ts_lshl2_add_u32 s40, s46, %[pfl]\n\ts_lshl2_add_u32 s41, s50, %[full]\n\t"
               "v_xor_b32 v146, 64, v144\n\tv_xor_b32 v147, 64, v145\n\tv_mov_b32 v148, s40\n\tv_mov_b32 v149, s41\n\t"
               "s_lshl1_add_u32 s44, s42, 2\n\ts_lshl_b32 s46, s46, 7\n\ts_mov_b64 vcc, %[dg]\n\ts_cmp_lt_u32 s49, s44\n\ts_cbranch_scc1 5f\n"
               "2:\n\tds_read_b32 v150, %[amr]\n\tds_read_b32 v151, v148 offset:64\n\t"
               S_R("24:27", 145, 0) S_R("48:51", 144, 0) S_R("28:31", 147, 0) S_R("52:55", 146, 0) S_R("32:35", 145, 128) S_R("56:59", 144, 128)
               S_R("36:39", 147, 128) S_R("60:63", 146, 128) S_R("40:43", 145, 256) S_R("64:67", 144, 256) S_R("44:47", 147, 256) S_R("68:71", 146, 256)
               S_W(10) S_M0("100:103", "48:51", "24:27") S_M0("104:107", "48:51", "48:51") S_R("72:75", 144, 6144) S_R("76:79", 146, 6144) S_M0("96:99", "24:27", "24:27")
               S_W(10) S_M("100:103", "52:55", "28:31") S_M("104:107", "52:55", "52:55") S_R("80:83", 144, 6272) S_R("84:87", 146, 6272) S_M("96:99", "28:31", "28:31")
               S_W(10) S_M("100:103", "56:59", "32:35") S_M("104:107", "56:59", "56:59") S_R("88:91", 144, 6400) S_R("92:95", 146, 6400) S_M("96:99", "32:35", "32:35")
               "s_mov_b64 exec, 1\n\tds_add_u32 v149, %[four] offset:64\n\ts_mov_b64 exec, -1\n\tv_readfirstlane_b32 s51, v151\n\ts_setprio 1\n\ts_nop 0\n\t"
               S_W(11) S_3("60:63", "36:39") S_W(9) S_3("64:67", "40:43") S_W(7) S_3("68:71", "44:47")
               S_W(6) S_M0("108:111", "72:75", "24:27") S_M0("112:115", "72:75", "72:75")
               S_W(5) S_M("108:111", "76:79", "28:31") S_M("112:115", "76:79", "76:79")
               S_W(4) S_M("108:111", "80:83", "32:35")
               "v_mov_b32_dpp v152, v96 quad_perm:[1,1,3,3]" S_D "v_mov_b32_dpp v153, v98 quad_perm:[1,1,3,3]" S_D
               S_M("112:115", "80:83", "80:83") S_C(154, 96, 98, r2) S_SQ(104, 100)
               S_W(3) S_M("108:111", "84:87", "36:39") S_SQD(105, 101) S_SQ(106, 102)
               S_M("112:115", "84:87", "84:87") S_SQD(107, 103) "v_add_f32 v154, %[eps], v154\n\t"
               S_W(2) S_M("108:111", "88:91", "40:43") S_U(128, 100, 152) S_U(129, 101, 152)
               S_M("112:115", "88:91", "88:91") S_U(130, 102, 153) S_U(131, 103, 153)
               S_W(1) S_M("108:111", "92:95", "44:47") S_C(104, 104, 105, r1) S_C(106, 106, 107, r1)
               S_M("112:115", "92:95", "92:95") S_C(155, 128, 129, r1) S_C(156, 130, 131, r1)
               "s_cmp_lt_u32 s42, %[early]\n\ts_cbranch_scc1 7f\n"
               "3:\n\t" S_M0("116:119", "24:27", "0:3") S_C(104, 104, 106, r2) S_C(155, 155, 156, r2)
               S_M("116:119", "28:31", "4:7") "v_add_f32_dpp v104, v154, v104 quad_perm:[0,0,2,2]" S_D S_U(132, 108, 152)
               S_M("116:119", "32:35", "8:11") S_U(133, 109, 152) S_U(134, 110, 153)
               S_M("116:119", "36:39", "12:15") S_U(135, 111, 153) S_SQ(112, 108)
               S_M("116:119", "40:43", "16:19") S_SQD(113, 109) S_SQ(114, 110)
               S_M("116:119", "44:47", "20:23") S_SQD(115, 111) S_C(156, 132, 133, r1) S_C(157, 134, 135, r1)
               S_C(112, 112, 113, r1) S_C(114, 114, 115, r1) S_C(156, 156, 157, r2) S_C(112, 112, 114, r2)
               "v_add_f32_dpp v112, v154, v112 quad_perm:[0,0,2,2]" S_D
               "v_mul_f32 v117, %[nic], v117\n\tv_cndmask_b32_dpp v155, v156, v155, vcc row_ror:4" S_D
               S_BASE("120:121", "128:129", "116:117") S_BASE("122:123", "130:131", "118:119")
               "v_cndmask_b32_dpp v104, v112, v104, vcc row_ror:4" S_D "v_mul_f32 v156, %[nic], v155\n\t"
               S_M("120:123", "48:51", "0:3") S_BASE("124:125", "132:133", "116:117") S_BASE("126:127", "134:135", "118:119")
               S_M("120:123", "52:55", "4:7") "v_fmac_f32 v104, v156, v155\n\tv_max_f32 v104, %[eps], v104\n\t"
               S_M("120:123", "56:59", "8:11") "v_rsq_f32 v157, v104\n\t"
               S_M("120:123", "60:63", "12:15") "v_mul_f32 v104, v104, v157\n\tv_mov_b32_dpp v157, v157 row_ror:12 row_mask:0x2 bank_mask:0xf\n\t"
               S_M("120:123", "64:67", "16:19") "v_cvt_pk_bf16_f32 v105, v155, v104\n\tv_mov_b32_dpp v157, v157 row_ror:8 row_mask:0x4 bank_mask:0xf\n\t"
               S_M("120:123", "68:71", "20:23") "v_lshlrev_b32 v106, 16, v105\n\tv_mov_b32_dpp v157, v157 row_ror:4 row_mask:0x8 bank_mask:0xf\n\t"
               S_M("124:127", "72:75", "0:3") "v_and_b32 v107, 0xffff0000, v105\n\t" S_B(136, 0)
               S_M("124:127", "76:79", "4:7") S_B(137, 1) S_B(138, 2)
               S_M("124:127", "80:83", "8:11") S_B(139, 3) S_LG("120:121", "136:137")
               S_M("124:127", "84:87", "12:15") S_LG("122:123", "138:139") S_SUB("128:129", "120:121")
               S_M("124:127", "88:91", "16:19") S_SUB("130:131", "122:123") S_E(128)
               S_M("124:127", "92:95", "20:23") S_E(129) S_E(130) S_E(131)
               S_B(140, 4) S_B(141, 5) S_B(142, 6) S_B(143, 7) "v_sub_f32 v106, v155, v106\n\t"
               S_LG("124:125", "140:141") S_LG("126:127", "142:143") "v_sub_f32 v107, v104, v107\n\tv_max3_f32 v152, v120, v121, v122\n\t"
               S_SUB("132:133", "124:125") S_SUB("134:135", "126:127") "v_max3_f32 v153, v123, v124, v125\n\t"
               S_E(132) S_E(133) S_E(134) S_E(135)
               "v_max3_f32 v152, v152, v126, v127\n\tv_add_f32 v154, %[thr], v150\n\tv_max_f32 v152, v152, v153\n\tv_cvt_pk_bf16_f32 v106, v106, v107\n\t"
               "v_lshl_add_u32 v144, s46, 3, %[apk]\n\tv_cmp_gt_f32 vcc, v152, v154\n\tv_add_u32 v145, s46, %[apm]\n\tv_lshl_add_u32 v146, s46, 1, %[api]\n\t"
               "s_cbranch_vccnz 8f\n"
               "4:\n\t" S_MUL("128:129", "136:137") S_MUL("130:131", "138:139") S_MUL("132:133", "140:141") S_MUL("134:135", "142:143")
               "v_cvt_pk_bf16_f32 v128, v128, v129\n\tv_cvt_pk_bf16_f32 v129, v130, v131\n\tv_cvt_pk_bf16_f32 v130, v132, v133\n\tv_cvt_pk_bf16_f32 v131, v134, v135\n\t"
               "s_cmp_lt_u32 s51, s48\n\ts_cbranch_scc1 6f\n"
               "10:\n\tds_read_b32 v151, v149\n\tds_write_b128 v144, v[128:131]\n\tds_write2_b32 v145, v150, v150 offset0:0 offset1:1\n\ts_mov_b64 exec, %[dg2]\n\t"
               "ds_write_b16 v146, v105\n\tds_write_b16 v146, v106 offset:16\n\tds_write_b16_d16_hi v146, v105 offset:32\n\tds_write_b16_d16_hi v146, v106 offset:48\n\t"
               "s_mov_b64 exec, 1\n\tds_add_u32 v148, %[inc]\n\ts_mov_b64 exec, -1\n\ts_setprio 0\n\ts_add_u32 s42, s42, %[ns]\n\t"
               S_W(7) "v_readfirstlane_b32 s49, v151\n\ts_branch 1b\n"
               S_POLL(5, 149, 0, "s44", "2b", "2")
               S_POLL(6, 148, 64, "s48", "10b", "64")
               "7:\n\tv_mov_b32 v144, %[qi]\n\tv_mov_b32 v145, v152\n\tv_mov_b32 v146, 1\n"
               S_POLL(70, 144, 0, "%[nq]", "71f", "64")
               "71:\n\tv_mov_b32 v152, v145\n\tds_read_b128 v[0:3], %[apk]\n\tds_read_b128 v[4:7], %[apk] offset:1024\n\tds_read_b128 v[8:11], %[apk] offset:2048\n\t"
               "ds_read_b128 v[12:15], %[apk] offset:3072\n\tds_read_b128 v[16:19], %[apk] offset:4096\n\tds_read_b128 v[20:23], %[apk] offset:5120\n\t"
               "v_subrev_u32 v144, %[mc], %[amr]\n\tds_read_b32 v150, %[amr]\n\ts_mov_b64 exec, 63\n\tds_add_u32 v144, v146\n\ts_mov_b64 exec, -1\n\t"
               "s_cmp_lg_u32 %[late], 0\n\ts_waitcnt lgkmcnt(0)\n\ts_nop 1\n\ts_cbranch_scc1 1b\n\ts_branch 3b\n"
               "8:\n\tv_mov_b32 v153, v152\n\ts_nop 1\n\tv_permlane16_swap_b32 v152, v153\n\tv_max_f32 v152, v152, v153\n\tv_mov_b32 v153, v152\n\ts_nop 1\n\t"
               "v_permlane32_swap_b32 v152, v153\n\tv_max_f32 v152, v152, v153\n\tv_mul_f32 v152, 0x3d000000, v152\n\tv_rndne_f32 v152, v152\n\t"
               "v_mul_f32 v152, 0x42000000, v152\n\tv_max_f32 v150, v150, v152\n\tds_max_f32 %[amr], v150\n\t"
               S_SUB("128:129", "120:121") S_SUB("130:131", "122:123") S_SUB("132:133", "124:125") S_SUB("134:135", "126:127")
               S_E(128) S_E(129) S_E(130) S_E(131) S_E(132) S_E(133) S_E(134) S_E(135) "s_branch 4b\n"
               "9:"
               : "=&{v[0:7]}"(c.q[0]), "=&{v[8:15]}"(c.q[1]), "=&{v[16:23]}"(c.q[2]), "=&{s[52:53]}"(tq)
               : [orb] "v"(c.o_rb), [oyb] "v"(c.o_yb), [ev] "v"(c.ev), [apk] "v"(c.apk), [apm] "v"(c.apm), [api] "v"(c.api), [amr] "v"(c.amr),
                 [two] "v"(2.f), [four] "v"(4u), [inc] "v"(2u * NPK), [k0] "s"(k0), [late] "s"(__builtin_amdgcn_readfirstlane(late)), [toff] "s"(f32x2{TOFF, TOFF}),
                 [r1] "s"(0xAAAAAAAAAAAAAAAAull), [r2] "s"(0xCCCCCCCCCCCCCCCCull), [dg] "s"(0xF0000F0000F0000Full), [dg2] "s"(0xF00FFF000FF000FFull),
                 [cap] "i"(SPIN_CAP), [end] "i"(TPW - NH1), [ns] "i"(NS), [pfl] "i"(CT_OFF + C_PFL), [full] "i"(CT_OFF + C_FULL), [early] "i"(NEARLY),
                 [qi] "i"(CT_OFF + C_QI), [nq] "i"(NS - NEARLY), [mc] "i"(C_MREF - C_CONS), [slot] "i"(SLOT),
                 [thr] "i"(__builtin_bit_cast(unsigned, THR)), [eps] "i"(__builtin_bit_cast(unsigned, EPS * CC)), [nic] "i"(__builtin_bit_cast(unsigned, -1.f / CC))
               : "memory", "scc", "vcc", "s40", "s41", "s42", "s43", "s44", "s45", "s46", "s47", "s48", "s49", "s50", "s51",
                 "v24", "v25", "v26", "v27", "v28", "v29", "v30", "v31", "v32", "v33", "v34", "v35", "v36", "v37", "v38", "v39", "v40", "v41", "v42", "v43", "v44", "v45", "v46", "v47", "v48", "v49", "v50", "v51", "v52", "v53", "v54", "v55", "v56", "v57", "v58", "v59", "v60", "v61", "v62", "v63", "v64", "v65", "v66", "v67", "v68", "v69", "v70", "v71", "v72", "v73", "v74", "v75", "v76", "v77", "v78", "v79", "v80", "v81", "v82", "v83", "v84", "v85", "v86", "v87", "v88", "v89", "v90", "v91", "v92", "v93", "v94", "v95", "v96", "v97", "v98", "v99", "v100", "v101", "v102", "v103", "v104", "v105", "v106", "v107", "v108", "v109", "v110", "v111", "v112", "v113", "v114", "v115", "v116", "v117", "v118", "v119", "v120", "v121", "v122", "v123", "v124", "v125", "v126", "v127", "v128", "v129", "v130", "v131", "v132", "v133", "v134", "v135", "v136", "v137", "v138", "v139", "v140", "v141", "v142", "v143", "v144", "v145", "v146", "v147", "v148", "v149", "v150", "v151", "v152", "v153", "v154", "v155", "v156", "v157");
  c.tq = (unsigned)tq;
}

__device__ __forceinline__ void s_half(SCtx& c, int k, int h) {
  const int s = k % NSLOT, e = k % NPK;
  const int sb_ = s * SLOT;
  int yba = c.ev * sb_ + c.o_yb;
  float hc = 1.f - 0.5f * c.ev;
  unsigned xa = (unsigned)(size_t)c.L0 + sb_ + h * 6144 + c.o_rb, xb = xa ^ 64, ai = c.api + e * 256 + h * 8;
  unsigned ap = c.apk + e * 1024 + h * 8, am = c.apm + e * 128 + h * 4, af = c.ct + C_PFL + e * 4, inc = NPK;
  float two = 2.f;
  f32x4 G = {EPS * CC, EPS * CC, EPS * CC, EPS * CC};
  poll_ge_sleep(c.ct + C_CONS + e * 4, (unsigned)(NE * (k / NPK) + NS));
  asm volatile("" : "+v"(yba), "+v"(hc), "+v"(xa), "+v"(xb), "+v"(ai), "+v"(ap), "+v"(am), "+v"(af), "+v"(inc), "+v"(two), "+{v[104:107]}"(G));
  poll_ge_sleep(c.ct + C_FULL, (unsigned)(2 * k + 1));
  __builtin_amdgcn_s_setprio(1);
  bf16x8 yb[6];
#pragma unroll
  for (int j = 0; j < 6; ++j) yb[j] = *(LDS_AS(bf16x8)*)(c.L0 + ((j & 1) ? (yba ^ 64) : yba) + (j >> 1) * 128);
  f32x4 Hx = {0.f, 0.f, 0.f, 0.f}, S = Hx;
#pragma unroll
  for (int j = 0; j < 6; ++j) { Hx = mfma(yb[j], yb[j], Hx); S = mfma(yb[j], s_qf(c, j), S); }
  const float nsq = S[1] * (-1.f / CC);
  Hx = f32x4{Hx[0] * hc, Hx[0] * hc, Hx[2] * hc, Hx[2] * hc};
  S = f32x4{S[0], S[0], S[2], S[2]};
  float mref, mt;
  f32x4 rs;
  unsigned long long raise;
#define SH_M(d, a, b) "v_mfma_f32_16x16x32_bf16 v[" d "], v[" a "], " b ", v[" d "]\n\t"
#define SH_X(n, a, y) "s_waitcnt lgkmcnt(" #n ")\n\t" SH_M("100:103", a, "%[" #y "]") SH_M("104:107", a, "v[" a "]")
#define SH_D " row_mask:0xf bank_mask:0xf\n\t"
  __builtin_amdgcn_s_setprio(0);
  if (HB == 0 || (HB == 1 && k < TPW - 1)) poll_ge_sleep(c.ct + C_FULL, (unsigned)(2 * k + 2));
  if (HB == 2) { if (k < TPW - 1) poll_ge_sleep(c.ct + C_FULL, (unsigned)(2 * k + 2)); else poll_ge(c.ct + C_FULL, (unsigned)(2 * TPW)); }
  asm volatile("s_mov_b32 s42, %[cap]\n"
               "1:\n\tds_read_b32 v136, %[afl]\n\tds_read_b128 v[112:115], %[xa]\n\tds_read_b128 v[116:119], %[xb]\n\tds_read_b128 v[120:123], %[xa] offset:128\n\t"
               "ds_read_b128 v[124:127], %[xb] offset:128\n\tds_read_b128 v[128:131], %[xa] offset:256\n\tds_read_b128 v[132:135], %[xb] offset:256\n\t"
               "ds_read_b32 %[mr], %[amr]\n\ts_sub_u32 s42, s42, 1\n\ts_cselect_b32 s41, 0, %[need]\n\ts_waitcnt lgkmcnt(7)\n\t"
               "v_readfirstlane_b32 s40, v136\n\ts_cmp_lt_u32 s40, s41\n\ts_cbranch_scc1 1b\n\ts_setprio 3\n\t"
               SH_X(6, "112:115", y0) SH_X(5, "116:119", y1) SH_X(4, "120:123", y2) SH_X(3, "124:127", y3) SH_X(2, "128:131", y4) SH_X(1, "132:135", y5)
               SH_M("108:111", "112:115", "%[q0]") SH_M("108:111", "116:119", "%[q1]")
               "s_waitcnt lgkmcnt(0)\n\tv_add_f32 v137, %[thr], %[mr]\n\tds_write_b32 %[am], %[mr]\n\ts_nop 1\n\t"
               "v_mov_b32_dpp v140, v100 quad_perm:[1,1,3,3]" SH_D "v_mov_b32_dpp v141, v101 quad_perm:[1,1,3,3]" SH_D
               "v_mov_b32_dpp v142, v102 quad_perm:[1,1,3,3]" SH_D "v_mov_b32_dpp v143, v103 quad_perm:[1,1,3,3]" SH_D
               SH_M("108:111", "120:123", "%[q2]")
               "v_fmac_f32 v104, 2.0, v100\n\tv_fmac_f32_dpp v105, v101, %[two] quad_perm:[0,0,2,2]" SH_D
               "v_fmac_f32 v106, 2.0, v102\n\tv_fmac_f32_dpp v107, v103, %[two] quad_perm:[0,0,2,2]" SH_D
               "v_cndmask_b32 v138, v140, v141, %[r1]\n\tv_cndmask_b32 v139, v142, v143, %[r1]\n\t"
               SH_M("108:111", "124:127", "%[q3]")
               "v_cndmask_b32 v104, v104, v105, %[r1]\n\tv_cndmask_b32 v106, v106, v107, %[r1]\n\tv_cndmask_b32 v138, v138, v139, %[r2]\n\t"
               "v_cndmask_b32 v104, v104, v106, %[r2]\n\tv_mul_f32 v139, %[nic], v138\n\t"
               SH_M("108:111", "128:131", "%[q4]")
               "v_fmac_f32 v104, v139, v138\n\tv_max_f32 v104, %[eps], v104\n\tv_rsq_f32 v105, v104\n\t"
               SH_M("108:111", "132:135", "%[q5]")
               "v_mul_f32 v104, v104, v105\n\t"
               "v_mov_b32_dpp v105, v105 row_ror:12 row_mask:0x2 bank_mask:0xf\n\tv_cvt_pk_bf16_f32 v106, v138, v104\n\ts_nop 0\n\t"
               "v_mov_b32_dpp v105, v105 row_ror:8 row_mask:0x4 bank_mask:0xf\n\tv_lshlrev_b32 v107, 16, v106\n\tv_and_b32 v139, 0xffff0000, v106\n\t"
               "v_mov_b32_dpp v105, v105 row_ror:4 row_mask:0x8 bank_mask:0xf\n\t"
               "v_fmac_f32 v108, v140, %[nsq]\n\tv_fmac_f32 v109, v141, %[nsq]\n\tv_fmac_f32 v110, v142, %[nsq]\n\tv_fmac_f32 v111, v143, %[nsq]\n\t"
               "v_mov_b32_dpp v100, v105 row_newbcast:0" SH_D "v_mov_b32_dpp v101, v105 row_newbcast:1" SH_D
               "v_mov_b32_dpp v102, v105 row_newbcast:2" SH_D "v_mov_b32_dpp v103, v105 row_newbcast:3" SH_D
               "v_fmaak_f32 v108, v108, v100, %[toff]\n\tv_fmaak_f32 v109, v109, v101, %[toff]\n\tv_fmaak_f32 v110, v110, v102, %[toff]\n\tv_fmaak_f32 v111, v111, v103, %[toff]\n\t"
               "v_sub_f32 v140, v108, %[mr]\n\tv_sub_f32 v141, v109, %[mr]\n\tv_sub_f32 v142, v110, %[mr]\n\tv_sub_f32 v143, v111, %[mr]\n\t"
               "v_exp_f32 v140, v140\n\tv_exp_f32 v141, v141\n\tv_exp_f32 v142, v142\n\tv_exp_f32 v143, v143\n\t"
               "v_max3_f32 %[mt], v108, v109, v110\n\tv_sub_f32 v138, v138, v107\n\tv_sub_f32 v104, v104, v139\n\tv_max_f32 %[mt], %[mt], v111\n\t"
               "v_pk_mul_f32 v[140:141], v[140:141], v[100:101]\n\tv_pk_mul_f32 v[142:143], v[142:143], v[102:103]\n\t"
               "v_cmp_gt_f32 %[ra], %[mt], v137\n\tv_cvt_pk_bf16_f32 v107, v138, v104\n\t"
               "v_cvt_pk_bf16_f32 v140, v140, v141\n\tv_cvt_pk_bf16_f32 v141, v142, v143\n\t"
               "ds_write_b64 %[ap], v[140:141]\n\ts_mov_b64 exec, %[dg]\n\t"
               "ds_write_b16 %[ai], v106\n\tds_write_b16 %[ai], v107 offset:16\n\tds_write_b16_d16_hi %[ai], v106 offset:32\n\tds_write_b16_d16_hi %[ai], v107 offset:48\n\t"
               "s_cmp_eq_u64 %[ra], 0\n\ts_cselect_b64 exec, 1, 0\n\tds_add_u32 %[af], %[inc]\n\ts_mov_b64 exec, -1\n\ts_setprio 0"
               : "+{v[100:103]}"(Hx), "+{v[104:107]}"(G), "+{v[108:111]}"(S), [mr] "=&v"(mref), [mt] "=&v"(mt), [ra] "=&s"(raise)
               : [y0] "v"(yb[0]), [y1] "v"(yb[1]), [y2] "v"(yb[2]), [y3] "v"(yb[3]), [y4] "v"(yb[4]), [y5] "v"(yb[5]),
                 [q0] "v"(s_qf(c, 0)), [q1] "v"(s_qf(c, 1)), [q2] "v"(s_qf(c, 2)), [q3] "v"(s_qf(c, 3)), [q4] "v"(s_qf(c, 4)), [q5] "v"(s_qf(c, 5)),
                 [afl] "v"(c.ct + C_FULL), [xa] "v"(xa), [xb] "v"(xb), [amr] "v"(c.amr), [am] "v"(am), [ap] "v"(ap), [ai] "v"(ai),
                 [af] "v"(af), [inc] "v"(inc), [nsq] "v"(nsq), [two] "v"(two),
                 [need] "s"(__builtin_amdgcn_readfirstlane(2 * k + 2)), [r1] "s"(0xAAAAAAAAAAAAAAAAull), [r2] "s"(0xCCCCCCCCCCCCCCCCull),
                 [dg] "s"(0xF0000F0000F0000Full), [cap] "i"(SPIN_CAP), [thr] "i"(__builtin_bit_cast(unsigned, THR)), [toff] "i"(__builtin_bit_cast(unsigned, TOFF)),
                 [eps] "i"(__builtin_bit_cast(unsigned, EPS * CC)), [nic] "i"(__builtin_bit_cast(unsigned, -1.f / CC))
               : "memory", "scc", "s40", "s41", "s42", "v112", "v113", "v114", "v115", "v116", "v117", "v118", "v119", "v120", "v121", "v122", "v123", "v124", "v125",
                 "v126", "v127", "v128", "v129", "v130", "v131", "v132", "v133", "v134", "v135", "v136", "v137", "v138", "v139", "v140", "v141", "v142", "v143");
  if (__builtin_expect(raise != 0ull, 0)) {
    rs = Hx;
    mref = fmaxf(mref, 32.f * __builtin_rintf(xrow16_max(mt) * (1.f / 32.f)));
    float pp[4];
#pragma unroll
    for (int i = 0; i < 4; ++i) pp[i] = __builtin_amdgcn_exp2f(S[i] - mref) * rs[i];
    asm volatile("ds_max_f32 %6, %3\n\tds_write_b32 %2, %3\n\tds_write_b64 %0, %1\n\ts_mov_b64 exec, 1\n\tds_add_u32 %4, %5\n\ts_mov_b64 exec, -1"
                 : : "v"(ap), "v"(u32x2{cvtpk(pp[0], pp[1]), cvtpk(pp[2], pp[3])}), "v"(am), "v"(mref), "v"(af), "v"((unsigned)NPK), "v"(c.amr) : "memory");
  }
}

using u32x16 = __attribute__((ext_vector_type(16))) unsigned;
typedef __attribute__((address_space(1))) unsigned short gus;
extern "C" __global__ __launch_bounds__(768, 3) void dino_main(const void*, const void*, const void*, const void*, const void*,
                                                               const void*, const void*, void*, void*) {
  u32x16 ka; u32x2 kb; unsigned long long tent64;
  __shared__ __attribute__((aligned(16))) char lds[LDS_BYTES];
  const int wave = __builtin_amdgcn_readfirstlane((int)threadIdx.x >> 6);
  const bool helper = blockIdx.x < 8;
  lds_ptr L0 = (lds_ptr)lds;
  const unsigned lbase = (unsigned)(size_t)L0;
  const unsigned ct = lbase + CT_OFF;
#define KARG(v, i) ((char*)(((unsigned long long)v[2 * (i) + 1] << 32) | v[2 * (i)]))
#define q KARG(ka, 0)
#define f0 KARG(ka, 1)
#define f1 KARG(ka, 2)
#define wq KARG(ka, 3)
#define wk KARG(ka, 4)
#define wv KARG(ka, 5)
#define wo KARG(ka, 6)
#define box KARG(ka, 7)
#define tent ((unsigned)tent64)
#define LANE_VARS int lane; asm volatile("v_mbcnt_lo_u32_b32 %0, -1, 0\n\tv_mbcnt_hi_u32_b32 %0, -1, %0" : "=v"(lane)); \
  const int r = lane & 15, g = lane >> 4; (void)r; (void)g;
#define DOFF_VARS unsigned doff[3];   \
  _Pragma("unroll") for (int i = 0; i < 3; ++i) doff[i] = (unsigned)(lane ^ ((swz_tbl(i) >> ((lane >> 3) * 4)) & 7)) << 4;
#define BARRIER() do { asm volatile("s_waitcnt lgkmcnt(0)" ::: "memory"); __builtin_amdgcn_s_barrier(); asm volatile("" ::: "memory"); } while (0)

  {
    unsigned doff[3];
    static_assert(swz_tbl(0) == swz_tbl(3) && swz_tbl(1) == swz_tbl(4) && swz_tbl(2) == swz_tbl(5), "");
    unsigned l16, db;
#define PD(d, b, off, pol) "global_load_lds_dwordx4 %[" #d "], s[" #b "] offset:" #off pol "\n\t"
    asm volatile("s_load_dwordx16 s[64:79], %[kp], 0x0\n\ts_load_dwordx2 s[80:81], %[kp], 0x40\n\ts_memrealtime s[82:83]\n\t"
                 "v_mbcnt_lo_u32_b32 %[l16], -1, 0\n\tv_mbcnt_hi_u32_b32 %[l16], -1, %[l16]\n\t"
                 "s_lshr_b32 s84, %[b], 3\n\ts_and_b32 s85, %[b], 7\n\t"
                 "v_lshrrev_b32 %[db], 1, %[l16]\n\tv_and_b32 %[db], -4, %[db]\n\t"
                 "s_mov_b32 s86, %[t0]\n\ts_mov_b32 s87, %[t1]\n\ts_mov_b32 s88, %[t2]\n\t"
                 "v_bfe_u32 %[d0], s86, %[db], 3\n\tv_bfe_u32 %[d1], s87, %[db], 3\n\tv_bfe_u32 %[d2], s88, %[db], 3\n\t"
                 "v_xor_b32 %[d0], %[d0], %[l16]\n\tv_xor_b32 %[d1], %[d1], %[l16]\n\tv_xor_b32 %[d2], %[d2], %[l16]\n\t"
                 "v_lshlrev_b32 %[d0], 4, %[d0]\n\tv_lshlrev_b32 %[d1], 4, %[d1]\n\tv_lshlrev_b32 %[d2], 4, %[d2]\n\t"
                 "v_lshlrev_b32 %[l16], 4, %[l16]\n\t"
                 "s_cmp_eq_u32 s84, 0\n\ts_cbranch_scc1 4f\n\t"
                 "s_cmp_lg_u32 %[w], 0\n\ts_cbranch_scc1 7f\n\t"
                 "s_setprio 3\n\tv_lshlrev_b32 %[db], 3, %[l16]\n\ts_mov_b32 m0, %[junk]\n\ts_lshl_b32 s89, s84, 13\n\t"
                 "s_cmp_gt_u32 s84, 3\n\ts_cbranch_scc1 2f\n\t"
                 "s_getpc_b64 s[90:91]\n1:\n\ts_sub_u32 s86, s89, 8192\n\ts_min_u32 s86, s86, dino_end-1b-8192\n\t"
                 "s_add_u32 s90, s90, s86\n\ts_addc_u32 s91, s91, 0\n\tglobal_load_lds_dword %[db], s[90:91]\n"
                 "2:\n\ts_mul_i32 s86, s85, 98304\n\ts_mul_i32 s87, s84, 3072\n\ts_sub_u32 s87, s87, 3072\n\t"
                 "s_add_u32 s88, s86, s87\n\ts_lshl1_add_u32 s86, s86, s87\n\ts_lshl_b32 s86, s86, 1\n\t"
                 "s_mul_i32 s87, s85, 12288\n\ts_lshr_b32 s90, s89, 1\n\ts_add_u32 s87, s87, s90\n\ts_sub_u32 s87, s87, 13 * 4096\n\t"
                 "s_sub_u32 s89, s89, 4 * 8192\n\ts_cmp_gt_u32 s85, 5\n\ts_cselect_b32 s89, -1, s89\n\ts_cmp_ge_u32 s89, 11 * 8192\n\t"
                 "s_waitcnt lgkmcnt(0)\n\ts_cbranch_scc1 3f\n\t"
                 "s_cmp_lt_u32 s89, 9 * 8192\n\ts_cselect_b64 s[90:91], s[70:71], s[72:73]\n\ts_cselect_b32 s89, s89, s87\n\t"
                 "s_add_u32 s90, s90, s89\n\ts_addc_u32 s91, s91, 0\n\tglobal_load_lds_dword %[db], s[90:91]\n"
                 "3:\n\ts_mov_b32 m0, 0x3000\n\t"
                 "s_add_u32 s90, s68, s88\n\ts_addc_u32 s91, s69, 0\n\t"
                 "s_add_u32 s92, s66, s86\n\ts_addc_u32 s93, s67, 0\n\t"
                 PD(d0, 90:91, 0, " nt") PD(d1, 90:91, 1024, " nt") PD(d2, 90:91, 2048, " nt")
                 "s_mov_b32 m0, 0\n\ts_nop 0\n\t"
                 PD(d0, 92:93, 0, " nt") PD(d1, 92:93, 1024, " nt")
                 "s_sub_u32 s89, s84, 15\n\ts_cmp_gt_u32 s85, 5\n\ts_cselect_b32 s89, -1, s89\n\ts_cmp_ge_u32 s89, 11\n\ts_cbranch_scc1 9f\n\t"
                 "s_mov_b32 m0, %[junk]\n\ts_sub_u32 s87, s87, 11 * 4096\n\ts_lshl_b32 s89, s89, 13\n\ts_cmp_lt_u32 s89, 9 * 8192\n\t"
                 "s_cselect_b64 s[90:91], s[76:77], s[74:75]\n\ts_cselect_b32 s89, s89, s87\n\t"
                 "s_add_u32 s90, s90, s89\n\ts_addc_u32 s91, s91, 0\n\tglobal_load_lds_dword %[db], s[90:91]\n\t"
                 "s_branch 9f\n"
                 "7:\n\ts_cmp_eq_u32 %[w], 1\n\ts_cbranch_scc1 8f\n\ts_barrier\n\ts_branch 8f\n"
                 "4:\n\t"
                 "s_cmp_gt_u32 %[b], 5\n\ts_cbranch_scc1 8f\n\ts_cmp_gt_u32 %[w], 9\n\ts_cbranch_scc1 8f\n\t"
                 "s_mov_b32 m0, %[img]\n\ts_mul_i32 s86, %[w], 9216\n\ts_and_b32 s88, %[w], 1\n\ts_lshl1_add_u32 s89, %[b], s88\n\t"
                 "s_mul_i32 s88, s88, 6144\n\ts_mul_i32 s89, s89, 6144\n\ts_add_u32 s88, s88, %[hr]\n\t"
                 "s_cmp_lg_u32 %[w], 0\n\ts_waitcnt lgkmcnt(0)\n\ts_cbranch_scc1 5f\n\t"
                 PD(d0, 64:65, 0, "") PD(d1, 64:65, 1024, "") PD(d2, 64:65, 2048, "") PD(d0, 64:65, 3072, "")
                 "s_mov_b32 m0, %[img] + 4096\n\ts_add_u32 s90, s64, 0x1000\n\ts_addc_u32 s91, s65, 0\n\t"
                 PD(d1, 90:91, 0, "") PD(d2, 90:91, 1024, "")
                 "\n5:\n\ts_cmp_gt_u32 %[w], 7\n\ts_cselect_b64 s[92:93], s[72:73], s[70:71]\n\ts_cselect_b32 m0, s88, s86\n\ts_cselect_b32 s86, s89, s86\n\t"
                 "s_cselect_b32 s87, 2, 3\n\ts_add_u32 s92, s92, s86\n\ts_addc_u32 s93, s93, 0\n"
                 "6:\n\ts_sub_u32 s87, s87, 1\n\t"
                 PD(d0, 92:93, 0, "") PD(d1, 92:93, 1024, "") PD(d2, 92:93, 2048, "")
                 "s_add_u32 m0, m0, 3072\n\ts_add_u32 s92, s92, 3072\n\ts_addc_u32 s93, s93, 0\n\ts_cmp_lg_u32 s87, 0\n\ts_cbranch_scc1 6b"
                 "\n8:\n\ts_waitcnt lgkmcnt(0)\n9:"
                 : "={s[64:79]}"(ka), "={s[80:81]}"(kb), "={s[82:83]}"(tent64),
                   [d0] "=&v"(doff[0]), [d1] "=&v"(doff[1]), [d2] "=&v"(doff[2]), [l16] "=&v"(l16), [db] "=&v"(db)
                 : [kp] "s"(__builtin_amdgcn_kernarg_segment_ptr()), [b] "s"(blockIdx.x), [w] "s"(wave),
                   [t0] "i"(swz_tbl(0)), [t1] "i"(swz_tbl(1)), [t2] "i"(swz_tbl(2)), [junk] "i"(JUNK), [img] "i"(IMG_A), [hr] "i"(H_R)
                 : "memory", "scc", "vcc", "s84", "s85", "s86", "s87", "s88", "s89", "s90", "s91", "s92", "s93", "s94", "s95", "s96", "s97", "s98", "s99");
  }
  const unsigned t0 = (unsigned)__builtin_amdgcn_s_memrealtime() - T0BACK;

  if (__builtin_expect(blockIdx.x < 6, 0)) {
    LANE_VARS
    DOFF_VARS
    const int j = blockIdx.x;
    TNOW(ta0);
    TNOW(ta1);
    const int qq = (lane >> 2) & 3, p4 = lane & 3, e4 = wave & 3;
    int ra = H_R + r * ROWB + ((g ^ swz(r)) << 4);
    bf16x8 a[6], b[6], w[18];
    auto rows = [&](int t, bf16x8* d) {
#pragma unroll
      for (int kk = 0; kk < 6; ++kk) d[kk] = *(LDS_AS(bf16x8)*)(L0 + t * 6144 + ((kk & 1) ? (ra ^ 64) : ra) + (kk >> 1) * 128);
    };
#define TSET(v)
    int o[3];
#pragma unroll
    for (int t = 0; t < 3; ++t) o[t] = tr_off(4 * g + qq, 4 * (3 * e4 + t) + p4);
    unsigned qo = lbase + H_Q + 3 * e4 * 512 + ((g >> 1) * 16 + r) * 16 + (g & 1) * 8, qa = lbase + IMG_A + tr_off(r, 8 * wave + g);
    if (wave == 11) {
      unsigned d;
      asm volatile("s_load_dword %0, %1, 0x40\n\ts_barrier\n\ts_barrier\n\ts_barrier\n\ts_waitcnt lgkmcnt(0)" : "=&s"(d) : "s"(box + EPOCH_OFF) : "memory");
    } else
    asm volatile("s_waitcnt vmcnt(9)\n\ts_barrier" : PIN3(o, 0), "+v"(qo), "+v"(qa) :: "memory");
    if (wave < 6 || wave == 10) {
      rows((IMG_A - H_R) / 6144, a);
      const u32x2 m0 = *(LDS_AS(u32x2)*)(size_t)qa, m1 = *(LDS_AS(u32x2)*)(size_t)(qa ^ 32);
      static_assert((IMG_A - H_R) % 6144 == 0, "");
      const bf16x8 ones = __builtin_bit_cast(bf16x8, u32x4{0x3f803f80u, 0x3f803f80u, 0x3f803f80u, 0x3f803f80u});
      f32x4 sm = {0.f, 0.f, 0.f, 0.f};
#pragma unroll
      for (int i = 0; i < 6; ++i) sm = mfma(ones, a[i], sm);
      const float mean = sm[0] * (1.f / CC);
      if (__builtin_expect(wave < 6, 1)) {
        img_st(L0, H_C, wave, lane, __builtin_bit_cast(bf16x8, u32x4{cvtpk(bflo(m0[0]) - mean, bfhi(m0[0]) - mean), cvtpk(bflo(m0[1]) - mean, bfhi(m0[1]) - mean),
                                                                   cvtpk(bflo(m1[0]) - mean, bfhi(m1[0]) - mean), cvtpk(bflo(m1[1]) - mean, bfhi(m1[1]) - mean)}));
      } else {
        asm volatile("s_barrier" : PIN3(a, 0), PIN3(a, 3) :: "memory");
        f32x2 v2 = {0.f, 0.f}, v3 = v2;
#pragma unroll
        for (int i = 0; i < 12; ++i) {
          const u32x4 d = __builtin_bit_cast(u32x4, a[i >> 1]);
          const unsigned d0 = d[2 * (i & 1)], d1 = d[2 * (i & 1) + 1];
          const f32x2 x0 = f32x2{bflo(d0), bfhi(d0)} - mean, x1 = f32x2{bflo(d1), bfhi(d1)} - mean;
          v2 += x0 * x0; v3 += x1 * x1;
        }
        v2 += v3;
        *(LDS_AS(float)*)(L0 + CT_OFF + r * 4) = LOG2E * __builtin_amdgcn_rsqf(xrow16_sum(v2[0] + v2[1]) * (1.f / CC) + EPS);
      }
      TSET(ta2);
    }
    if (wave < 4) {
      wait_vm<0>();
      TSET(ta3);
      BARRIER();
      TSET(th1);
#pragma unroll
      for (int kk = 0; kk < 6; ++kk) {
        b[kk] = img_ld(L0, H_C, kk, lane);
        w[kk] = mk8(tr_read(L0 + kk * 12288 + o[0]), tr_read(L0 + kk * 12288 + 6144 + o[0]));
        __builtin_amdgcn_sched_barrier(0);
      }
#pragma unroll
      for (int i = 6; i < 18; ++i) {
        w[i] = mk8(tr_read(L0 + (i % 6) * 12288 + o[i / 6]), tr_read(L0 + (i % 6) * 12288 + 6144 + o[i / 6]));
        __builtin_amdgcn_sched_barrier(0);
      }
#pragma unroll
      for (int t = 0; t < 3; ++t) {
        f32x4 D = {0.f, 0.f, 0.f, 0.f};
#pragma unroll
        for (int kk = 0; kk < 6; ++kk) D = mfma(w[6 * t + kk], b[kk], D);
        *(LDS_AS(u32x2)*)(size_t)(qo + t * 512) = u32x2{cvtpk(D[0], D[1]), cvtpk(D[2], D[3])};
      }
      TSET(ta4);
      BARRIER();
    } else if (wave == 8 || wave == 9) {
      unsigned so = lane * 16 + (2 * j + (wave & 1)) * 1024;
      unsigned epoch;
      asm volatile("global_load_dword %0, %1, %2 sc1\n\ts_barrier\n\ts_waitcnt vmcnt(0)" : "=v"(epoch) : "v"(EPOCH_OFF), "s"(box) : "memory");
      TSET(ta3);
      rows(wave & 1, a);
      unsigned ta = line_tag(epoch, 8), tb = line_tag(epoch, 9);
      asm volatile("" : PIN3(a, 0), PIN3(a, 3), "+v"(ta), "+v"(tb), "+v"(so));
      TSET(ta4);
      BARRIER();
      TSET(ta5);
#pragma unroll
      for (int kk = 0; kk < 6; ++kk) b[kk] = img_ld(L0, H_Q, kk, lane);
      const float sc = *(LDS_AS(float)*)(L0 + CT_OFF + r * 4);
      __builtin_amdgcn_sched_barrier(0);
      f32x4 D = {0.f, 0.f, 0.f, 0.f};
#pragma unroll
      for (int kk = 0; kk < 6; ++kk) D = mfma(a[kk], b[kk], D);
      D *= sc;
      const unsigned p0 = cvtpk(D[0], D[1]), p1 = cvtpk(D[2], D[3]);
      asm volatile("global_store_dwordx4 %0, %1, %2 sc1\n\ts_nop 1" : : "v"(so), "v"(u32x4{p0, p1, p0 ^ p1 ^ ta, tb}), "s"(box + QK_OFF) : "memory");
      TSET(th2);
    } else if (wave != 11) {
      if (wave != 10) asm volatile("s_waitcnt vmcnt(0) lgkmcnt(0)\n\ts_barrier" ::: "memory");
      BARRIER();
    }
    BARRIER();
    issue_rows16(wo + wave * 6144, lbase + wave * 6144, doff);
    if (wave < 2) issue_rows16(wv + (2 * j + wave) * 6144, lbase + H_R + wave * 6144, doff);
    const unsigned epoch = epoch_load(box);
    BARRIER();
    TSET(ta3);
    {
      const int o0 = tr_off(8 * g + qq, 4 * wave + p4), o1 = tr_off(8 * g + 4 + qq, 4 * wave + p4);
#pragma unroll
      for (int kk = 0; kk < 6; ++kk) b[kk] = mk8(tr_read(L0 + kk * 12288 + o0), tr_read(L0 + kk * 12288 + o1));
      f32x4 D[2];
#pragma unroll
      for (int h = 0; h < 2; ++h) {
        rows(h, a);
        D[h] = f32x4{0.f, 0.f, 0.f, 0.f};
#pragma unroll
        for (int kk = 0; kk < 6; ++kk) D[h] = mfma(a[kk], b[kk], D[h]);
      }
      const int e = wave / 3;
      asm volatile("global_store_dwordx4 %0, %1, %2 sc1\n\ts_waitcnt vmcnt(0)"
                   : : "v"(lane * 16 + (e * 15 + 3 * j + wave) * 1024), "v"(pack8(D[0] * SQRT_C, D[1] * SQRT_C)), "s"(box + W2_OFF) : "memory");
    }
    BARRIER();
    if (wave == 11)
      asm volatile("s_mov_b64 exec, 1\n\tglobal_store_dwordx2 %0, %1, %2 sc1\n\ts_mov_b64 exec, -1\n\ts_nop 1"
                   : : "v"(j * 128), "v"(u32x2{line_tag(epoch, 10), line_tag(epoch, 11)}), "s"(box + W2F_OFF) : "memory");
    TPUT(60 + wave * 5, ta0); TPUT(61 + wave * 5, ta1); TPUT(62 + wave * 5, ta2); TPUT(63 + wave * 5, ta3); TPUT(64 + wave * 5, ta4);
  }

  int rslot = -1;
  if (__builtin_expect(wave == 0, 1)) {
    LANE_VARS
    DOFF_VARS
    if (helper) BARRIER();
    const unsigned afull = ct + C_FULL + r * 4;
    unsigned ts = 0;
    {
      const int ti = helper ? 1023 : (lane * 31 + (int)(blockIdx.x >> 3) - 1) & 1023;
      const int sp = ti >> 5, yy = sp * 8 + ((blockIdx.x + sp) & 7), xx = (ti + 5 * sp) & 31;
      unsigned tmp, pub = helper ? 2 * (TPW - 1) : 0, pre = 0, afr = ct + C_FREE + (helper ? ((TPW - 1) % NSLOT) * 4 : 0), e0, e1, e2, g0, g1, g2;
      asm volatile("s_mov_b32 s44, %[due0]\n\ts_mov_b32 s46, %[y0]\n\ts_mov_b32 s47, %[cap]\n\ts_mov_b32 s48, %[i0]\n\ts_mov_b32 s49, %[s0]\n\t"
                   "s_mov_b32 s51, 0\n\ts_mov_b32 s45, 0\n\ts_movk_i32 s39, %[p3s]\n"
                   "1:\n\tv_readlane_b32 s53, %[ob], s48\n\tv_readlane_b32 s52, %[oa], s48\n\ts_add_u32 m0, s49, 0x3000\n\tv_cmp_gt_u32 vcc, s51, %[pre]\n\t"
                   "v_add_u32 %[g0], s53, %[d0]\n\tv_add_u32 %[g1], s53, %[d1]\n\tv_add_u32 %[g2], s53, %[d2]\n\t"
                   "v_add_u32 %[e0], s52, %[d0]\n\tv_add_u32 %[e1], s52, %[d1]\n\tv_add_u32 %[e2], s52, %[d2]\n\t"
                   "s_cbranch_vccnz 5f\n\t"
                   "s_cmp_eq_u32 s48, 0\n\ts_cbranch_scc1 2f\n\ts_cmp_eq_u32 s48, 2\n\ts_cbranch_scc1 8f\n"
                   LD_GATE(1, "")
                   LD1(g0, 68:69, 0) LD1(g1, 68:69, 1024) LD1(g2, 68:69, 2048) "s_mov_b32 m0, s49\n\ts_nop 0\n\t" LD1(e0, 66:67, 0) LD1(e1, 66:67, 1024)
                   LD_NEXT
                   "\n2:\n\tv_add_u32 %[afr], 4, %[afr]\n\ts_cmp_eq_u32 s49, %[lasts]\n\ts_cbranch_scc1 7f\n"
                   "3:\n\tv_mov_b32 %[pre], 0\n\ts_add_u32 m0, s49, 0x800\n\tds_read_b32 %[pre], %[afr] ; NOTRACK\n"
                   LD_GATE(2, "")
                   LD1(e2, 56:57, 0) LD1(e0, 56:57, 1024) LD1(e1, 56:57, 2048) LD1(e2, 56:57, 3072) "s_add_u32 m0, s49, 0x1800\n\ts_nop 0\n\t" LD1(e0, 58:59, 0)
                   LD_NEXT
                   "\n29:\n\ts_add_u32 m0, s49, 0x1800\n\ts_add_u32 s48, s48, 1\n"
                   LD_GATE(3, "s_cmp_gt_u32 s48, %[tpw] - 3\n\ts_cbranch_scc1 38f\n39:\n\t")
                   LD1(e1, 58:59, 1024) LD1(e2, 58:59, 2048) LD1(e0, 58:59, 3072) "s_add_u32 m0, s49, 0x2800\n\ts_add_u32 s49, s49, %[slot]\n\t" LD1(e1, 60:61, 0) LD1(e2, 60:61, 1024)
                   LD_NEXT
                   "s_cmp_eq_u32 s49, %[ring]\n\ts_cselect_b32 s49, 0, s49\n\ts_cmp_gt_i32 s46, 30\n\ts_cbranch_scc1 6f\n"
                   "4:\n\ts_cmp_eq_u32 s48, %[pk]\n\ts_cmovk_i32 s39, %[p3]\n\ts_cmp_lg_u32 s48, %[tpw]\n\ts_cbranch_scc1 1b\n\ts_branch 9f\n"
                   LD_PUB(1, 0, 15, 30) LD_PUB(2, 5, 20, 35) LD_PUB(3, 10, 25, 40)
                   "38:\n\ts_cmp_eq_u32 s46, 10\n\ts_cselect_b32 s43, 23, 0\n\ts_cmp_lt_u32 s42, s43\n\ts_cbranch_scc0 39b\n\tv_add_u32 %[tmp], 1, %[pub]\n\ts_waitcnt vmcnt(22)\n\tds_write_b32 %[af], %[tmp] ; NOTRACK\n\ts_branch 39b\n"
                   "5:\n\ts_movk_i32 s40, 0x100\n"
                   "40:\n\tds_read_b32 %[pre], %[afr]\n\t" LD_VM
                   "s_sub_u32 s40, s40, 1\n\ts_cselect_b32 s42, 0, s42\n\ts_cselect_b32 s40, 0, s40\n\t"
                   "s_cmp_le_i32 s42, s46\n\ts_cbranch_scc1 42f\n"
                   "41:\n\ts_sub_i32 s47, s47, 1\n\ts_cmp_lt_i32 s47, 0\n\ts_waitcnt lgkmcnt(0)\n\ts_cbranch_scc1 10b\n\t"
                   "v_cmp_gt_u32 vcc, s51, %[pre]\n\ts_cbranch_vccnz 40b\n\ts_branch 10b\n"
                   LD_PUB0(4, 0, 15, 30)
                   "6:\n\ts_waitcnt vmcnt(45)\n\tv_add_u32 %[pub], 2, %[pub]\n\tds_write_b32 %[af], %[pub]\n\ts_sub_u32 s46, s46, 15\n\ts_branch 4b\n"
                   "7:\n\ts_waitcnt lgkmcnt(0)\n\ts_add_u32 s51, s51, 8\n\tv_subrev_u32 %[afr], %[frb], %[afr]\n\ts_branch 3b\n"
                   "8:\n\ts_barrier\n\ts_add_u32 s46, s46, 64\n\ts_branch 10b\n"
                   "9:\n\ts_cmp_lt_i32 s46, 30\n\ts_cbranch_scc1 91f\n\ts_waitcnt vmcnt(30)\n\tv_add_u32 %[pub], 2, %[pub]\n\tds_write_b32 %[af], %[pub]\n"
                   "91:\n\ts_cmp_lt_i32 s46, 15\n\ts_cbranch_scc1 92f\n\tv_add_u32 %[tmp], 1, %[pub]\n\ts_waitcnt vmcnt(27)\n\tds_write_b32 %[af], %[tmp]\n"
                   "92:\n\ts_waitcnt lgkmcnt(0)"
                   : [pub] "+v"(pub), [pre] "+v"(pre), [afr] "+v"(afr), [ts] "+v"(ts), [tmp] "=&v"(tmp),
                     [e0] "=&v"(e0), [e1] "=&v"(e1), [e2] "=&v"(e2), [g0] "=&v"(g0), [g1] "=&v"(g1), [g2] "=&v"(g2)
                   : "{s[64:79]}"(ka), "{s[56:57]}"(f0 + 0x800), "{s[58:59]}"(f0 + 0x30000), "{s[60:61]}"(f0 + 0x31000), [t0] "s"(t0), [af] "v"(afull),
                     [d0] "v"(doff[0]), [d1] "v"(doff[1]), [d2] "v"(doff[2]), [oa] "v"(yy * 393216 + xx * 6144), [ob] "v"(yy * 98304 + xx * 3072),
                     [due0] "s"(helper ? HDUE * (PACE16 / 3) : PRE5 * (PACES / 3)), [y0] "s"(helper ? -15 : -79 + 5 * PRE5), [i0] "s"(helper ? TPW - 1 : 0),
                     [s0] "s"(helper ? ((TPW - 1) % NSLOT) * SLOT : 0),
                     [cap] "i"(SPIN_CAP), [win] "i"(PWIN), [ahi] "s"(((blockIdx.x >> 3) & 3) == 1 ? AHIR : AHI), [adl] "i"(ADL), [amg] "i"(AMG), [p3] "i"(PACE16 / 3), [p3s] "i"(PACES / 3), [pk] "i"(PK1), [slot] "i"(SLOT),
                     [ring] "i"(NSLOT * SLOT), [frb] "i"(NSLOT * 4), [lasts] "i"((NSLOT - 1) * SLOT), [tpw] "i"(TPW)
                   : "memory", "scc", "vcc", "s39", "s40", "s41", "s42", "s43", "s44", "s45", "s46", "s47", "s48", "s49", "s51", "s52", "s53", "s62", "s63");
    }
    lds_st32(ct + C_ISS, 1u);
#define CLR(b) "global_store_dwordx4 %0, %1, %" #b " sc1\n\t"
    auto clear = [&]() {
      asm volatile(CLR(2) CLR(3) CLR(4) CLR(5) CLR(6) CLR(7) CLR(8) CLR(9) "s_nop 1"
                 : : "v"(((lane >> 3) * NWG + blockIdx.x) * 128 + (lane & 7) * 16), "v"(u32x4{0u, 0u, 0u, 0u}), "s"(box), "s"(box + 1 * 262144), "s"(box + 2 * 262144),
                     "s"(box + 3 * 262144), "s"(box + 4 * 262144), "s"(box + 5 * 262144), "s"(box + 6 * 262144), "s"(box + 7 * 262144) : "memory");
    };
    if (PRECLR == 1) clear();
    wait_vm<PRECLR == 1 ? 23 : 15>();
    lds_st32(afull, (unsigned)(2 * TPW - 2));
    if (PRECLR == 3) clear();
    wait_vm<(PRECLR == 1 || PRECLR == 3) ? 20 : 12>();
    lds_st32(afull, (unsigned)(2 * TPW - 1));
    wait_vm<(PRECLR == 1 || PRECLR == 3) ? 8 : 0>();
    lds_st32(afull, ~0u);
    TNOW(tl);
    if (PRECLR == 2) clear();
    if (PRECLR == 4) { unsigned d; asm volatile("global_load_dword %0, %1, %2 sc1\n\ts_waitcnt vmcnt(0)" : "=v"(d) : "v"(blockIdx.x * 128), "s"(box) : "memory"); }
    __builtin_amdgcn_s_setprio(0);
    wait_vm<0>();
    TNOW(tlx);
    if (PRECLR != 5) lds_add(ct + C_X1, 1u);
    rslot = 7;
  } else {
    if (wave == 1) {
      LANE_VARS
    {
      const unsigned o = lane * 8;
      unsigned z = (o >= C_MREF && o < C_MREF + 64) ? 0xff800000u : 0u;
      if (helper && o >= C_CONS && o < C_MREF) z = NE * ((TPW - 1) / NPK);
      u32x2 zz = {z, z};
      if (o >= C_PFL && o < C_CONS) zz = u32x2{(o - C_PFL) / 2 + 2, (o - C_PFL) / 2 + 4} + (helper ? 2u * NPK * ((TPW - 1) / NPK) : 0u);
      *(LDS_AS(u32x2)*)(L0 + CT_OFF + o) = zz;
      *(LDS_AS(u32x2)*)(L0 + ONES_OFF + (lane < 48 ? o : 0)) = u32x2{0x3f803f80u, 0x3f803f80u};
    }
    }
    if (helper || wave == 1) BARRIER();
    if (wave <= NE) {
      LANE_VARS
      const int ej = wave - 1;
      const int qq = (lane >> 2) & 3, p4 = lane & 3;
      const unsigned apk = lbase + IMG_A + lane * 16;
      const unsigned epoch = epoch_load(box);
      __builtin_amdgcn_s_setprio(2);
      if (helper) { while ((unsigned)__builtin_amdgcn_s_memrealtime() - tent < RELAYT) __builtin_amdgcn_s_sleep(8); }
      unsigned long long tpv, tp7, tx1;
      {
        static_assert((TPW & 1) == 1 && C_CONS == C_PFL + 64 && C_FREE == C_FULL + 64, "");
        const int k0 = helper ? TPW - 1 : 0;
        unsigned need = 2 * k0 + 2;
        asm volatile("s_mov_b32 s46, 0\n\ts_mov_b64 exec, 0\n\ts_nop 3\n\ts_branch 99f\n"
                     "2:\n\ts_mov_b64 exec, -1\n\ts_nop 7\n\t" PV_Z4(72, 73, 74, 75) PV_Z4(76, 77, 78, 79) PV_Z4(80, 81, 82, 83) PV_Z4(84, 85, 86, 87)
                     "v_mov_b32 v88, 0xff800000\n\t"
                     "s_mov_b32 s46, %[k0]\n\ts_mov_b32 s45, %[s0]\n\ts_mov_b32 s44, %[e0]\n\ts_mov_b32 s47, %[cap]\n\t"
                     "s_cmp_lg_u32 s46, 0\n\ts_cbranch_scc1 32f\n"
                     "3:\n\tv_mov_b32 v148, %[ca]\n\t" PV_WAITF(4, 148) PV_FETCH_A
                     "s_mov_b64 exec, 2\n\tds_add_u32 v148, %[one] offset:64\n\ts_mov_b64 exec, -2\n\t"
                     "v_add_u32 %[need], 2, %[need]\n\tv_add_u32 v148, 4, v148\n\ts_mov_b64 exec, -1\n\t"
                     PV_WAITF(5, 148)
                     "v_mov_b32 v149, v148\n\ts_mov_b64 exec, -2\n\tv_add_u32 %[need], 2, %[need]\n\tv_add_u32 v148, 4, v148\n\t"
                     "s_mov_b64 exec, 1\n\tv_add_u32 %[need], %[npk2], %[need]\n\ts_mov_b64 exec, -1\n\t"
                     PV_REQ("126:129", "130:133", "134:135", 136, 148) PV_FETCH_B
                     "s_mov_b64 exec, 2\n\tds_add_u32 v149, %[one] offset:64\n\ts_mov_b64 exec, -1\n"
                     "6:\n\t"
                     PV_STEP(1, "126:129", "130:133", "134:135", 134, 135, 136, 148, "138:141", "142:145", "146:147", 137, 149,
                             "92:95", "98:101", "104:107", "90:93", "96:99", "102:105",
                             PV_MOV(92, 90) PV_MOV(93, 91), PV_MOV(98, 96) PV_MOV(99, 97), PV_MOV(104, 102) PV_MOV(105, 103),
                             "s_cmp_eq_u32 s46, %[last]\n\ts_cbranch_scc1 99f\n\t")
                     "s_cbranch_vccnz 7f\n"
                     "16:\n\t" PV_FETCH_A PV_REL(148)
                     "s_lshl1_add_u32 s54, s46, -2\n\ts_lshr_b64 s[54:55], %[sch], s54\n\ts_and_b32 s54, s54, 15\n\ts_cbranch_scc1 31f\n"
                     "30:\n\t"
                     PV_STEP(2, "138:141", "142:145", "146:147", 146, 147, 137, 149, "126:129", "130:133", "134:135", 136, 148,
                             "110:113", "116:119", "122:125", "108:111", "114:117", "120:123",
                             PV_MOV(110, 108) PV_MOV(111, 109), PV_MOV(116, 114) PV_MOV(117, 115), PV_MOV(122, 120) PV_MOV(123, 121),
                             "s_cmp_eq_u32 s46, %[last] - 1\n\ts_cbranch_scc1 98f\n\t")
                     "s_cbranch_vccnz 8f\n"
                     "26:\n\t" PV_FETCH_B PV_REL(149)
                     "s_branch 6b\n"
                     PV_WAITF(7, 148) "s_branch 16b\n"
                     PV_WAITF(8, 149) "s_branch 26b\n"
                     "98:\n\t" PV_MF("76:79", "114:117", "138:141") PV_MF("80:83", "120:123", "138:141")
                     "v_add_u32 %[need], 2, %[need]\n\ts_add_u32 s46, s46, 1\n\ts_branch 13f\n"
                     PV_SLOW(1, "126:129", "130:133", "134:135", 134, 135, 136, 148, 126, 127, 128, 129)
                     PV_SLOW(2, "138:141", "142:145", "146:147", 146, 147, 137, 149, 138, 139, 140, 141)
                     "31:\n\ts_cmp_lg_u32 s54, 8\n\ts_cbranch_scc1 34f\n"
                     "32:\n\tv_mbcnt_lo_u32_b32 v152, 31, 0\n\tv_lshlrev_b32 v152, 7, v152\n\ts_mov_b32 s55, %[gcap]\n"
                     "33:\n\tglobal_load_dwordx2 v[150:151], v152, %[fl] sc1\n\ts_sub_u32 s55, s55, 1\n\ts_cselect_b32 s56, 0, -1\n\ts_waitcnt vmcnt(0) lgkmcnt(0)\n\t"
                     "v_cmp_ne_u64 s[48:49], %[tag], v[150:151]\n\ts_sleep 2\n\ts_and_b32 s48, s48, s56\n\ts_cbranch_scc1 33b\n\t"
                     "s_mov_b32 s57, 0\n\tv_mov_b32 v150, %[apk]\n\ts_cmp_lg_u32 s46, %[last]\n\ts_cbranch_scc1 30b\n\ts_mov_b32 s54, 7\n\ts_branch 38f\n"
                     "34:\n\ts_cmp_lg_u32 s46, %[last] - 3\n\ts_cbranch_scc1 36f\n\tv_mov_b32 v151, %[iss]\n"
                     "35:\n\tds_read_b32 v150, v151\n\ts_sub_i32 s47, s47, 1\n\ts_cmp_lt_i32 s47, 0\n\ts_cselect_b32 s55, 0, 1\n\ts_waitcnt lgkmcnt(0)\n\t"
                     "v_readfirstlane_b32 s56, v150\n\ts_cmp_lt_u32 s56, s55\n\ts_cbranch_scc1 35b\n"
                     "36:\n\ts_mul_i32 s55, s57, 3072\n\tv_add_u32 v150, s55, %[apk]\n\ts_getpc_b64 s[48:49]\n"
                     "37:\n\ts_mul_i32 s55, s57, 40\n\ts_add_u32 s55, s55, 38f-37b\n\ts_add_u32 s48, s48, s55\n\ts_addc_u32 s49, s49, 0\n\t"
                     "s_add_u32 s57, s57, s54\n\ts_sub_u32 s54, s54, 1\n\ts_setpc_b64 s[48:49]\n"
                     "38:\n\t" PV_W2(0, 3, 4, 7, 8, 11) PV_W2(12, 15, 16, 19, 20, 23) PV_W2(24, 27, 28, 31, 32, 35) PV_W2(36, 39, 40, 43, 44, 47)
                     PV_W2(48, 51, 52, 55, 56, 59) PV_W2(60, 63, 64, 67, 68, 71)
                     "s_waitcnt lgkmcnt(0)\n\ts_branch 3b\n"
                     "99:\n\t" PV_MF("76:79", "96:99", "126:129") PV_MF("80:83", "102:105", "126:129") PV_T(58)
                     "v_add_f32 v153, v86, v87\n\tv_add_f32 v151, v84, v85\n\tv_rcp_f32 v150, v153\n\tv_mul_f32 v151, %[nic], v151\n\t"
                     "v_add_u32 v152, %[aw], %[apk]\n\tv_mul_f32 v151, v151, v150\n\t"
                     PV_NRM(72, 73, 74, 75, 0) PV_NRM(76, 77, 78, 79, 2048) PV_NRM(80, 81, 82, 83, 4096)
                     "v_mov_b32 v154, %[x1]\n\ts_and_b64 exec, exec, 1\n\tds_add_u32 v154, %[one]\n\ts_cselect_b64 exec, -1, 0\n\ts_cselect_b32 s55, %[x1n], 0\n\t" PV_T(60)
                     "v_log_f32 v153, v153\n\tv_subrev_f32 v88, %[toff], v88\n\tv_lshrrev_b32 v151, 1, %[apm]\n\tv_add_f32 v88, v88, v153\n\tv_add_u32 v151, %[lwo], v151\n\t"
                     "v_mbcnt_lo_u32_b32 v155, -1, 0\n\tv_mbcnt_hi_u32_b32 v155, -1, v155\n\tds_write_b32 v151, v88\n\t"
                     "v_and_b32 v151, 1, v155\n\tv_and_b32 v155, 48, v155\n\tv_lshl_add_u32 v155, v151, 2, v155\n\tv_add_u32 v155, %[lw], v155\n\t"
                     "ds_read2_b32 v[114:115], v155 offset1:2\n\tv_add_u32 v152, %[ar], %[apk]\n"
                     "97:\n\tds_read_b32 v150, v154\n\tds_read_b128 v[90:93], v152\n\tds_read_b128 v[94:97], v152 offset:1024\n\tds_read_b128 v[98:101], v152 offset:2048\n\t"
                     "ds_read_b128 v[102:105], v152 offset:3072\n\tds_read_b128 v[106:109], v152 offset:4096\n\tds_read_b128 v[110:113], v152 offset:5120\n\t"
                     "s_sub_i32 s47, s47, 1\n\ts_cmp_lt_i32 s47, 0\n\ts_cselect_b32 s55, 0, s55\n\ts_waitcnt vmcnt(0) lgkmcnt(6)\n\t"
                     "v_readfirstlane_b32 s56, v150\n\ts_cmp_lt_u32 s56, s55\n\ts_cbranch_scc1 97b\n\ts_cmp_eq_u32 s46, 0\n\t"
                     PV_G(5, "90:93", "0:3", "4:7", "8:11", "0", "0", "0")
                     PV_G(4, "94:97", "12:15", "16:19", "20:23", "v[72:75]", "v[76:79]", "v[80:83]")
                     PV_G(3, "98:101", "24:27", "28:31", "32:35", "v[72:75]", "v[76:79]", "v[80:83]")
                     PV_G(2, "102:105", "36:39", "40:43", "44:47", "v[72:75]", "v[76:79]", "v[80:83]")
                     PV_G(1, "106:109", "48:51", "52:55", "56:59", "v[72:75]", "v[76:79]", "v[80:83]")
                     PV_G(0, "110:113", "60:63", "64:67", "68:71", "v[72:75]", "v[76:79]", "v[80:83]")
                     "v_xor_b32 v114, v114, %[ltag]\n\tv_xor_b32 v115, v115, %[ltag]\n\ts_nop 3\n\t"
                     "v_cvt_pk_bf16_f32 v116, v72, v73\n\tv_cvt_pk_bf16_f32 v120, v74, v75\n\tv_cvt_pk_bf16_f32 v117, v76, v77\n\tv_cvt_pk_bf16_f32 v121, v78, v79\n\t"
                     "v_cvt_pk_bf16_f32 v118, v80, v81\n\tv_bitop3_b32 v119, v116, v117, v114 bitop3:0x96\n\tv_cvt_pk_bf16_f32 v122, v82, v83\n\t"
                     "v_bitop3_b32 v123, v120, v121, v115 bitop3:0x96\n\tv_xor_b32 v119, v119, v118\n\tv_xor_b32 v123, v123, v122\n\t"
                     "s_cbranch_scc1 2b\n\t"
                     "global_store_dwordx4 %[so], v[116:119], %[bx]" SPOL "\n\tglobal_store_dwordx4 %[so], v[120:123], %[bx2]" SPOL "\n\t"
                     PV_T(62) "s_waitcnt lgkmcnt(0)\n\ts_nop 0"
                     : [need] "+v"(need), "={s[58:59]}"(tpv), "={s[60:61]}"(tp7), "={s[62:63]}"(tx1)
                     : [so] "v"(((ej * 16 + (lane >> 3)) * NWG + blockIdx.x) * 128 + (lane & 7) * 16), [ltag] "v"(line_tag(epoch, lane & 7)),
                       [bx] "s"(box), [bx2] "s"(box + 8 * NWG * 128), [aw] "s"(A_IMG - IMG_A + (ej >> 1) * 1024 + (ej & 1) * 8), [ar] "i"(A_IMG - IMG_A),
                       [nic] "i"(__builtin_bit_cast(unsigned, -1.f / CC)), [toff] "i"(__builtin_bit_cast(unsigned, TOFF)),
                       [lwo] "i"(CT_OFF + C_LW - PKM / 2), [lw] "i"(CT_OFF + C_LW), [x1] "i"(CT_OFF + C_X1), [x1n] "i"(X1N),
                       [apk] "v"(apk), [apm] "v"(lbase + PKM + r * 8), [apu] "v"(lbase + PKU + g * 64 + (r & 3) * 16),
                       [ox] "v"(lbase + tr_off(4 * g + qq, 4 * ej + p4)), [oy] "v"(lbase + 32 * ROWB + tr_off(2 * g + (qq >> 1), 4 * ej + p4)),
                       [ca] "v"(ct + (lane == 0 ? C_PFL + (k0 % NPK) * 4 : C_FULL + (k0 % NSLOT) * 4)),
                       [lim] "v"(ct + (lane == 0 ? C_PFL + NPK * 4 : C_FULL + NSLOT * 4)), [base] "v"(ct + (lane == 0 ? C_PFL : C_FULL)), [one] "v"(1u),
                       [k0] "s"(k0), [s0] "s"(helper ? ((TPW - 1) % NSLOT) * SLOT : 0), [e0] "s"(helper ? ((TPW - 1) % NPK) * 128 : 0),
                       [w2] "s"(box + W2_OFF + ej * 18432 - (lbase + IMG_A)), [fl] "s"(box + W2F_OFF),
                       [tag] "s"(u32x2{line_tag(epoch, 10), line_tag(epoch, 11)}), [gcap] "i"(GSPIN_CAP), [iss] "i"(CT_OFF + C_ISS), [cap] "i"(SPIN_CAP), [slot] "i"(SLOT), [ring] "i"(NSLOT * SLOT), [pring] "i"(NPK * 128),
                       [npk2] "i"(2 * NPK), [last] "i"(TPW - 1), [tight] "i"(TPW - 5),  [sch] "s"(W2SCH)
                     : "memory", "scc", "vcc", "s44", "s45", "s46", "s47", "s48", "s49", "s50", "s51", "s52", "s53", "s54", "s55", "s56", "s57",
                       "v0", "v1", "v2", "v3", "v4", "v5", "v6", "v7", "v8", "v9", "v10", "v11", "v12", "v13", "v14", "v15", "v16", "v17", "v18", "v19", "v20", "v21", "v22", "v23", "v24", "v25", "v26", "v27", "v28", "v29", "v30", "v31", "v32", "v33", "v34", "v35", "v36", "v37", "v38", "v39", "v40", "v41", "v42", "v43", "v44", "v45", "v46", "v47", "v48", "v49", "v50", "v51", "v52", "v53", "v54", "v55", "v56", "v57", "v58", "v59", "v60", "v61", "v62", "v63", "v64", "v65", "v66", "v67", "v68", "v69", "v70", "v71", "v72", "v73", "v74", "v75", "v76", "v77", "v78", "v79", "v80", "v81", "v82", "v83", "v84", "v85", "v86", "v87", "v88",
                       "v90", "v91", "v92", "v93", "v94", "v95", "v96", "v97", "v98", "v99", "v100", "v101", "v102", "v103", "v104", "v105", "v106", "v107",
                       "v108", "v109", "v110", "v111", "v112", "v113", "v114", "v115", "v116", "v117", "v118", "v119", "v120", "v121", "v122", "v123",
                       "v124", "v125", "v126", "v127", "v128", "v129", "v130", "v131", "v132", "v133", "v134", "v135", "v136", "v137", "v138", "v139",
                       "v140", "v141", "v142", "v143", "v144", "v145", "v146", "v147", "v148", "v149", "v150", "v151", "v152", "v153", "v154", "v155");
      }
    } else {
      LANE_VARS
      const int c = wave - 1 - NE;
      SCtx sc;
      sc.L0 = L0; sc.ct = ct; sc.apk = lbase + IMG_A + lane * 16; sc.apm = lbase + PKM + r * 8;
      sc.api = lbase + PKU + g * 64 + (r & 3) * 2 + ((r >> 2) == g ? 0 : 8); sc.amr = ct + C_MREF + r * 4;
      sc.o_rb = r * ROWB + ((g ^ swz(r)) << 4);
      sc.ev = (r & 1) ^ 1;
      sc.o_yb = (r & 1) ? ONES_OFF + (g << 4) : (32 + (r >> 1)) * ROWB + ((g ^ swz(r >> 1)) << 4);
      TNOW(tq0);
      if (c >= NEARLY) {
        static_assert(NS - NEARLY == 3, "");
        const int u = c - NEARLY;
        const unsigned epoch = epoch_load(box);
        const unsigned ta = line_tag(epoch, 8), tb = line_tag(epoch, 9);
        while ((unsigned)__builtin_amdgcn_s_memrealtime() - tent < QKWAIT) __builtin_amdgcn_s_sleep(4);
        u32x4 v[4];
#pragma unroll 1
        for (int sp = 0; sp < GSPIN_CAP; ++sp) {
          asm volatile("global_load_dwordx4 %0, %4, %5 sc1\n\tglobal_load_dwordx4 %1, %4, %5 offset:1024 sc1\n\tglobal_load_dwordx4 %2, %4, %5 offset:2048 sc1\n\t"
                       "global_load_dwordx4 %3, %4, %5 offset:3072 sc1\n\ts_waitcnt vmcnt(0)"
                       : "=&v"(v[0]), "=&v"(v[1]), "=&v"(v[2]), "=&v"(v[3]) : "v"(lane * 16), "s"(box + QK_OFF + u * 4096) : "memory");
          unsigned bad = 0;
#pragma unroll
          for (int t = 0; t < 4; ++t) bad |= (v[t][0] ^ v[t][1] ^ v[t][2] ^ ta) | (v[t][3] ^ tb);
          if (!__any(bad != 0u)) break;
        }
        TSET(tqa);
#pragma unroll
        for (int t = 0; t < 4; ++t)
          *(LDS_AS(u32x2)*)(L0 + IMG_A + (4 * u + t) * 512 + ((g >> 1) * 16 + r) * 16 + (g & 1) * 8) = u32x2{v[t][0], v[t][1]};
        asm volatile("" ::: "memory");
        lds_add(ct + C_QI, 1u);
        lds_st32(ct + C_EP, epoch);
      }
      static_assert(NE == 4 && NPK == 6 && NSLOT == 10, "");
      s_items(sc, helper ? TPW : c, helper || c >= NEARLY);
      const int hi = (HMAP >> (4 * c)) & 15;
      if (hi < 6 && (!helper || hi >= 4)) s_half(sc, TPW - NH1 + (hi >> 1), hi & 1);
      TNOW(tse);
      rslot = c;
    }
  }

  if (rslot >= 0 && ((blockIdx.x >> 3) & 3) == 1) {
    LANE_VARS
    __builtin_amdgcn_s_setprio(0);
    const int rid = ((blockIdx.x >> 5) << 3) | (blockIdx.x & 7);
    const int qa = ((rid >> 1) & 3) * 4 + ((rid >> 3) & 1) * 2;
    const char* src = box + (size_t)(rid * NWG + rslot * 32) * 128;
    const int e = lane & 7;
    const int oidx = (qa + (e & 1)) * CC + 48 * (rid >> 4) + 16 * (e < 6 ? e >> 1 : 0) + 8 * (rid & 1) + (lane >> 3);
    const unsigned short qv = ((const gus*)q)[oidx];
    u32x4 am;
#pragma unroll
    for (int d = 0; d < 4; ++d) am[d] = (e >> 1) == d ? ((e & 1) ? 0xffff0000u : 0x0000ffffu) : 0u;
    const unsigned epoch = *(LDS_AS(unsigned)*)(L0 + CT_OFF + C_EP);
    const unsigned arf = lbase + RED_OFF + 4096 + (lane & 1) * 4;
    poll_ge_sleep(ct + C_FREE + ((TPW - NPK) % NSLOT) * 4, 8u * ((TPW - NPK) / NSLOT + 1));
    lds_st32(arf + rslot * 8, 0u);
    unsigned long long tr;
#define R_LD(d, m, o) "s_mov_b64 exec, s[" m "]\n\tglobal_load_dwordx4 v[" d "], %[vo], %[src] offset:" #o " sc1\n\t"
#define R_W(l, a0, a1, a2, a3) "v_cvt_pk_bf16_f32 v" #a3 ", v" #a3 ", v" #a3 "\n\tv_and_b32 v" #a0 ", v60, v" #a3 "\n\tv_and_b32 v" #a1 ", v61, v" #a3 "\n\t" \
                               "v_and_b32 v" #a2 ", v62, v" #a3 "\n\tv_and_b32 v" #a3 ", v63, v" #a3 "\n\t"
#define R_G(n, w, m, l, d0, d1, d2, d3, a0, a1, a2, a3, c) \
  "s_mov_b64 exec, s[" m "]\n\ts_cbranch_execz " #n "f\n\ts_waitcnt vmcnt(" #w ")\n\tv_bitop3_b32 v" #l ", v" #d0 ", v" #d1 ", v" #d2 " bitop3:0x96\n\t" \
  "v_bitop3_b32 v" #l ", v" #l ", v" #d3 ", %[tag] bitop3:0x96\n\tv_mov_b32 v" #d3 ", s49\n\ts_mov_b64 exec, -1\n\tv_exp_f32 v" #a3 ", v" #l "\n\t" \
  "v_xor_b32_dpp v59, v" #l ", v" #l " quad_perm:[2,3,0,1] row_mask:0xf bank_mask:0x5\n\tv_xor_b32_dpp v59, v" #l ", v" #l " row_shr:4 row_mask:0xf bank_mask:0xa\n\t" \
  R_W(l, a0, a1, a2, a3) "v_cmp_ne_u32 s[" m "], 0, v59\n\ts_nop 0\n" \
  "1" #n ":\n\tv_mfma_f32_16x16x32_bf16 v[36:39], v[" #a0 ":" #a3 "], v[" #d0 ":" #d3 "], " c "\n\t"
#define R_X(n) #n ":\n\ts_mov_b64 exec, -1\n\ts_nop 2\n\ts_branch 1" #n "b\n"
#define R_F(l, d0, d3, a0, a1, a2, a3, c) "v_sub_f32 v" #a3 ", v" #l ", v56\n\tv_exp_f32 v" #a3 ", v" #a3 "\n\ts_nop 0\n\t" R_W(l, a0, a1, a2, a3) \
  "s_nop 1\n\tv_mfma_f32_16x16x32_bf16 v[36:39], v[" #a0 ":" #a3 "], v[" #d0 ":" #d3 "], " c "\n\t"
#define R_RD(d, a, b) "ds_read2st64_b32 v[" d "], %[ard] offset0:" #a " offset1:" #b "\n\t"
#define R_A(d, a) "v_pk_add_f32 v[" d "], v[" d "], v[" a "]\n\t"
#define R_S(x, f) "v_sub_f32 v" #f ", v" #f ", v56\n\tv_exp_f32 v" #f ", v" #f "\n\ts_nop 0\n\tv_mul_f32 v[" #x "], v[" #x "], v" #f "\n\tv_mul_f32 v[" #x " + 1], v[" #x " + 1], v" #f "\n\t"
    asm volatile("s_mov_b64 s[52:53], 0\n\ts_mov_b64 s[40:41], -1\n\ts_mov_b64 s[42:43], -1\n\ts_mov_b64 s[44:45], -1\n\ts_mov_b64 s[46:47], -1\n\ts_mov_b32 s48, %[cap]\n\ts_mov_b32 s49, 0x3f803f80\n\t"
                 "v_mov_b32 v57, 1\n\tv_mov_b32 v58, %[x1]\n"
                 "20:\n\ts_sleep 1\n\tds_read_b32 v56, v58\n\ts_sub_u32 s48, s48, 1\n\ts_cselect_b32 s50, 0, %[x1n]\n\ts_waitcnt lgkmcnt(0)\n\t"
                 "v_readfirstlane_b32 s51, v56\n\ts_cmp_lt_u32 s51, s50\n\ts_cbranch_scc1 20b\n\tv_mov_b32 v58, %[red]\n\ts_mov_b32 s48, %[gcap]\n\ts_sleep %[rs]\n"
                 "21:\n\t" R_LD("0:3", "40:41", 0) R_LD("4:7", "42:43", 1024) R_LD("8:11", "44:45", 2048) R_LD("12:15", "46:47", 3072)
                 "s_cmp_eq_u32 s48, %[gcap]\n\ts_cbranch_scc1 24f\n\ts_waitcnt vmcnt(0)\n24:\n\t"
                 R_G(1, 3, "40:41", 32, 0, 1, 2, 3, 16, 17, 18, 19, "0") R_G(2, 2, "42:43", 33, 4, 5, 6, 7, 20, 21, 22, 23, "v[36:39]")
                 R_G(3, 1, "44:45", 34, 8, 9, 10, 11, 24, 25, 26, 27, "v[36:39]") R_G(4, 0, "46:47", 35, 12, 13, 14, 15, 28, 29, 30, 31, "v[36:39]")
                 "v_max3_f32 v56, v32, v33, v34\n\ts_sub_u32 s48, s48, 1\n\ts_cbranch_scc1 22f\n\ts_or_b64 s[50:51], s[40:41], s[42:43]\n\tv_max_f32 v56, v56, v35\n\t"
                 "s_or_b64 s[50:51], s[50:51], s[44:45]\n\ts_or_b64 s[50:51], s[50:51], s[46:47]\n\tv_cmp_nlt_f32 vcc, |v56|, %[lim]\n\ts_cbranch_scc1 21b\n\t"
                 "s_cbranch_vccnz 25f\n"
                 "22:\n\ts_mov_b64 exec, %[wm]\n\tds_write_b128 %[awr], v[36:39]\n\ts_mov_b64 exec, 1\n\tds_add_rtn_u32 v59, v58, v57\n\ts_mov_b64 exec, -1\n\t"
                 R_RD("40:41", 0, 1) R_RD("42:43", 2, 3) R_RD("44:45", 4, 5) R_RD("46:47", 6, 7) R_RD("48:49", 8, 9) R_RD("50:51", 10, 11) R_RD("52:53", 12, 13) R_RD("54:55", 14, 15)
                 "s_waitcnt lgkmcnt(8)\n\tv_add_u32 v59, v59, v57\n\ts_nop 0\n\tv_readfirstlane_b32 s50, v59\n\ts_cmp_lg_u32 s50, 8\n\ts_cbranch_scc1 26f\n"
                 "23:\n\ts_waitcnt lgkmcnt(6)\n\t" R_A("40:41", "42:43") "s_waitcnt lgkmcnt(4)\n\t" R_A("44:45", "46:47") "s_waitcnt lgkmcnt(2)\n\t" R_A("48:49", "50:51")
                 R_A("40:41", "44:45") "s_waitcnt lgkmcnt(0)\n\t" R_A("52:53", "54:55") R_A("48:49", "52:53") R_A("40:41", "48:49")
                 "v_add_f32 v40, v40, v41\n\ts_nop 1\n\tv_mov_b32_dpp v41, v40 quad_perm:[2,3,2,3] row_mask:0xf bank_mask:0xf\n\ts_nop 1\n\t"
                 "v_mov_b32_dpp v41, v41 row_shl:4 row_mask:0xf bank_mask:0x5\n\tv_rcp_f32 v41, v41\n\ts_mov_b64 exec, %[om]\n\tv_mul_f32 v40, v40, v41\n\t"
                 "v_cvt_pk_bf16_f32 v40, v40, v40\n\tv_lshlrev_b32 v40, 16, v40\n\tv_add_f32 v40, v40, %[qf]\n\tv_cvt_pk_bf16_f32 v40, v40, v40\n\t"
                 "global_store_short %[oo], v40, %[out]\n\t"
                 "s_cmp_lg_u32 %[rid], 0\n\ts_cbranch_scc1 29f\n\ts_mov_b64 exec, 1\n\tv_mov_b32 v41, %[eo]\n\tglobal_store_dword v41, %[ep], %[bx] sc1\n\ts_branch 29f\n"
                 R_X(1) R_X(2) R_X(3) R_X(4)
                 "25:\n\tv_max_f32_dpp v56, v56, v56 row_ror:8 row_mask:0xf bank_mask:0xf\n\tv_mov_b32 v59, v56\n\ts_nop 1\n\tv_permlane16_swap_b32 v56, v59\n\tv_max_f32 v56, v56, v59\n\t"
                 "v_mov_b32 v59, v56\n\ts_nop 1\n\tv_permlane32_swap_b32 v56, v59\n\tv_max_f32 v56, v56, v59\n\tv_mov_b32 v57, 0x101\n\tv_add_u32 v59, %[rs8], %[arf]\n\tds_write_b32 v59, v56\n\t"
                 R_F(32, 0, 3, 16, 17, 18, 19, "0") R_F(33, 4, 7, 20, 21, 22, 23, "v[36:39]") R_F(34, 8, 11, 24, 25, 26, 27, "v[36:39]") R_F(35, 12, 15, 28, 29, 30, 31, "v[36:39]")
                 "s_nop 7\n\ts_branch 22b\n"
                 "26:\n\ts_waitcnt vmcnt(0)\n\ts_and_b32 s50, s50, 0xff\n\ts_cmp_lg_u32 s50, 8\n\ts_cbranch_scc1 29f\n\t"
                 "ds_read2_b32 v[0:1], %[arf] offset1:2\n\tds_read2_b32 v[2:3], %[arf] offset0:4 offset1:6\n\tds_read2_b32 v[4:5], %[arf] offset0:8 offset1:10\n\t"
                 "ds_read2_b32 v[6:7], %[arf] offset0:12 offset1:14\n\ts_waitcnt lgkmcnt(0)\n\tv_max3_f32 v56, v0, v1, v2\n\tv_max3_f32 v56, v56, v3, v4\n\t"
                 "v_max3_f32 v56, v56, v5, v6\n\tv_max_f32 v56, v56, v7\n\t"
                 R_S(40, 0) R_S(42, 1) R_S(44, 2) R_S(46, 3) R_S(48, 4) R_S(50, 5) R_S(52, 6) R_S(54, 7) "s_branch 23b\n"
                 "29:\n\ts_mov_b64 exec, -1\n\ts_waitcnt lgkmcnt(0)"
                 : "=&{s[52:53]}"(tr)
                 : "{v[60:63]}"(am), [vo] "v"(lane * 16), [src] "s"(src), [tag] "v"(line_tag(epoch, e)),
                   [awr] "v"(lbase + RED_OFF + (rslot * 2 + (r >> 3)) * 256 + (r & 7) * 32 + (g & 1) * 16), [ard] "v"(lbase + RED_OFF + lane * 4), [arf] "v"(arf),
                   [qf] "v"((unsigned)qv << 16), [oo] "v"(oidx * 2), [out] "s"(KARG(kb, 0)), [ep] "v"(epoch + 1u), [bx] "s"(box), [rid] "s"(rid), [rs8] "s"(rslot * 8),
                   [wm] "s"(0xff00ff0000ff00ffull), [om] "s"(0x3f3f3f3f3f3f3f3full), [lim] "s"(64.f),
                   [cap] "i"(SPIN_CAP), [gcap] "i"(GSPIN_CAP), [x1] "i"(CT_OFF + C_X1), [x1n] "i"(X1N), [red] "i"(CT_OFF + C_RED), [rs] "i"(RSLEEP), [eo] "i"(EPOCH_OFF)
                 : "memory", "scc", "vcc", "s40", "s41", "s42", "s43", "s44", "s45", "s46", "s47", "s48", "s49", "s50", "s51", "v0", "v1", "v2", "v3", "v4", "v5", "v6", "v7", "v8", "v9", "v10", "v11", "v12", "v13", "v14", "v15", "v16", "v17", "v18", "v19", "v20", "v21", "v22", "v23", "v24", "v25", "v26", "v27", "v28", "v29", "v30", "v31", "v32", "v33", "v34", "v35", "v36", "v37", "v38", "v39", "v40", "v41", "v42", "v43", "v44", "v45", "v46", "v47", "v48", "v49", "v50", "v51", "v52", "v53", "v54", "v55", "v56", "v57", "v58", "v59");
  }
}

#undef q
#undef f0
#undef f1
#undef wq
#undef wk
#undef wv
#undef wo
#undef box
extern "C" __global__ void dino_end() {}

extern "C" void kernel_launch(void* const* d_in, const int* in_sizes, int n_in,
                              void* d_out, int out_size, void* d_ws, size_t ws_size,
                              hipStream_t stream) {
  dino_main<<<NWG, 768, 0, stream>>>((const unsigned short*)d_in[0], (const char*)d_in[1], (const char*)d_in[2], (const char*)d_in[3],
                                     (const char*)d_in[4], (const char*)d_in[5], (const char*)d_in[6], (char*)d_ws + WSOFF,
                                     (unsigned short*)d_out);
}
```

```cpp
#include <hip/hip_runtime.h>
#include <hip/hip_bf16.h>
#include <stdint.h>

#define PROFOFF (380ll << 20)
#define TNOW(v) do {} while (0)
#define TPUT(id, v) do {} while (0)

using bf16x8  = __attribute__((ext_vector_type(8))) short;
using f32x4   = __attribute__((ext_vector_type(4))) float;
using f32x2   = __attribute__((ext_vector_type(2))) float;
using u32x4   = __attribute__((ext_vector_type(4))) unsigned;
using u32x2   = __attribute__((ext_vector_type(2))) unsigned;
using v4i16_t = __attribute__((ext_vector_type(4))) short;
typedef __attribute__((address_space(3))) char* lds_ptr;
#define LDS_AS(T) __attribute__((address_space(3))) T

constexpr int CC = 192, IMW = 512;
constexpr int NWG = 256, NS = 7, NE = 4;
constexpr int TPW = 33;
constexpr int ROWB = 384;
constexpr int SLOT = 40 * ROWB;
constexpr int NSLOT = 10;
constexpr int BOX_BYTES = 64 * NWG * 128, EPOCH_OFF = BOX_BYTES, QK_OFF = EPOCH_OFF + 4096, W2F_OFF = QK_OFF + 12288, W2_OFF = W2F_OFF + 1024;
constexpr int GSPIN_CAP = 1 << 16;
#define PACE16 777
#define QKWAIT 300
#define NEARLY 4
#define PRECLR 4
#define RSLEEP 24
#define HDUE 93
#define PACES 810
#define PK1 14
#define AG 100
#define AHI 22
#define ADL 60
#define AMG 200
#define NORELAY
#define HMAP 0x4321085
#define HB 1
#define WSOFF ((ws_size - (4u << 20)) & ~(size_t)0x1fffff)
#define EPM 0
#define RSTART 1880
#define RMODE 1
#define CLRK 17
#define X1N (PRECLR == 5 ? 11u : 5u)
#define RPOL " sc1"
#define SPOL " sc1"
#define RGAP 0
#define RDRY 0
#define RWAIT 76
#define RMIN 30
#define PROBE 0xffffffffffffffffull
#define RELAYT 0
#define PRE5 1
#define SL_S 2
#define SL_FAR 12
#define SL_P 2
#define SL_L 1
#define STR_(x) #x
#define STR(x) STR_(x)
#define AHIR AHI
#define T0BACK 7
#define W2SCH 0x0311180000000000ull
#define NH1 3
constexpr int NPK = 6;
constexpr float EPS = 1e-5f;
constexpr float SQRT_C = 13.856406460551018f;
constexpr float LOG2E = 1.4426950408889634f;
constexpr float THR = 20.0f;
constexpr float TOFF = 192.0f;
constexpr int SPIN_CAP = 1 << 22;

constexpr int IMG_A = NSLOT * SLOT;
constexpr int CT_OFF = IMG_A + 6144;
constexpr int PKM = CT_OFF + 512, PKU = PKM + NPK * 128;
constexpr int ONES_OFF = PKU + NPK * 256;
constexpr int JUNK = ONES_OFF + ROWB;
constexpr int LDS_BYTES = JUNK + 256;
constexpr int H_R = 192 * ROWB, H_Q = H_R + 32 * ROWB, H_C = H_Q + 6144;
#define PWIN 35
static_assert(LDS_BYTES <= 163840 && (PKU & 15) == 0 && (ONES_OFF & 127) == 0, "lds");
constexpr int C_FULL = 0, C_FREE = 64, C_EP = 128, C_RED = 144, C_ISS = 152, C_X1 = 156, C_QI = 160, C_PFL = 192, C_CONS = 256,
              C_MREF = 320, C_LW = 384;

typedef __bf16 bf16x2_t __attribute__((ext_vector_type(2)));
__device__ __forceinline__ unsigned cvtpk(float lo, float hi) {
  return __builtin_bit_cast(unsigned, __builtin_convertvector(f32x2{lo, hi}, bf16x2_t));
}
__device__ __forceinline__ float rbf(float x) { return __uint_as_float(cvtpk(x, x) << 16); }
__device__ __forceinline__ float bflo(unsigned p) { return __uint_as_float(p << 16); }
__device__ __forceinline__ float bfhi(unsigned p) { return __uint_as_float(p & 0xffff0000u); }
__device__ __forceinline__ bf16x8 pack8(const f32x4& a, const f32x4& b) {
  return __builtin_bit_cast(bf16x8, u32x4{cvtpk(a[0], a[1]), cvtpk(a[2], a[3]), cvtpk(b[0], b[1]), cvtpk(b[2], b[3])});
}
__device__ __forceinline__ float xrow16_max(float x) {
  auto s = __builtin_amdgcn_permlane16_swap(__float_as_uint(x), __float_as_uint(x), false, false);
  x = fmaxf(__uint_as_float(s[0]), __uint_as_float(s[1]));
  auto t = __builtin_amdgcn_permlane32_swap(__float_as_uint(x), __float_as_uint(x), false, false);
  return fmaxf(__uint_as_float(t[0]), __uint_as_float(t[1]));
}
__device__ __forceinline__ float xrow16_sum(float x) {
  auto s = __builtin_amdgcn_permlane16_swap(__float_as_uint(x), __float_as_uint(x), false, false);
  x = __uint_as_float(s[0]) + __uint_as_float(s[1]);
  auto t = __builtin_amdgcn_permlane32_swap(__float_as_uint(x), __float_as_uint(x), false, false);
  return __uint_as_float(t[0]) + __uint_as_float(t[1]);
}
__device__ __forceinline__ v4i16_t tr_read(lds_ptr p) {
  return __builtin_amdgcn_ds_read_tr16_b64_v4i16((LDS_AS(v4i16_t)*)p);
}
__device__ __forceinline__ f32x4 mfma(bf16x8 a, bf16x8 b, f32x4 c) {
  return __builtin_amdgcn_mfma_f32_16x16x32_bf16(a, b, c, 0, 0, 0);
}
__device__ __forceinline__ int swz(int p) { return (p & 6) | ((p >> 2) & 1); }
constexpr unsigned swz_tbl(int i) {
  unsigned t = 0;
  for (int j = 0; j < 8; ++j) { const int p = (8 * i + j) / 3; t |= (unsigned)((p & 6) | ((p >> 2) & 1)) << (4 * j); }
  return t;
}
__device__ __forceinline__ bf16x8 mk8(v4i16_t a, v4i16_t b) { return bf16x8{a[0], a[1], a[2], a[3], b[0], b[1], b[2], b[3]}; }

__device__ __forceinline__ void lds_st32(unsigned addr, unsigned v) {
  asm volatile("ds_write_b32 %0, %1" : : "v"(addr), "v"(v) : "memory");
}
__device__ __forceinline__ unsigned lds_ld32(unsigned addr) {
  unsigned v;
  asm volatile("ds_read_b32 %0, %1\n\ts_waitcnt lgkmcnt(0)" : "=v"(v) : "v"(addr) : "memory");
  return __builtin_amdgcn_readfirstlane(v);
}
__device__ __forceinline__ void lds_add(unsigned addr, unsigned n) {
  asm volatile("s_mov_b64 exec, 1\n\tds_add_u32 %0, %1\n\ts_mov_b64 exec, -1\n\ts_nop 3" : : "v"(addr), "v"(n) : "memory");
}
__device__ __forceinline__ void poll_ge(unsigned addr, unsigned need) {
  unsigned v, s, n2, cnt = SPIN_CAP;
  asm volatile("1:\n\tds_read_b32 %0, %4\n\ts_sub_u32 %3, %3, 1\n\ts_cselect_b32 %2, 0, %5\n\ts_waitcnt lgkmcnt(0)\n\t"
               "v_readfirstlane_b32 %1, %0\n\ts_cmp_lt_u32 %1, %2\n\ts_cbranch_scc1 1b"
               : "=&v"(v), "=&s"(s), "=&s"(n2), "+s"(cnt) : "v"(addr), "s"(need) : "memory", "scc");
}
__device__ __forceinline__ void poll_ge_sleep(unsigned addr, unsigned need) {
  need = __builtin_amdgcn_readfirstlane(need);
  unsigned v, s, n2, cnt = SPIN_CAP;
  asm volatile("s_branch 2f\n1:\n\ts_sleep " STR(SL_S) "\n2:\n\tds_read_b32 %0, %4\n\ts_sub_u32 %3, %3, 1\n\ts_cselect_b32 %2, 0, %5\n\ts_waitcnt lgkmcnt(0)\n\t"
               "v_readfirstlane_b32 %1, %0\n\ts_cmp_lt_u32 %1, %2\n\ts_cbranch_scc1 1b"
               : "=&v"(v), "=&s"(s), "=&s"(n2), "+s"(cnt) : "v"(addr), "s"(need) : "memory", "scc");
}
__device__ __forceinline__ bf16x8 img_ld(lds_ptr L0, int off, int kk, int lane) { return *(LDS_AS(bf16x8)*)(L0 + off + kk * 1024 + lane * 16); }
__device__ __forceinline__ void img_st(lds_ptr L0, int off, int kk, int lane, bf16x8 v) { *(LDS_AS(bf16x8)*)(L0 + off + kk * 1024 + lane * 16) = v; }

template <int N> __device__ __forceinline__ void wait_vm() { asm volatile("s_waitcnt vmcnt(%0)" :: "i"(N) : "memory"); }
constexpr int A_IMG = ((TPW + 1 - NPK) % NSLOT) * SLOT, RED_OFF = ((TPW - NPK) % NSLOT) * SLOT;
__device__ __forceinline__ unsigned line_tag(unsigned epoch, unsigned i) {
  unsigned x = (epoch + 1u + i * 0x9E3779B9u) * 0x85EBCA6Bu;
  x ^= x >> 15; x *= 0xC2B2AE35u;
  return x ^ (x >> 13);
}
__device__ __forceinline__ unsigned epoch_load(const char* box) {
  unsigned e;
  asm volatile("global_load_dword %0, %1, %2 sc1\n\ts_waitcnt vmcnt(0)" : "=v"(e) : "v"(EPOCH_OFF), "s"(box) : "memory");
  return __builtin_amdgcn_readfirstlane(e);
}
__device__ __forceinline__ int tr_off(int row, int col4) {
  const int ch = col4 >> 1;
  return row * ROWB + (((ch & ~7) | ((ch & 7) ^ swz(row & 15))) << 4) + 8 * (col4 & 1);
}
#define PIN3(a, i) "+v"(a[i]), "+v"(a[i + 1]), "+v"(a[i + 2])

#define DMA1(d, b, off, pol) "global_load_lds_dwordx4 %[" #d "], %[" #b "] offset:" #off pol "\n\t"
__device__ __forceinline__ void issue_rows16(const char* a0, unsigned dst, const unsigned (&d)[3]) {
  unsigned keep;
  asm volatile("s_mov_b32 %[keep], m0\n\ts_mov_b32 m0, %[dst]\n\ts_nop 0\n\t"
               DMA1(d0, a, 0, "") DMA1(d1, a, 1024, "") DMA1(d2, a, 2048, "") DMA1(d0, a, 3072, "")
               "s_add_u32 m0, %[dst], 0x1000\n\ts_nop 0\n\t" DMA1(d1, b, 0, "") DMA1(d2, b, 1024, "") "s_mov_b32 m0, %[keep]"
               : [keep] "=&s"(keep) : [dst] "s"(dst), [a] "s"(a0), [b] "s"(a0 + 4096), [d0] "v"(d[0]), [d1] "v"(d[1]), [d2] "v"(d[2]) : "memory", "scc");
}
#define LD_VM "s_getreg_b32 s43, hwreg(HW_REG_IB_STS, 22, 2)\n\ts_getreg_b32 s42, hwreg(HW_REG_IB_STS, 0, 4)\n\ts_lshl4_add_u32 s42, s43, s42\n\t"
#define LD_GATE(n, X) \
  #n "0:\n\t" LD_VM "s_cmp_le_i32 s42, s46\n\ts_cbranch_scc1 " #n "2f\n" \
  #n "1:\n\t" X "s_cmp_gt_u32 s42, %[win]\n\ts_cbranch_scc1 " #n "5f\n\ts_cmp_ge_u32 s45, s44\n\ts_cbranch_scc1 " #n "7f\n" \
  #n "6:\n\ts_memrealtime s[40:41]\n\ts_sleep " STR(SL_L) "\n\ts_waitcnt lgkmcnt(0)\n\ts_sub_u32 s45, s40, %[t0]\n\ts_lshl_b32 s45, s45, 4\n\ts_cmp_lt_u32 s45, s44\n\ts_cbranch_scc1 " #n "6b\n" \
  #n "7:\n\t" LD_VM
#define LD_NEXT \
  "s_cmp_gt_u32 s42, %[ahi]\n\ts_cselect_b32 s42, %[amg], 0\n\ts_sub_u32 s43, s45, s44\n\ts_cmp_lt_u32 s43, s42\n\ts_cselect_b32 s42, %[p3] + %[adl], s39\n\t" \
  "s_add_u32 s44, s44, s42\n\ts_add_u32 s46, s46, 5\n\t"
#define LD_PUB0(n, a, b, c) \
  #n "2:\n\tv_add_u32 %[pub], 2, %[pub]\n\ts_cmp_ge_u32 s46, 30\n\ts_cbranch_scc1 " #n "4f\n\ts_cmp_ge_u32 s46, 15\n\ts_cbranch_scc1 " #n "3f\n\t" \
  "s_waitcnt vmcnt(" #a ")\n" #n "3:\n\ts_waitcnt vmcnt(" #b ")\n" #n "4:\n\ts_waitcnt vmcnt(" #c ")\n\tds_write_b32 %[af], %[pub] ; NOTRACK\n\t" \
  "s_sub_u32 s46, s46, 15\n\ts_branch " #n "1b\n"
#define LD_PUB(n, a, b, c) LD_PUB0(n, a, b, c) \
  #n "5:\n\ts_sleep 1\n\ts_sub_i32 s47, s47, 1\n\ts_cmp_gt_i32 s47, 0\n\ts_cbranch_scc1 " #n "0b\n\ts_waitcnt vmcnt(%[win])\n\ts_branch " #n "6b\n"
#define LD1(d, b, off) "global_load_lds_dwordx4 %[" #d "], s[" #b "] offset:" #off " nt\n\t"

#define PV_REQ(P, U, M, C, CA) \
  "v_lshl_add_u32 v150, s44, 3, %[apk]\n\tv_lshl_add_u32 v151, s44, 1, %[apu]\n\tv_add_u32 v152, s44, %[apm]\n\t" \
  "ds_read_b32 v" #C ", v" #CA "\n\tds_read_b128 v[" P "], v150\n\tds_read_b64 v[" M "], v152\n\tds_read_b128 v[" U "], v151\n\t"
#define PV_TR(d0, d1, a, o) "ds_read_b64_tr_b16 v[" #d0 ":" #d1 "], v" #a " offset:" #o "\n\t"
#define PV_FETCH(y0, y0b, a0, a0b, b0, b0b, y1, y1b, a1, a1b, b1, b1b, y2, y2b, a2, a2b, b2, b2b) \
  "v_add_u32 v153, s45, %[ox]\n\tv_add_u32 v154, s45, %[oy]\n\t" \
  PV_TR(a0, a0b, 153, 0) PV_TR(b0, b0b, 153, 6144) PV_TR(a1, a1b, 153, 128) PV_TR(b1, b1b, 153, 6272) PV_TR(a2, a2b, 153, 256) PV_TR(b2, b2b, 153, 6400) \
  PV_TR(y0, y0b, 154, 0) PV_TR(y1, y1b, 154, 128) PV_TR(y2, y2b, 154, 256) \
  "s_add_u32 s45, s45, %[slot]\n\ts_cmp_eq_u32 s45, %[ring]\n\ts_cselect_b32 s45, 0, s45\n\t"
#define PV_FETCH_A PV_FETCH(90, 91, 92, 93, 94, 95, 96, 97, 98, 99, 100, 101, 102, 103, 104, 105, 106, 107)
#define PV_FETCH_B PV_FETCH(108, 109, 110, 111, 112, 113, 114, 115, 116, 117, 118, 119, 120, 121, 122, 123, 124, 125)
#define PV_MF(d, a, p) "v_mfma_f32_16x16x32_bf16 v[" d "], v[" a "], v[" p "], v[" d "]\n\t"
#define PV_MOV(d, s) "v_mov_b32 v" #d ", v" #s "\n\t"
#define PV_WAITF(n, CA)   \
  #n ":\n\tds_read_b32 v155, v" #CA "\n\ts_sub_i32 s47, s47, 1\n\ts_cmp_lt_i32 s47, 0\n\ts_cbranch_scc1 " #n "9f\n\ts_waitcnt lgkmcnt(0)\n\t" \
  "v_cmp_lt_u32 vcc, v155, %[need]\n\ts_and_b64 vcc, vcc, 2\n\ts_cbranch_scc0 " #n "9f\n\ts_sleep " STR(SL_P) "\n\ts_branch " #n "b\n" #n "9:\n\ts_waitcnt lgkmcnt(0)\n\t"
#define PV_STEP(n, P, U, M, m0, m1, C, CA, P2, U2, M2, C2, CA2, X0, X1, X2, Y0, Y1, Y2, MV0, MV1, MV2, HOOK) \
  "s_waitcnt lgkmcnt(10)\n\t" \
  "v_cmp_lt_u32 vcc, v" #C ", %[need]\n\tv_cmp_neq_f32 s[48:49], v" #m0 ", v88\n\tv_cmp_neq_f32 s[50:51], v" #m1 ", v88\n\t" \
  "v_add_u32 v" #CA2 ", 4, v" #CA "\n\ts_or_b64 s[48:49], s[48:49], vcc\n\tv_cmp_eq_u32 s[52:53], v" #CA2 ", %[lim]\n\t" \
  "s_or_b64 s[48:49], s[48:49], s[50:51]\n\ts_cbranch_scc1 " #n "1f\n" \
  #n "0:\n\ts_add_u32 s44, s44, 128\n\ts_cmp_eq_u32 s44, %[pring]\n\ts_cselect_b32 s44, 0, s44\n\t" \
  "v_cndmask_b32 v" #CA2 ", v" #CA2 ", %[base], s[52:53]\n\t" \
  PV_MF("84:87", U, P) PV_MF("72:75", X0, P) PV_MF("76:79", X1, P) MV0 PV_MF("80:83", X2, P) MV1 \
  PV_MF("72:75", Y0, P) MV2 HOOK PV_REQ(P2, U2, M2, C2, CA2) PV_MF("76:79", Y1, P) PV_MF("80:83", Y2, P)
#define PV_REL(CA) \
  "s_mov_b64 exec, 3\n\tds_add_u32 v" #CA ", %[one] offset:64\n\ts_mov_b64 exec, -1\n\tv_add_u32 %[need], 2, %[need]\n\ts_add_u32 s46, s46, 1\n\t"
#define PV_SC1(p) "v_lshlrev_b32 v151, 16, v" #p "\n\tv_and_b32 v152, 0xffff0000, v" #p "\n\tv_mul_f32 v151, v151, v150\n\tv_mul_f32 v152, v152, v150\n\t" \
                  "v_cvt_pk_bf16_f32 v" #p ", v151, v152\n\t"
#define PV_MUL4(a, b, c, d) "v_mul_f32 v" #a ", v" #a ", v150\n\tv_mul_f32 v" #b ", v" #b ", v150\n\tv_mul_f32 v" #c ", v" #c ", v150\n\tv_mul_f32 v" #d ", v" #d ", v150\n\t"
#define PV_SLOW(n, P, U, M, m0, m1, C, CA, p0, p1, p2, p3) \
  #n "1:\n\ts_bitcmp0_b32 vcc_lo, 0\n\ts_cbranch_scc1 " #n "4f\n\ts_cmp_gt_u32 s46, %[tight]\n\ts_cbranch_scc1 " #n "3f\n" \
  #n "2:\n\ts_sleep " STR(SL_P) "\n\tds_read_b32 v" #C ", v" #CA "\n\ts_sub_i32 s47, s47, 1\n\ts_cmp_lt_i32 s47, 0\n\ts_cbranch_scc1 " #n "3f\n\ts_waitcnt lgkmcnt(0)\n\t" \
  "v_cmp_lt_u32 vcc, v" #C ", %[need]\n\ts_and_b64 vcc, vcc, 1\n\ts_cbranch_scc1 " #n "2b\n" \
  #n "3:\n\t" PV_REQ(P, U, M, C, CA) "s_sub_i32 s47, s47, 1\n\ts_cmp_lt_i32 s47, 0\n\ts_cselect_b32 s54, 0, 1\n\ts_waitcnt lgkmcnt(0)\n\t" \
  "v_cmp_lt_u32 vcc, v" #C ", %[need]\n\tv_cmp_neq_f32 s[48:49], v" #m0 ", v88\n\tv_cmp_neq_f32 s[50:51], v" #m1 ", v88\n\t" \
  "s_and_b32 s54, s54, vcc_lo\n\ts_cbranch_scc1 " #n "3b\n\ts_or_b64 s[48:49], s[48:49], s[50:51]\n\ts_cbranch_scc0 " #n "0b\n\ts_branch " #n "5f\n" \
  #n "4:\n\tv_cmp_neq_f32 s[48:49], v" #m0 ", v88\n\ts_or_b64 s[48:49], s[48:49], s[50:51]\n\ts_cbranch_scc0 " #n "0b\n" \
  #n "5:\n\tv_mov_b32 v150, v88\n\tv_max3_f32 v88, v88, v" #m0 ", v" #m1 "\n\tv_sub_f32 v150, v150, v88\n\tv_exp_f32 v150, v150\n\ts_nop 1\n\t" \
  PV_MUL4(72, 73, 74, 75) PV_MUL4(76, 77, 78, 79) PV_MUL4(80, 81, 82, 83) PV_MUL4(84, 85, 86, 87) \
  "v_sub_f32 v150, v" #m0 ", v88\n\tv_exp_f32 v150, v150\n\ts_nop 1\n\t" PV_SC1(p0) PV_SC1(p1) \
  "v_sub_f32 v150, v" #m1 ", v88\n\tv_exp_f32 v150, v150\n\ts_nop 1\n\t" PV_SC1(p2) PV_SC1(p3) \
  "s_branch " #n "0b\n"
#define PV_W2(a0, a1, b0, b1, c0, c1) \
  "global_load_dwordx4 v[" #a0 ":" #a1 "], v150, %[w2] sc1\n\tglobal_load_dwordx4 v[" #b0 ":" #b1 "], v150, %[w2] offset:1024 sc1\n\t" \
  "global_load_dwordx4 v[" #c0 ":" #c1 "], v150, %[w2] offset:2048 sc1\n\tv_add_u32 v150, 0xc00, v150\n\ts_sub_u32 s54, s54, 1\n\ts_cbranch_scc1 30b\n\t"

#define PV_NRM(a, b, c, d, o)   \
  "v_pk_fma_f32 v[" #a ":" #b "], v[" #a ":" #b "], v[150:151], v[150:151] op_sel:[0,0,1] op_sel_hi:[1,0,1]\n\t" \
  "v_pk_fma_f32 v[" #c ":" #d "], v[" #c ":" #d "], v[150:151], v[150:151] op_sel:[0,0,1] op_sel_hi:[1,0,1]\n\t" \
  "v_cvt_pk_bf16_f32 v" #a ", v" #a ", v" #b "\n\tv_cvt_pk_bf16_f32 v" #b ", v" #c ", v" #d "\n\tds_write_b64 v152, v[" #a ":" #b "] offset:" #o "\n\t"
#define PV_G(n, a, w0, w1, w2, c0, c1, c2) "s_waitcnt lgkmcnt(" #n ")\n\tv_mfma_f32_16x16x32_bf16 v[72:75], v[" a "], v[" w0 "], " c0 "\n\t" \
  "v_mfma_f32_16x16x32_bf16 v[76:79], v[" a "], v[" w1 "], " c1 "\n\tv_mfma_f32_16x16x32_bf16 v[80:83], v[" a "], v[" w2 "], " c2 "\n\t"
#define PV_Z4(a, b, c, d) "v_mov_b32 v" #a ", 0\n\tv_mov_b32 v" #b ", 0\n\tv_mov_b32 v" #c ", 0\n\tv_mov_b32 v" #d ", 0\n\t"
#define PV_T(n)

using u32x8 = __attribute__((ext_vector_type(8))) unsigned;
struct SCtx {
  lds_ptr L0;
  unsigned ct, apk, apm, api, amr;
  int o_rb, o_yb, ev;
  u32x8 q[3];
  unsigned tq;
};
__device__ __forceinline__ bf16x8 s_qf(const SCtx& c, int j) {
  const u32x8& v = c.q[j >> 1];
  const int o = 4 * (j & 1);
  return __builtin_bit_cast(bf16x8, u32x4{v[o], v[o + 1], v[o + 2], v[o + 3]});
}
#define S_M(d, a, b) "v_mfma_f32_16x16x32_bf16 v[" d "], v[" a "], v[" b "], v[" d "]\n\t"
#define S_M0(d, a, b) "v_mfma_f32_16x16x32_bf16 v[" d "], v[" a "], v[" b "], 0\n\t"
#define S_R(d, a, o) "ds_read_b128 v[" d "], v" #a " offset:" #o "\n\t"
#define S_W(n) "s_waitcnt lgkmcnt(" #n ")\n\t"
#define S_D " row_mask:0xf bank_mask:0xf\n\t"
#define S_3(x, y) S_M("100:103", x, y) S_M("104:107", x, x) S_M("96:99", y, y)
#define S_SQ(g, h) "v_fmac_f32 v" #g ", 2.0, v" #h "\n\t"
#define S_SQD(g, h) "v_fmac_f32_dpp v" #g ", v" #h ", %[two] quad_perm:[0,0,2,2]" S_D
#define S_U(u, h, y) "v_add_f32_dpp v" #u ", v" #h ", v" #y " quad_perm:[1,1,3,3]" S_D
#define S_C(d, a, b, m) "v_cndmask_b32 v" #d ", v" #a ", v" #b ", %[" #m "]\n\t"
#define S_B(d, n) "v_mov_b32_dpp v" #d ", v157 row_newbcast:" #n S_D
#define S_BASE(d, u, t) "v_pk_fma_f32 v[" d "], v[" u "], v[116:117], v[" t "] op_sel:[0,1,0] op_sel_hi:[1,1,0]\n\t"
#define S_LG(d, r) "v_pk_fma_f32 v[" d "], v[" d "], v[" r "], %[toff] op_sel_hi:[1,1,0]\n\t"
#define S_SUB(p, d) "v_pk_add_f32 v[" p "], v[" d "], v[150:151] op_sel_hi:[1,0] neg_lo:[0,1] neg_hi:[0,1]\n\t"
#define S_E(p) "v_exp_f32 v" #p ", v" #p "\n\t"
#define S_MUL(p, r) "v_pk_mul_f32 v[" p "], v[" p "], v[" r "]\n\t"
#define S_POS   \
  "s_mul_i32 s50, s42, 26\n\ts_lshr_b32 s50, s50, 8\n\ts_mul_i32 s50, s50, -10\n\ts_add_u32 s50, s50, s42\n\ts_mul_i32 s45, s50, %[slot]\n\ts_mov_b32 s43, s45\n\t"
#define S_POLL(n, a, o, need, back, far) \
  #n ":\n\tds_read_b32 v152, v" #a " offset:" #o "\n\ts_sub_i32 s47, s47, 1\n\ts_cmp_lt_i32 s47, 0\n\ts_waitcnt lgkmcnt(0)\n\ts_cbranch_scc1 " back "\n\t" \
  "v_readfirstlane_b32 s40, v152\n\ts_cmp_ge_u32 s40, " need "\n\ts_cbranch_scc1 " back "\n\ts_sleep " STR(SL_S) "\n\t" \
  "s_add_u32 s40, s40, " far "\n\ts_cmp_ge_u32 s40, " need "\n\ts_cbranch_scc1 " #n "b\n\ts_sleep " STR(SL_FAR) "\n\ts_branch " #n "b\n"
__device__ __forceinline__ void s_items(SCtx& c, int k0, int late) {
  unsigned long long tq;
  asm volatile("s_mov_b32 s47, %[cap]\n\ts_mov_b32 s49, 0\n\ts_mov_b32 s42, %[k0]\n\ts_cmp_lg_u32 %[late], 0\n\ts_cbranch_scc1 7f\n"
               "1:\n\ts_cmp_ge_u32 s42, %[end]\n\ts_cbranch_scc1 9f\n\t"
               S_POS
               "s_mul_i32 s48, s42, 43\n\ts_lshr_b32 s48, s48, 8\n\ts_mul_i32 s46, s48, -6\n\ts_add_u32 s46, s46, s42\n\ts_lshl2_add_u32 s48, s48, %[ns]\n\t"
               "v_add_u32 v144, s45, %[orb]\n\tv_mad_u32_u24 v145, s43, %[ev], %[oyb]\n\ts_lshl2_add_u32 s40, s46, %[pfl]\n\ts_lshl2_add_u32 s41, s50, %[full]\n\t"
               "v_xor_b32 v146, 64, v144\n\tv_xor_b32 v147, 64, v145\n\tv_mov_b32 v148, s40\n\tv_mov_b32 v149, s41\n\t"
               "s_lshl1_add_u32 s44, s42, 2\n\ts_lshl_b32 s46, s46, 7\n\ts_mov_b64 vcc, %[dg]\n\ts_cmp_lt_u32 s49, s44\n\ts_cbranch_scc1 5f\n"
               "2:\n\tds_read_b32 v150, %[amr]\n\tds_read_b32 v151, v148 offset:64\n\t"
               S_R("24:27", 145, 0) S_R("48:51", 144, 0) S_R("28:31", 147, 0) S_R("52:55", 146, 0) S_R("32:35", 145, 128) S_R("56:59", 144, 128)
               S_R("36:39", 147, 128) S_R("60:63", 146, 128) S_R("40:43", 145, 256) S_R("64:67", 144, 256) S_R("44:47", 147, 256) S_R("68:71", 146, 256)
               S_W(10) S_M0("100:103", "48:51", "24:27") S_M0("104:107", "48:51", "48:51") S_R("72:75", 144, 6144) S_R("76:79", 146, 6144) S_M0("96:99", "24:27", "24:27")
               S_W(10) S_M("100:103", "52:55", "28:31") S_M("104:107", "52:55", "52:55") S_R("80:83", 144, 6272) S_R("84:87", 146, 6272) S_M("96:99", "28:31", "28:31")
               S_W(10) S_M("100:103", "56:59", "32:35") S_M("104:107", "56:59", "56:59") S_R("88:91", 144, 6400) S_R("92:95", 146, 6400) S_M("96:99", "32:35", "32:35")
               "s_mov_b64 exec, 1\n\tds_add_u32 v149, %[four] offset:64\n\ts_mov_b64 exec, -1\n\tv_readfirstlane_b32 s51, v151\n\ts_setprio 1\n\ts_nop 0\n\t"
               S_W(11) S_3("60:63", "36:39") S_W(9) S_3("64:67", "40:43") S_W(7) S_3("68:71", "44:47")
               S_W(6) S_M0("108:111", "72:75", "24:27") S_M0("112:115", "72:75", "72:75")
               S_W(5) S_M("108:111", "76:79", "28:31") S_M("112:115", "76:79", "76:79")
               S_W(4) S_M("108:111", "80:83", "32:35")
               "v_mov_b32_dpp v152, v96 quad_perm:[1,1,3,3]" S_D "v_mov_b32_dpp v153, v98 quad_perm:[1,1,3,3]" S_D
               S_M("112:115", "80:83", "80:83") S_C(154, 96, 98, r2) S_SQ(104, 100)
               S_W(3) S_M("108:111", "84:87", "36:39") S_SQD(105, 101) S_SQ(106, 102)
               S_M("112:115", "84:87", "84:87") S_SQD(107, 103) "v_add_f32 v154, %[eps], v154\n\t"
               S_W(2) S_M("108:111", "88:91", "40:43") S_U(128, 100, 152) S_U(129, 101, 152)
               S_M("112:115", "88:91", "88:91") S_U(130, 102, 153) S_U(131, 103, 153)
               S_W(1) S_M("108:111", "92:95", "44:47") S_C(104, 104, 105, r1) S_C(106, 106, 107, r1)
               S_M("112:115", "92:95", "92:95") S_C(155, 128, 129, r1) S_C(156, 130, 131, r1)
               "s_cmp_lt_u32 s42, %[early]\n\ts_cbranch_scc1 7f\n"
               "3:\n\t" S_M0("116:119", "24:27", "0:3") S_C(104, 104, 106, r2) S_C(155, 155, 156, r2)
               S_M("116:119", "28:31", "4:7") "v_add_f32_dpp v104, v154, v104 quad_perm:[0,0,2,2]" S_D S_U(132, 108, 152)
               S_M("116:119", "32:35", "8:11") S_U(133, 109, 152) S_U(134, 110, 153)
               S_M("116:119", "36:39", "12:15") S_U(135, 111, 153) S_SQ(112, 108)
               S_M("116:119", "40:43", "16:19") S_SQD(113, 109) S_SQ(114, 110)
               S_M("116:119", "44:47", "20:23") S_SQD(115, 111) S_C(156, 132, 133, r1) S_C(157, 134, 135, r1)
               S_C(112, 112, 113, r1) S_C(114, 114, 115, r1) S_C(156, 156, 157, r2) S_C(112, 112, 114, r2)
               "v_add_f32_dpp v112, v154, v112 quad_perm:[0,0,2,2]" S_D
               "v_mul_f32 v117, %[nic], v117\n\tv_cndmask_b32_dpp v155, v156, v155, vcc row_ror:4" S_D
               S_BASE("120:121", "128:129", "116:117") S_BASE("122:123", "130:131", "118:119")
               "v_cndmask_b32_dpp v104, v112, v104, vcc row_ror:4" S_D "v_mul_f32 v156, %[nic], v155\n\t"
               S_M("120:123", "48:51", "0:3") S_BASE("124:125", "132:133", "116:117") S_BASE("126:127", "134:135", "118:119")
               S_M("120:123", "52:55", "4:7") "v_fmac_f32 v104, v156, v155\n\tv_max_f32 v104, %[eps], v104\n\t"
               S_M("120:123", "56:59", "8:11") "v_rsq_f32 v157, v104\n\t"
               S_M("120:123", "60:63", "12:15") "v_mul_f32 v104, v104, v157\n\tv_mov_b32_dpp v157, v157 row_ror:12 row_mask:0x2 bank_mask:0xf\n\t"
               S_M("120:123", "64:67", "16:19") "v_cvt_pk_bf16_f32 v105, v155, v104\n\tv_mov_b32_dpp v157, v157 row_ror:8 row_mask:0x4 bank_mask:0xf\n\t"
               S_M("120:123", "68:71", "20:23") "v_lshlrev_b32 v106, 16, v105\n\tv_mov_b32_dpp v157, v157 row_ror:4 row_mask:0x8 bank_mask:0xf\n\t"
               S_M("124:127", "72:75", "0:3") "v_and_b32 v107, 0xffff0000, v105\n\t" S_B(136, 0)
               S_M("124:127", "76:79", "4:7") S_B(137, 1) S_B(138, 2)
               S_M("124:127", "80:83", "8:11") S_B(139, 3) S_LG("120:121", "136:137")
               S_M("124:127", "84:87", "12:15") S_LG("122:123", "138:139") S_SUB("128:129", "120:121")
               S_M("124:127", "88:91", "16:19") S_SUB("130:131", "122:123") S_E(128)
               S_M("124:127", "92:95", "20:23") S_E(129) S_E(130) S_E(131)
               S_B(140, 4) S_B(141, 5) S_B(142, 6) S_B(143, 7) "v_sub_f32 v106, v155, v106\n\t"
               S_LG("124:125", "140:141") S_LG("126:127", "142:143") "v_sub_f32 v107, v104, v107\n\tv_max3_f32 v152, v120, v121, v122\n\t"
               S_SUB("132:133", "124:125") S_SUB("134:135", "126:127") "v_max3_f32 v153, v123, v124, v125\n\t"
               S_E(132) S_E(133) S_E(134) S_E(135)
               "v_max3_f32 v152, v152, v126, v127\n\tv_add_f32 v154, %[thr], v150\n\tv_max_f32 v152, v152, v153\n\tv_cvt_pk_bf16_f32 v106, v106, v107\n\t"
               "v_lshl_add_u32 v144, s46, 3, %[apk]\n\tv_cmp_gt_f32 vcc, v152, v154\n\tv_add_u32 v145, s46, %[apm]\n\tv_lshl_add_u32 v146, s46, 1, %[api]\n\t"
               "s_cbranch_vccnz 8f\n"
               "4:\n\t" S_MUL("128:129", "136:137") S_MUL("130:131", "138:139") S_MUL("132:133", "140:141") S_MUL("134:135", "142:143")
               "v_cvt_pk_bf16_f32 v128, v128, v129\n\tv_cvt_pk_bf16_f32 v129, v130, v131\n\tv_cvt_pk_bf16_f32 v130, v132, v133\n\tv_cvt_pk_bf16_f32 v131, v134, v135\n\t"
               "s_cmp_lt_u32 s51, s48\n\ts_cbranch_scc1 6f\n"
               "10:\n\tds_read_b32 v151, v149\n\tds_write_b128 v144, v[128:131]\n\tds_write2_b32 v145, v150, v150 offset0:0 offset1:1\n\ts_mov_b64 exec, %[dg2]\n\t"
               "ds_write_b16 v146, v105\n\tds_write_b16 v146, v106 offset:16\n\tds_write_b16_d16_hi v146, v105 offset:32\n\tds_write_b16_d16_hi v146, v106 offset:48\n\t"
               "s_mov_b64 exec, 1\n\tds_add_u32 v148, %[inc]\n\ts_mov_b64 exec, -1\n\ts_setprio 0\n\ts_add_u32 s42, s42, %[ns]\n\t"
               S_W(7) "v_readfirstlane_b32 s49, v151\n\ts_branch 1b\n"
               S_POLL(5, 149, 0, "s44", "2b", "2")
               S_POLL(6, 148, 64, "s48", "10b", "64")
               "7:\n\tv_mov_b32 v144, %[qi]\n\tv_mov_b32 v145, v152\n\tv_mov_b32 v146, 1\n"
               S_POLL(70, 144, 0, "%[nq]", "71f", "64")
               "71:\n\tv_mov_b32 v152, v145\n\tds_read_b128 v[0:3], %[apk]\n\tds_read_b128 v[4:7], %[apk] offset:1024\n\tds_read_b128 v[8:11], %[apk] offset:2048\n\t"
               "ds_read_b128 v[12:15], %[apk] offset:3072\n\tds_read_b128 v[16:19], %[apk] offset:4096\n\tds_read_b128 v[20:23], %[apk] offset:5120\n\t"
               "v_subrev_u32 v144, %[mc], %[amr]\n\tds_read_b32 v150, %[amr]\n\ts_mov_b64 exec, 63\n\tds_add_u32 v144, v146\n\ts_mov_b64 exec, -1\n\t"
               "s_cmp_lg_u32 %[late], 0\n\ts_waitcnt lgkmcnt(0)\n\ts_nop 1\n\ts_cbranch_scc1 1b\n\ts_branch 3b\n"
               "8:\n\tv_mov_b32 v153, v152\n\ts_nop 1\n\tv_permlane16_swap_b32 v152, v153\n\tv_max_f32 v152, v152, v153\n\tv_mov_b32 v153, v152\n\ts_nop 1\n\t"
               "v_permlane32_swap_b32 v152, v153\n\tv_max_f32 v152, v152, v153\n\tv_mul_f32 v152, 0x3d000000, v152\n\tv_rndne_f32 v152, v152\n\t"
               "v_mul_f32 v152, 0x42000000, v152\n\tv_max_f32 v150, v150, v152\n\tds_max_f32 %[amr], v150\n\t"
               S_SUB("128:129", "120:121") S_SUB("130:131", "122:123") S_SUB("132:133", "124:125") S_SUB("134:135", "126:127")
               S_E(128) S_E(129) S_E(130) S_E(131) S_E(132) S_E(133) S_E(134) S_E(135) "s_branch 4b\n"
               "9:"
               : "=&{v[0:7]}"(c.q[0]), "=&{v[8:15]}"(c.q[1]), "=&{v[16:23]}"(c.q[2]), "=&{s[52:53]}"(tq)
               : [orb] "v"(c.o_rb), [oyb] "v"(c.o_yb), [ev] "v"(c.ev), [apk] "v"(c.apk), [apm] "v"(c.apm), [api] "v"(c.api), [amr] "v"(c.amr),
                 [two] "v"(2.f), [four] "v"(4u), [inc] "v"(2u * NPK), [k0] "s"(k0), [late] "s"(__builtin_amdgcn_readfirstlane(late)), [toff] "s"(f32x2{TOFF, TOFF}),
                 [r1] "s"(0xAAAAAAAAAAAAAAAAull), [r2] "s"(0xCCCCCCCCCCCCCCCCull), [dg] "s"(0xF0000F0000F0000Full), [dg2] "s"(0xF00FFF000FF000FFull),
                 [cap] "i"(SPIN_CAP), [end] "i"(TPW - NH1), [ns] "i"(NS), [pfl] "i"(CT_OFF + C_PFL), [full] "i"(CT_OFF + C_FULL), [early] "i"(NEARLY),
                 [qi] "i"(CT_OFF + C_QI), [nq] "i"(NS - NEARLY), [mc] "i"(C_MREF - C_CONS), [slot] "i"(SLOT),
                 [thr] "i"(__builtin_bit_cast(unsigned, THR)), [eps] "i"(__builtin_bit_cast(unsigned, EPS * CC)), [nic] "i"(__builtin_bit_cast(unsigned, -1.f / CC))
               : "memory", "scc", "vcc", "s40", "s41", "s42", "s43", "s44", "s45", "s46", "s47", "s48", "s49", "s50", "s51",
                 "v24", "v25", "v26", "v27", "v28", "v29", "v30", "v31", "v32", "v33", "v34", "v35", "v36", "v37", "v38", "v39", "v40", "v41", "v42", "v43", "v44", "v45", "v46", "v47", "v48", "v49", "v50", "v51", "v52", "v53", "v54", "v55", "v56", "v57", "v58", "v59", "v60", "v61", "v62", "v63", "v64", "v65", "v66", "v67", "v68", "v69", "v70", "v71", "v72", "v73", "v74", "v75", "v76", "v77", "v78", "v79", "v80", "v81", "v82", "v83", "v84", "v85", "v86", "v87", "v88", "v89", "v90", "v91", "v92", "v93", "v94", "v95", "v96", "v97", "v98", "v99", "v100", "v101", "v102", "v103", "v104", "v105", "v106", "v107", "v108", "v109", "v110", "v111", "v112", "v113", "v114", "v115", "v116", "v117", "v118", "v119", "v120", "v121", "v122", "v123", "v124", "v125", "v126", "v127", "v128", "v129", "v130", "v131", "v132", "v133", "v134", "v135", "v136", "v137", "v138", "v139", "v140", "v141", "v142", "v143", "v144", "v145", "v146", "v147", "v148", "v149", "v150", "v151", "v152", "v153", "v154", "v155", "v156", "v157");
  c.tq = (unsigned)tq;
}

__device__ __forceinline__ void s_half(SCtx& c, int k, int h) {
  const int s = k % NSLOT, e = k % NPK;
  const int sb_ = s * SLOT;
  int yba = c.ev * sb_ + c.o_yb;
  float hc = 1.f - 0.5f * c.ev;
  unsigned xa = (unsigned)(size_t)c.L0 + sb_ + h * 6144 + c.o_rb, xb = xa ^ 64, ai = c.api + e * 256 + h * 8;
  unsigned ap = c.apk + e * 1024 + h * 8, am = c.apm + e * 128 + h * 4, af = c.ct + C_PFL + e * 4, inc = NPK;
  float two = 2.f;
  f32x4 G = {EPS * CC, EPS * CC, EPS * CC, EPS * CC};
  poll_ge_sleep(c.ct + C_CONS + e * 4, (unsigned)(NE * (k / NPK) + NS));
  asm volatile("" : "+v"(yba), "+v"(hc), "+v"(xa), "+v"(xb), "+v"(ai), "+v"(ap), "+v"(am), "+v"(af), "+v"(inc), "+v"(two), "+{v[104:107]}"(G));
  poll_ge_sleep(c.ct + C_FULL, (unsigned)(2 * k + 1));
  __builtin_amdgcn_s_setprio(1);
  bf16x8 yb[6];
#pragma unroll
  for (int j = 0; j < 6; ++j) yb[j] = *(LDS_AS(bf16x8)*)(c.L0 + ((j & 1) ? (yba ^ 64) : yba) + (j >> 1) * 128);
  f32x4 Hx = {0.f, 0.f, 0.f, 0.f}, S = Hx;
#pragma unroll
  for (int j = 0; j < 6; ++j) { Hx = mfma(yb[j], yb[j], Hx); S = mfma(yb[j], s_qf(c, j), S); }
  const float nsq = S[1] * (-1.f / CC);
  Hx = f32x4{Hx[0] * hc, Hx[0] * hc, Hx[2] * hc, Hx[2] * hc};
  S = f32x4{S[0], S[0], S[2], S[2]};
  float mref, mt;
  f32x4 rs;
  unsigned long long raise;
#define SH_M(d, a, b) "v_mfma_f32_16x16x32_bf16 v[" d "], v[" a "], " b ", v[" d "]\n\t"
#define SH_X(n, a, y) "s_waitcnt lgkmcnt(" #n ")\n\t" SH_M("100:103", a, "%[" #y "]") SH_M("104:107", a, "v[" a "]")
#define SH_D " row_mask:0xf bank_mask:0xf\n\t"
  __builtin_amdgcn_s_setprio(0);
  if (HB == 0 || (HB == 1 && k < TPW - 1)) poll_ge_sleep(c.ct + C_FULL, (unsigned)(2 * k + 2));
  if (HB == 2) { if (k < TPW - 1) poll_ge_sleep(c.ct + C_FULL, (unsigned)(2 * k + 2)); else poll_ge(c.ct + C_FULL, (unsigned)(2 * TPW)); }
  asm volatile("s_mov_b32 s42, %[cap]\n"
               "1:\n\tds_read_b32 v136, %[afl]\n\tds_read_b128 v[112:115], %[xa]\n\tds_read_b128 v[116:119], %[xb]\n\tds_read_b128 v[120:123], %[xa] offset:128\n\t"
               "ds_read_b128 v[124:127], %[xb] offset:128\n\tds_read_b128 v[128:131], %[xa] offset:256\n\tds_read_b128 v[132:135], %[xb] offset:256\n\t"
               "ds_read_b32 %[mr], %[amr]\n\ts_sub_u32 s42, s42, 1\n\ts_cselect_b32 s41, 0, %[need]\n\ts_waitcnt lgkmcnt(7)\n\t"
               "v_readfirstlane_b32 s40, v136\n\ts_cmp_lt_u32 s40, s41\n\ts_cbranch_scc1 1b\n\ts_setprio 3\n\t"
               SH_X(6, "112:115", y0) SH_X(5, "116:119", y1) SH_X(4, "120:123", y2) SH_X(3, "124:127", y3) SH_X(2, "128:131", y4) SH_X(1, "132:135", y5)
               SH_M("108:111", "112:115", "%[q0]") SH_M("108:111", "116:119", "%[q1]")
               "s_waitcnt lgkmcnt(0)\n\tv_add_f32 v137, %[thr], %[mr]\n\tds_write_b32 %[am], %[mr]\n\ts_nop 1\n\t"
               "v_mov_b32_dpp v140, v100 quad_perm:[1,1,3,3]" SH_D "v_mov_b32_dpp v141, v101 quad_perm:[1,1,3,3]" SH_D
               "v_mov_b32_dpp v142, v102 quad_perm:[1,1,3,3]" SH_D "v_mov_b32_dpp v143, v103 quad_perm:[1,1,3,3]" SH_D
               SH_M("108:111", "120:123", "%[q2]")
               "v_fmac_f32 v104, 2.0, v100\n\tv_fmac_f32_dpp v105, v101, %[two] quad_perm:[0,0,2,2]" SH_D
               "v_fmac_f32 v106, 2.0, v102\n\tv_fmac_f32_dpp v107, v103, %[two] quad_perm:[0,0,2,2]" SH_D
               "v_cndmask_b32 v138, v140, v141, %[r1]\n\tv_cndmask_b32 v139, v142, v143, %[r1]\n\t"
               SH_M("108:111", "124:127", "%[q3]")
               "v_cndmask_b32 v104, v104, v105, %[r1]\n\tv_cndmask_b32 v106, v106, v107, %[r1]\n\tv_cndmask_b32 v138, v138, v139, %[r2]\n\t"
               "v_cndmask_b32 v104, v104, v106, %[r2]\n\tv_mul_f32 v139, %[nic], v138\n\t"
               SH_M("108:111", "128:131", "%[q4]")
               "v_fmac_f32 v104, v139, v138\n\tv_max_f32 v104, %[eps], v104\n\tv_rsq_f32 v105, v104\n\t"
               SH_M("108:111", "132:135", "%[q5]")
               "v_mul_f32 v104, v104, v105\n\t"
               "v_mov_b32_dpp v105, v105 row_ror:12 row_mask:0x2 bank_mask:0xf\n\tv_cvt_pk_bf16_f32 v106, v138, v104\n\ts_nop 0\n\t"
               "v_mov_b32_dpp v105, v105 row_ror:8 row_mask:0x4 bank_mask:0xf\n\tv_lshlrev_b32 v107, 16, v106\n\tv_and_b32 v139, 0xffff0000, v106\n\t"
               "v_mov_b32_dpp v105, v105 row_ror:4 row_mask:0x8 bank_mask:0xf\n\t"
               "v_fmac_f32 v108, v140, %[nsq]\n\tv_fmac_f32 v109, v141, %[nsq]\n\tv_fmac_f32 v110, v142, %[nsq]\n\tv_fmac_f32 v111, v143, %[nsq]\n\t"
               "v_mov_b32_dpp v100, v105 row_newbcast:0" SH_D "v_mov_b32_dpp v101, v105 row_newbcast:1" SH_D
               "v_mov_b32_dpp v102, v105 row_newbcast:2" SH_D "v_mov_b32_dpp v103, v105 row_newbcast:3" SH_D
               "v_fmaak_f32 v108, v108, v100, %[toff]\n\tv_fmaak_f32 v109, v109, v101, %[toff]\n\tv_fmaak_f32 v110, v110, v102, %[toff]\n\tv_fmaak_f32 v111, v111, v103, %[toff]\n\t"
               "v_sub_f32 v140, v108, %[mr]\n\tv_sub_f32 v141, v109, %[mr]\n\tv_sub_f32 v142, v110, %[mr]\n\tv_sub_f32 v143, v111, %[mr]\n\t"
               "v_exp_f32 v140, v140\n\tv_exp_f32 v141, v141\n\tv_exp_f32 v142, v142\n\tv_exp_f32 v143, v143\n\t"
               "v_max3_f32 %[mt], v108, v109, v110\n\tv_sub_f32 v138, v138, v107\n\tv_sub_f32 v104, v104, v139\n\tv_max_f32 %[mt], %[mt], v111\n\t"
               "v_pk_mul_f32 v[140:141], v[140:141], v[100:101]\n\tv_pk_mul_f32 v[142:143], v[142:143], v[102:103]\n\t"
               "v_cmp_gt_f32 %[ra], %[mt], v137\n\tv_cvt_pk_bf16_f32 v107, v138, v104\n\t"
               "v_cvt_pk_bf16_f32 v140, v140, v141\n\tv_cvt_pk_bf16_f32 v141, v142, v143\n\t"
               "ds_write_b64 %[ap], v[140:141]\n\ts_mov_b64 exec, %[dg]\n\t"
               "ds_write_b16 %[ai], v106\n\tds_write_b16 %[ai], v107 offset:16\n\tds_write_b16_d16_hi %[ai], v106 offset:32\n\tds_write_b16_d16_hi %[ai], v107 offset:48\n\t"
               "s_cmp_eq_u64 %[ra], 0\n\ts_cselect_b64 exec, 1, 0\n\tds_add_u32 %[af], %[inc]\n\ts_mov_b64 exec, -1\n\ts_setprio 0"
               : "+{v[100:103]}"(Hx), "+{v[104:107]}"(G), "+{v[108:111]}"(S), [mr] "=&v"(mref), [mt] "=&v"(mt), [ra] "=&s"(raise)
               : [y0] "v"(yb[0]), [y1] "v"(yb[1]), [y2] "v"(yb[2]), [y3] "v"(yb[3]), [y4] "v"(yb[4]), [y5] "v"(yb[5]),
                 [q0] "v"(s_qf(c, 0)), [q1] "v"(s_qf(c, 1)), [q2] "v"(s_qf(c, 2)), [q3] "v"(s_qf(c, 3)), [q4] "v"(s_qf(c, 4)), [q5] "v"(s_qf(c, 5)),
                 [afl] "v"(c.ct + C_FULL), [xa] "v"(xa), [xb] "v"(xb), [amr] "v"(c.amr), [am] "v"(am), [ap] "v"(ap), [ai] "v"(ai),
                 [af] "v"(af), [inc] "v"(inc), [nsq] "v"(nsq), [two] "v"(two),
                 [need] "s"(__builtin_amdgcn_readfirstlane(2 * k + 2)), [r1] "s"(0xAAAAAAAAAAAAAAAAull), [r2] "s"(0xCCCCCCCCCCCCCCCCull),
                 [dg] "s"(0xF0000F0000F0000Full), [cap] "i"(SPIN_CAP), [thr] "i"(__builtin_bit_cast(unsigned, THR)), [toff] "i"(__builtin_bit_cast(unsigned, TOFF)),
                 [eps] "i"(__builtin_bit_cast(unsigned, EPS * CC)), [nic] "i"(__builtin_bit_cast(unsigned, -1.f / CC))
               : "memory", "scc", "s40", "s41", "s42", "v112", "v113", "v114", "v115", "v116", "v117", "v118", "v119", "v120", "v121", "v122", "v123", "v124", "v125",
                 "v126", "v127", "v128", "v129", "v130", "v131", "v132", "v133", "v134", "v135", "v136", "v137", "v138", "v139", "v140", "v141", "v142", "v143");
  if (__builtin_expect(raise != 0ull, 0)) {
    rs = Hx;
    mref = fmaxf(mref, 32.f * __builtin_rintf(xrow16_max(mt) * (1.f / 32.f)));
    float pp[4];
#pragma unroll
    for (int i = 0; i < 4; ++i) pp[i] = __builtin_amdgcn_exp2f(S[i] - mref) * rs[i];
    asm volatile("ds_max_f32 %6, %3\n\tds_write_b32 %2, %3\n\tds_write_b64 %0, %1\n\ts_mov_b64 exec, 1\n\tds_add_u32 %4, %5\n\ts_mov_b64 exec, -1"
                 : : "v"(ap), "v"(u32x2{cvtpk(pp[0], pp[1]), cvtpk(pp[2], pp[3])}), "v"(am), "v"(mref), "v"(af), "v"((unsigned)NPK), "v"(c.amr) : "memory");
  }
}

using u32x16 = __attribute__((ext_vector_type(16))) unsigned;
typedef __attribute__((address_space(1))) unsigned short gus;
extern "C" __global__ __launch_bounds__(768, 3) void dino_main(const void*, const void*, const void*, const void*, const void*,
                                                               const void*, const void*, void*, void*) {
  u32x16 ka; u32x2 kb; unsigned long long tent64;
  __shared__ __attribute__((aligned(16))) char lds[LDS_BYTES];
  const int wave = __builtin_amdgcn_readfirstlane((int)threadIdx.x >> 6);
  const bool helper = blockIdx.x < 8;
  lds_ptr L0 = (lds_ptr)lds;
  const unsigned lbase = (unsigned)(size_t)L0;
  const unsigned ct = lbase + CT_OFF;
#define KARG(v, i) ((char*)(((unsigned long long)v[2 * (i) + 1] << 32) | v[2 * (i)]))
#define q KARG(ka, 0)
#define f0 KARG(ka, 1)
#define f1 KARG(ka, 2)
#define wq KARG(ka, 3)
#define wk KARG(ka, 4)
#define wv KARG(ka, 5)
#define wo KARG(ka, 6)
#define box KARG(ka, 7)
#define tent ((unsigned)tent64)
#define LANE_VARS int lane; asm volatile("v_mbcnt_lo_u32_b32 %0, -1, 0\n\tv_mbcnt_hi_u32_b32 %0, -1, %0" : "=v"(lane)); \
  const int r = lane & 15, g = lane >> 4; (void)r; (void)g;
#define DOFF_VARS unsigned doff[3];   \
  _Pragma("unroll") for (int i = 0; i < 3; ++i) doff[i] = (unsigned)(lane ^ ((swz_tbl(i) >> ((lane >> 3) * 4)) & 7)) << 4;
#define BARRIER() do { asm volatile("s_waitcnt lgkmcnt(0)" ::: "memory"); __builtin_amdgcn_s_barrier(); asm volatile("" ::: "memory"); } while (0)

  {
    unsigned doff[3];
    static_assert(swz_tbl(0) == swz_tbl(3) && swz_tbl(1) == swz_tbl(4) && swz_tbl(2) == swz_tbl(5), "");
    unsigned l16, db;
#define PD(d, b, off, pol) "global_load_lds_dwordx4 %[" #d "], s[" #b "] offset:" #off pol "\n\t"
    asm volatile("s_load_dwordx16 s[64:79], %[kp], 0x0\n\ts_load_dwordx2 s[80:81], %[kp], 0x40\n\ts_memrealtime s[82:83]\n\t"
                 "v_mbcnt_lo_u32_b32 %[l16], -1, 0\n\tv_mbcnt_hi_u32_b32 %[l16], -1, %[l16]\n\t"
                 "s_lshr_b32 s84, %[b], 3\n\ts_and_b32 s85, %[b], 7\n\t"
                 "v_lshrrev_b32 %[db], 1, %[l16]\n\tv_and_b32 %[db], -4, %[db]\n\t"
                 "s_mov_b32 s86, %[t0]\n\ts_mov_b32 s87, %[t1]\n\ts_mov_b32 s88, %[t2]\n\t"
                 "v_bfe_u32 %[d0], s86, %[db], 3\n\tv_bfe_u32 %[d1], s87, %[db], 3\n\tv_bfe_u32 %[d2], s88, %[db], 3\n\t"
                 "v_xor_b32 %[d0], %[d0], %[l16]\n\tv_xor_b32 %[d1], %[d1], %[l16]\n\tv_xor_b32 %[d2], %[d2], %[l16]\n\t"
                 "v_lshlrev_b32 %[d0], 4, %[d0]\n\tv_lshlrev_b32 %[d1], 4, %[d1]\n\tv_lshlrev_b32 %[d2], 4, %[d2]\n\t"
                 "v_lshlrev_b32 %[l16], 4, %[l16]\n\t"
                 "s_cmp_eq_u32 s84, 0\n\ts_cbranch_scc1 4f\n\t"
                 "s_cmp_lg_u32 %[w], 0\n\ts_cbranch_scc1 7f\n\t"
                 "s_setprio 3\n\tv_lshlrev_b32 %[db], 3, %[l16]\n\ts_mov_b32 m0, %[junk]\n\ts_lshl_b32 s89, s84, 13\n\t"
                 "s_cmp_gt_u32 s84, 3\n\ts_cbranch_scc1 2f\n\t"
                 "s_getpc_b64 s[90:91]\n1:\n\ts_sub_u32 s86, s89, 8192\n\ts_min_u32 s86, s86, dino_end-1b-8192\n\t"
                 "s_add_u32 s90, s90, s86\n\ts_addc_u32 s91, s91, 0\n\tglobal_load_lds_dword %[db], s[90:91]\n"
                 "2:\n\ts_mul_i32 s86, s85, 98304\n\ts_mul_i32 s87, s84, 3072\n\ts_sub_u32 s87, s87, 3072\n\t"
                 "s_add_u32 s88, s86, s87\n\ts_lshl1_add_u32 s86, s86, s87\n\ts_lshl_b32 s86, s86, 1\n\t"
                 "s_mul_i32 s87, s85, 12288\n\ts_lshr_b32 s90, s89, 1\n\ts_add_u32 s87, s87, s90\n\ts_sub_u32 s87, s87, 13 * 4096\n\t"
                 "s_sub_u32 s89, s89, 4 * 8192\n\ts_cmp_gt_u32 s85, 5\n\ts_cselect_b32 s89, -1, s89\n\ts_cmp_ge_u32 s89, 11 * 8192\n\t"
                 "s_waitcnt lgkmcnt(0)\n\ts_cbranch_scc1 3f\n\t"
                 "s_cmp_lt_u32 s89, 9 * 8192\n\ts_cselect_b64 s[90:91], s[70:71], s[72:73]\n\ts_cselect_b32 s89, s89, s87\n\t"
                 "s_add_u32 s90, s90, s89\n\ts_addc_u32 s91, s91, 0\n\tglobal_load_lds_dword %[db], s[90:91]\n"
                 "3:\n\ts_mov_b32 m0, 0x3000\n\t"
                 "s_add_u32 s90, s68, s88\n\ts_addc_u32 s91, s69, 0\n\t"
                 "s_add_u32 s92, s66, s86\n\ts_addc_u32 s93, s67, 0\n\t"
                 PD(d0, 90:91, 0, " nt") PD(d1, 90:91, 1024, " nt") PD(d2, 90:91, 2048, " nt")
                 "s_mov_b32 m0, 0\n\ts_nop 0\n\t"
                 PD(d0, 92:93, 0, " nt") PD(d1, 92:93, 1024, " nt")
                 "s_sub_u32 s89, s84, 15\n\ts_cmp_gt_u32 s85, 5\n\ts_cselect_b32 s89, -1, s89\n\ts_cmp_ge_u32 s89, 11\n\ts_cbranch_scc1 9f\n\t"
                 "s_mov_b32 m0, %[junk]\n\ts_sub_u32 s87, s87, 11 * 4096\n\ts_lshl_b32 s89, s89, 13\n\ts_cmp_lt_u32 s89, 9 * 8192\n\t"
                 "s_cselect_b64 s[90:91], s[76:77], s[74:75]\n\ts_cselect_b32 s89, s89, s87\n\t"
                 "s_add_u32 s90, s90, s89\n\ts_addc_u32 s91, s91, 0\n\tglobal_load_lds_dword %[db], s[90:91]\n\t"
                 "s_branch 9f\n"
                 "7:\n\ts_cmp_eq_u32 %[w], 1\n\ts_cbranch_scc1 8f\n\ts_barrier\n\ts_branch 8f\n"
                 "4:\n\t"
                 "s_cmp_gt_u32 %[b], 5\n\ts_cbranch_scc1 8f\n\ts_cmp_gt_u32 %[w], 9\n\ts_cbranch_scc1 8f\n\t"
                 "s_mov_b32 m0, %[img]\n\ts_mul_i32 s86, %[w], 9216\n\ts_and_b32 s88, %[w], 1\n\ts_lshl1_add_u32 s89, %[b], s88\n\t"
                 "s_mul_i32 s88, s88, 6144\n\ts_mul_i32 s89, s89, 6144\n\ts_add_u32 s88, s88, %[hr]\n\t"
                 "s_cmp_lg_u32 %[w], 0\n\ts_waitcnt lgkmcnt(0)\n\ts_cbranch_scc1 5f\n\t"
                 PD(d0, 64:65, 0, "") PD(d1, 64:65, 1024, "") PD(d2, 64:65, 2048, "") PD(d0, 64:65, 3072, "")
                 "s_mov_b32 m0, %[img] + 4096\n\ts_add_u32 s90, s64, 0x1000\n\ts_addc_u32 s91, s65, 0\n\t"
                 PD(d1, 90:91, 0, "") PD(d2, 90:91, 1024, "")
                 "\n5:\n\ts_cmp_gt_u32 %[w], 7\n\ts_cselect_b64 s[92:93], s[72:73], s[70:71]\n\ts_cselect_b32 m0, s88, s86\n\ts_cselect_b32 s86, s89, s86\n\t"
                 "s_cselect_b32 s87, 2, 3\n\ts_add_u32 s92, s92, s86\n\ts_addc_u32 s93, s93, 0\n"
                 "6:\n\ts_sub_u32 s87, s87, 1\n\t"
                 PD(d0, 92:93, 0, "") PD(d1, 92:93, 1024, "") PD(d2, 92:93, 2048, "")
                 "s_add_u32 m0, m0, 3072\n\ts_add_u32 s92, s92, 3072\n\ts_addc_u32 s93, s93, 0\n\ts_cmp_lg_u32 s87, 0\n\ts_cbranch_scc1 6b"
                 "\n8:\n\ts_waitcnt lgkmcnt(0)\n9:"
                 : "={s[64:79]}"(ka), "={s[80:81]}"(kb), "={s[82:83]}"(tent64),
                   [d0] "=&v"(doff[0]), [d1] "=&v"(doff[1]), [d2] "=&v"(doff[2]), [l16] "=&v"(l16), [db] "=&v"(db)
                 : [kp] "s"(__builtin_amdgcn_kernarg_segment_ptr()), [b] "s"(blockIdx.x), [w] "s"(wave),
                   [t0] "i"(swz_tbl(0)), [t1] "i"(swz_tbl(1)), [t2] "i"(swz_tbl(2)), [junk] "i"(JUNK), [img] "i"(IMG_A), [hr] "i"(H_R)
                 : "memory", "scc", "vcc", "s84", "s85", "s86", "s87", "s88", "s89", "s90", "s91", "s92", "s93", "s94", "s95", "s96", "s97", "s98", "s99");
  }
  const unsigned t0 = (unsigned)__builtin_amdgcn_s_memrealtime() - T0BACK;

  if (__builtin_expect(blockIdx.x < 6, 0)) {
    LANE_VARS
    DOFF_VARS
    const int j = blockIdx.x;
    TNOW(ta0);
    TNOW(ta1);
    const int qq = (lane >> 2) & 3, p4 = lane & 3, e4 = wave & 3;
    int ra = H_R + r * ROWB + ((g ^ swz(r)) << 4);
    bf16x8 a[6], b[6], w[18];
    auto rows = [&](int t, bf16x8* d) {
#pragma unroll
      for (int kk = 0; kk < 6; ++kk) d[kk] = *(LDS_AS(bf16x8)*)(L0 + t * 6144 + ((kk & 1) ? (ra ^ 64) : ra) + (kk >> 1) * 128);
    };
#define TSET(v)
    int o[3];
#pragma unroll
    for (int t = 0; t < 3; ++t) o[t] = tr_off(4 * g + qq, 4 * (3 * e4 + t) + p4);
    unsigned qo = lbase + H_Q + 3 * e4 * 512 + ((g >> 1) * 16 + r) * 16 + (g & 1) * 8, qa = lbase + IMG_A + tr_off(r, 8 * wave + g);
    if (wave == 11) {
      unsigned d;
      asm volatile("s_load_dword %0, %1, 0x40\n\ts_barrier\n\ts_barrier\n\ts_barrier\n\ts_waitcnt lgkmcnt(0)" : "=&s"(d) : "s"(box + EPOCH_OFF) : "memory");
    } else
    asm volatile("s_waitcnt vmcnt(9)\n\ts_barrier" : PIN3(o, 0), "+v"(qo), "+v"(qa) :: "memory");
    if (wave < 6 || wave == 10) {
      rows((IMG_A - H_R) / 6144, a);
      const u32x2 m0 = *(LDS_AS(u32x2)*)(size_t)qa, m1 = *(LDS_AS(u32x2)*)(size_t)(qa ^ 32);
      static_assert((IMG_A - H_R) % 6144 == 0, "");
      const bf16x8 ones = __builtin_bit_cast(bf16x8, u32x4{0x3f803f80u, 0x3f803f80u, 0x3f803f80u, 0x3f803f80u});
      f32x4 sm = {0.f, 0.f, 0.f, 0.f};
#pragma unroll
      for (int i = 0; i < 6; ++i) sm = mfma(ones, a[i], sm);
      const float mean = sm[0] * (1.f / CC);
      if (__builtin_expect(wave < 6, 1)) {
        img_st(L0, H_C, wave, lane, __builtin_bit_cast(bf16x8, u32x4{cvtpk(bflo(m0[0]) - mean, bfhi(m0[0]) - mean), cvtpk(bflo(m0[1]) - mean, bfhi(m0[1]) - mean),
                                                                   cvtpk(bflo(m1[0]) - mean, bfhi(m1[0]) - mean), cvtpk(bflo(m1[1]) - mean, bfhi(m1[1]) - mean)}));
      } else {
        asm volatile("s_barrier" : PIN3(a, 0), PIN3(a, 3) :: "memory");
        f32x2 v2 = {0.f, 0.f}, v3 = v2;
#pragma unroll
        for (int i = 0; i < 12; ++i) {
          const u32x4 d = __builtin_bit_cast(u32x4, a[i >> 1]);
          const unsigned d0 = d[2 * (i & 1)], d1 = d[2 * (i & 1) + 1];
          const f32x2 x0 = f32x2{bflo(d0), bfhi(d0)} - mean, x1 = f32x2{bflo(d1), bfhi(d1)} - mean;
          v2 += x0 * x0; v3 += x1 * x1;
        }
        v2 += v3;
        *(LDS_AS(float)*)(L0 + CT_OFF + r * 4) = LOG2E * __builtin_amdgcn_rsqf(xrow16_sum(v2[0] + v2[1]) * (1.f / CC) + EPS);
      }
      TSET(ta2);
    }
    if (wave < 4) {
      wait_vm<0>();
      TSET(ta3);
      BARRIER();
      TSET(th1);
#pragma unroll
      for (int kk = 0; kk < 6; ++kk) {
        b[kk] = img_ld(L0, H_C, kk, lane);
        w[kk] = mk8(tr_read(L0 + kk * 12288 + o[0]), tr_read(L0 + kk * 12288 + 6144 + o[0]));
        __builtin_amdgcn_sched_barrier(0);
      }
#pragma unroll
      for (int i = 6; i < 18; ++i) {
        w[i] = mk8(tr_read(L0 + (i % 6) * 12288 + o[i / 6]), tr_read(L0 + (i % 6) * 12288 + 6144 + o[i / 6]));
        __builtin_amdgcn_sched_barrier(0);
      }
#pragma unroll
      for (int t = 0; t < 3; ++t) {
        f32x4 D = {0.f, 0.f, 0.f, 0.f};
#pragma unroll
        for (int kk = 0; kk < 6; ++kk) D = mfma(w[6 * t + kk], b[kk], D);
        *(LDS_AS(u32x2)*)(size_t)(qo + t * 512) = u32x2{cvtpk(D[0], D[1]), cvtpk(D[2], D[3])};
      }
      TSET(ta4);
      BARRIER();
    } else if (wave == 8 || wave == 9) {
      unsigned so = lane * 16 + (2 * j + (wave & 1)) * 1024;
      unsigned epoch;
      asm volatile("global_load_dword %0, %1, %2 sc1\n\ts_barrier\n\ts_waitcnt vmcnt(0)" : "=v"(epoch) : "v"(EPOCH_OFF), "s"(box) : "memory");
      TSET(ta3);
      rows(wave & 1, a);
      unsigned ta = line_tag(epoch, 8), tb = line_tag(epoch, 9);
      asm volatile("" : PIN3(a, 0), PIN3(a, 3), "+v"(ta), "+v"(tb), "+v"(so));
      TSET(ta4);
      BARRIER();
      TSET(ta5);
#pragma unroll
      for (int kk = 0; kk < 6; ++kk) b[kk] = img_ld(L0, H_Q, kk, lane);
      const float sc = *(LDS_AS(float)*)(L0 + CT_OFF + r * 4);
      __builtin_amdgcn_sched_barrier(0);
      f32x4 D = {0.f, 0.f, 0.f, 0.f};
#pragma unroll
      for (int kk = 0; kk < 6; ++kk) D = mfma(a[kk], b[kk], D);
      D *= sc;
      const unsigned p0 = cvtpk(D[0], D[1]), p1 = cvtpk(D[2], D[3]);
      asm volatile("global_store_dwordx4 %0, %1, %2 sc1\n\ts_nop 1" : : "v"(so), "v"(u32x4{p0, p1, p0 ^ p1 ^ ta, tb}), "s"(box + QK_OFF) : "memory");
      TSET(th2);
    } else if (wave != 11) {
      if (wave != 10) asm volatile("s_waitcnt vmcnt(0) lgkmcnt(0)\n\ts_barrier" ::: "memory");
      BARRIER();
    }
    BARRIER();
    issue_rows16(wo + wave * 6144, lbase + wave * 6144, doff);
    if (wave < 2) issue_rows16(wv + (2 * j + wave) * 6144, lbase + H_R + wave * 6144, doff);
    const unsigned epoch = epoch_load(box);
    BARRIER();
    TSET(ta3);
    {
      const int o0 = tr_off(8 * g + qq, 4 * wave + p4), o1 = tr_off(8 * g + 4 + qq, 4 * wave + p4);
#pragma unroll
      for (int kk = 0; kk < 6; ++kk) b[kk] = mk8(tr_read(L0 + kk * 12288 + o0), tr_read(L0 + kk * 12288 + o1));
      f32x4 D[2];
#pragma unroll
      for (int h = 0; h < 2; ++h) {
        rows(h, a);
        D[h] = f32x4{0.f, 0.f, 0.f, 0.f};
#pragma unroll
        for (int kk = 0; kk < 6; ++kk) D[h] = mfma(a[kk], b[kk], D[h]);
      }
      const int e = wave / 3;
      asm volatile("global_store_dwordx4 %0, %1, %2 sc1\n\ts_waitcnt vmcnt(0)"
                   : : "v"(lane * 16 + (e * 15 + 3 * j + wave) * 1024), "v"(pack8(D[0] * SQRT_C, D[1] * SQRT_C)), "s"(box + W2_OFF) : "memory");
    }
    BARRIER();
    if (wave == 11)
      asm volatile("s_mov_b64 exec, 1\n\tglobal_store_dwordx2 %0, %1, %2 sc1\n\ts_mov_b64 exec, -1\n\ts_nop 1"
                   : : "v"(j * 128), "v"(u32x2{line_tag(epoch, 10), line_tag(epoch, 11)}), "s"(box + W2F_OFF) : "memory");
    TPUT(60 + wave * 5, ta0); TPUT(61 + wave * 5, ta1); TPUT(62 + wave * 5, ta2); TPUT(63 + wave * 5, ta3); TPUT(64 + wave * 5, ta4);
  }

  if (__builtin_expect(helper, 0) && wave == 6) {
    LANE_VARS
    unsigned d0, d1;
    asm volatile("global_load_dword %0, %2, %3 sc1\n\tglobal_load_dword %1, %4, %5 sc1\n\ts_waitcnt vmcnt(0)"
                 : "=&v"(d0), "=&v"(d1) : "v"((lane * NWG + blockIdx.x) * 128), "s"(box), "v"((lane & 1) * 4096), "s"(KARG(kb, 0)) : "memory");
  }
  int rslot = -1;
  if (__builtin_expect(wave == 0, 1)) {
    LANE_VARS
    DOFF_VARS
    if (helper) BARRIER();
    const unsigned afull = ct + C_FULL + r * 4;
    unsigned ts = 0;
    {
      const int ti = helper ? 1023 : (lane * 31 + (int)(blockIdx.x >> 3) - 1) & 1023;
      const int sp = ti >> 5, yy = sp * 8 + ((blockIdx.x + sp) & 7), xx = (ti + 5 * sp) & 31;
      unsigned tmp, pub = helper ? 2 * (TPW - 1) : 0, pre = 0, afr = ct + C_FREE + (helper ? ((TPW - 1) % NSLOT) * 4 : 0), e0, e1, e2, g0, g1, g2;
      asm volatile("s_mov_b32 s44, %[due0]\n\ts_mov_b32 s46, %[y0]\n\ts_mov_b32 s47, %[cap]\n\ts_mov_b32 s48, %[i0]\n\ts_mov_b32 s49, %[s0]\n\t"
                   "s_mov_b32 s51, 0\n\ts_mov_b32 s45, 0\n\ts_movk_i32 s39, %[p3s]\n"
                   "1:\n\tv_readlane_b32 s53, %[ob], s48\n\tv_readlane_b32 s52, %[oa], s48\n\ts_add_u32 m0, s49, 0x3000\n\tv_cmp_gt_u32 vcc, s51, %[pre]\n\t"
                   "v_add_u32 %[g0], s53, %[d0]\n\tv_add_u32 %[g1], s53, %[d1]\n\tv_add_u32 %[g2], s53, %[d2]\n\t"
                   "v_add_u32 %[e0], s52, %[d0]\n\tv_add_u32 %[e1], s52, %[d1]\n\tv_add_u32 %[e2], s52, %[d2]\n\t"
                   "s_cbranch_vccnz 5f\n\t"
                   "s_cmp_eq_u32 s48, 0\n\ts_cbranch_scc1 2f\n\ts_cmp_eq_u32 s48, 2\n\ts_cbranch_scc1 8f\n"
                   LD_GATE(1, "")
                   LD1(g0, 68:69, 0) LD1(g1, 68:69, 1024) LD1(g2, 68:69, 2048) "s_mov_b32 m0, s49\n\ts_nop 0\n\t" LD1(e0, 66:67, 0) LD1(e1, 66:67, 1024)
                   LD_NEXT
                   "\n2:\n\tv_add_u32 %[afr], 4, %[afr]\n\ts_cmp_eq_u32 s49, %[lasts]\n\ts_cbranch_scc1 7f\n"
                   "3:\n\tv_mov_b32 %[pre], 0\n\ts_add_u32 m0, s49, 0x800\n\tds_read_b32 %[pre], %[afr] ; NOTRACK\n"
                   LD_GATE(2, "")
                   LD1(e2, 56:57, 0) LD1(e0, 56:57, 1024) LD1(e1, 56:57, 2048) LD1(e2, 56:57, 3072) "s_add_u32 m0, s49, 0x1800\n\ts_nop 0\n\t" LD1(e0, 58:59, 0)
                   LD_NEXT
                   "\n29:\n\ts_add_u32 m0, s49, 0x1800\n\ts_add_u32 s48, s48, 1\n"
                   LD_GATE(3, "s_cmp_gt_u32 s48, %[tpw] - 3\n\ts_cbranch_scc1 38f\n39:\n\t")
                   LD1(e1, 58:59, 1024) LD1(e2, 58:59, 2048) LD1(e0, 58:59, 3072) "s_add_u32 m0, s49, 0x2800\n\ts_add_u32 s49, s49, %[slot]\n\t" LD1(e1, 60:61, 0) LD1(e2, 60:61, 1024)
                   LD_NEXT
                   "s_cmp_eq_u32 s49, %[ring]\n\ts_cselect_b32 s49, 0, s49\n\ts_cmp_gt_i32 s46, 30\n\ts_cbranch_scc1 6f\n"
                   "4:\n\ts_cmp_eq_u32 s48, %[pk]\n\ts_cmovk_i32 s39, %[p3]\n\ts_cmp_lg_u32 s48, %[tpw]\n\ts_cbranch_scc1 1b\n\ts_branch 9f\n"
                   LD_PUB(1, 0, 15, 30) LD_PUB(2, 5, 20, 35) LD_PUB(3, 10, 25, 40)
                   "38:\n\ts_cmp_eq_u32 s46, 10\n\ts_cselect_b32 s43, 23, 0\n\ts_cmp_lt_u32 s42, s43\n\ts_cbranch_scc0 39b\n\tv_add_u32 %[tmp], 1, %[pub]\n\ts_waitcnt vmcnt(22)\n\tds_write_b32 %[af], %[tmp] ; NOTRACK\n\ts_branch 39b\n"
                   "5:\n\ts_movk_i32 s40, 0x100\n"
                   "40:\n\tds_read_b32 %[pre], %[afr]\n\t" LD_VM
                   "s_sub_u32 s40, s40, 1\n\ts_cselect_b32 s42, 0, s42\n\ts_cselect_b32 s40, 0, s40\n\t"
                   "s_cmp_le_i32 s42, s46\n\ts_cbranch_scc1 42f\n"
                   "41:\n\ts_sub_i32 s47, s47, 1\n\ts_cmp_lt_i32 s47, 0\n\ts_waitcnt lgkmcnt(0)\n\ts_cbranch_scc1 10b\n\t"
                   "v_cmp_gt_u32 vcc, s51, %[pre]\n\ts_cbranch_vccnz 40b\n\ts_branch 10b\n"
                   LD_PUB0(4, 0, 15, 30)
                   "6:\n\ts_waitcnt vmcnt(45)\n\tv_add_u32 %[pub], 2, %[pub]\n\tds_write_b32 %[af], %[pub]\n\ts_sub_u32 s46, s46, 15\n\ts_branch 4b\n"
                   "7:\n\ts_waitcnt lgkmcnt(0)\n\ts_add_u32 s51, s51, 8\n\tv_subrev_u32 %[afr], %[frb], %[afr]\n\ts_branch 3b\n"
                   "8:\n\ts_barrier\n\ts_add_u32 s46, s46, 64\n\ts_branch 10b\n"
                   "9:\n\ts_cmp_lt_i32 s46, 30\n\ts_cbranch_scc1 91f\n\ts_waitcnt vmcnt(30)\n\tv_add_u32 %[pub], 2, %[pub]\n\tds_write_b32 %[af], %[pub]\n"
                   "91:\n\ts_cmp_lt_i32 s46, 15\n\ts_cbranch_scc1 92f\n\tv_add_u32 %[tmp], 1, %[pub]\n\ts_waitcnt vmcnt(27)\n\tds_write_b32 %[af], %[tmp]\n"
                   "92:\n\ts_waitcnt lgkmcnt(0)"
                   : [pub] "+v"(pub), [pre] "+v"(pre), [afr] "+v"(afr), [ts] "+v"(ts), [tmp] "=&v"(tmp),
                     [e0] "=&v"(e0), [e1] "=&v"(e1), [e2] "=&v"(e2), [g0] "=&v"(g0), [g1] "=&v"(g1), [g2] "=&v"(g2)
                   : "{s[64:79]}"(ka), "{s[56:57]}"(f0 + 0x800), "{s[58:59]}"(f0 + 0x30000), "{s[60:61]}"(f0 + 0x31000), [t0] "s"(t0), [af] "v"(afull),
                     [d0] "v"(doff[0]), [d1] "v"(doff[1]), [d2] "v"(doff[2]), [oa] "v"(yy * 393216 + xx * 6144), [ob] "v"(yy * 98304 + xx * 3072),
                     [due0] "s"(helper ? HDUE * (PACE16 / 3) : PRE5 * (PACES / 3)), [y0] "s"(helper ? -15 : -79 + 5 * PRE5), [i0] "s"(helper ? TPW - 1 : 0),
                     [s0] "s"(helper ? ((TPW - 1) % NSLOT) * SLOT : 0),
                     [cap] "i"(SPIN_CAP), [win] "i"(PWIN), [ahi] "s"(((blockIdx.x >> 3) & 3) == 1 ? AHIR : AHI), [adl] "i"(ADL), [amg] "i"(AMG), [p3] "i"(PACE16 / 3), [p3s] "i"(PACES / 3), [pk] "i"(PK1), [slot] "i"(SLOT),
                     [ring] "i"(NSLOT * SLOT), [frb] "i"(NSLOT * 4), [lasts] "i"((NSLOT - 1) * SLOT), [tpw] "i"(TPW)
                   : "memory", "scc", "vcc", "s39", "s40", "s41", "s42", "s43", "s44", "s45", "s46", "s47", "s48", "s49", "s51", "s52", "s53", "s62", "s63");
    }
    lds_st32(ct + C_ISS, 1u);
#define CLR(b) "global_store_dwordx4 %0, %1, %" #b " sc1\n\t"
    auto clear = [&]() {
      asm volatile(CLR(2) CLR(3) CLR(4) CLR(5) CLR(6) CLR(7) CLR(8) CLR(9) "s_nop 1"
                 : : "v"(((lane >> 3) * NWG + blockIdx.x) * 128 + (lane & 7) * 16), "v"(u32x4{0u, 0u, 0u, 0u}), "s"(box), "s"(box + 1 * 262144), "s"(box + 2 * 262144),
                     "s"(box + 3 * 262144), "s"(box + 4 * 262144), "s"(box + 5 * 262144), "s"(box + 6 * 262144), "s"(box + 7 * 262144) : "memory");
    };
    if (PRECLR == 1) clear();
    wait_vm<PRECLR == 1 ? 23 : 15>();
    lds_st32(afull, (unsigned)(2 * TPW - 2));
    if (PRECLR == 3) clear();
    wait_vm<(PRECLR == 1 || PRECLR == 3) ? 20 : 12>();
    lds_st32(afull, (unsigned)(2 * TPW - 1));
    wait_vm<(PRECLR == 1 || PRECLR == 3) ? 8 : 0>();
    lds_st32(afull, ~0u);
    TNOW(tl);
    if (PRECLR == 2) clear();
    if (PRECLR == 4) { unsigned d; asm volatile("global_load_dword %0, %1, %2 sc1\n\ts_waitcnt vmcnt(0)" : "=v"(d) : "v"(blockIdx.x * 128), "s"(box) : "memory"); }
    __builtin_amdgcn_s_setprio(0);
    wait_vm<0>();
    TNOW(tlx);
    if (PRECLR != 5) lds_add(ct + C_X1, 1u);
    rslot = 7;
  } else {
    if (wave == 1) {
      LANE_VARS
    {
      const unsigned o = lane * 8;
      unsigned z = (o >= C_MREF && o < C_MREF + 64) ? 0xff800000u : 0u;
      if (helper && o >= C_CONS && o < C_MREF) z = NE * ((TPW - 1) / NPK);
      u32x2 zz = {z, z};
      if (o >= C_PFL && o < C_CONS) zz = u32x2{(o - C_PFL) / 2 + 2, (o - C_PFL) / 2 + 4} + (helper ? 2u * NPK * ((TPW - 1) / NPK) : 0u);
      *(LDS_AS(u32x2)*)(L0 + CT_OFF + o) = zz;
      *(LDS_AS(u32x2)*)(L0 + ONES_OFF + (lane < 48 ? o : 0)) = u32x2{0x3f803f80u, 0x3f803f80u};
    }
    }
    if (helper || wave == 1) BARRIER();
    if (wave <= NE) {
      LANE_VARS
      const int ej = wave - 1;
      const int qq = (lane >> 2) & 3, p4 = lane & 3;
      const unsigned apk = lbase + IMG_A + lane * 16;
      const unsigned epoch = epoch_load(box);
      __builtin_amdgcn_s_setprio(2);
      if (helper) { while ((unsigned)__builtin_amdgcn_s_memrealtime() - tent < RELAYT) __builtin_amdgcn_s_sleep(8); }
      unsigned long long tpv, tp7, tx1;
      {
        static_assert((TPW & 1) == 1 && C_CONS == C_PFL + 64 && C_FREE == C_FULL + 64, "");
        const int k0 = helper ? TPW - 1 : 0;
        unsigned need = 2 * k0 + 2;
        asm volatile("s_mov_b32 s46, 0\n\ts_mov_b64 exec, 0\n\ts_nop 3\n\ts_branch 99f\n"
                     "2:\n\ts_mov_b64 exec, -1\n\ts_nop 7\n\t" PV_Z4(72, 73, 74, 75) PV_Z4(76, 77, 78, 79) PV_Z4(80, 81, 82, 83) PV_Z4(84, 85, 86, 87)
                     "v_mov_b32 v88, 0xff800000\n\t"
                     "s_mov_b32 s46, %[k0]\n\ts_mov_b32 s45, %[s0]\n\ts_mov_b32 s44, %[e0]\n\ts_mov_b32 s47, %[cap]\n\t"
                     "s_cmp_lg_u32 s46, 0\n\ts_cbranch_scc1 32f\n"
                     "3:\n\tv_mov_b32 v148, %[ca]\n\t" PV_WAITF(4, 148) PV_FETCH_A
                     "s_mov_b64 exec, 2\n\tds_add_u32 v148, %[one] offset:64\n\ts_mov_b64 exec, -2\n\t"
                     "v_add_u32 %[need], 2, %[need]\n\tv_add_u32 v148, 4, v148\n\ts_mov_b64 exec, -1\n\t"
                     PV_WAITF(5, 148)
                     "v_mov_b32 v149, v148\n\ts_mov_b64 exec, -2\n\tv_add_u32 %[need], 2, %[need]\n\tv_add_u32 v148, 4, v148\n\t"
                     "s_mov_b64 exec, 1\n\tv_add_u32 %[need], %[npk2], %[need]\n\ts_mov_b64 exec, -1\n\t"
                     PV_REQ("126:129", "130:133", "134:135", 136, 148) PV_FETCH_B
                     "s_mov_b64 exec, 2\n\tds_add_u32 v149, %[one] offset:64\n\ts_mov_b64 exec, -1\n"
                     "6:\n\t"
                     PV_STEP(1, "126:129", "130:133", "134:135", 134, 135, 136, 148, "138:141", "142:145", "146:147", 137, 149,
                             "92:95", "98:101", "104:107", "90:93", "96:99", "102:105",
                             PV_MOV(92, 90) PV_MOV(93, 91), PV_MOV(98, 96) PV_MOV(99, 97), PV_MOV(104, 102) PV_MOV(105, 103),
                             "s_cmp_eq_u32 s46, %[last]\n\ts_cbranch_scc1 99f\n\t")
                     "s_cbranch_vccnz 7f\n"
                     "16:\n\t" PV_FETCH_A PV_REL(148)
                     "s_lshl1_add_u32 s54, s46, -2\n\ts_lshr_b64 s[54:55], %[sch], s54\n\ts_and_b32 s54, s54, 15\n\ts_cbranch_scc1 31f\n"
                     "30:\n\t"
                     PV_STEP(2, "138:141", "142:145", "146:147", 146, 147, 137, 149, "126:129", "130:133", "134:135", 136, 148,
                             "110:113", "116:119", "122:125", "108:111", "114:117", "120:123",
                             PV_MOV(110, 108) PV_MOV(111, 109), PV_MOV(116, 114) PV_MOV(117, 115), PV_MOV(122, 120) PV_MOV(123, 121),
                             "s_cmp_eq_u32 s46, %[last] - 1\n\ts_cbranch_scc1 98f\n\t")
                     "s_cbranch_vccnz 8f\n"
                     "26:\n\t" PV_FETCH_B PV_REL(149)
                     "s_branch 6b\n"
                     PV_WAITF(7, 148) "s_branch 16b\n"
                     PV_WAITF(8, 149) "s_branch 26b\n"
                     "98:\n\t" PV_MF("76:79", "114:117", "138:141") PV_MF("80:83", "120:123", "138:141")
                     "v_add_u32 %[need], 2, %[need]\n\ts_add_u32 s46, s46, 1\n\ts_branch 13f\n"
                     PV_SLOW(1, "126:129", "130:133", "134:135", 134, 135, 136, 148, 126, 127, 128, 129)
                     PV_SLOW(2, "138:141", "142:145", "146:147", 146, 147, 137, 149, 138, 139, 140, 141)
                     "31:\n\ts_cmp_lg_u32 s54, 8\n\ts_cbranch_scc1 34f\n"
                     "32:\n\tv_mbcnt_lo_u32_b32 v152, 31, 0\n\tv_lshlrev_b32 v152, 7, v152\n\ts_mov_b32 s55, %[gcap]\n"
                     "33:\n\tglobal_load_dwordx2 v[150:151], v152, %[fl] sc1\n\ts_sub_u32 s55, s55, 1\n\ts_cselect_b32 s56, 0, -1\n\ts_waitcnt vmcnt(0) lgkmcnt(0)\n\t"
                     "v_cmp_ne_u64 s[48:49], %[tag], v[150:151]\n\ts_sleep 2\n\ts_and_b32 s48, s48, s56\n\ts_cbranch_scc1 33b\n\t"
                     "s_mov_b32 s57, 0\n\tv_mov_b32 v150, %[apk]\n\ts_cmp_lg_u32 s46, %[last]\n\ts_cbranch_scc1 30b\n\ts_mov_b32 s54, 7\n\ts_branch 38f\n"
                     "34:\n\ts_cmp_lg_u32 s46, %[last] - 3\n\ts_cbranch_scc1 36f\n\tv_mov_b32 v151, %[iss]\n"
                     "35:\n\tds_read_b32 v150, v151\n\ts_sub_i32 s47, s47, 1\n\ts_cmp_lt_i32 s47, 0\n\ts_cselect_b32 s55, 0, 1\n\ts_waitcnt lgkmcnt(0)\n\t"
                     "v_readfirstlane_b32 s56, v150\n\ts_cmp_lt_u32 s56, s55\n\ts_cbranch_scc1 35b\n"
                     "36:\n\ts_mul_i32 s55, s57, 3072\n\tv_add_u32 v150, s55, %[apk]\n\ts_getpc_b64 s[48:49]\n"
                     "37:\n\ts_mul_i32 s55, s57, 40\n\ts_add_u32 s55, s55, 38f-37b\n\ts_add_u32 s48, s48, s55\n\ts_addc_u32 s49, s49, 0\n\t"
                     "s_add_u32 s57, s57, s54\n\ts_sub_u32 s54, s54, 1\n\ts_setpc_b64 s[48:49]\n"
                     "38:\n\t" PV_W2(0, 3, 4, 7, 8, 11) PV_W2(12, 15, 16, 19, 20, 23) PV_W2(24, 27, 28, 31, 32, 35) PV_W2(36, 39, 40, 43, 44, 47)
                     PV_W2(48, 51, 52, 55, 56, 59) PV_W2(60, 63, 64, 67, 68, 71)
                     "s_waitcnt lgkmcnt(0)\n\ts_branch 3b\n"
                     "99:\n\t" PV_MF("76:79", "96:99", "126:129") PV_MF("80:83", "102:105", "126:129") PV_T(58)
                     "v_add_f32 v153, v86, v87\n\tv_add_f32 v151, v84, v85\n\tv_rcp_f32 v150, v153\n\tv_mul_f32 v151, %[nic], v151\n\t"
                     "v_add_u32 v152, %[aw], %[apk]\n\tv_mul_f32 v151, v151, v150\n\t"
                     PV_NRM(72, 73, 74, 75, 0) PV_NRM(76, 77, 78, 79, 2048) PV_NRM(80, 81, 82, 83, 4096)
                     "v_mov_b32 v154, %[x1]\n\ts_and_b64 exec, exec, 1\n\tds_add_u32 v154, %[one]\n\ts_cselect_b64 exec, -1, 0\n\ts_cselect_b32 s55, %[x1n], 0\n\t" PV_T(60)
                     "v_log_f32 v153, v153\n\tv_subrev_f32 v88, %[toff], v88\n\tv_lshrrev_b32 v151, 1, %[apm]\n\tv_add_f32 v88, v88, v153\n\tv_add_u32 v151, %[lwo], v151\n\t"
                     "v_mbcnt_lo_u32_b32 v155, -1, 0\n\tv_mbcnt_hi_u32_b32 v155, -1, v155\n\tds_write_b32 v151, v88\n\t"
                     "v_and_b32 v151, 1, v155\n\tv_and_b32 v155, 48, v155\n\tv_lshl_add_u32 v155, v151, 2, v155\n\tv_add_u32 v155, %[lw], v155\n\t"
                     "ds_read2_b32 v[114:115], v155 offset1:2\n\tv_add_u32 v152, %[ar], %[apk]\n"
                     "97:\n\tds_read_b32 v150, v154\n\tds_read_b128 v[90:93], v152\n\tds_read_b128 v[94:97], v152 offset:1024\n\tds_read_b128 v[98:101], v152 offset:2048\n\t"
                     "ds_read_b128 v[102:105], v152 offset:3072\n\tds_read_b128 v[106:109], v152 offset:4096\n\tds_read_b128 v[110:113], v152 offset:5120\n\t"
                     "s_sub_i32 s47, s47, 1\n\ts_cmp_lt_i32 s47, 0\n\ts_cselect_b32 s55, 0, s55\n\ts_waitcnt vmcnt(0) lgkmcnt(6)\n\t"
                     "v_readfirstlane_b32 s56, v150\n\ts_cmp_lt_u32 s56, s55\n\ts_cbranch_scc1 97b\n\ts_cmp_eq_u32 s46, 0\n\t"
                     PV_G(5, "90:93", "0:3", "4:7", "8:11", "0", "0", "0")
                     PV_G(4, "94:97", "12:15", "16:19", "20:23", "v[72:75]", "v[76:79]", "v[80:83]")
                     PV_G(3, "98:101", "24:27", "28:31", "32:35", "v[72:75]", "v[76:79]", "v[80:83]")
                     PV_G(2, "102:105", "36:39", "40:43", "44:47", "v[72:75]", "v[76:79]", "v[80:83]")
                     PV_G(1, "106:109", "48:51", "52:55", "56:59", "v[72:75]", "v[76:79]", "v[80:83]")
                     PV_G(0, "110:113", "60:63", "64:67", "68:71", "v[72:75]", "v[76:79]", "v[80:83]")
                     "v_xor_b32 v114, v114, %[ltag]\n\tv_xor_b32 v115, v115, %[ltag]\n\ts_nop 3\n\t"
                     "v_cvt_pk_bf16_f32 v116, v72, v73\n\tv_cvt_pk_bf16_f32 v120, v74, v75\n\tv_cvt_pk_bf16_f32 v117, v76, v77\n\tv_cvt_pk_bf16_f32 v121, v78, v79\n\t"
                     "v_cvt_pk_bf16_f32 v118, v80, v81\n\tv_bitop3_b32 v119, v116, v117, v114 bitop3:0x96\n\tv_cvt_pk_bf16_f32 v122, v82, v83\n\t"
                     "v_bitop3_b32 v123, v120, v121, v115 bitop3:0x96\n\tv_xor_b32 v119, v119, v118\n\tv_xor_b32 v123, v123, v122\n\t"
                     "s_cbranch_scc1 2b\n\t"
                     "global_store_dwordx4 %[so], v[116:119], %[bx]" SPOL "\n\tglobal_store_dwordx4 %[so], v[120:123], %[bx2]" SPOL "\n\t"
                     PV_T(62) "s_waitcnt lgkmcnt(0)\n\ts_nop 0"
                     : [need] "+v"(need), "={s[58:59]}"(tpv), "={s[60:61]}"(tp7), "={s[62:63]}"(tx1)
                     : [so] "v"(((ej * 16 + (lane >> 3)) * NWG + blockIdx.x) * 128 + (lane & 7) * 16), [ltag] "v"(line_tag(epoch, lane & 7)),
                       [bx] "s"(box), [bx2] "s"(box + 8 * NWG * 128), [aw] "s"(A_IMG - IMG_A + (ej >> 1) * 1024 + (ej & 1) * 8), [ar] "i"(A_IMG - IMG_A),
                       [nic] "i"(__builtin_bit_cast(unsigned, -1.f / CC)), [toff] "i"(__builtin_bit_cast(unsigned, TOFF)),
                       [lwo] "i"(CT_OFF + C_LW - PKM / 2), [lw] "i"(CT_OFF + C_LW), [x1] "i"(CT_OFF + C_X1), [x1n] "i"(X1N),
                       [apk] "v"(apk), [apm] "v"(lbase + PKM + r * 8), [apu] "v"(lbase + PKU + g * 64 + (r & 3) * 16),
                       [ox] "v"(lbase + tr_off(4 * g + qq, 4 * ej + p4)), [oy] "v"(lbase + 32 * ROWB + tr_off(2 * g + (qq >> 1), 4 * ej + p4)),
                       [ca] "v"(ct + (lane == 0 ? C_PFL + (k0 % NPK) * 4 : C_FULL + (k0 % NSLOT) * 4)),
                       [lim] "v"(ct + (lane == 0 ? C_PFL + NPK * 4 : C_FULL + NSLOT * 4)), [base] "v"(ct + (lane == 0 ? C_PFL : C_FULL)), [one] "v"(1u),
                       [k0] "s"(k0), [s0] "s"(helper ? ((TPW - 1) % NSLOT) * SLOT : 0), [e0] "s"(helper ? ((TPW - 1) % NPK) * 128 : 0),
                       [w2] "s"(box + W2_OFF + ej * 18432 - (lbase + IMG_A)), [fl] "s"(box + W2F_OFF),
                       [tag] "s"(u32x2{line_tag(epoch, 10), line_tag(epoch, 11)}), [gcap] "i"(GSPIN_CAP), [iss] "i"(CT_OFF + C_ISS), [cap] "i"(SPIN_CAP), [slot] "i"(SLOT), [ring] "i"(NSLOT * SLOT), [pring] "i"(NPK * 128),
                       [npk2] "i"(2 * NPK), [last] "i"(TPW - 1), [tight] "i"(TPW - 5),  [sch] "s"(W2SCH)
                     : "memory", "scc", "vcc", "s44", "s45", "s46", "s47", "s48", "s49", "s50", "s51", "s52", "s53", "s54", "s55", "s56", "s57",
                       "v0", "v1", "v2", "v3", "v4", "v5", "v6", "v7", "v8", "v9", "v10", "v11", "v12", "v13", "v14", "v15", "v16", "v17", "v18", "v19", "v20", "v21", "v22", "v23", "v24", "v25", "v26", "v27", "v28", "v29", "v30", "v31", "v32", "v33", "v34", "v35", "v36", "v37", "v38", "v39", "v40", "v41", "v42", "v43", "v44", "v45", "v46", "v47", "v48", "v49", "v50", "v51", "v52", "v53", "v54", "v55", "v56", "v57", "v58", "v59", "v60", "v61", "v62", "v63", "v64", "v65", "v66", "v67", "v68", "v69", "v70", "v71", "v72", "v73", "v74", "v75", "v76", "v77", "v78", "v79", "v80", "v81", "v82", "v83", "v84", "v85", "v86", "v87", "v88",
                       "v90", "v91", "v92", "v93", "v94", "v95", "v96", "v97", "v98", "v99", "v100", "v101", "v102", "v103", "v104", "v105", "v106", "v107",
                       "v108", "v109", "v110", "v111", "v112", "v113", "v114", "v115", "v116", "v117", "v118", "v119", "v120", "v121", "v122", "v123",
                       "v124", "v125", "v126", "v127", "v128", "v129", "v130", "v131", "v132", "v133", "v134", "v135", "v136", "v137", "v138", "v139",
                       "v140", "v141", "v142", "v143", "v144", "v145", "v146", "v147", "v148", "v149", "v150", "v151", "v152", "v153", "v154", "v155");
      }
    } else {
      LANE_VARS
      const int c = wave - 1 - NE;
      SCtx sc;
      sc.L0 = L0; sc.ct = ct; sc.apk = lbase + IMG_A + lane * 16; sc.apm = lbase + PKM + r * 8;
      sc.api = lbase + PKU + g * 64 + (r & 3) * 2 + ((r >> 2) == g ? 0 : 8); sc.amr = ct + C_MREF + r * 4;
      sc.o_rb = r * ROWB + ((g ^ swz(r)) << 4);
      sc.ev = (r & 1) ^ 1;
      sc.o_yb = (r & 1) ? ONES_OFF + (g << 4) : (32 + (r >> 1)) * ROWB + ((g ^ swz(r >> 1)) << 4);
      TNOW(tq0);
      if (c >= NEARLY) {
        static_assert(NS - NEARLY == 3, "");
        const int u = c - NEARLY;
        const unsigned epoch = epoch_load(box);
        const unsigned ta = line_tag(epoch, 8), tb = line_tag(epoch, 9);
        while ((unsigned)__builtin_amdgcn_s_memrealtime() - tent < QKWAIT) __builtin_amdgcn_s_sleep(4);
        u32x4 v[4];
#pragma unroll 1
        for (int sp = 0; sp < GSPIN_CAP; ++sp) {
          asm volatile("global_load_dwordx4 %0, %4, %5 sc1\n\tglobal_load_dwordx4 %1, %4, %5 offset:1024 sc1\n\tglobal_load_dwordx4 %2, %4, %5 offset:2048 sc1\n\t"
                       "global_load_dwordx4 %3, %4, %5 offset:3072 sc1\n\ts_waitcnt vmcnt(0)"
                       : "=&v"(v[0]), "=&v"(v[1]), "=&v"(v[2]), "=&v"(v[3]) : "v"(lane * 16), "s"(box + QK_OFF + u * 4096) : "memory");
          unsigned bad = 0;
#pragma unroll
          for (int t = 0; t < 4; ++t) bad |= (v[t][0] ^ v[t][1] ^ v[t][2] ^ ta) | (v[t][3] ^ tb);
          if (!__any(bad != 0u)) break;
        }
        TSET(tqa);
#pragma unroll
        for (int t = 0; t < 4; ++t)
          *(LDS_AS(u32x2)*)(L0 + IMG_A + (4 * u + t) * 512 + ((g >> 1) * 16 + r) * 16 + (g & 1) * 8) = u32x2{v[t][0], v[t][1]};
        asm volatile("" ::: "memory");
        lds_add(ct + C_QI, 1u);
        lds_st32(ct + C_EP, epoch);
      }
      static_assert(NE == 4 && NPK == 6 && NSLOT == 10, "");
      s_items(sc, helper ? TPW : c, helper || c >= NEARLY);
      const int hi = (HMAP >> (4 * c)) & 15;
      if (hi < 6 && (!helper || hi >= 4)) s_half(sc, TPW - NH1 + (hi >> 1), hi & 1);
      TNOW(tse);
      rslot = c;
    }
  }

  if (rslot >= 0 && ((blockIdx.x >> 3) & 3) == 1) {
    LANE_VARS
    __builtin_amdgcn_s_setprio(0);
    const int rid = ((blockIdx.x >> 5) << 3) | (blockIdx.x & 7);
    const int qa = ((rid >> 1) & 3) * 4 + ((rid >> 3) & 1) * 2;
    const char* src = box + (size_t)(rid * NWG + rslot * 32) * 128;
    const int e = lane & 7;
    const int oidx = (qa + (e & 1)) * CC + 48 * (rid >> 4) + 16 * (e < 6 ? e >> 1 : 0) + 8 * (rid & 1) + (lane >> 3);
    const unsigned short qv = ((const gus*)q)[oidx];
    u32x4 am;
#pragma unroll
    for (int d = 0; d < 4; ++d) am[d] = (e >> 1) == d ? ((e & 1) ? 0xffff0000u : 0x0000ffffu) : 0u;
    const unsigned epoch = *(LDS_AS(unsigned)*)(L0 + CT_OFF + C_EP);
    const unsigned arf = lbase + RED_OFF + 4096 + (lane & 1) * 4;
    poll_ge_sleep(ct + C_FREE + ((TPW - NPK) % NSLOT) * 4, 8u * ((TPW - NPK) / NSLOT + 1));
    lds_st32(arf + rslot * 8, 0u);
    unsigned long long tr;
#define R_LD(d, m, o) "s_mov_b64 exec, s[" m "]\n\tglobal_load_dwordx4 v[" d "], %[vo], %[src] offset:" #o " sc1\n\t"
#define R_W(l, a0, a1, a2, a3) "v_cvt_pk_bf16_f32 v" #a3 ", v" #a3 ", v" #a3 "\n\tv_and_b32 v" #a0 ", v60, v" #a3 "\n\tv_and_b32 v" #a1 ", v61, v" #a3 "\n\t" \
                               "v_and_b32 v" #a2 ", v62, v" #a3 "\n\tv_and_b32 v" #a3 ", v63, v" #a3 "\n\t"
#define R_G(n, w, m, l, d0, d1, d2, d3, a0, a1, a2, a3, c) \
  "s_mov_b64 exec, s[" m "]\n\ts_cbranch_execz " #n "f\n\ts_waitcnt vmcnt(" #w ")\n\tv_bitop3_b32 v" #l ", v" #d0 ", v" #d1 ", v" #d2 " bitop3:0x96\n\t" \
  "v_bitop3_b32 v" #l ", v" #l ", v" #d3 ", %[tag] bitop3:0x96\n\tv_mov_b32 v" #d3 ", s49\n\ts_mov_b64 exec, -1\n\tv_exp_f32 v" #a3 ", v" #l "\n\t" \
  "v_xor_b32_dpp v59, v" #l ", v" #l " quad_perm:[2,3,0,1] row_mask:0xf bank_mask:0x5\n\tv_xor_b32_dpp v59, v" #l ", v" #l " row_shr:4 row_mask:0xf bank_mask:0xa\n\t" \
  R_W(l, a0, a1, a2, a3) "v_cmp_ne_u32 s[" m "], 0, v59\n\ts_nop 0\n" \
  "1" #n ":\n\tv_mfma_f32_16x16x32_bf16 v[36:39], v[" #a0 ":" #a3 "], v[" #d0 ":" #d3 "], " c "\n\t"
#define R_X(n) #n ":\n\ts_mov_b64 exec, -1\n\ts_nop 2\n\ts_branch 1" #n "b\n"
#define R_F(l, d0, d3, a0, a1, a2, a3, c) "v_sub_f32 v" #a3 ", v" #l ", v56\n\tv_exp_f32 v" #a3 ", v" #a3 "\n\ts_nop 0\n\t" R_W(l, a0, a1, a2, a3) \
  "s_nop 1\n\tv_mfma_f32_16x16x32_bf16 v[36:39], v[" #a0 ":" #a3 "], v[" #d0 ":" #d3 "], " c "\n\t"
#define R_RD(d, a, b) "ds_read2st64_b32 v[" d "], %[ard] offset0:" #a " offset1:" #b "\n\t"
#define R_A(d, a) "v_pk_add_f32 v[" d "], v[" d "], v[" a "]\n\t"
#define R_S(x, f) "v_sub_f32 v" #f ", v" #f ", v56\n\tv_exp_f32 v" #f ", v" #f "\n\ts_nop 0\n\tv_mul_f32 v[" #x "], v[" #x "], v" #f "\n\tv_mul_f32 v[" #x " + 1], v[" #x " + 1], v" #f "\n\t"
    asm volatile("s_mov_b64 s[52:53], 0\n\ts_mov_b64 s[40:41], -1\n\ts_mov_b64 s[42:43], -1\n\ts_mov_b64 s[44:45], -1\n\ts_mov_b64 s[46:47], -1\n\ts_mov_b32 s48, %[cap]\n\ts_mov_b32 s49, 0x3f803f80\n\t"
                 "v_mov_b32 v57, 1\n\tv_mov_b32 v58, %[x1]\n"
                 "20:\n\ts_sleep 1\n\tds_read_b32 v56, v58\n\ts_sub_u32 s48, s48, 1\n\ts_cselect_b32 s50, 0, %[x1n]\n\ts_waitcnt lgkmcnt(0)\n\t"
                 "v_readfirstlane_b32 s51, v56\n\ts_cmp_lt_u32 s51, s50\n\ts_cbranch_scc1 20b\n\tv_mov_b32 v58, %[red]\n\ts_mov_b32 s48, %[gcap]\n\ts_sleep %[rs]\n"
                 "21:\n\t" R_LD("0:3", "40:41", 0) R_LD("4:7", "42:43", 1024) R_LD("8:11", "44:45", 2048) R_LD("12:15", "46:47", 3072)
                 "s_cmp_eq_u32 s48, %[gcap]\n\ts_cbranch_scc1 24f\n\ts_waitcnt vmcnt(0)\n24:\n\t"
                 R_G(1, 3, "40:41", 32, 0, 1, 2, 3, 16, 17, 18, 19, "0") R_G(2, 2, "42:43", 33, 4, 5, 6, 7, 20, 21, 22, 23, "v[36:39]")
                 R_G(3, 1, "44:45", 34, 8, 9, 10, 11, 24, 25, 26, 27, "v[36:39]") R_G(4, 0, "46:47", 35, 12, 13, 14, 15, 28, 29, 30, 31, "v[36:39]")
                 "v_max3_f32 v56, v32, v33, v34\n\ts_sub_u32 s48, s48, 1\n\ts_cbranch_scc1 22f\n\ts_or_b64 s[50:51], s[40:41], s[42:43]\n\tv_max_f32 v56, v56, v35\n\t"
                 "s_or_b64 s[50:51], s[50:51], s[44:45]\n\ts_or_b64 s[50:51], s[50:51], s[46:47]\n\tv_cmp_nlt_f32 vcc, |v56|, %[lim]\n\ts_cbranch_scc1 21b\n\t"
                 "s_cbranch_vccnz 25f\n"
                 "22:\n\ts_mov_b64 exec, %[wm]\n\tds_write_b128 %[awr], v[36:39]\n\ts_mov_b64 exec, 1\n\tds_add_rtn_u32 v59, v58, v57\n\ts_mov_b64 exec, -1\n\t"
                 R_RD("40:41", 0, 1) R_RD("42:43", 2, 3) R_RD("44:45", 4, 5) R_RD("46:47", 6, 7) R_RD("48:49", 8, 9) R_RD("50:51", 10, 11) R_RD("52:53", 12, 13) R_RD("54:55", 14, 15)
                 "s_waitcnt lgkmcnt(8)\n\tv_add_u32 v59, v59, v57\n\ts_nop 0\n\tv_readfirstlane_b32 s50, v59\n\ts_cmp_lg_u32 s50, 8\n\ts_cbranch_scc1 26f\n"
                 "23:\n\ts_waitcnt lgkmcnt(6)\n\t" R_A("40:41", "42:43") "s_waitcnt lgkmcnt(4)\n\t" R_A("44:45", "46:47") "s_waitcnt lgkmcnt(2)\n\t" R_A("48:49", "50:51")
                 R_A("40:41", "44:45") "s_waitcnt lgkmcnt(0)\n\t" R_A("52:53", "54:55") R_A("48:49", "52:53") R_A("40:41", "48:49")
                 "v_add_f32 v40, v40, v41\n\ts_nop 1\n\tv_mov_b32_dpp v41, v40 quad_perm:[2,3,2,3] row_mask:0xf bank_mask:0xf\n\ts_nop 1\n\t"
                 "v_mov_b32_dpp v41, v41 row_shl:4 row_mask:0xf bank_mask:0x5\n\tv_rcp_f32 v41, v41\n\ts_mov_b64 exec, %[om]\n\tv_mul_f32 v40, v40, v41\n\t"
                 "v_cvt_pk_bf16_f32 v40, v40, v40\n\tv_lshlrev_b32 v40, 16, v40\n\tv_add_f32 v40, v40, %[qf]\n\tv_cvt_pk_bf16_f32 v40, v40, v40\n\t"
                 "global_store_short %[oo], v40, %[out]\n\t"
                 "s_cmp_lg_u32 %[rid], 0\n\ts_cbranch_scc1 29f\n\ts_mov_b64 exec, 1\n\tv_mov_b32 v41, %[eo]\n\tglobal_store_dword v41, %[ep], %[bx] sc1\n\ts_branch 29f\n"
                 R_X(1) R_X(2) R_X(3) R_X(4)
                 "25:\n\tv_max_f32_dpp v56, v56, v56 row_ror:8 row_mask:0xf bank_mask:0xf\n\tv_mov_b32 v59, v56\n\ts_nop 1\n\tv_permlane16_swap_b32 v56, v59\n\tv_max_f32 v56, v56, v59\n\t"
                 "v_mov_b32 v59, v56\n\ts_nop 1\n\tv_permlane32_swap_b32 v56, v59\n\tv_max_f32 v56, v56, v59\n\tv_mov_b32 v57, 0x101\n\tv_add_u32 v59, %[rs8], %[arf]\n\tds_write_b32 v59, v56\n\t"
                 R_F(32, 0, 3, 16, 17, 18, 19, "0") R_F(33, 4, 7, 20, 21, 22, 23, "v[36:39]") R_F(34, 8, 11, 24, 25, 26, 27, "v[36:39]") R_F(35, 12, 15, 28, 29, 30, 31, "v[36:39]")
                 "s_nop 7\n\ts_branch 22b\n"
                 "26:\n\ts_waitcnt vmcnt(0)\n\ts_and_b32 s50, s50, 0xff\n\ts_cmp_lg_u32 s50, 8\n\ts_cbranch_scc1 29f\n\t"
                 "ds_read2_b32 v[0:1], %[arf] offset1:2\n\tds_read2_b32 v[2:3], %[arf] offset0:4 offset1:6\n\tds_read2_b32 v[4:5], %[arf] offset0:8 offset1:10\n\t"
                 "ds_read2_b32 v[6:7], %[arf] offset0:12 offset1:14\n\ts_waitcnt lgkmcnt(0)\n\tv_max3_f32 v56, v0, v1, v2\n\tv_max3_f32 v56, v56, v3, v4\n\t"
                 "v_max3_f32 v56, v56, v5, v6\n\tv_max_f32 v56, v56, v7\n\t"
                 R_S(40, 0) R_S(42, 1) R_S(44, 2) R_S(46, 3) R_S(48, 4) R_S(50, 5) R_S(52, 6) R_S(54, 7) "s_branch 23b\n"
                 "29:\n\ts_mov_b64 exec, -1\n\ts_waitcnt lgkmcnt(0)"
                 : "=&{s[52:53]}"(tr)
                 : "{v[60:63]}"(am), [vo] "v"(lane * 16), [src] "s"(src), [tag] "v"(line_tag(epoch, e)),
                   [awr] "v"(lbase + RED_OFF + (rslot * 2 + (r >> 3)) * 256 + (r & 7) * 32 + (g & 1) * 16), [ard] "v"(lbase + RED_OFF + lane * 4), [arf] "v"(arf),
                   [qf] "v"((unsigned)qv << 16), [oo] "v"(oidx * 2), [out] "s"(KARG(kb, 0)), [ep] "v"(epoch + 1u), [bx] "s"(box), [rid] "s"(rid), [rs8] "s"(rslot * 8),
                   [wm] "s"(0xff00ff0000ff00ffull), [om] "s"(0x3f3f3f3f3f3f3f3full), [lim] "s"(64.f),
                   [cap] "i"(SPIN_CAP), [gcap] "i"(GSPIN_CAP), [x1] "i"(CT_OFF + C_X1), [x1n] "i"(X1N), [red] "i"(CT_OFF + C_RED), [rs] "i"(RSLEEP), [eo] "i"(EPOCH_OFF)
                 : "memory", "scc", "vcc", "s40", "s41", "s42", "s43", "s44", "s45", "s46", "s47", "s48", "s49", "s50", "s51", "v0", "v1", "v2", "v3", "v4", "v5", "v6", "v7", "v8", "v9", "v10", "v11", "v12", "v13", "v14", "v15", "v16", "v17", "v18", "v19", "v20", "v21", "v22", "v23", "v24", "v25", "v26", "v27", "v28", "v29", "v30", "v31", "v32", "v33", "v34", "v35", "v36", "v37", "v38", "v39", "v40", "v41", "v42", "v43", "v44", "v45", "v46", "v47", "v48", "v49", "v50", "v51", "v52", "v53", "v54", "v55", "v56", "v57", "v58", "v59");
  }
}

#undef q
#undef f0
#undef f1
#undef wq
#undef wk
#undef wv
#undef wo
#undef box
extern "C" __global__ void dino_end() {}

extern "C" void kernel_launch(void* const* d_in, const int* in_sizes, int n_in,
                              void* d_out, int out_size, void* d_ws, size_t ws_size,
                              hipStream_t stream) {
  dino_main<<<NWG, 768, 0, stream>>>((const unsigned short*)d_in[0], (const char*)d_in[1], (const char*)d_in[2], (const char*)d_in[3],
                                     (const char*)d_in[4], (const char*)d_in[5], (const char*)d_in[6], (char*)d_ws + WSOFF,
                                     (unsigned short*)d_out);
}
```

```cpp
#include <hip/hip_runtime.h>
#include <hip/hip_bf16.h>
#include <stdint.h>

#define PROFOFF (380ll << 20)
#define TNOW(v) do {} while (0)
#define TPUT(id, v) do {} while (0)

using bf16x8  = __attribute__((ext_vector_type(8))) short;
using f32x4   = __attribute__((ext_vector_type(4))) float;
using f32x2   = __attribute__((ext_vector_type(2))) float;
using u32x4   = __attribute__((ext_vector_type(4))) unsigned;
using u32x2   = __attribute__((ext_vector_type(2))) unsigned;
using v4i16_t = __attribute__((ext_vector_type(4))) short;
typedef __attribute__((address_space(3))) char* lds_ptr;
#define LDS_AS(T) __attribute__((address_space(3))) T

constexpr int CC = 192, IMW = 512;
constexpr int NWG = 256, NS = 7, NE = 4;
constexpr int TPW = 33;
constexpr int ROWB = 384;
constexpr int SLOT = 40 * ROWB;
constexpr int NSLOT = 10;
constexpr int BOX_BYTES = 64 * NWG * 128, EPOCH_OFF = BOX_BYTES, QK_OFF = EPOCH_OFF + 4096, W2F_OFF = QK_OFF + 12288, W2_OFF = W2F_OFF + 1024;
constexpr int GSPIN_CAP = 1 << 16;
#define PACE16 777
#define QKWAIT 300
#define NEARLY 4
#define PRECLR 4
#define RSLEEP 24
#define HDUE 93
#define PACES 810
#define PK1 14
#define AG 100
#define AHI 22
#define ADL 60
#define AMG 200
#define NORELAY
#define HMAP 0x4321085
#define HB 1
#define WSOFF ((ws_size - (4u << 20)) & ~(size_t)0x1fffff)
#define EPM 0
#define RSTART 1880
#define RMODE 1
#define CLRK 17
#define X1N (PRECLR == 5 ? 11u : 5u)
#define RPOL " sc1"
#define SPOL " sc1"
#define RGAP 0
#define RDRY 0
#define RWAIT 76
#define RMIN 30
#define PROBE 0xffffffffffffffffull
#define RELAYT 0
#define PRE5 1
#define SL_S 2
#define SL_FAR 12
#define SL_P 2
#define SL_L 1
#define STR_(x) #x
#define STR(x) STR_(x)
#define AHIR AHI
#define OTOUCH 0
#define TLBPF 1
#define T0BACK 7
#define W2SCH 0x0311180000000000ull
#define NH1 3
constexpr int NPK = 6;
constexpr float EPS = 1e-5f;
constexpr float SQRT_C = 13.856406460551018f;
constexpr float LOG2E = 1.4426950408889634f;
constexpr float THR = 20.0f;
constexpr float TOFF = 192.0f;
constexpr int SPIN_CAP = 1 << 22;

constexpr int IMG_A = NSLOT * SLOT;
constexpr int CT_OFF = IMG_A + 6144;
constexpr int PKM = CT_OFF + 512, PKU = PKM + NPK * 128;
constexpr int ONES_OFF = PKU + NPK * 256;
constexpr int JUNK = ONES_OFF + ROWB;
constexpr int LDS_BYTES = JUNK + 256;
constexpr int H_R = 192 * ROWB, H_Q = H_R + 32 * ROWB, H_C = H_Q + 6144;
#define PWIN 35
static_assert(LDS_BYTES <= 163840 && (PKU & 15) == 0 && (ONES_OFF & 127) == 0, "lds");
constexpr int C_FULL = 0, C_FREE = 64, C_EP = 128, C_RED = 144, C_ISS = 152, C_X1 = 156, C_QI = 160, C_PFL = 192, C_CONS = 256,
              C_MREF = 320, C_LW = 384;

typedef __bf16 bf16x2_t __attribute__((ext_vector_type(2)));
__device__ __forceinline__ unsigned cvtpk(float lo, float hi) {
  return __builtin_bit_cast(unsigned, __builtin_convertvector(f32x2{lo, hi}, bf16x2_t));
}
__device__ __forceinline__ float rbf(float x) { return __uint_as_float(cvtpk(x, x) << 16); }
__device__ __forceinline__ float bflo(unsigned p) { return __uint_as_float(p << 16); }
__device__ __forceinline__ float bfhi(unsigned p) { return __uint_as_float(p & 0xffff0000u); }
__device__ __forceinline__ bf16x8 pack8(const f32x4& a, const f32x4& b) {
  return __builtin_bit_cast(bf16x8, u32x4{cvtpk(a[0], a[1]), cvtpk(a[2], a[3]), cvtpk(b[0], b[1]), cvtpk(b[2], b[3])});
}
__device__ __forceinline__ float xrow16_max(float x) {
  auto s = __builtin_amdgcn_permlane16_swap(__float_as_uint(x), __float_as_uint(x), false, false);
  x = fmaxf(__uint_as_float(s[0]), __uint_as_float(s[1]));
  auto t = __builtin_amdgcn_permlane32_swap(__float_as_uint(x), __float_as_uint(x), false, false);
  return fmaxf(__uint_as_float(t[0]), __uint_as_float(t[1]));
}
__device__ __forceinline__ float xrow16_sum(float x) {
  auto s = __builtin_amdgcn_permlane16_swap(__float_as_uint(x), __float_as_uint(x), false, false);
  x = __uint_as_float(s[0]) + __uint_as_float(s[1]);
  auto t = __builtin_amdgcn_permlane32_swap(__float_as_uint(x), __float_as_uint(x), false, false);
  return __uint_as_float(t[0]) + __uint_as_float(t[1]);
}
__device__ __forceinline__ v4i16_t tr_read(lds_ptr p) {
  return __builtin_amdgcn_ds_read_tr16_b64_v4i16((LDS_AS(v4i16_t)*)p);
}
__device__ __forceinline__ f32x4 mfma(bf16x8 a, bf16x8 b, f32x4 c) {
  return __builtin_amdgcn_mfma_f32_16x16x32_bf16(a, b, c, 0, 0, 0);
}
__device__ __forceinline__ int swz(int p) { return (p & 6) | ((p >> 2) & 1); }
constexpr unsigned swz_tbl(int i) {
  unsigned t = 0;
  for (int j = 0; j < 8; ++j) { const int p = (8 * i + j) / 3; t |= (unsigned)((p & 6) | ((p >> 2) & 1)) << (4 * j); }
  return t;
}
__device__ __forceinline__ bf16x8 mk8(v4i16_t a, v4i16_t b) { return bf16x8{a[0], a[1], a[2], a[3], b[0], b[1], b[2], b[3]}; }

__device__ __forceinline__ void lds_st32(unsigned addr, unsigned v) {
  asm volatile("ds_write_b32 %0, %1" : : "v"(addr), "v"(v) : "memory");
}
__device__ __forceinline__ unsigned lds_ld32(unsigned addr) {
  unsigned v;
  asm volatile("ds_read_b32 %0, %1\n\ts_waitcnt lgkmcnt(0)" : "=v"(v) : "v"(addr) : "memory");
  return __builtin_amdgcn_readfirstlane(v);
}
__device__ __forceinline__ void lds_add(unsigned addr, unsigned n) {
  asm volatile("s_mov_b64 exec, 1\n\tds_add_u32 %0, %1\n\ts_mov_b64 exec, -1\n\ts_nop 3" : : "v"(addr), "v"(n) : "memory");
}
__device__ __forceinline__ void poll_ge(unsigned addr, unsigned need) {
  unsigned v, s, n2, cnt = SPIN_CAP;
  asm volatile("1:\n\tds_read_b32 %0, %4\n\ts_sub_u32 %3, %3, 1\n\ts_cselect_b32 %2, 0, %5\n\ts_waitcnt lgkmcnt(0)\n\t"
               "v_readfirstlane_b32 %1, %0\n\ts_cmp_lt_u32 %1, %2\n\ts_cbranch_scc1 1b"
               : "=&v"(v), "=&s"(s), "=&s"(n2), "+s"(cnt) : "v"(addr), "s"(need) : "memory", "scc");
}
__device__ __forceinline__ void poll_ge_sleep(unsigned addr, unsigned need) {
  need = __builtin_amdgcn_readfirstlane(need);
  unsigned v, s, n2, cnt = SPIN_CAP;
  asm volatile("s_branch 2f\n1:\n\ts_sleep " STR(SL_S) "\n2:\n\tds_read_b32 %0, %4\n\ts_sub_u32 %3, %3, 1\n\ts_cselect_b32 %2, 0, %5\n\ts_waitcnt lgkmcnt(0)\n\t"
               "v_readfirstlane_b32 %1, %0\n\ts_cmp_lt_u32 %1, %2\n\ts_cbranch_scc1 1b"
               : "=&v"(v), "=&s"(s), "=&s"(n2), "+s"(cnt) : "v"(addr), "s"(need) : "memory", "scc");
}
__device__ __forceinline__ bf16x8 img_ld(lds_ptr L0, int off, int kk, int lane) { return *(LDS_AS(bf16x8)*)(L0 + off + kk * 1024 + lane * 16); }
__device__ __forceinline__ void img_st(lds_ptr L0, int off, int kk, int lane, bf16x8 v) { *(LDS_AS(bf16x8)*)(L0 + off + kk * 1024 + lane * 16) = v; }

template <int N> __device__ __forceinline__ void wait_vm() { asm volatile("s_waitcnt vmcnt(%0)" :: "i"(N) : "memory"); }
constexpr int A_IMG = ((TPW + 1 - NPK) % NSLOT) * SLOT, RED_OFF = ((TPW - NPK) % NSLOT) * SLOT;
__device__ __forceinline__ unsigned line_tag(unsigned epoch, unsigned i) {
  unsigned x = (epoch + 1u + i * 0x9E3779B9u) * 0x85EBCA6Bu;
  x ^= x >> 15; x *= 0xC2B2AE35u;
  return x ^ (x >> 13);
}
__device__ __forceinline__ unsigned epoch_load(const char* box) {
  unsigned e;
  asm volatile("global_load_dword %0, %1, %2 sc1\n\ts_waitcnt vmcnt(0)" : "=v"(e) : "v"(EPOCH_OFF), "s"(box) : "memory");
  return __builtin_amdgcn_readfirstlane(e);
}
__device__ __forceinline__ int tr_off(int row, int col4) {
  const int ch = col4 >> 1;
  return row * ROWB + (((ch & ~7) | ((ch & 7) ^ swz(row & 15))) << 4) + 8 * (col4 & 1);
}
#define PIN3(a, i) "+v"(a[i]), "+v"(a[i + 1]), "+v"(a[i + 2])

#define DMA1(d, b, off, pol) "global_load_lds_dwordx4 %[" #d "], %[" #b "] offset:" #off pol "\n\t"
__device__ __forceinline__ void issue_rows16(const char* a0, unsigned dst, const unsigned (&d)[3]) {
  unsigned keep;
  asm volatile("s_mov_b32 %[keep], m0\n\ts_mov_b32 m0, %[dst]\n\ts_nop 0\n\t"
               DMA1(d0, a, 0, "") DMA1(d1, a, 1024, "") DMA1(d2, a, 2048, "") DMA1(d0, a, 3072, "")
               "s_add_u32 m0, %[dst], 0x1000\n\ts_nop 0\n\t" DMA1(d1, b, 0, "") DMA1(d2, b, 1024, "") "s_mov_b32 m0, %[keep]"
               : [keep] "=&s"(keep) : [dst] "s"(dst), [a] "s"(a0), [b] "s"(a0 + 4096), [d0] "v"(d[0]), [d1] "v"(d[1]), [d2] "v"(d[2]) : "memory", "scc");
}
#define LD_VM "s_getreg_b32 s43, hwreg(HW_REG_IB_STS, 22, 2)\n\ts_getreg_b32 s42, hwreg(HW_REG_IB_STS, 0, 4)\n\ts_lshl4_add_u32 s42, s43, s42\n\t"
#define LD_GATE(n, X) \
  #n "0:\n\t" LD_VM "s_cmp_le_i32 s42, s46\n\ts_cbranch_scc1 " #n "2f\n" \
  #n "1:\n\t" X "s_cmp_gt_u32 s42, %[win]\n\ts_cbranch_scc1 " #n "5f\n\ts_cmp_ge_u32 s45, s44\n\ts_cbranch_scc1 " #n "7f\n" \
  #n "6:\n\ts_memrealtime s[40:41]\n\ts_sleep " STR(SL_L) "\n\ts_waitcnt lgkmcnt(0)\n\ts_sub_u32 s45, s40, %[t0]\n\ts_lshl_b32 s45, s45, 4\n\ts_cmp_lt_u32 s45, s44\n\ts_cbranch_scc1 " #n "6b\n" \
  #n "7:\n\t" LD_VM
#define LD_NEXT \
  "s_cmp_gt_u32 s42, %[ahi]\n\ts_cselect_b32 s42, %[amg], 0\n\ts_sub_u32 s43, s45, s44\n\ts_cmp_lt_u32 s43, s42\n\ts_cselect_b32 s42, %[p3] + %[adl], s39\n\t" \
  "s_add_u32 s44, s44, s42\n\ts_add_u32 s46, s46, 5\n\t"
#define LD_PUB0(n, a, b, c) \
  #n "2:\n\tv_add_u32 %[pub], 2, %[pub]\n\ts_cmp_ge_u32 s46, 30\n\ts_cbranch_scc1 " #n "4f\n\ts_cmp_ge_u32 s46, 15\n\ts_cbranch_scc1 " #n "3f\n\t" \
  "s_waitcnt vmcnt(" #a ")\n" #n "3:\n\ts_waitcnt vmcnt(" #b ")\n" #n "4:\n\ts_waitcnt vmcnt(" #c ")\n\tds_write_b32 %[af], %[pub] ; NOTRACK\n\t" \
  "s_sub_u32 s46, s46, 15\n\ts_branch " #n "1b\n"
#define LD_PUB(n, a, b, c) LD_PUB0(n, a, b, c) \
  #n "5:\n\ts_sleep 1\n\ts_sub_i32 s47, s47, 1\n\ts_cmp_gt_i32 s47, 0\n\ts_cbranch_scc1 " #n "0b\n\ts_waitcnt vmcnt(%[win])\n\ts_branch " #n "6b\n"
#define LD1(d, b, off) "global_load_lds_dwordx4 %[" #d "], s[" #b "] offset:" #off " nt\n\t"

#define PV_REQ(P, U, M, C, CA) \
  "v_lshl_add_u32 v150, s44, 3, %[apk]\n\tv_lshl_add_u32 v151, s44, 1, %[apu]\n\tv_add_u32 v152, s44, %[apm]\n\t" \
  "ds_read_b32 v" #C ", v" #CA "\n\tds_read_b128 v[" P "], v150\n\tds_read_b64 v[" M "], v152\n\tds_read_b128 v[" U "], v151\n\t"
#define PV_TR(d0, d1, a, o) "ds_read_b64_tr_b16 v[" #d0 ":" #d1 "], v" #a " offset:" #o "\n\t"
#define PV_FETCH(y0, y0b, a0, a0b, b0, b0b, y1, y1b, a1, a1b, b1, b1b, y2, y2b, a2, a2b, b2, b2b) \
  "v_add_u32 v153, s45, %[ox]\n\tv_add_u32 v154, s45, %[oy]\n\t" \
  PV_TR(a0, a0b, 153, 0) PV_TR(b0, b0b, 153, 6144) PV_TR(a1, a1b, 153, 128) PV_TR(b1, b1b, 153, 6272) PV_TR(a2, a2b, 153, 256) PV_TR(b2, b2b, 153, 6400) \
  PV_TR(y0, y0b, 154, 0) PV_TR(y1, y1b, 154, 128) PV_TR(y2, y2b, 154, 256) \
  "s_add_u32 s45, s45, %[slot]\n\ts_cmp_eq_u32 s45, %[ring]\n\ts_cselect_b32 s45, 0, s45\n\t"
#define PV_FETCH_A PV_FETCH(90, 91, 92, 93, 94, 95, 96, 97, 98, 99, 100, 101, 102, 103, 104, 105, 106, 107)
#define PV_FETCH_B PV_FETCH(108, 109, 110, 111, 112, 113, 114, 115, 116, 117, 118, 119, 120, 121, 122, 123, 124, 125)
#define PV_MF(d, a, p) "v_mfma_f32_16x16x32_bf16 v[" d "], v[" a "], v[" p "], v[" d "]\n\t"
#define PV_MOV(d, s) "v_mov_b32 v" #d ", v" #s "\n\t"
#define PV_WAITF(n, CA)   \
  #n ":\n\tds_read_b32 v155, v" #CA "\n\ts_sub_i32 s47, s47, 1\n\ts_cmp_lt_i32 s47, 0\n\ts_cbranch_scc1 " #n "9f\n\ts_waitcnt lgkmcnt(0)\n\t" \
  "v_cmp_lt_u32 vcc, v155, %[need]\n\ts_and_b64 vcc, vcc, 2\n\ts_cbranch_scc0 " #n "9f\n\ts_sleep " STR(SL_P) "\n\ts_branch " #n "b\n" #n "9:\n\ts_waitcnt lgkmcnt(0)\n\t"
#define PV_STEP(n, P, U, M, m0, m1, C, CA, P2, U2, M2, C2, CA2, X0, X1, X2, Y0, Y1, Y2, MV0, MV1, MV2, HOOK) \
  "s_waitcnt lgkmcnt(10)\n\t" \
  "v_cmp_lt_u32 vcc, v" #C ", %[need]\n\tv_cmp_neq_f32 s[48:49], v" #m0 ", v88\n\tv_cmp_neq_f32 s[50:51], v" #m1 ", v88\n\t" \
  "v_add_u32 v" #CA2 ", 4, v" #CA "\n\ts_or_b64 s[48:49], s[48:49], vcc\n\tv_cmp_eq_u32 s[52:53], v" #CA2 ", %[lim]\n\t" \
  "s_or_b64 s[48:49], s[48:49], s[50:51]\n\ts_cbranch_scc1 " #n "1f\n" \
  #n "0:\n\ts_add_u32 s44, s44, 128\n\ts_cmp_eq_u32 s44, %[pring]\n\ts_cselect_b32 s44, 0, s44\n\t" \
  "v_cndmask_b32 v" #CA2 ", v" #CA2 ", %[base], s[52:53]\n\t" \
  PV_MF("84:87", U, P) PV_MF("72:75", X0, P) PV_MF("76:79", X1, P) MV0 PV_MF("80:83", X2, P) MV1 \
  PV_MF("72:75", Y0, P) MV2 HOOK PV_REQ(P2, U2, M2, C2, CA2) PV_MF("76:79", Y1, P) PV_MF("80:83", Y2, P)
#define PV_REL(CA) \
  "s_mov_b64 exec, 3\n\tds_add_u32 v" #CA ", %[one] offset:64\n\ts_mov_b64 exec, -1\n\tv_add_u32 %[need], 2, %[need]\n\ts_add_u32 s46, s46, 1\n\t"
#define PV_SC1(p) "v_lshlrev_b32 v151, 16, v" #p "\n\tv_and_b32 v152, 0xffff0000, v" #p "\n\tv_mul_f32 v151, v151, v150\n\tv_mul_f32 v152, v152, v150\n\t" \
                  "v_cvt_pk_bf16_f32 v" #p ", v151, v152\n\t"
#define PV_MUL4(a, b, c, d) "v_mul_f32 v" #a ", v" #a ", v150\n\tv_mul_f32 v" #b ", v" #b ", v150\n\tv_mul_f32 v" #c ", v" #c ", v150\n\tv_mul_f32 v" #d ", v" #d ", v150\n\t"
#define PV_SLOW(n, P, U, M, m0, m1, C, CA, p0, p1, p2, p3) \
  #n "1:\n\ts_bitcmp0_b32 vcc_lo, 0\n\ts_cbranch_scc1 " #n "4f\n\ts_cmp_gt_u32 s46, %[tight]\n\ts_cbranch_scc1 " #n "3f\n" \
  #n "2:\n\ts_sleep " STR(SL_P) "\n\tds_read_b32 v" #C ", v" #CA "\n\ts_sub_i32 s47, s47, 1\n\ts_cmp_lt_i32 s47, 0\n\ts_cbranch_scc1 " #n "3f\n\ts_waitcnt lgkmcnt(0)\n\t" \
  "v_cmp_lt_u32 vcc, v" #C ", %[need]\n\ts_and_b64 vcc, vcc, 1\n\ts_cbranch_scc1 " #n "2b\n" \
  #n "3:\n\t" PV_REQ(P, U, M, C, CA) "s_sub_i32 s47, s47, 1\n\ts_cmp_lt_i32 s47, 0\n\ts_cselect_b32 s54, 0, 1\n\ts_waitcnt lgkmcnt(0)\n\t" \
  "v_cmp_lt_u32 vcc, v" #C ", %[need]\n\tv_cmp_neq_f32 s[48:49], v" #m0 ", v88\n\tv_cmp_neq_f32 s[50:51], v" #m1 ", v88\n\t" \
  "s_and_b32 s54, s54, vcc_lo\n\ts_cbranch_scc1 " #n "3b\n\ts_or_b64 s[48:49], s[48:49], s[50:51]\n\ts_cbranch_scc0 " #n "0b\n\ts_branch " #n "5f\n" \
  #n "4:\n\tv_cmp_neq_f32 s[48:49], v" #m0 ", v88\n\ts_or_b64 s[48:49], s[48:49], s[50:51]\n\ts_cbranch_scc0 " #n "0b\n" \
  #n "5:\n\tv_mov_b32 v150, v88\n\tv_max3_f32 v88, v88, v" #m0 ", v" #m1 "\n\tv_sub_f32 v150, v150, v88\n\tv_exp_f32 v150, v150\n\ts_nop 1\n\t" \
  PV_MUL4(72, 73, 74, 75) PV_MUL4(76, 77, 78, 79) PV_MUL4(80, 81, 82, 83) PV_MUL4(84, 85, 86, 87) \
  "v_sub_f32 v150, v" #m0 ", v88\n\tv_exp_f32 v150, v150\n\ts_nop 1\n\t" PV_SC1(p0) PV_SC1(p1) \
  "v_sub_f32 v150, v" #m1 ", v88\n\tv_exp_f32 v150, v150\n\ts_nop 1\n\t" PV_SC1(p2) PV_SC1(p3) \
  "s_branch " #n "0b\n"
#define PV_W2(a0, a1, b0, b1, c0, c1) \
  "global_load_dwordx4 v[" #a0 ":" #a1 "], v150, %[w2] sc1\n\tglobal_load_dwordx4 v[" #b0 ":" #b1 "], v150, %[w2] offset:1024 sc1\n\t" \
  "global_load_dwordx4 v[" #c0 ":" #c1 "], v150, %[w2] offset:2048 sc1\n\tv_add_u32 v150, 0xc00, v150\n\ts_sub_u32 s54, s54, 1\n\ts_cbranch_scc1 30b\n\t"

#define PV_NRM(a, b, c, d, o)   \
  "v_pk_fma_f32 v[" #a ":" #b "], v[" #a ":" #b "], v[150:151], v[150:151] op_sel:[0,0,1] op_sel_hi:[1,0,1]\n\t" \
  "v_pk_fma_f32 v[" #c ":" #d "], v[" #c ":" #d "], v[150:151], v[150:151] op_sel:[0,0,1] op_sel_hi:[1,0,1]\n\t" \
  "v_cvt_pk_bf16_f32 v" #a ", v" #a ", v" #b "\n\tv_cvt_pk_bf16_f32 v" #b ", v" #c ", v" #d "\n\tds_write_b64 v152, v[" #a ":" #b "] offset:" #o "\n\t"
#define PV_G(n, a, w0, w1, w2, c0, c1, c2) "s_waitcnt lgkmcnt(" #n ")\n\tv_mfma_f32_16x16x32_bf16 v[72:75], v[" a "], v[" w0 "], " c0 "\n\t" \
  "v_mfma_f32_16x16x32_bf16 v[76:79], v[" a "], v[" w1 "], " c1 "\n\tv_mfma_f32_16x16x32_bf16 v[80:83], v[" a "], v[" w2 "], " c2 "\n\t"
#define PV_Z4(a, b, c, d) "v_mov_b32 v" #a ", 0\n\tv_mov_b32 v" #b ", 0\n\tv_mov_b32 v" #c ", 0\n\tv_mov_b32 v" #d ", 0\n\t"
#define PV_T(n)

using u32x8 = __attribute__((ext_vector_type(8))) unsigned;
struct SCtx {
  lds_ptr L0;
  unsigned ct, apk, apm, api, amr;
  int o_rb, o_yb, ev;
  u32x8 q[3];
  unsigned tq;
};
__device__ __forceinline__ bf16x8 s_qf(const SCtx& c, int j) {
  const u32x8& v = c.q[j >> 1];
  const int o = 4 * (j & 1);
  return __builtin_bit_cast(bf16x8, u32x4{v[o], v[o + 1], v[o + 2], v[o + 3]});
}
#define S_M(d, a, b) "v_mfma_f32_16x16x32_bf16 v[" d "], v[" a "], v[" b "], v[" d "]\n\t"
#define S_M0(d, a, b) "v_mfma_f32_16x16x32_bf16 v[" d "], v[" a "], v[" b "], 0\n\t"
#define S_R(d, a, o) "ds_read_b128 v[" d "], v" #a " offset:" #o "\n\t"
#define S_W(n) "s_waitcnt lgkmcnt(" #n ")\n\t"
#define S_D " row_mask:0xf bank_mask:0xf\n\t"
#define S_3(x, y) S_M("100:103", x, y) S_M("104:107", x, x) S_M("96:99", y, y)
#define S_SQ(g, h) "v_fmac_f32 v" #g ", 2.0, v" #h "\n\t"
#define S_SQD(g, h) "v_fmac_f32_dpp v" #g ", v" #h ", %[two] quad_perm:[0,0,2,2]" S_D
#define S_U(u, h, y) "v_add_f32_dpp v" #u ", v" #h ", v" #y " quad_perm:[1,1,3,3]" S_D
#define S_C(d, a, b, m) "v_cndmask_b32 v" #d ", v" #a ", v" #b ", %[" #m "]\n\t"
#define S_B(d, n) "v_mov_b32_dpp v" #d ", v157 row_newbcast:" #n S_D
#define S_BASE(d, u, t) "v_pk_fma_f32 v[" d "], v[" u "], v[116:117], v[" t "] op_sel:[0,1,0] op_sel_hi:[1,1,0]\n\t"
#define S_LG(d, r) "v_pk_fma_f32 v[" d "], v[" d "], v[" r "], %[toff] op_sel_hi:[1,1,0]\n\t"
#define S_SUB(p, d) "v_pk_add_f32 v[" p "], v[" d "], v[150:151] op_sel_hi:[1,0] neg_lo:[0,1] neg_hi:[0,1]\n\t"
#define S_E(p) "v_exp_f32 v" #p ", v" #p "\n\t"
#define S_MUL(p, r) "v_pk_mul_f32 v[" p "], v[" p "], v[" r "]\n\t"
#define S_POS   \
  "s_mul_i32 s50, s42, 26\n\ts_lshr_b32 s50, s50, 8\n\ts_mul_i32 s50, s50, -10\n\ts_add_u32 s50, s50, s42\n\ts_mul_i32 s45, s50, %[slot]\n\ts_mov_b32 s43, s45\n\t"
#define S_POLL(n, a, o, need, back, far) \
  #n ":\n\tds_read_b32 v152, v" #a " offset:" #o "\n\ts_sub_i32 s47, s47, 1\n\ts_cmp_lt_i32 s47, 0\n\ts_waitcnt lgkmcnt(0)\n\ts_cbranch_scc1 " back "\n\t" \
  "v_readfirstlane_b32 s40, v152\n\ts_cmp_ge_u32 s40, " need "\n\ts_cbranch_scc1 " back "\n\ts_sleep " STR(SL_S) "\n\t" \
  "s_add_u32 s40, s40, " far "\n\ts_cmp_ge_u32 s40, " need "\n\ts_cbranch_scc1 " #n "b\n\ts_sleep " STR(SL_FAR) "\n\ts_branch " #n "b\n"
__device__ __forceinline__ void s_items(SCtx& c, int k0, int late) {
  unsigned long long tq;
  asm volatile("s_mov_b32 s47, %[cap]\n\ts_mov_b32 s49, 0\n\ts_mov_b32 s42, %[k0]\n\ts_cmp_lg_u32 %[late], 0\n\ts_cbranch_scc1 7f\n"
               "1:\n\ts_cmp_ge_u32 s42, %[end]\n\ts_cbranch_scc1 9f\n\t"
               S_POS
               "s_mul_i32 s48, s42, 43\n\ts_lshr_b32 s48, s48, 8\n\ts_mul_i32 s46, s48, -6\n\ts_add_u32 s46, s46, s42\n\ts_lshl2_add_u32 s48, s48, %[ns]\n\t"
               "v_add_u32 v144, s45, %[orb]\n\tv_mad_u32_u24 v145, s43, %[ev], %[oyb]\n\ts_lshl2_add_u32 s40, s46, %[pfl]\n\ts_lshl2_add_u32 s41, s50, %[full]\n\t"
               "v_xor_b32 v146, 64, v144\n\tv_xor_b32 v147, 64, v145\n\tv_mov_b32 v148, s40\n\tv_mov_b32 v149, s41\n\t"
               "s_lshl1_add_u32 s44, s42, 2\n\ts_lshl_b32 s46, s46, 7\n\ts_mov_b64 vcc, %[dg]\n\ts_cmp_lt_u32 s49, s44\n\ts_cbranch_scc1 5f\n"
               "2:\n\tds_read_b32 v150, %[amr]\n\tds_read_b32 v151, v148 offset:64\n\t"
               S_R("24:27", 145, 0) S_R("48:51", 144, 0) S_R("28:31", 147, 0) S_R("52:55", 146, 0) S_R("32:35", 145, 128) S_R("56:59", 144, 128)
               S_R("36:39", 147, 128) S_R("60:63", 146, 128) S_R("40:43", 145, 256) S_R("64:67", 144, 256) S_R("44:47", 147, 256) S_R("68:71", 146, 256)
               S_W(10) S_M0("100:103", "48:51", "24:27") S_M0("104:107", "48:51", "48:51") S_R("72:75", 144, 6144) S_R("76:79", 146, 6144) S_M0("96:99", "24:27", "24:27")
               S_W(10) S_M("100:103", "52:55", "28:31") S_M("104:107", "52:55", "52:55") S_R("80:83", 144, 6272) S_R("84:87", 146, 6272) S_M("96:99", "28:31", "28:31")
               S_W(10) S_M("100:103", "56:59", "32:35") S_M("104:107", "56:59", "56:59") S_R("88:91", 144, 6400) S_R("92:95", 146, 6400) S_M("96:99", "32:35", "32:35")
               "s_mov_b64 exec, 1\n\tds_add_u32 v149, %[four] offset:64\n\ts_mov_b64 exec, -1\n\tv_readfirstlane_b32 s51, v151\n\ts_setprio 1\n\ts_nop 0\n\t"
               S_W(11) S_3("60:63", "36:39") S_W(9) S_3("64:67", "40:43") S_W(7) S_3("68:71", "44:47")
               S_W(6) S_M0("108:111", "72:75", "24:27") S_M0("112:115", "72:75", "72:75")
               S_W(5) S_M("108:111", "76:79", "28:31") S_M("112:115", "76:79", "76:79")
               S_W(4) S_M("108:111", "80:83", "32:35")
               "v_mov_b32_dpp v152, v96 quad_perm:[1,1,3,3]" S_D "v_mov_b32_dpp v153, v98 quad_perm:[1,1,3,3]" S_D
               S_M("112:115", "80:83", "80:83") S_C(154, 96, 98, r2) S_SQ(104, 100)
               S_W(3) S_M("108:111", "84:87", "36:39") S_SQD(105, 101) S_SQ(106, 102)
               S_M("112:115", "84:87", "84:87") S_SQD(107, 103) "v_add_f32 v154, %[eps], v154\n\t"
               S_W(2) S_M("108:111", "88:91", "40:43") S_U(128, 100, 152) S_U(129, 101, 152)
               S_M("112:115", "88:91", "88:91") S_U(130, 102, 153) S_U(131, 103, 153)
               S_W(1) S_M("108:111", "92:95", "44:47") S_C(104, 104, 105, r1) S_C(106, 106, 107, r1)
               S_M("112:115", "92:95", "92:95") S_C(155, 128, 129, r1) S_C(156, 130, 131, r1)
               "s_cmp_lt_u32 s42, %[early]\n\ts_cbranch_scc1 7f\n"
               "3:\n\t" S_M0("116:119", "24:27", "0:3") S_C(104, 104, 106, r2) S_C(155, 155, 156, r2)
               S_M("116:119", "28:31", "4:7") "v_add_f32_dpp v104, v154, v104 quad_perm:[0,0,2,2]" S_D S_U(132, 108, 152)
               S_M("116:119", "32:35", "8:11") S_U(133, 109, 152) S_U(134, 110, 153)
               S_M("116:119", "36:39", "12:15") S_U(135, 111, 153) S_SQ(112, 108)
               S_M("116:119", "40:43", "16:19") S_SQD(113, 109) S_SQ(114, 110)
               S_M("116:119", "44:47", "20:23") S_SQD(115, 111) S_C(156, 132, 133, r1) S_C(157, 134, 135, r1)
               S_C(112, 112, 113, r1) S_C(114, 114, 115, r1) S_C(156, 156, 157, r2) S_C(112, 112, 114, r2)
               "v_add_f32_dpp v112, v154, v112 quad_perm:[0,0,2,2]" S_D
               "v_mul_f32 v117, %[nic], v117\n\tv_cndmask_b32_dpp v155, v156, v155, vcc row_ror:4" S_D
               S_BASE("120:121", "128:129", "116:117") S_BASE("122:123", "130:131", "118:119")
               "v_cndmask_b32_dpp v104, v112, v104, vcc row_ror:4" S_D "v_mul_f32 v156, %[nic], v155\n\t"
               S_M("120:123", "48:51", "0:3") S_BASE("124:125", "132:133", "116:117") S_BASE("126:127", "134:135", "118:119")
               S_M("120:123", "52:55", "4:7") "v_fmac_f32 v104, v156, v155\n\tv_max_f32 v104, %[eps], v104\n\t"
               S_M("120:123", "56:59", "8:11") "v_rsq_f32 v157, v104\n\t"
               S_M("120:123", "60:63", "12:15") "v_mul_f32 v104, v104, v157\n\tv_mov_b32_dpp v157, v157 row_ror:12 row_mask:0x2 bank_mask:0xf\n\t"
               S_M("120:123", "64:67", "16:19") "v_cvt_pk_bf16_f32 v105, v155, v104\n\tv_mov_b32_dpp v157, v157 row_ror:8 row_mask:0x4 bank_mask:0xf\n\t"
               S_M("120:123", "68:71", "20:23") "v_lshlrev_b32 v106, 16, v105\n\tv_mov_b32_dpp v157, v157 row_ror:4 row_mask:0x8 bank_mask:0xf\n\t"
               S_M("124:127", "72:75", "0:3") "v_and_b32 v107, 0xffff0000, v105\n\t" S_B(136, 0)
               S_M("124:127", "76:79", "4:7") S_B(137, 1) S_B(138, 2)
               S_M("124:127", "80:83", "8:11") S_B(139, 3) S_LG("120:121", "136:137")
               S_M("124:127", "84:87", "12:15") S_LG("122:123", "138:139") S_SUB("128:129", "120:121")
               S_M("124:127", "88:91", "16:19") S_SUB("130:131", "122:123") S_E(128)
               S_M("124:127", "92:95", "20:23") S_E(129) S_E(130) S_E(131)
               S_B(140, 4) S_B(141, 5) S_B(142, 6) S_B(143, 7) "v_sub_f32 v106, v155, v106\n\t"
               S_LG("124:125", "140:141") S_LG("126:127", "142:143") "v_sub_f32 v107, v104, v107\n\tv_max3_f32 v152, v120, v121, v122\n\t"
               S_SUB("132:133", "124:125") S_SUB("134:135", "126:127") "v_max3_f32 v153, v123, v124, v125\n\t"
               S_E(132) S_E(133) S_E(134) S_E(135)
               "v_max3_f32 v152, v152, v126, v127\n\tv_add_f32 v154, %[thr], v150\n\tv_max_f32 v152, v152, v153\n\tv_cvt_pk_bf16_f32 v106, v106, v107\n\t"
               "v_lshl_add_u32 v144, s46, 3, %[apk]\n\tv_cmp_gt_f32 vcc, v152, v154\n\tv_add_u32 v145, s46, %[apm]\n\tv_lshl_add_u32 v146, s46, 1, %[api]\n\t"
               "s_cbranch_vccnz 8f\n"
               "4:\n\t" S_MUL("128:129", "136:137") S_MUL("130:131", "138:139") S_MUL("132:133", "140:141") S_MUL("134:135", "142:143")
               "v_cvt_pk_bf16_f32 v128, v128, v129\n\tv_cvt_pk_bf16_f32 v129, v130, v131\n\tv_cvt_pk_bf16_f32 v130, v132, v133\n\tv_cvt_pk_bf16_f32 v131, v134, v135\n\t"
               "s_cmp_lt_u32 s51, s48\n\ts_cbranch_scc1 6f\n"
               "10:\n\tds_read_b32 v151, v149\n\tds_write_b128 v144, v[128:131]\n\tds_write2_b32 v145, v150, v150 offset0:0 offset1:1\n\ts_mov_b64 exec, %[dg2]\n\t"
               "ds_write_b16 v146, v105\n\tds_write_b16 v146, v106 offset:16\n\tds_write_b16_d16_hi v146, v105 offset:32\n\tds_write_b16_d16_hi v146, v106 offset:48\n\t"
               "s_mov_b64 exec, 1\n\tds_add_u32 v148, %[inc]\n\ts_mov_b64 exec, -1\n\ts_setprio 0\n\ts_add_u32 s42, s42, %[ns]\n\t"
               S_W(7) "v_readfirstlane_b32 s49, v151\n\ts_branch 1b\n"
               S_POLL(5, 149, 0, "s44", "2b", "2")
               S_POLL(6, 148, 64, "s48", "10b", "64")
               "7:\n\tv_mov_b32 v144, %[qi]\n\tv_mov_b32 v145, v152\n\tv_mov_b32 v146, 1\n"
               S_POLL(70, 144, 0, "%[nq]", "71f", "64")
               "71:\n\tv_mov_b32 v152, v145\n\tds_read_b128 v[0:3], %[apk]\n\tds_read_b128 v[4:7], %[apk] offset:1024\n\tds_read_b128 v[8:11], %[apk] offset:2048\n\t"
               "ds_read_b128 v[12:15], %[apk] offset:3072\n\tds_read_b128 v[16:19], %[apk] offset:4096\n\tds_read_b128 v[20:23], %[apk] offset:5120\n\t"
               "v_subrev_u32 v144, %[mc], %[amr]\n\tds_read_b32 v150, %[amr]\n\ts_mov_b64 exec, 63\n\tds_add_u32 v144, v146\n\ts_mov_b64 exec, -1\n\t"
               "s_cmp_lg_u32 %[late], 0\n\ts_waitcnt lgkmcnt(0)\n\ts_nop 1\n\ts_cbranch_scc1 1b\n\ts_branch 3b\n"
               "8:\n\tv_mov_b32 v153, v152\n\ts_nop 1\n\tv_permlane16_swap_b32 v152, v153\n\tv_max_f32 v152, v152, v153\n\tv_mov_b32 v153, v152\n\ts_nop 1\n\t"
               "v_permlane32_swap_b32 v152, v153\n\tv_max_f32 v152, v152, v153\n\tv_mul_f32 v152, 0x3d000000, v152\n\tv_rndne_f32 v152, v152\n\t"
               "v_mul_f32 v152, 0x42000000, v152\n\tv_max_f32 v150, v150, v152\n\tds_max_f32 %[amr], v150\n\t"
               S_SUB("128:129", "120:121") S_SUB("130:131", "122:123") S_SUB("132:133", "124:125") S_SUB("134:135", "126:127")
               S_E(128) S_E(129) S_E(130) S_E(131) S_E(132) S_E(133) S_E(134) S_E(135) "s_branch 4b\n"
               "9:"
               : "=&{v[0:7]}"(c.q[0]), "=&{v[8:15]}"(c.q[1]), "=&{v[16:23]}"(c.q[2]), "=&{s[52:53]}"(tq)
               : [orb] "v"(c.o_rb), [oyb] "v"(c.o_yb), [ev] "v"(c.ev), [apk] "v"(c.apk), [apm] "v"(c.apm), [api] "v"(c.api), [amr] "v"(c.amr),
                 [two] "v"(2.f), [four] "v"(4u), [inc] "v"(2u * NPK), [k0] "s"(k0), [late] "s"(__builtin_amdgcn_readfirstlane(late)), [toff] "s"(f32x2{TOFF, TOFF}),
                 [r1] "s"(0xAAAAAAAAAAAAAAAAull), [r2] "s"(0xCCCCCCCCCCCCCCCCull), [dg] "s"(0xF0000F0000F0000Full), [dg2] "s"(0xF00FFF000FF000FFull),
                 [cap] "i"(SPIN_CAP), [end] "i"(TPW - NH1), [ns] "i"(NS), [pfl] "i"(CT_OFF + C_PFL), [full] "i"(CT_OFF + C_FULL), [early] "i"(NEARLY),
                 [qi] "i"(CT_OFF + C_QI), [nq] "i"(NS - NEARLY), [mc] "i"(C_MREF - C_CONS), [slot] "i"(SLOT),
                 [thr] "i"(__builtin_bit_cast(unsigned, THR)), [eps] "i"(__builtin_bit_cast(unsigned, EPS * CC)), [nic] "i"(__builtin_bit_cast(unsigned, -1.f / CC))
               : "memory", "scc", "vcc", "s40", "s41", "s42", "s43", "s44", "s45", "s46", "s47", "s48", "s49", "s50", "s51",
                 "v24", "v25", "v26", "v27", "v28", "v29", "v30", "v31", "v32", "v33", "v34", "v35", "v36", "v37", "v38", "v39", "v40", "v41", "v42", "v43", "v44", "v45", "v46", "v47", "v48", "v49", "v50", "v51", "v52", "v53", "v54", "v55", "v56", "v57", "v58", "v59", "v60", "v61", "v62", "v63", "v64", "v65", "v66", "v67", "v68", "v69", "v70", "v71", "v72", "v73", "v74", "v75", "v76", "v77", "v78", "v79", "v80", "v81", "v82", "v83", "v84", "v85", "v86", "v87", "v88", "v89", "v90", "v91", "v92", "v93", "v94", "v95", "v96", "v97", "v98", "v99", "v100", "v101", "v102", "v103", "v104", "v105", "v106", "v107", "v108", "v109", "v110", "v111", "v112", "v113", "v114", "v115", "v116", "v117", "v118", "v119", "v120", "v121", "v122", "v123", "v124", "v125", "v126", "v127", "v128", "v129", "v130", "v131", "v132", "v133", "v134", "v135", "v136", "v137", "v138", "v139", "v140", "v141", "v142", "v143", "v144", "v145", "v146", "v147", "v148", "v149", "v150", "v151", "v152", "v153", "v154", "v155", "v156", "v157");
  c.tq = (unsigned)tq;
}

__device__ __forceinline__ void s_half(SCtx& c, int k, int h) {
  const int s = k % NSLOT, e = k % NPK;
  const int sb_ = s * SLOT;
  int yba = c.ev * sb_ + c.o_yb;
  float hc = 1.f - 0.5f * c.ev;
  unsigned xa = (unsigned)(size_t)c.L0 + sb_ + h * 6144 + c.o_rb, xb = xa ^ 64, ai = c.api + e * 256 + h * 8;
  unsigned ap = c.apk + e * 1024 + h * 8, am = c.apm + e * 128 + h * 4, af = c.ct + C_PFL + e * 4, inc = NPK;
  float two = 2.f;
  f32x4 G = {EPS * CC, EPS * CC, EPS * CC, EPS * CC};
  poll_ge_sleep(c.ct + C_CONS + e * 4, (unsigned)(NE * (k / NPK) + NS));
  asm volatile("" : "+v"(yba), "+v"(hc), "+v"(xa), "+v"(xb), "+v"(ai), "+v"(ap), "+v"(am), "+v"(af), "+v"(inc), "+v"(two), "+{v[104:107]}"(G));
  poll_ge_sleep(c.ct + C_FULL, (unsigned)(2 * k + 1));
  __builtin_amdgcn_s_setprio(1);
  bf16x8 yb[6];
#pragma unroll
  for (int j = 0; j < 6; ++j) yb[j] = *(LDS_AS(bf16x8)*)(c.L0 + ((j & 1) ? (yba ^ 64) : yba) + (j >> 1) * 128);
  f32x4 Hx = {0.f, 0.f, 0.f, 0.f}, S = Hx;
#pragma unroll
  for (int j = 0; j < 6; ++j) { Hx = mfma(yb[j], yb[j], Hx); S = mfma(yb[j], s_qf(c, j), S); }
  const float nsq = S[1] * (-1.f / CC);
  Hx = f32x4{Hx[0] * hc, Hx[0] * hc, Hx[2] * hc, Hx[2] * hc};
  S = f32x4{S[0], S[0], S[2], S[2]};
  float mref, mt;
  f32x4 rs;
  unsigned long long raise;
#define SH_M(d, a, b) "v_mfma_f32_16x16x32_bf16 v[" d "], v[" a "], " b ", v[" d "]\n\t"
#define SH_X(n, a, y) "s_waitcnt lgkmcnt(" #n ")\n\t" SH_M("100:103", a, "%[" #y "]") SH_M("104:107", a, "v[" a "]")
#define SH_D " row_mask:0xf bank_mask:0xf\n\t"
  __builtin_amdgcn_s_setprio(0);
  if (HB == 0 || (HB == 1 && k < TPW - 1)) poll_ge_sleep(c.ct + C_FULL, (unsigned)(2 * k + 2));
  if (HB == 2) { if (k < TPW - 1) poll_ge_sleep(c.ct + C_FULL, (unsigned)(2 * k + 2)); else poll_ge(c.ct + C_FULL, (unsigned)(2 * TPW)); }
  asm volatile("s_mov_b32 s42, %[cap]\n"
               "1:\n\tds_read_b32 v136, %[afl]\n\tds_read_b128 v[112:115], %[xa]\n\tds_read_b128 v[116:119], %[xb]\n\tds_read_b128 v[120:123], %[xa] offset:128\n\t"
               "ds_read_b128 v[124:127], %[xb] offset:128\n\tds_read_b128 v[128:131], %[xa] offset:256\n\tds_read_b128 v[132:135], %[xb] offset:256\n\t"
               "ds_read_b32 %[mr], %[amr]\n\ts_sub_u32 s42, s42, 1\n\ts_cselect_b32 s41, 0, %[need]\n\ts_waitcnt lgkmcnt(7)\n\t"
               "v_readfirstlane_b32 s40, v136\n\ts_cmp_lt_u32 s40, s41\n\ts_cbranch_scc1 1b\n\ts_setprio 3\n\t"
               SH_X(6, "112:115", y0) SH_X(5, "116:119", y1) SH_X(4, "120:123", y2) SH_X(3, "124:127", y3) SH_X(2, "128:131", y4) SH_X(1, "132:135", y5)
               SH_M("108:111", "112:115", "%[q0]") SH_M("108:111", "116:119", "%[q1]")
               "s_waitcnt lgkmcnt(0)\n\tv_add_f32 v137, %[thr], %[mr]\n\tds_write_b32 %[am], %[mr]\n\ts_nop 1\n\t"
               "v_mov_b32_dpp v140, v100 quad_perm:[1,1,3,3]" SH_D "v_mov_b32_dpp v141, v101 quad_perm:[1,1,3,3]" SH_D
               "v_mov_b32_dpp v142, v102 quad_perm:[1,1,3,3]" SH_D "v_mov_b32_dpp v143, v103 quad_perm:[1,1,3,3]" SH_D
               SH_M("108:111", "120:123", "%[q2]")
               "v_fmac_f32 v104, 2.0, v100\n\tv_fmac_f32_dpp v105, v101, %[two] quad_perm:[0,0,2,2]" SH_D
               "v_fmac_f32 v106, 2.0, v102\n\tv_fmac_f32_dpp v107, v103, %[two] quad_perm:[0,0,2,2]" SH_D
               "v_cndmask_b32 v138, v140, v141, %[r1]\n\tv_cndmask_b32 v139, v142, v143, %[r1]\n\t"
               SH_M("108:111", "124:127", "%[q3]")
               "v_cndmask_b32 v104, v104, v105, %[r1]\n\tv_cndmask_b32 v106, v106, v107, %[r1]\n\tv_cndmask_b32 v138, v138, v139, %[r2]\n\t"
               "v_cndmask_b32 v104, v104, v106, %[r2]\n\tv_mul_f32 v139, %[nic], v138\n\t"
               SH_M("108:111", "128:131", "%[q4]")
               "v_fmac_f32 v104, v139, v138\n\tv_max_f32 v104, %[eps], v104\n\tv_rsq_f32 v105, v104\n\t"
               SH_M("108:111", "132:135", "%[q5]")
               "v_mul_f32 v104, v104, v105\n\t"
               "v_mov_b32_dpp v105, v105 row_ror:12 row_mask:0x2 bank_mask:0xf\n\tv_cvt_pk_bf16_f32 v106, v138, v104\n\ts_nop 0\n\t"
               "v_mov_b32_dpp v105, v105 row_ror:8 row_mask:0x4 bank_mask:0xf\n\tv_lshlrev_b32 v107, 16, v106\n\tv_and_b32 v139, 0xffff0000, v106\n\t"
               "v_mov_b32_dpp v105, v105 row_ror:4 row_mask:0x8 bank_mask:0xf\n\t"
               "v_fmac_f32 v108, v140, %[nsq]\n\tv_fmac_f32 v109, v141, %[nsq]\n\tv_fmac_f32 v110, v142, %[nsq]\n\tv_fmac_f32 v111, v143, %[nsq]\n\t"
               "v_mov_b32_dpp v100, v105 row_newbcast:0" SH_D "v_mov_b32_dpp v101, v105 row_newbcast:1" SH_D
               "v_mov_b32_dpp v102, v105 row_newbcast:2" SH_D "v_mov_b32_dpp v103, v105 row_newbcast:3" SH_D
               "v_fmaak_f32 v108, v108, v100, %[toff]\n\tv_fmaak_f32 v109, v109, v101, %[toff]\n\tv_fmaak_f32 v110, v110, v102, %[toff]\n\tv_fmaak_f32 v111, v111, v103, %[toff]\n\t"
               "v_sub_f32 v140, v108, %[mr]\n\tv_sub_f32 v141, v109, %[mr]\n\tv_sub_f32 v142, v110, %[mr]\n\tv_sub_f32 v143, v111, %[mr]\n\t"
               "v_exp_f32 v140, v140\n\tv_exp_f32 v141, v141\n\tv_exp_f32 v142, v142\n\tv_exp_f32 v143, v143\n\t"
               "v_max3_f32 %[mt], v108, v109, v110\n\tv_sub_f32 v138, v138, v107\n\tv_sub_f32 v104, v104, v139\n\tv_max_f32 %[mt], %[mt], v111\n\t"
               "v_pk_mul_f32 v[140:141], v[140:141], v[100:101]\n\tv_pk_mul_f32 v[142:143], v[142:143], v[102:103]\n\t"
               "v_cmp_gt_f32 %[ra], %[mt], v137\n\tv_cvt_pk_bf16_f32 v107, v138, v104\n\t"
               "v_cvt_pk_bf16_f32 v140, v140, v141\n\tv_cvt_pk_bf16_f32 v141, v142, v143\n\t"
               "ds_write_b64 %[ap], v[140:141]\n\ts_mov_b64 exec, %[dg]\n\t"
               "ds_write_b16 %[ai], v106\n\tds_write_b16 %[ai], v107 offset:16\n\tds_write_b16_d16_hi %[ai], v106 offset:32\n\tds_write_b16_d16_hi %[ai], v107 offset:48\n\t"
               "s_cmp_eq_u64 %[ra], 0\n\ts_cselect_b64 exec, 1, 0\n\tds_add_u32 %[af], %[inc]\n\ts_mov_b64 exec, -1\n\ts_setprio 0"
               : "+{v[100:103]}"(Hx), "+{v[104:107]}"(G), "+{v[108:111]}"(S), [mr] "=&v"(mref), [mt] "=&v"(mt), [ra] "=&s"(raise)
               : [y0] "v"(yb[0]), [y1] "v"(yb[1]), [y2] "v"(yb[2]), [y3] "v"(yb[3]), [y4] "v"(yb[4]), [y5] "v"(yb[5]),
                 [q0] "v"(s_qf(c, 0)), [q1] "v"(s_qf(c, 1)), [q2] "v"(s_qf(c, 2)), [q3] "v"(s_qf(c, 3)), [q4] "v"(s_qf(c, 4)), [q5] "v"(s_qf(c, 5)),
                 [afl] "v"(c.ct + C_FULL), [xa] "v"(xa), [xb] "v"(xb), [amr] "v"(c.amr), [am] "v"(am), [ap] "v"(ap), [ai] "v"(ai),
                 [af] "v"(af), [inc] "v"(inc), [nsq] "v"(nsq), [two] "v"(two),
                 [need] "s"(__builtin_amdgcn_readfirstlane(2 * k + 2)), [r1] "s"(0xAAAAAAAAAAAAAAAAull), [r2] "s"(0xCCCCCCCCCCCCCCCCull),
                 [dg] "s"(0xF0000F0000F0000Full), [cap] "i"(SPIN_CAP), [thr] "i"(__builtin_bit_cast(unsigned, THR)), [toff] "i"(__builtin_bit_cast(unsigned, TOFF)),
                 [eps] "i"(__builtin_bit_cast(unsigned, EPS * CC)), [nic] "i"(__builtin_bit_cast(unsigned, -1.f / CC))
               : "memory", "scc", "s40", "s41", "s42", "v112", "v113", "v114", "v115", "v116", "v117", "v118", "v119", "v120", "v121", "v122", "v123", "v124", "v125",
                 "v126", "v127", "v128", "v129", "v130", "v131", "v132", "v133", "v134", "v135", "v136", "v137", "v138", "v139", "v140", "v141", "v142", "v143");
  if (__builtin_expect(raise != 0ull, 0)) {
    rs = Hx;
    mref = fmaxf(mref, 32.f * __builtin_rintf(xrow16_max(mt) * (1.f / 32.f)));
    float pp[4];
#pragma unroll
    for (int i = 0; i < 4; ++i) pp[i] = __builtin_amdgcn_exp2f(S[i] - mref) * rs[i];
    asm volatile("ds_max_f32 %6, %3\n\tds_write_b32 %2, %3\n\tds_write_b64 %0, %1\n\ts_mov_b64 exec, 1\n\tds_add_u32 %4, %5\n\ts_mov_b64 exec, -1"
                 : : "v"(ap), "v"(u32x2{cvtpk(pp[0], pp[1]), cvtpk(pp[2], pp[3])}), "v"(am), "v"(mref), "v"(af), "v"((unsigned)NPK), "v"(c.amr) : "memory");
  }
}

using u32x16 = __attribute__((ext_vector_type(16))) unsigned;
typedef __attribute__((address_space(1))) unsigned short gus;
extern "C" __global__ __launch_bounds__(768, 3) void dino_main(const void*, const void*, const void*, const void*, const void*,
                                                               const void*, const void*, void*, void*) {
  u32x16 ka; u32x2 kb; unsigned long long tent64;
  __shared__ __attribute__((aligned(16))) char lds[LDS_BYTES];
  const int wave = __builtin_amdgcn_readfirstlane((int)threadIdx.x >> 6);
  const bool helper = blockIdx.x < 8;
  lds_ptr L0 = (lds_ptr)lds;
  const unsigned lbase = (unsigned)(size_t)L0;
  const unsigned ct = lbase + CT_OFF;
#define KARG(v, i) ((char*)(((unsigned long long)v[2 * (i) + 1] << 32) | v[2 * (i)]))
#define q KARG(ka, 0)
#define f0 KARG(ka, 1)
#define f1 KARG(ka, 2)
#define wq KARG(ka, 3)
#define wk KARG(ka, 4)
#define wv KARG(ka, 5)
#define wo KARG(ka, 6)
#define box KARG(ka, 7)
#define tent ((unsigned)tent64)
#define LANE_VARS int lane; asm volatile("v_mbcnt_lo_u32_b32 %0, -1, 0\n\tv_mbcnt_hi_u32_b32 %0, -1, %0" : "=v"(lane)); \
  const int r = lane & 15, g = lane >> 4; (void)r; (void)g;
#define DOFF_VARS unsigned doff[3];   \
  _Pragma("unroll") for (int i = 0; i < 3; ++i) doff[i] = (unsigned)(lane ^ ((swz_tbl(i) >> ((lane >> 3) * 4)) & 7)) << 4;
#define BARRIER() do { asm volatile("s_waitcnt lgkmcnt(0)" ::: "memory"); __builtin_amdgcn_s_barrier(); asm volatile("" ::: "memory"); } while (0)

  {
    unsigned doff[3];
    static_assert(swz_tbl(0) == swz_tbl(3) && swz_tbl(1) == swz_tbl(4) && swz_tbl(2) == swz_tbl(5), "");
    unsigned l16, db;
#define PD(d, b, off, pol) "global_load_lds_dwordx4 %[" #d "], s[" #b "] offset:" #off pol "\n\t"
    asm volatile("s_load_dwordx16 s[64:79], %[kp], 0x0\n\ts_load_dwordx2 s[80:81], %[kp], 0x40\n\ts_memrealtime s[82:83]\n\t"
                 "v_mbcnt_lo_u32_b32 %[l16], -1, 0\n\tv_mbcnt_hi_u32_b32 %[l16], -1, %[l16]\n\t"
                 "s_lshr_b32 s84, %[b], 3\n\ts_and_b32 s85, %[b], 7\n\t"
                 "v_lshrrev_b32 %[db], 1, %[l16]\n\tv_and_b32 %[db], -4, %[db]\n\t"
                 "s_mov_b32 s86, %[t0]\n\ts_mov_b32 s87, %[t1]\n\ts_mov_b32 s88, %[t2]\n\t"
                 "v_bfe_u32 %[d0], s86, %[db], 3\n\tv_bfe_u32 %[d1], s87, %[db], 3\n\tv_bfe_u32 %[d2], s88, %[db], 3\n\t"
                 "v_xor_b32 %[d0], %[d0], %[l16]\n\tv_xor_b32 %[d1], %[d1], %[l16]\n\tv_xor_b32 %[d2], %[d2], %[l16]\n\t"
                 "v_lshlrev_b32 %[d0], 4, %[d0]\n\tv_lshlrev_b32 %[d1], 4, %[d1]\n\tv_lshlrev_b32 %[d2], 4, %[d2]\n\t"
                 "v_lshlrev_b32 %[l16], 4, %[l16]\n\t"
                 "s_cmp_eq_u32 s84, 0\n\ts_cbranch_scc1 4f\n\t"
                 "s_cmp_lg_u32 %[w], 0\n\ts_cbranch_scc1 7f\n\t"
                 "s_setprio 3\n\tv_lshlrev_b32 %[db], 3, %[l16]\n\ts_mov_b32 m0, %[junk]\n\ts_lshl_b32 s89, s84, 13\n\t"
                 "s_cmp_gt_u32 s84, 3\n\ts_cbranch_scc1 2f\n\t"
                 "s_getpc_b64 s[90:91]\n1:\n\ts_sub_u32 s86, s89, 8192\n\ts_min_u32 s86, s86, dino_end-1b-8192\n\t"
                 "s_add_u32 s90, s90, s86\n\ts_addc_u32 s91, s91, 0\n\tglobal_load_lds_dword %[db], s[90:91]\n"
                 "2:\n\ts_mul_i32 s86, s85, 98304\n\ts_mul_i32 s87, s84, 3072\n\ts_sub_u32 s87, s87, 3072\n\t"
                 "s_add_u32 s88, s86, s87\n\ts_lshl1_add_u32 s86, s86, s87\n\ts_lshl_b32 s86, s86, 1\n\t"
                 "s_mul_i32 s87, s85, 12288\n\ts_lshr_b32 s90, s89, 1\n\ts_add_u32 s87, s87, s90\n\ts_sub_u32 s87, s87, 13 * 4096\n\t"
                 "s_sub_u32 s89, s89, 4 * 8192\n\ts_cmp_gt_u32 s85, 5\n\ts_cselect_b32 s89, -1, s89\n\ts_cmp_ge_u32 s89, 11 * 8192\n\t"
                 "s_waitcnt lgkmcnt(0)\n\ts_cbranch_scc1 3f\n\t"
                 "s_cmp_lt_u32 s89, 9 * 8192\n\ts_cselect_b64 s[90:91], s[70:71], s[72:73]\n\ts_cselect_b32 s89, s89, s87\n\t"
                 "s_add_u32 s90, s90, s89\n\ts_addc_u32 s91, s91, 0\n\tglobal_load_lds_dword %[db], s[90:91]\n"
                 "3:\n\ts_mov_b32 m0, 0x3000\n\t"
                 "s_add_u32 s90, s68, s88\n\ts_addc_u32 s91, s69, 0\n\t"
                 "s_add_u32 s92, s66, s86\n\ts_addc_u32 s93, s67, 0\n\t"
                 PD(d0, 90:91, 0, " nt") PD(d1, 90:91, 1024, " nt") PD(d2, 90:91, 2048, " nt")
                 "s_mov_b32 m0, 0\n\ts_nop 0\n\t"
                 PD(d0, 92:93, 0, " nt") PD(d1, 92:93, 1024, " nt")
                 "s_sub_u32 s89, s84, 15\n\ts_cmp_gt_u32 s85, 5\n\ts_cselect_b32 s89, -1, s89\n\ts_cmp_ge_u32 s89, 11\n\ts_cbranch_scc1 9f\n\t"
                 "s_mov_b32 m0, %[junk]\n\ts_sub_u32 s87, s87, 11 * 4096\n\ts_lshl_b32 s89, s89, 13\n\ts_cmp_lt_u32 s89, 9 * 8192\n\t"
                 "s_cselect_b64 s[90:91], s[76:77], s[74:75]\n\ts_cselect_b32 s89, s89, s87\n\t"
                 "s_add_u32 s90, s90, s89\n\ts_addc_u32 s91, s91, 0\n\tglobal_load_lds_dword %[db], s[90:91]\n\t"
                 "s_branch 9f\n"
                 "7:\n\ts_cmp_eq_u32 %[w], 1\n\ts_cbranch_scc1 8f\n\ts_barrier\n\ts_branch 8f\n"
                 "4:\n\t"
                 "s_cmp_gt_u32 %[b], 5\n\ts_cbranch_scc1 8f\n\ts_cmp_gt_u32 %[w], 9\n\ts_cbranch_scc1 8f\n\t"
                 "s_mov_b32 m0, %[img]\n\ts_mul_i32 s86, %[w], 9216\n\ts_and_b32 s88, %[w], 1\n\ts_lshl1_add_u32 s89, %[b], s88\n\t"
                 "s_mul_i32 s88, s88, 6144\n\ts_mul_i32 s89, s89, 6144\n\ts_add_u32 s88, s88, %[hr]\n\t"
                 "s_cmp_lg_u32 %[w], 0\n\ts_waitcnt lgkmcnt(0)\n\ts_cbranch_scc1 5f\n\t"
                 PD(d0, 64:65, 0, "") PD(d1, 64:65, 1024, "") PD(d2, 64:65, 2048, "") PD(d0, 64:65, 3072, "")
                 "s_mov_b32 m0, %[img] + 4096\n\ts_add_u32 s90, s64, 0x1000\n\ts_addc_u32 s91, s65, 0\n\t"
                 PD(d1, 90:91, 0, "") PD(d2, 90:91, 1024, "")
                 "\n5:\n\ts_cmp_gt_u32 %[w], 7\n\ts_cselect_b64 s[92:93], s[72:73], s[70:71]\n\ts_cselect_b32 m0, s88, s86\n\ts_cselect_b32 s86, s89, s86\n\t"
                 "s_cselect_b32 s87, 2, 3\n\ts_add_u32 s92, s92, s86\n\ts_addc_u32 s93, s93, 0\n"
                 "6:\n\ts_sub_u32 s87, s87, 1\n\t"
                 PD(d0, 92:93, 0, "") PD(d1, 92:93, 1024, "") PD(d2, 92:93, 2048, "")
                 "s_add_u32 m0, m0, 3072\n\ts_add_u32 s92, s92, 3072\n\ts_addc_u32 s93, s93, 0\n\ts_cmp_lg_u32 s87, 0\n\ts_cbranch_scc1 6b"
                 "\n8:\n\ts_waitcnt lgkmcnt(0)\n9:"
                 : "={s[64:79]}"(ka), "={s[80:81]}"(kb), "={s[82:83]}"(tent64),
                   [d0] "=&v"(doff[0]), [d1] "=&v"(doff[1]), [d2] "=&v"(doff[2]), [l16] "=&v"(l16), [db] "=&v"(db)
                 : [kp] "s"(__builtin_amdgcn_kernarg_segment_ptr()), [b] "s"(blockIdx.x), [w] "s"(wave),
                   [t0] "i"(swz_tbl(0)), [t1] "i"(swz_tbl(1)), [t2] "i"(swz_tbl(2)), [junk] "i"(JUNK), [img] "i"(IMG_A), [hr] "i"(H_R)
                 : "memory", "scc", "vcc", "s84", "s85", "s86", "s87", "s88", "s89", "s90", "s91", "s92", "s93", "s94", "s95", "s96", "s97", "s98", "s99");
  }
  const unsigned t0 = (unsigned)__builtin_amdgcn_s_memrealtime() - T0BACK;

  if (__builtin_expect(blockIdx.x < 6, 0)) {
    LANE_VARS
    DOFF_VARS
    const int j = blockIdx.x;
    TNOW(ta0);
    TNOW(ta1);
    const int qq = (lane >> 2) & 3, p4 = lane & 3, e4 = wave & 3;
    int ra = H_R + r * ROWB + ((g ^ swz(r)) << 4);
    bf16x8 a[6], b[6], w[18];
    auto rows = [&](int t, bf16x8* d) {
#pragma unroll
      for (int kk = 0; kk < 6; ++kk) d[kk] = *(LDS_AS(bf16x8)*)(L0 + t * 6144 + ((kk & 1) ? (ra ^ 64) : ra) + (kk >> 1) * 128);
    };
#define TSET(v)
    int o[3];
#pragma unroll
    for (int t = 0; t < 3; ++t) o[t] = tr_off(4 * g + qq, 4 * (3 * e4 + t) + p4);
    unsigned qo = lbase + H_Q + 3 * e4 * 512 + ((g >> 1) * 16 + r) * 16 + (g & 1) * 8, qa = lbase + IMG_A + tr_off(r, 8 * wave + g);
    if (wave == 11) {
      unsigned d;
      asm volatile("s_load_dword %0, %1, 0x40\n\ts_barrier\n\ts_barrier\n\ts_barrier\n\ts_waitcnt lgkmcnt(0)" : "=&s"(d) : "s"(box + EPOCH_OFF) : "memory");
    } else
    asm volatile("s_waitcnt vmcnt(9)\n\ts_barrier" : PIN3(o, 0), "+v"(qo), "+v"(qa) :: "memory");
    if (wave < 6 || wave == 10) {
      rows((IMG_A - H_R) / 6144, a);
      const u32x2 m0 = *(LDS_AS(u32x2)*)(size_t)qa, m1 = *(LDS_AS(u32x2)*)(size_t)(qa ^ 32);
      static_assert((IMG_A - H_R) % 6144 == 0, "");
      const bf16x8 ones = __builtin_bit_cast(bf16x8, u32x4{0x3f803f80u, 0x3f803f80u, 0x3f803f80u, 0x3f803f80u});
      f32x4 sm = {0.f, 0.f, 0.f, 0.f};
#pragma unroll
      for (int i = 0; i < 6; ++i) sm = mfma(ones, a[i], sm);
      const float mean = sm[0] * (1.f / CC);
      if (__builtin_expect(wave < 6, 1)) {
        img_st(L0, H_C, wave, lane, __builtin_bit_cast(bf16x8, u32x4{cvtpk(bflo(m0[0]) - mean, bfhi(m0[0]) - mean), cvtpk(bflo(m0[1]) - mean, bfhi(m0[1]) - mean),
                                                                   cvtpk(bflo(m1[0]) - mean, bfhi(m1[0]) - mean), cvtpk(bflo(m1[1]) - mean, bfhi(m1[1]) - mean)}));
      } else {
        asm volatile("s_barrier" : PIN3(a, 0), PIN3(a, 3) :: "memory");
        f32x2 v2 = {0.f, 0.f}, v3 = v2;
#pragma unroll
        for (int i = 0; i < 12; ++i) {
          const u32x4 d = __builtin_bit_cast(u32x4, a[i >> 1]);
          const unsigned d0 = d[2 * (i & 1)], d1 = d[2 * (i & 1) + 1];
          const f32x2 x0 = f32x2{bflo(d0), bfhi(d0)} - mean, x1 = f32x2{bflo(d1), bfhi(d1)} - mean;
          v2 += x0 * x0; v3 += x1 * x1;
        }
        v2 += v3;
        *(LDS_AS(float)*)(L0 + CT_OFF + r * 4) = LOG2E * __builtin_amdgcn_rsqf(xrow16_sum(v2[0] + v2[1]) * (1.f / CC) + EPS);
      }
      TSET(ta2);
    }
    if (wave < 4) {
      wait_vm<0>();
      TSET(ta3);
      BARRIER();
      TSET(th1);
#pragma unroll
      for (int kk = 0; kk < 6; ++kk) {
        b[kk] = img_ld(L0, H_C, kk, lane);
        w[kk] = mk8(tr_read(L0 + kk * 12288 + o[0]), tr_read(L0 + kk * 12288 + 6144 + o[0]));
        __builtin_amdgcn_sched_barrier(0);
      }
#pragma unroll
      for (int i = 6; i < 18; ++i) {
        w[i] = mk8(tr_read(L0 + (i % 6) * 12288 + o[i / 6]), tr_read(L0 + (i % 6) * 12288 + 6144 + o[i / 6]));
        __builtin_amdgcn_sched_barrier(0);
      }
#pragma unroll
      for (int t = 0; t < 3; ++t) {
        f32x4 D = {0.f, 0.f, 0.f, 0.f};
#pragma unroll
        for (int kk = 0; kk < 6; ++kk) D = mfma(w[6 * t + kk], b[kk], D);
        *(LDS_AS(u32x2)*)(size_t)(qo + t * 512) = u32x2{cvtpk(D[0], D[1]), cvtpk(D[2], D[3])};
      }
      TSET(ta4);
      BARRIER();
    } else if (wave == 8 || wave == 9) {
      unsigned so = lane * 16 + (2 * j + (wave & 1)) * 1024;
      unsigned epoch;
      asm volatile("global_load_dword %0, %1, %2 sc1\n\ts_barrier\n\ts_waitcnt vmcnt(0)" : "=v"(epoch) : "v"(EPOCH_OFF), "s"(box) : "memory");
      TSET(ta3);
      rows(wave & 1, a);
      unsigned ta = line_tag(epoch, 8), tb = line_tag(epoch, 9);
      asm volatile("" : PIN3(a, 0), PIN3(a, 3), "+v"(ta), "+v"(tb), "+v"(so));
      TSET(ta4);
      BARRIER();
      TSET(ta5);
#pragma unroll
      for (int kk = 0; kk < 6; ++kk) b[kk] = img_ld(L0, H_Q, kk, lane);
      const float sc = *(LDS_AS(float)*)(L0 + CT_OFF + r * 4);
      __builtin_amdgcn_sched_barrier(0);
      f32x4 D = {0.f, 0.f, 0.f, 0.f};
#pragma unroll
      for (int kk = 0; kk < 6; ++kk) D = mfma(a[kk], b[kk], D);
      D *= sc;
      const unsigned p0 = cvtpk(D[0], D[1]), p1 = cvtpk(D[2], D[3]);
      asm volatile("global_store_dwordx4 %0, %1, %2 sc1\n\ts_nop 1" : : "v"(so), "v"(u32x4{p0, p1, p0 ^ p1 ^ ta, tb}), "s"(box + QK_OFF) : "memory");
      TSET(th2);
    } else if (wave != 11) {
      if (wave != 10) asm volatile("s_waitcnt vmcnt(0) lgkmcnt(0)\n\ts_barrier" ::: "memory");
      BARRIER();
    }
    BARRIER();
    issue_rows16(wo + wave * 6144, lbase + wave * 6144, doff);
    if (wave < 2) issue_rows16(wv + (2 * j + wave) * 6144, lbase + H_R + wave * 6144, doff);
    const unsigned epoch = epoch_load(box);
    BARRIER();
    TSET(ta3);
    {
      const int o0 = tr_off(8 * g + qq, 4 * wave + p4), o1 = tr_off(8 * g + 4 + qq, 4 * wave + p4);
#pragma unroll
      for (int kk = 0; kk < 6; ++kk) b[kk] = mk8(tr_read(L0 + kk * 12288 + o0), tr_read(L0 + kk * 12288 + o1));
      f32x4 D[2];
#pragma unroll
      for (int h = 0; h < 2; ++h) {
        rows(h, a);
        D[h] = f32x4{0.f, 0.f, 0.f, 0.f};
#pragma unroll
        for (int kk = 0; kk < 6; ++kk) D[h] = mfma(a[kk], b[kk], D[h]);
      }
      const int e = wave / 3;
      asm volatile("global_store_dwordx4 %0, %1, %2 sc1\n\ts_waitcnt vmcnt(0)"
                   : : "v"(lane * 16 + (e * 15 + 3 * j + wave) * 1024), "v"(pack8(D[0] * SQRT_C, D[1] * SQRT_C)), "s"(box + W2_OFF) : "memory");
    }
    BARRIER();
    if (wave == 11)
      asm volatile("s_mov_b64 exec, 1\n\tglobal_store_dwordx2 %0, %1, %2 sc1\n\ts_mov_b64 exec, -1\n\ts_nop 1"
                   : : "v"(j * 128), "v"(u32x2{line_tag(epoch, 10), line_tag(epoch, 11)}), "s"(box + W2F_OFF) : "memory");
    TPUT(60 + wave * 5, ta0); TPUT(61 + wave * 5, ta1); TPUT(62 + wave * 5, ta2); TPUT(63 + wave * 5, ta3); TPUT(64 + wave * 5, ta4);
  }

  if (__builtin_expect(helper, 0) && wave == 6) {
    LANE_VARS
    unsigned d0, d1, d2;
    asm volatile("global_load_dword %0, %3, %4 sc1\n\tglobal_load_dword %1, %5, %6 sc1\n\tglobal_load_dword %2, %5, %7\n\ts_waitcnt vmcnt(0)"
                 : "=&v"(d0), "=&v"(d1), "=&v"(d2) : "v"((lane * NWG + blockIdx.x) * 128), "s"(box), "v"((lane & 1) * 6140), "s"(KARG(kb, 0)), "s"(q) : "memory");
  }
  if (__builtin_expect(helper, 0) && wave == 7) {
    LANE_VARS
    unsigned d0, d1;
    asm volatile("global_load_dword %0, %2, %3 nt\n\tglobal_load_dword %1, %4, %5 nt\n\ts_waitcnt vmcnt(0)"
                 : "=&v"(d0), "=&v"(d1) : "v"(lane * 1572864 + 1572800), "s"(f0), "v"(lane * 393216 + 393152), "s"(f1) : "memory");
  }
  int rslot = -1;
  if (__builtin_expect(wave == 0, 1)) {
    LANE_VARS
    DOFF_VARS
    if (helper) BARRIER();
    const unsigned afull = ct + C_FULL + r * 4;
    unsigned ts = 0;
    {
      const int ti = helper ? 1023 : (lane * 31 + (int)(blockIdx.x >> 3) - 1) & 1023;
      const int sp = ti >> 5, yy = sp * 8 + ((blockIdx.x + sp) & 7), xx = (ti + 5 * sp) & 31;
      unsigned tmp, pub = helper ? 2 * (TPW - 1) : 0, pre = 0, afr = ct + C_FREE + (helper ? ((TPW - 1) % NSLOT) * 4 : 0), e0, e1, e2, g0, g1, g2;
      asm volatile("s_mov_b32 s44, %[due0]\n\ts_mov_b32 s46, %[y0]\n\ts_mov_b32 s47, %[cap]\n\ts_mov_b32 s48, %[i0]\n\ts_mov_b32 s49, %[s0]\n\t"
                   "s_mov_b32 s51, 0\n\ts_mov_b32 s45, 0\n\ts_movk_i32 s39, %[p3s]\n"
                   "1:\n\tv_readlane_b32 s53, %[ob], s48\n\tv_readlane_b32 s52, %[oa], s48\n\ts_add_u32 m0, s49, 0x3000\n\tv_cmp_gt_u32 vcc, s51, %[pre]\n\t"
                   "v_add_u32 %[g0], s53, %[d0]\n\tv_add_u32 %[g1], s53, %[d1]\n\tv_add_u32 %[g2], s53, %[d2]\n\t"
                   "v_add_u32 %[e0], s52, %[d0]\n\tv_add_u32 %[e1], s52, %[d1]\n\tv_add_u32 %[e2], s52, %[d2]\n\t"
                   "s_cbranch_vccnz 5f\n\t"
                   "s_cmp_eq_u32 s48, 0\n\ts_cbranch_scc1 2f\n\ts_cmp_eq_u32 s48, 2\n\ts_cbranch_scc1 8f\n"
                   LD_GATE(1, "")
                   LD1(g0, 68:69, 0) LD1(g1, 68:69, 1024) LD1(g2, 68:69, 2048) "s_mov_b32 m0, s49\n\ts_nop 0\n\t" LD1(e0, 66:67, 0) LD1(e1, 66:67, 1024)
                   LD_NEXT
                   "\n2:\n\tv_add_u32 %[afr], 4, %[afr]\n\ts_cmp_eq_u32 s49, %[lasts]\n\ts_cbranch_scc1 7f\n"
                   "3:\n\tv_mov_b32 %[pre], 0\n\ts_add_u32 m0, s49, 0x800\n\tds_read_b32 %[pre], %[afr] ; NOTRACK\n"
                   LD_GATE(2, "")
                   LD1(e2, 56:57, 0) LD1(e0, 56:57, 1024) LD1(e1, 56:57, 2048) LD1(e2, 56:57, 3072) "s_add_u32 m0, s49, 0x1800\n\ts_nop 0\n\t" LD1(e0, 58:59, 0)
                   LD_NEXT
                   "\n29:\n\ts_add_u32 m0, s49, 0x1800\n\ts_add_u32 s48, s48, 1\n"
                   LD_GATE(3, "s_cmp_gt_u32 s48, %[tpw] - 3\n\ts_cbranch_scc1 38f\n39:\n\t")
                   LD1(e1, 58:59, 1024) LD1(e2, 58:59, 2048) LD1(e0, 58:59, 3072) "s_add_u32 m0, s49, 0x2800\n\ts_add_u32 s49, s49, %[slot]\n\t" LD1(e1, 60:61, 0) LD1(e2, 60:61, 1024)
                   LD_NEXT
                   "s_cmp_eq_u32 s49, %[ring]\n\ts_cselect_b32 s49, 0, s49\n\ts_cmp_gt_i32 s46, 30\n\ts_cbranch_scc1 6f\n"
                   "4:\n\ts_cmp_eq_u32 s48, %[pk]\n\ts_cmovk_i32 s39, %[p3]\n\ts_cmp_lg_u32 s48, %[tpw]\n\ts_cbranch_scc1 1b\n\ts_branch 9f\n"
                   LD_PUB(1, 0, 15, 30) LD_PUB(2, 5, 20, 35) LD_PUB(3, 10, 25, 40)
                   "38:\n\ts_cmp_eq_u32 s46, 10\n\ts_cselect_b32 s43, 23, 0\n\ts_cmp_lt_u32 s42, s43\n\ts_cbranch_scc0 39b\n\tv_add_u32 %[tmp], 1, %[pub]\n\ts_waitcnt vmcnt(22)\n\tds_write_b32 %[af], %[tmp] ; NOTRACK\n\ts_branch 39b\n"
                   "5:\n\ts_movk_i32 s40, 0x100\n"
                   "40:\n\tds_read_b32 %[pre], %[afr]\n\t" LD_VM
                   "s_sub_u32 s40, s40, 1\n\ts_cselect_b32 s42, 0, s42\n\ts_cselect_b32 s40, 0, s40\n\t"
                   "s_cmp_le_i32 s42, s46\n\ts_cbranch_scc1 42f\n"
                   "41:\n\ts_sub_i32 s47, s47, 1\n\ts_cmp_lt_i32 s47, 0\n\ts_waitcnt lgkmcnt(0)\n\ts_cbranch_scc1 10b\n\t"
                   "v_cmp_gt_u32 vcc, s51, %[pre]\n\ts_cbranch_vccnz 40b\n\ts_branch 10b\n"
                   LD_PUB0(4, 0, 15, 30)
                   "6:\n\ts_waitcnt vmcnt(45)\n\tv_add_u32 %[pub], 2, %[pub]\n\tds_write_b32 %[af], %[pub]\n\ts_sub_u32 s46, s46, 15\n\ts_branch 4b\n"
                   "7:\n\ts_waitcnt lgkmcnt(0)\n\ts_add_u32 s51, s51, 8\n\tv_subrev_u32 %[afr], %[frb], %[afr]\n\ts_branch 3b\n"
                   "8:\n\ts_barrier\n\ts_add_u32 s46, s46, 64\n\ts_branch 10b\n"
                   "9:\n\ts_cmp_lt_i32 s46, 30\n\ts_cbranch_scc1 91f\n\ts_waitcnt vmcnt(30)\n\tv_add_u32 %[pub], 2, %[pub]\n\tds_write_b32 %[af], %[pub]\n"
                   "91:\n\ts_cmp_lt_i32 s46, 15\n\ts_cbranch_scc1 92f\n\tv_add_u32 %[tmp], 1, %[pub]\n\ts_waitcnt vmcnt(27)\n\tds_write_b32 %[af], %[tmp]\n"
                   "92:\n\ts_waitcnt lgkmcnt(0)"
                   : [pub] "+v"(pub), [pre] "+v"(pre), [afr] "+v"(afr), [ts] "+v"(ts), [tmp] "=&v"(tmp),
                     [e0] "=&v"(e0), [e1] "=&v"(e1), [e2] "=&v"(e2), [g0] "=&v"(g0), [g1] "=&v"(g1), [g2] "=&v"(g2)
                   : "{s[64:79]}"(ka), "{s[56:57]}"(f0 + 0x800), "{s[58:59]}"(f0 + 0x30000), "{s[60:61]}"(f0 + 0x31000), [t0] "s"(t0), [af] "v"(afull),
                     [d0] "v"(doff[0]), [d1] "v"(doff[1]), [d2] "v"(doff[2]), [oa] "v"(yy * 393216 + xx * 6144), [ob] "v"(yy * 98304 + xx * 3072),
                     [due0] "s"(helper ? HDUE * (PACE16 / 3) : PRE5 * (PACES / 3)), [y0] "s"(helper ? -15 : -79 + 5 * PRE5), [i0] "s"(helper ? TPW - 1 : 0),
                     [s0] "s"(helper ? ((TPW - 1) % NSLOT) * SLOT : 0),
                     [cap] "i"(SPIN_CAP), [win] "i"(PWIN), [ahi] "s"(((blockIdx.x >> 3) & 3) == 1 ? AHIR : AHI), [adl] "i"(ADL), [amg] "i"(AMG), [p3] "i"(PACE16 / 3), [p3s] "i"(PACES / 3), [pk] "i"(PK1), [slot] "i"(SLOT),
                     [ring] "i"(NSLOT * SLOT), [frb] "i"(NSLOT * 4), [lasts] "i"((NSLOT - 1) * SLOT), [tpw] "i"(TPW)
                   : "memory", "scc", "vcc", "s39", "s40", "s41", "s42", "s43", "s44", "s45", "s46", "s47", "s48", "s49", "s51", "s52", "s53", "s62", "s63");
    }
    lds_st32(ct + C_ISS, 1u);
#define CLR(b) "global_store_dwordx4 %0, %1, %" #b " sc1\n\t"
    auto clear = [&]() {
      asm volatile(CLR(2) CLR(3) CLR(4) CLR(5) CLR(6) CLR(7) CLR(8) CLR(9) "s_nop 1"
                 : : "v"(((lane >> 3) * NWG + blockIdx.x) * 128 + (lane & 7) * 16), "v"(u32x4{0u, 0u, 0u, 0u}), "s"(box), "s"(box + 1 * 262144), "s"(box + 2 * 262144),
                     "s"(box + 3 * 262144), "s"(box + 4 * 262144), "s"(box + 5 * 262144), "s"(box + 6 * 262144), "s"(box + 7 * 262144) : "memory");
    };
    if (PRECLR == 1) clear();
    wait_vm<PRECLR == 1 ? 23 : 15>();
    lds_st32(afull, (unsigned)(2 * TPW - 2));
    if (PRECLR == 3) clear();
    wait_vm<(PRECLR == 1 || PRECLR == 3) ? 20 : 12>();
    lds_st32(afull, (unsigned)(2 * TPW - 1));
    wait_vm<(PRECLR == 1 || PRECLR == 3) ? 8 : 0>();
    lds_st32(afull, ~0u);
    TNOW(tl);
    if (PRECLR == 2) clear();
    if (PRECLR == 4) { unsigned d; asm volatile("global_load_dword %0, %1, %2 sc1\n\ts_waitcnt vmcnt(0)" : "=v"(d) : "v"(blockIdx.x * 128), "s"(box) : "memory"); }
    __builtin_amdgcn_s_setprio(0);
    wait_vm<0>();
    TNOW(tlx);
    if (PRECLR != 5) lds_add(ct + C_X1, 1u);
    rslot = 7;
  } else {
    if (wave == 1) {
      LANE_VARS
    {
      const unsigned o = lane * 8;
      unsigned z = (o >= C_MREF && o < C_MREF + 64) ? 0xff800000u : 0u;
      if (helper && o >= C_CONS && o < C_MREF) z = NE * ((TPW - 1) / NPK);
      u32x2 zz = {z, z};
      if (o >= C_PFL && o < C_CONS) zz = u32x2{(o - C_PFL) / 2 + 2, (o - C_PFL) / 2 + 4} + (helper ? 2u * NPK * ((TPW - 1) / NPK) : 0u);
      *(LDS_AS(u32x2)*)(L0 + CT_OFF + o) = zz;
      *(LDS_AS(u32x2)*)(L0 + ONES_OFF + (lane < 48 ? o : 0)) = u32x2{0x3f803f80u, 0x3f803f80u};
    }
    }
    if (helper || wave == 1) BARRIER();
    if (wave <= NE) {
      LANE_VARS
      const int ej = wave - 1;
      const int qq = (lane >> 2) & 3, p4 = lane & 3;
      const unsigned apk = lbase + IMG_A + lane * 16;
      const unsigned epoch = epoch_load(box);
      __builtin_amdgcn_s_setprio(2);
      if (helper) { while ((unsigned)__builtin_amdgcn_s_memrealtime() - tent < RELAYT) __builtin_amdgcn_s_sleep(8); }
      unsigned long long tpv, tp7, tx1;
      {
        static_assert((TPW & 1) == 1 && C_CONS == C_PFL + 64 && C_FREE == C_FULL + 64, "");
        const int k0 = helper ? TPW - 1 : 0;
        unsigned need = 2 * k0 + 2;
        asm volatile("s_mov_b32 s46, 0\n\ts_mov_b64 exec, 0\n\ts_nop 3\n\ts_branch 99f\n"
                     "2:\n\ts_mov_b64 exec, -1\n\ts_nop 7\n\t" PV_Z4(72, 73, 74, 75) PV_Z4(76, 77, 78, 79) PV_Z4(80, 81, 82, 83) PV_Z4(84, 85, 86, 87)
                     "v_mov_b32 v88, 0xff800000\n\t"
                     "s_mov_b32 s46, %[k0]\n\ts_mov_b32 s45, %[s0]\n\ts_mov_b32 s44, %[e0]\n\ts_mov_b32 s47, %[cap]\n\t"
                     "s_cmp_lg_u32 s46, 0\n\ts_cbranch_scc1 32f\n"
                     "3:\n\tv_mov_b32 v148, %[ca]\n\t" PV_WAITF(4, 148) PV_FETCH_A
                     "s_mov_b64 exec, 2\n\tds_add_u32 v148, %[one] offset:64\n\ts_mov_b64 exec, -2\n\t"
                     "v_add_u32 %[need], 2, %[need]\n\tv_add_u32 v148, 4, v148\n\ts_mov_b64 exec, -1\n\t"
                     PV_WAITF(5, 148)
                     "v_mov_b32 v149, v148\n\ts_mov_b64 exec, -2\n\tv_add_u32 %[need], 2, %[need]\n\tv_add_u32 v148, 4, v148\n\t"
                     "s_mov_b64 exec, 1\n\tv_add_u32 %[need], %[npk2], %[need]\n\ts_mov_b64 exec, -1\n\t"
                     PV_REQ("126:129", "130:133", "134:135", 136, 148) PV_FETCH_B
                     "s_mov_b64 exec, 2\n\tds_add_u32 v149, %[one] offset:64\n\ts_mov_b64 exec, -1\n"
                     "6:\n\t"
                     PV_STEP(1, "126:129", "130:133", "134:135", 134, 135, 136, 148, "138:141", "142:145", "146:147", 137, 149,
                             "92:95", "98:101", "104:107", "90:93", "96:99", "102:105",
                             PV_MOV(92, 90) PV_MOV(93, 91), PV_MOV(98, 96) PV_MOV(99, 97), PV_MOV(104, 102) PV_MOV(105, 103),
                             "s_cmp_eq_u32 s46, %[last]\n\ts_cbranch_scc1 99f\n\t")
                     "s_cbranch_vccnz 7f\n"
                     "16:\n\t" PV_FETCH_A PV_REL(148)
                     "s_lshl1_add_u32 s54, s46, -2\n\ts_lshr_b64 s[54:55], %[sch], s54\n\ts_and_b32 s54, s54, 15\n\ts_cbranch_scc1 31f\n"
                     "30:\n\t"
                     PV_STEP(2, "138:141", "142:145", "146:147", 146, 147, 137, 149, "126:129", "130:133", "134:135", 136, 148,
                             "110:113", "116:119", "122:125", "108:111", "114:117", "120:123",
                             PV_MOV(110, 108) PV_MOV(111, 109), PV_MOV(116, 114) PV_MOV(117, 115), PV_MOV(122, 120) PV_MOV(123, 121),
                             "s_cmp_eq_u32 s46, %[last] - 1\n\ts_cbranch_scc1 98f\n\t")
                     "s_cbranch_vccnz 8f\n"
                     "26:\n\t" PV_FETCH_B PV_REL(149)
                     "s_branch 6b\n"
                     PV_WAITF(7, 148) "s_branch 16b\n"
                     PV_WAITF(8, 149) "s_branch 26b\n"
                     "98:\n\t" PV_MF("76:79", "114:117", "138:141") PV_MF("80:83", "120:123", "138:141")
                     "v_add_u32 %[need], 2, %[need]\n\ts_add_u32 s46, s46, 1\n\ts_branch 13f\n"
                     PV_SLOW(1, "126:129", "130:133", "134:135", 134, 135, 136, 148, 126, 127, 128, 129)
                     PV_SLOW(2, "138:141", "142:145", "146:147", 146, 147, 137, 149, 138, 139, 140, 141)
                     "31:\n\ts_cmp_lg_u32 s54, 8\n\ts_cbranch_scc1 34f\n"
                     "32:\n\tv_mbcnt_lo_u32_b32 v152, 31, 0\n\tv_lshlrev_b32 v152, 7, v152\n\ts_mov_b32 s55, %[gcap]\n"
                     "33:\n\tglobal_load_dwordx2 v[150:151], v152, %[fl] sc1\n\ts_sub_u32 s55, s55, 1\n\ts_cselect_b32 s56, 0, -1\n\ts_waitcnt vmcnt(0) lgkmcnt(0)\n\t"
                     "v_cmp_ne_u64 s[48:49], %[tag], v[150:151]\n\ts_sleep 2\n\ts_and_b32 s48, s48, s56\n\ts_cbranch_scc1 33b\n\t"
                     "s_mov_b32 s57, 0\n\tv_mov_b32 v150, %[apk]\n\ts_cmp_lg_u32 s46, %[last]\n\ts_cbranch_scc1 30b\n\ts_mov_b32 s54, 7\n\ts_branch 38f\n"
                     "34:\n\ts_cmp_lg_u32 s46, %[last] - 3\n\ts_cbranch_scc1 36f\n\tv_mov_b32 v151, %[iss]\n"
                     "35:\n\tds_read_b32 v150, v151\n\ts_sub_i32 s47, s47, 1\n\ts_cmp_lt_i32 s47, 0\n\ts_cselect_b32 s55, 0, 1\n\ts_waitcnt lgkmcnt(0)\n\t"
                     "v_readfirstlane_b32 s56, v150\n\ts_cmp_lt_u32 s56, s55\n\ts_cbranch_scc1 35b\n"
                     "36:\n\ts_mul_i32 s55, s57, 3072\n\tv_add_u32 v150, s55, %[apk]\n\ts_getpc_b64 s[48:49]\n"
                     "37:\n\ts_mul_i32 s55, s57, 40\n\ts_add_u32 s55, s55, 38f-37b\n\ts_add_u32 s48, s48, s55\n\ts_addc_u32 s49, s49, 0\n\t"
                     "s_add_u32 s57, s57, s54\n\ts_sub_u32 s54, s54, 1\n\ts_setpc_b64 s[48:49]\n"
                     "38:\n\t" PV_W2(0, 3, 4, 7, 8, 11) PV_W2(12, 15, 16, 19, 20, 23) PV_W2(24, 27, 28, 31, 32, 35) PV_W2(36, 39, 40, 43, 44, 47)
                     PV_W2(48, 51, 52, 55, 56, 59) PV_W2(60, 63, 64, 67, 68, 71)
                     "s_waitcnt lgkmcnt(0)\n\ts_branch 3b\n"
                     "99:\n\t" PV_MF("76:79", "96:99", "126:129") PV_MF("80:83", "102:105", "126:129") PV_T(58)
                     "v_add_f32 v153, v86, v87\n\tv_add_f32 v151, v84, v85\n\tv_rcp_f32 v150, v153\n\tv_mul_f32 v151, %[nic], v151\n\t"
                     "v_add_u32 v152, %[aw], %[apk]\n\tv_mul_f32 v151, v151, v150\n\t"
                     PV_NRM(72, 73, 74, 75, 0) PV_NRM(76, 77, 78, 79, 2048) PV_NRM(80, 81, 82, 83, 4096)
                     "v_mov_b32 v154, %[x1]\n\ts_and_b64 exec, exec, 1\n\tds_add_u32 v154, %[one]\n\ts_cselect_b64 exec, -1, 0\n\ts_cselect_b32 s55, %[x1n], 0\n\t" PV_T(60)
                     "v_log_f32 v153, v153\n\tv_subrev_f32 v88, %[toff], v88\n\tv_lshrrev_b32 v151, 1, %[apm]\n\tv_add_f32 v88, v88, v153\n\tv_add_u32 v151, %[lwo], v151\n\t"
                     "v_mbcnt_lo_u32_b32 v155, -1, 0\n\tv_mbcnt_hi_u32_b32 v155, -1, v155\n\tds_write_b32 v151, v88\n\t"
                     "v_and_b32 v151, 1, v155\n\tv_and_b32 v155, 48, v155\n\tv_lshl_add_u32 v155, v151, 2, v155\n\tv_add_u32 v155, %[lw], v155\n\t"
                     "ds_read2_b32 v[114:115], v155 offset1:2\n\tv_add_u32 v152, %[ar], %[apk]\n"
                     "97:\n\tds_read_b32 v150, v154\n\tds_read_b128 v[90:93], v152\n\tds_read_b128 v[94:97], v152 offset:1024\n\tds_read_b128 v[98:101], v152 offset:2048\n\t"
                     "ds_read_b128 v[102:105], v152 offset:3072\n\tds_read_b128 v[106:109], v152 offset:4096\n\tds_read_b128 v[110:113], v152 offset:5120\n\t"
                     "s_sub_i32 s47, s47, 1\n\ts_cmp_lt_i32 s47, 0\n\ts_cselect_b32 s55, 0, s55\n\ts_waitcnt vmcnt(0) lgkmcnt(6)\n\t"
                     "v_readfirstlane_b32 s56, v150\n\ts_cmp_lt_u32 s56, s55\n\ts_cbranch_scc1 97b\n\ts_cmp_eq_u32 s46, 0\n\t"
                     PV_G(5, "90:93", "0:3", "4:7", "8:11", "0", "0", "0")
                     PV_G(4, "94:97", "12:15", "16:19", "20:23", "v[72:75]", "v[76:79]", "v[80:83]")
                     PV_G(3, "98:101", "24:27", "28:31", "32:35", "v[72:75]", "v[76:79]", "v[80:83]")
                     PV_G(2, "102:105", "36:39", "40:43", "44:47", "v[72:75]", "v[76:79]", "v[80:83]")
                     PV_G(1, "106:109", "48:51", "52:55", "56:59", "v[72:75]", "v[76:79]", "v[80:83]")
                     PV_G(0, "110:113", "60:63", "64:67", "68:71", "v[72:75]", "v[76:79]", "v[80:83]")
                     "v_xor_b32 v114, v114, %[ltag]\n\tv_xor_b32 v115, v115, %[ltag]\n\ts_nop 3\n\t"
                     "v_cvt_pk_bf16_f32 v116, v72, v73\n\tv_cvt_pk_bf16_f32 v120, v74, v75\n\tv_cvt_pk_bf16_f32 v117, v76, v77\n\tv_cvt_pk_bf16_f32 v121, v78, v79\n\t"
                     "v_cvt_pk_bf16_f32 v118, v80, v81\n\tv_bitop3_b32 v119, v116, v117, v114 bitop3:0x96\n\tv_cvt_pk_bf16_f32 v122, v82, v83\n\t"
                     "v_bitop3_b32 v123, v120, v121, v115 bitop3:0x96\n\tv_xor_b32 v119, v119, v118\n\tv_xor_b32 v123, v123, v122\n\t"
                     "s_cbranch_scc1 2b\n\t"
                     "global_store_dwordx4 %[so], v[116:119], %[bx]" SPOL "\n\tglobal_store_dwordx4 %[so], v[120:123], %[bx2]" SPOL "\n\t"
                     PV_T(62) "s_waitcnt lgkmcnt(0)\n\ts_nop 0"
                     : [need] "+v"(need), "={s[58:59]}"(tpv), "={s[60:61]}"(tp7), "={s[62:63]}"(tx1)
                     : [so] "v"(((ej * 16 + (lane >> 3)) * NWG + blockIdx.x) * 128 + (lane & 7) * 16), [ltag] "v"(line_tag(epoch, lane & 7)),
                       [bx] "s"(box), [bx2] "s"(box + 8 * NWG * 128), [aw] "s"(A_IMG - IMG_A + (ej >> 1) * 1024 + (ej & 1) * 8), [ar] "i"(A_IMG - IMG_A),
                       [nic] "i"(__builtin_bit_cast(unsigned, -1.f / CC)), [toff] "i"(__builtin_bit_cast(unsigned, TOFF)),
                       [lwo] "i"(CT_OFF + C_LW - PKM / 2), [lw] "i"(CT_OFF + C_LW), [x1] "i"(CT_OFF + C_X1), [x1n] "i"(X1N),
                       [apk] "v"(apk), [apm] "v"(lbase + PKM + r * 8), [apu] "v"(lbase + PKU + g * 64 + (r & 3) * 16),
                       [ox] "v"(lbase + tr_off(4 * g + qq, 4 * ej + p4)), [oy] "v"(lbase + 32 * ROWB + tr_off(2 * g + (qq >> 1), 4 * ej + p4)),
                       [ca] "v"(ct + (lane == 0 ? C_PFL + (k0 % NPK) * 4 : C_FULL + (k0 % NSLOT) * 4)),
                       [lim] "v"(ct + (lane == 0 ? C_PFL + NPK * 4 : C_FULL + NSLOT * 4)), [base] "v"(ct + (lane == 0 ? C_PFL : C_FULL)), [one] "v"(1u),
                       [k0] "s"(k0), [s0] "s"(helper ? ((TPW - 1) % NSLOT) * SLOT : 0), [e0] "s"(helper ? ((TPW - 1) % NPK) * 128 : 0),
                       [w2] "s"(box + W2_OFF + ej * 18432 - (lbase + IMG_A)), [fl] "s"(box + W2F_OFF),
                       [tag] "s"(u32x2{line_tag(epoch, 10), line_tag(epoch, 11)}), [gcap] "i"(GSPIN_CAP), [iss] "i"(CT_OFF + C_ISS), [cap] "i"(SPIN_CAP), [slot] "i"(SLOT), [ring] "i"(NSLOT * SLOT), [pring] "i"(NPK * 128),
                       [npk2] "i"(2 * NPK), [last] "i"(TPW - 1), [tight] "i"(TPW - 5),  [sch] "s"(W2SCH)
                     : "memory", "scc", "vcc", "s44", "s45", "s46", "s47", "s48", "s49", "s50", "s51", "s52", "s53", "s54", "s55", "s56", "s57",
                       "v0", "v1", "v2", "v3", "v4", "v5", "v6", "v7", "v8", "v9", "v10", "v11", "v12", "v13", "v14", "v15", "v16", "v17", "v18", "v19", "v20", "v21", "v22", "v23", "v24", "v25", "v26", "v27", "v28", "v29", "v30", "v31", "v32", "v33", "v34", "v35", "v36", "v37", "v38", "v39", "v40", "v41", "v42", "v43", "v44", "v45", "v46", "v47", "v48", "v49", "v50", "v51", "v52", "v53", "v54", "v55", "v56", "v57", "v58", "v59", "v60", "v61", "v62", "v63", "v64", "v65", "v66", "v67", "v68", "v69", "v70", "v71", "v72", "v73", "v74", "v75", "v76", "v77", "v78", "v79", "v80", "v81", "v82", "v83", "v84", "v85", "v86", "v87", "v88",
                       "v90", "v91", "v92", "v93", "v94", "v95", "v96", "v97", "v98", "v99", "v100", "v101", "v102", "v103", "v104", "v105", "v106", "v107",
                       "v108", "v109", "v110", "v111", "v112", "v113", "v114", "v115", "v116", "v117", "v118", "v119", "v120", "v121", "v122", "v123",
                       "v124", "v125", "v126", "v127", "v128", "v129", "v130", "v131", "v132", "v133", "v134", "v135", "v136", "v137", "v138", "v139",
                       "v140", "v141", "v142", "v143", "v144", "v145", "v146", "v147", "v148", "v149", "v150", "v151", "v152", "v153", "v154", "v155");
      }
    } else {
      LANE_VARS
      const int c = wave - 1 - NE;
      SCtx sc;
      sc.L0 = L0; sc.ct = ct; sc.apk = lbase + IMG_A + lane * 16; sc.apm = lbase + PKM + r * 8;
      sc.api = lbase + PKU + g * 64 + (r & 3) * 2 + ((r >> 2) == g ? 0 : 8); sc.amr = ct + C_MREF + r * 4;
      sc.o_rb = r * ROWB + ((g ^ swz(r)) << 4);
      sc.ev = (r & 1) ^ 1;
      sc.o_yb = (r & 1) ? ONES_OFF + (g << 4) : (32 + (r >> 1)) * ROWB + ((g ^ swz(r >> 1)) << 4);
      TNOW(tq0);
      if (c >= NEARLY) {
        static_assert(NS - NEARLY == 3, "");
        const int u = c - NEARLY;
        const unsigned epoch = epoch_load(box);
        const unsigned ta = line_tag(epoch, 8), tb = line_tag(epoch, 9);
        while ((unsigned)__builtin_amdgcn_s_memrealtime() - tent < QKWAIT) __builtin_amdgcn_s_sleep(4);
        u32x4 v[4];
#pragma unroll 1
        for (int sp = 0; sp < GSPIN_CAP; ++sp) {
          asm volatile("global_load_dwordx4 %0, %4, %5 sc1\n\tglobal_load_dwordx4 %1, %4, %5 offset:1024 sc1\n\tglobal_load_dwordx4 %2, %4, %5 offset:2048 sc1\n\t"
                       "global_load_dwordx4 %3, %4, %5 offset:3072 sc1\n\ts_waitcnt vmcnt(0)"
                       : "=&v"(v[0]), "=&v"(v[1]), "=&v"(v[2]), "=&v"(v[3]) : "v"(lane * 16), "s"(box + QK_OFF + u * 4096) : "memory");
          unsigned bad = 0;
#pragma unroll
          for (int t = 0; t < 4; ++t) bad |= (v[t][0] ^ v[t][1] ^ v[t][2] ^ ta) | (v[t][3] ^ tb);
          if (!__any(bad != 0u)) break;
        }
        TSET(tqa);
#pragma unroll
        for (int t = 0; t < 4; ++t)
          *(LDS_AS(u32x2)*)(L0 + IMG_A + (4 * u + t) * 512 + ((g >> 1) * 16 + r) * 16 + (g & 1) * 8) = u32x2{v[t][0], v[t][1]};
        asm volatile("" ::: "memory");
        lds_add(ct + C_QI, 1u);
        lds_st32(ct + C_EP, epoch);
      }
      static_assert(NE == 4 && NPK == 6 && NSLOT == 10, "");
      s_items(sc, helper ? TPW : c, helper || c >= NEARLY);
      const int hi = (HMAP >> (4 * c)) & 15;
      if (hi < 6 && (!helper || hi >= 4)) s_half(sc, TPW - NH1 + (hi >> 1), hi & 1);
      TNOW(tse);
      rslot = c;
    }
  }

  if (rslot >= 0 && ((blockIdx.x >> 3) & 3) == 1) {
    LANE_VARS
    __builtin_amdgcn_s_setprio(0);
    const int rid = ((blockIdx.x >> 5) << 3) | (blockIdx.x & 7);
    const int qa = ((rid >> 1) & 3) * 4 + ((rid >> 3) & 1) * 2;
    const char* src = box + (size_t)(rid * NWG + rslot * 32) * 128;
    const int e = lane & 7;
    const int oidx = (qa + (e & 1)) * CC + 48 * (rid >> 4) + 16 * (e < 6 ? e >> 1 : 0) + 8 * (rid & 1) + (lane >> 3);
    const unsigned short qv = ((const gus*)q)[oidx];
    u32x4 am;
#pragma unroll
    for (int d = 0; d < 4; ++d) am[d] = (e >> 1) == d ? ((e & 1) ? 0xffff0000u : 0x0000ffffu) : 0u;
    const unsigned epoch = *(LDS_AS(unsigned)*)(L0 + CT_OFF + C_EP);
    const unsigned arf = lbase + RED_OFF + 4096 + (lane & 1) * 4;
    poll_ge_sleep(ct + C_FREE + ((TPW - NPK) % NSLOT) * 4, 8u * ((TPW - NPK) / NSLOT + 1));
    lds_st32(arf + rslot * 8, 0u);
    unsigned long long tr;
#define R_LD(d, m, o) "s_mov_b64 exec, s[" m "]\n\tglobal_load_dwordx4 v[" d "], %[vo], %[src] offset:" #o " sc1\n\t"
#define R_W(l, a0, a1, a2, a3) "v_cvt_pk_bf16_f32 v" #a3 ", v" #a3 ", v" #a3 "\n\tv_and_b32 v" #a0 ", v60, v" #a3 "\n\tv_and_b32 v" #a1 ", v61, v" #a3 "\n\t" \
                               "v_and_b32 v" #a2 ", v62, v" #a3 "\n\tv_and_b32 v" #a3 ", v63, v" #a3 "\n\t"
#define R_G(n, w, m, l, d0, d1, d2, d3, a0, a1, a2, a3, c) \
  "s_mov_b64 exec, s[" m "]\n\ts_cbranch_execz " #n "f\n\ts_waitcnt vmcnt(" #w ")\n\tv_bitop3_b32 v" #l ", v" #d0 ", v" #d1 ", v" #d2 " bitop3:0x96\n\t" \
  "v_bitop3_b32 v" #l ", v" #l ", v" #d3 ", %[tag] bitop3:0x96\n\tv_mov_b32 v" #d3 ", s49\n\ts_mov_b64 exec, -1\n\tv_exp_f32 v" #a3 ", v" #l "\n\t" \
  "v_xor_b32_dpp v59, v" #l ", v" #l " quad_perm:[2,3,0,1] row_mask:0xf bank_mask:0x5\n\tv_xor_b32_dpp v59, v" #l ", v" #l " row_shr:4 row_mask:0xf bank_mask:0xa\n\t" \
  R_W(l, a0, a1, a2, a3) "v_cmp_ne_u32 s[" m "], 0, v59\n\ts_nop 0\n" \
  "1" #n ":\n\tv_mfma_f32_16x16x32_bf16 v[36:39], v[" #a0 ":" #a3 "], v[" #d0 ":" #d3 "], " c "\n\t"
#define R_X(n) #n ":\n\ts_mov_b64 exec, -1\n\ts_nop 2\n\ts_branch 1" #n "b\n"
#define R_F(l, d0, d3, a0, a1, a2, a3, c) "v_sub_f32 v" #a3 ", v" #l ", v56\n\tv_exp_f32 v" #a3 ", v" #a3 "\n\ts_nop 0\n\t" R_W(l, a0, a1, a2, a3) \
  "s_nop 1\n\tv_mfma_f32_16x16x32_bf16 v[36:39], v[" #a0 ":" #a3 "], v[" #d0 ":" #d3 "], " c "\n\t"
#define R_RD(d, a, b) "ds_read2st64_b32 v[" d "], %[ard] offset0:" #a " offset1:" #b "\n\t"
#define R_A(d, a) "v_pk_add_f32 v[" d "], v[" d "], v[" a "]\n\t"
#define R_S(x, f) "v_sub_f32 v" #f ", v" #f ", v56\n\tv_exp_f32 v" #f ", v" #f "\n\ts_nop 0\n\tv_mul_f32 v[" #x "], v[" #x "], v" #f "\n\tv_mul_f32 v[" #x " + 1], v[" #x " + 1], v" #f "\n\t"
    asm volatile("s_mov_b64 s[52:53], 0\n\ts_mov_b64 s[40:41], -1\n\ts_mov_b64 s[42:43], -1\n\ts_mov_b64 s[44:45], -1\n\ts_mov_b64 s[46:47], -1\n\ts_mov_b32 s48, %[cap]\n\ts_mov_b32 s49, 0x3f803f80\n\t"
                 "v_mov_b32 v57, 1\n\tv_mov_b32 v58, %[x1]\n"
                 "20:\n\ts_sleep 1\n\tds_read_b32 v56, v58\n\ts_sub_u32 s48, s48, 1\n\ts_cselect_b32 s50, 0, %[x1n]\n\ts_waitcnt lgkmcnt(0)\n\t"
                 "v_readfirstlane_b32 s51, v56\n\ts_cmp_lt_u32 s51, s50\n\ts_cbranch_scc1 20b\n\tv_mov_b32 v58, %[red]\n\ts_mov_b32 s48, %[gcap]\n\ts_sleep %[rs]\n"
                 "21:\n\t" R_LD("0:3", "40:41", 0) R_LD("4:7", "42:43", 1024) R_LD("8:11", "44:45", 2048) R_LD("12:15", "46:47", 3072)
                 "s_cmp_eq_u32 s48, %[gcap]\n\ts_cbranch_scc1 24f\n\ts_waitcnt vmcnt(0)\n24:\n\t"
                 R_G(1, 3, "40:41", 32, 0, 1, 2, 3, 16, 17, 18, 19, "0") R_G(2, 2, "42:43", 33, 4, 5, 6, 7, 20, 21, 22, 23, "v[36:39]")
                 R_G(3, 1, "44:45", 34, 8, 9, 10, 11, 24, 25, 26, 27, "v[36:39]") R_G(4, 0, "46:47", 35, 12, 13, 14, 15, 28, 29, 30, 31, "v[36:39]")
                 "v_max3_f32 v56, v32, v33, v34\n\ts_sub_u32 s48, s48, 1\n\ts_cbranch_scc1 22f\n\ts_or_b64 s[50:51], s[40:41], s[42:43]\n\tv_max_f32 v56, v56, v35\n\t"
                 "s_or_b64 s[50:51], s[50:51], s[44:45]\n\ts_or_b64 s[50:51], s[50:51], s[46:47]\n\tv_cmp_nlt_f32 vcc, |v56|, %[lim]\n\ts_cbranch_scc1 21b\n\t"
                 "s_cbranch_vccnz 25f\n"
                 "22:\n\ts_mov_b64 exec, %[wm]\n\tds_write_b128 %[awr], v[36:39]\n\ts_mov_b64 exec, 1\n\tds_add_rtn_u32 v59, v58, v57\n\ts_mov_b64 exec, -1\n\t"
                 R_RD("40:41", 0, 1) R_RD("42:43", 2, 3) R_RD("44:45", 4, 5) R_RD("46:47", 6, 7) R_RD("48:49", 8, 9) R_RD("50:51", 10, 11) R_RD("52:53", 12, 13) R_RD("54:55", 14, 15)
                 "s_waitcnt lgkmcnt(8)\n\tv_add_u32 v59, v59, v57\n\ts_nop 0\n\tv_readfirstlane_b32 s50, v59\n\ts_cmp_lg_u32 s50, 8\n\ts_cbranch_scc1 26f\n"
                 "23:\n\ts_waitcnt lgkmcnt(6)\n\t" R_A("40:41", "42:43") "s_waitcnt lgkmcnt(4)\n\t" R_A("44:45", "46:47") "s_waitcnt lgkmcnt(2)\n\t" R_A("48:49", "50:51")
                 R_A("40:41", "44:45") "s_waitcnt lgkmcnt(0)\n\t" R_A("52:53", "54:55") R_A("48:49", "52:53") R_A("40:41", "48:49")
                 "v_add_f32 v40, v40, v41\n\ts_nop 1\n\tv_mov_b32_dpp v41, v40 quad_perm:[2,3,2,3] row_mask:0xf bank_mask:0xf\n\ts_nop 1\n\t"
                 "v_mov_b32_dpp v41, v41 row_shl:4 row_mask:0xf bank_mask:0x5\n\tv_rcp_f32 v41, v41\n\ts_mov_b64 exec, %[om]\n\tv_mul_f32 v40, v40, v41\n\t"
                 "v_cvt_pk_bf16_f32 v40, v40, v40\n\tv_lshlrev_b32 v40, 16, v40\n\tv_add_f32 v40, v40, %[qf]\n\tv_cvt_pk_bf16_f32 v40, v40, v40\n\t"
                 "global_store_short %[oo], v40, %[out]\n\t"
                 "s_cmp_lg_u32 %[rid], 0\n\ts_cbranch_scc1 29f\n\ts_mov_b64 exec, 1\n\tv_mov_b32 v41, %[eo]\n\tglobal_store_dword v41, %[ep], %[bx] sc1\n\ts_branch 29f\n"
                 R_X(1) R_X(2) R_X(3) R_X(4)
                 "25:\n\tv_max_f32_dpp v56, v56, v56 row_ror:8 row_mask:0xf bank_mask:0xf\n\tv_mov_b32 v59, v56\n\ts_nop 1\n\tv_permlane16_swap_b32 v56, v59\n\tv_max_f32 v56, v56, v59\n\t"
                 "v_mov_b32 v59, v56\n\ts_nop 1\n\tv_permlane32_swap_b32 v56, v59\n\tv_max_f32 v56, v56, v59\n\tv_mov_b32 v57, 0x101\n\tv_add_u32 v59, %[rs8], %[arf]\n\tds_write_b32 v59, v56\n\t"
                 R_F(32, 0, 3, 16, 17, 18, 19, "0") R_F(33, 4, 7, 20, 21, 22, 23, "v[36:39]") R_F(34, 8, 11, 24, 25, 26, 27, "v[36:39]") R_F(35, 12, 15, 28, 29, 30, 31, "v[36:39]")
                 "s_nop 7\n\ts_branch 22b\n"
                 "26:\n\ts_waitcnt vmcnt(0)\n\ts_and_b32 s50, s50, 0xff\n\ts_cmp_lg_u32 s50, 8\n\ts_cbranch_scc1 29f\n\t"
                 "ds_read2_b32 v[0:1], %[arf] offset1:2\n\tds_read2_b32 v[2:3], %[arf] offset0:4 offset1:6\n\tds_read2_b32 v[4:5], %[arf] offset0:8 offset1:10\n\t"
                 "ds_read2_b32 v[6:7], %[arf] offset0:12 offset1:14\n\ts_waitcnt lgkmcnt(0)\n\tv_max3_f32 v56, v0, v1, v2\n\tv_max3_f32 v56, v56, v3, v4\n\t"
                 "v_max3_f32 v56, v56, v5, v6\n\tv_max_f32 v56, v56, v7\n\t"
                 R_S(40, 0) R_S(42, 1) R_S(44, 2) R_S(46, 3) R_S(48, 4) R_S(50, 5) R_S(52, 6) R_S(54, 7) "s_branch 23b\n"
                 "29:\n\ts_mov_b64 exec, -1\n\ts_waitcnt lgkmcnt(0)"
                 : "=&{s[52:53]}"(tr)
                 : "{v[60:63]}"(am), [vo] "v"(lane * 16), [src] "s"(src), [tag] "v"(line_tag(epoch, e)),
                   [awr] "v"(lbase + RED_OFF + (rslot * 2 + (r >> 3)) * 256 + (r & 7) * 32 + (g & 1) * 16), [ard] "v"(lbase + RED_OFF + lane * 4), [arf] "v"(arf),
                   [qf] "v"((unsigned)qv << 16), [oo] "v"(oidx * 2), [out] "s"(KARG(kb, 0)), [ep] "v"(epoch + 1u), [bx] "s"(box), [rid] "s"(rid), [rs8] "s"(rslot * 8),
                   [wm] "s"(0xff00ff0000ff00ffull), [om] "s"(0x3f3f3f3f3f3f3f3full), [lim] "s"(64.f),
                   [cap] "i"(SPIN_CAP), [gcap] "i"(GSPIN_CAP), [x1] "i"(CT_OFF + C_X1), [x1n] "i"(X1N), [red] "i"(CT_OFF + C_RED), [rs] "i"(RSLEEP), [eo] "i"(EPOCH_OFF)
                 : "memory", "scc", "vcc", "s40", "s41", "s42", "s43", "s44", "s45", "s46", "s47", "s48", "s49", "s50", "s51", "v0", "v1", "v2", "v3", "v4", "v5", "v6", "v7", "v8", "v9", "v10", "v11", "v12", "v13", "v14", "v15", "v16", "v17", "v18", "v19", "v20", "v21", "v22", "v23", "v24", "v25", "v26", "v27", "v28", "v29", "v30", "v31", "v32", "v33", "v34", "v35", "v36", "v37", "v38", "v39", "v40", "v41", "v42", "v43", "v44", "v45", "v46", "v47", "v48", "v49", "v50", "v51", "v52", "v53", "v54", "v55", "v56", "v57", "v58", "v59");
  }
}

#undef q
#undef f0
#undef f1
#undef wq
#undef wk
#undef wv
#undef wo
#undef box
extern "C" __global__ void dino_end() {}

extern "C" void kernel_launch(void* const* d_in, const int* in_sizes, int n_in,
                              void* d_out, int out_size, void* d_ws, size_t ws_size,
                              hipStream_t stream) {
  dino_main<<<NWG, 768, 0, stream>>>((const unsigned short*)d_in[0], (const char*)d_in[1], (const char*)d_in[2], (const char*)d_in[3],
                                     (const char*)d_in[4], (const char*)d_in[5], (const char*)d_in[6], (char*)d_ws + WSOFF,
                                     (unsigned short*)d_out);
}
```

```cpp
#include <hip/hip_runtime.h>
#include <hip/hip_bf16.h>
#include <stdint.h>

#define PROFOFF (380ll << 20)
#define TNOW(v) do {} while (0)
#define TPUT(id, v) do {} while (0)

using bf16x8  = __attribute__((ext_vector_type(8))) short;
using f32x4   = __attribute__((ext_vector_type(4))) float;
using f32x2   = __attribute__((ext_vector_type(2))) float;
using u32x4   = __attribute__((ext_vector_type(4))) unsigned;
using u32x2   = __attribute__((ext_vector_type(2))) unsigned;
using v4i16_t = __attribute__((ext_vector_type(4))) short;
typedef __attribute__((address_space(3))) char* lds_ptr;
#define LDS_AS(T) __attribute__((address_space(3))) T

constexpr int CC = 192, IMW = 512;
constexpr int NWG = 256, NS = 7, NE = 4;
constexpr int TPW = 33;
constexpr int ROWB = 384;
constexpr int SLOT = 40 * ROWB;
constexpr int NSLOT = 10;
constexpr int BOX_BYTES = 64 * NWG * 128, EPOCH_OFF = BOX_BYTES, QK_OFF = EPOCH_OFF + 4096, W2F_OFF = QK_OFF + 12288, W2_OFF = W2F_OFF + 1024;
constexpr int GSPIN_CAP = 1 << 16;
#define PACE16 777
#define QKWAIT 300
#define NEARLY 4
#define PRECLR 4
#define RSLEEP 24
#define HDUE 93
#define PACES 810
#define PK1 14
#define AG 100
#define AHI 22
#define ADL 60
#define AMG 200
#define NORELAY
#define HMAP 0x4321085
#define HB 1
#define WSOFF ((ws_size - (4u << 20)) & ~(size_t)0x1fffff)
#define EPM 0
#define RSTART 1880
#define RMODE 1
#define CLRK 17
#define X1N (PRECLR == 5 ? 11u : 5u)
#define RPOL " sc1"
#define SPOL " sc1"
#define RGAP 0
#define RDRY 0
#define RWAIT 76
#define RMIN 30
#define PROBE 0xffffffffffffffffull
#define RELAYT 0
#define PRE5 1
#define SL_S 2
#define SL_FAR 12
#define SL_P 2
#define SL_L 1
#define STR_(x) #x
#define STR(x) STR_(x)
#define AHIR AHI
#define OTOUCH 0
#define TLBPF 0
#define T0BACK 7
#define W2SCH 0x0311180000000000ull
#define NH1 3
constexpr int NPK = 6;
constexpr float EPS = 1e-5f;
constexpr float SQRT_C = 13.856406460551018f;
constexpr float LOG2E = 1.4426950408889634f;
constexpr float THR = 20.0f;
constexpr float TOFF = 192.0f;
constexpr int SPIN_CAP = 1 << 22;

constexpr int IMG_A = NSLOT * SLOT;
constexpr int CT_OFF = IMG_A + 6144;
constexpr int PKM = CT_OFF + 512, PKU = PKM + NPK * 128;
constexpr int ONES_OFF = PKU + NPK * 256;
constexpr int JUNK = ONES_OFF + ROWB;
constexpr int LDS_BYTES = JUNK + 256;
constexpr int H_R = 192 * ROWB, H_Q = H_R + 32 * ROWB, H_C = H_Q + 6144;
#define PWIN 35
static_assert(LDS_BYTES <= 163840 && (PKU & 15) == 0 && (ONES_OFF & 127) == 0, "lds");
constexpr int C_FULL = 0, C_FREE = 64, C_EP = 128, C_RED = 144, C_ISS = 152, C_X1 = 156, C_QI = 160, C_PFL = 192, C_CONS = 256,
              C_MREF = 320, C_LW = 384;

typedef __bf16 bf16x2_t __attribute__((ext_vector_type(2)));
__device__ __forceinline__ unsigned cvtpk(float lo, float hi) {
  return __builtin_bit_cast(unsigned, __builtin_convertvector(f32x2{lo, hi}, bf16x2_t));
}
__device__ __forceinline__ float rbf(float x) { return __uint_as_float(cvtpk(x, x) << 16); }
__device__ __forceinline__ float bflo(unsigned p) { return __uint_as_float(p << 16); }
__device__ __forceinline__ float bfhi(unsigned p) { return __uint_as_float(p & 0xffff0000u); }
__device__ __forceinline__ bf16x8 pack8(const f32x4& a, const f32x4& b) {
  return __builtin_bit_cast(bf16x8, u32x4{cvtpk(a[0], a[1]), cvtpk(a[2], a[3]), cvtpk(b[0], b[1]), cvtpk(b[2], b[3])});
}
__device__ __forceinline__ float xrow16_max(float x) {
  auto s = __builtin_amdgcn_permlane16_swap(__float_as_uint(x), __float_as_uint(x), false, false);
  x = fmaxf(__uint_as_float(s[0]), __uint_as_float(s[1]));
  auto t = __builtin_amdgcn_permlane32_swap(__float_as_uint(x), __float_as_uint(x), false, false);
  return fmaxf(__uint_as_float(t[0]), __uint_as_float(t[1]));
}
__device__ __forceinline__ float xrow16_sum(float x) {
  auto s = __builtin_amdgcn_permlane16_swap(__float_as_uint(x), __float_as_uint(x), false, false);
  x = __uint_as_float(s[0]) + __uint_as_float(s[1]);
  auto t = __builtin_amdgcn_permlane32_swap(__float_as_uint(x), __float_as_uint(x), false, false);
  return __uint_as_float(t[0]) + __uint_as_float(t[1]);
}
__device__ __forceinline__ v4i16_t tr_read(lds_ptr p) {
  return __builtin_amdgcn_ds_read_tr16_b64_v4i16((LDS_AS(v4i16_t)*)p);
}
__device__ __forceinline__ f32x4 mfma(bf16x8 a, bf16x8 b, f32x4 c) {
  return __builtin_amdgcn_mfma_f32_16x16x32_bf16(a, b, c, 0, 0, 0);
}
__device__ __forceinline__ int swz(int p) { return (p & 6) | ((p >> 2) & 1); }
constexpr unsigned swz_tbl(int i) {
  unsigned t = 0;
  for (int j = 0; j < 8; ++j) { const int p = (8 * i + j) / 3; t |= (unsigned)((p & 6) | ((p >> 2) & 1)) << (4 * j); }
  return t;
}
__device__ __forceinline__ bf16x8 mk8(v4i16_t a, v4i16_t b) { return bf16x8{a[0], a[1], a[2], a[3], b[0], b[1], b[2], b[3]}; }

__device__ __forceinline__ void lds_st32(unsigned addr, unsigned v) {
  asm volatile("ds_write_b32 %0, %1" : : "v"(addr), "v"(v) : "memory");
}
__device__ __forceinline__ unsigned lds_ld32(unsigned addr) {
  unsigned v;
  asm volatile("ds_read_b32 %0, %1\n\ts_waitcnt lgkmcnt(0)" : "=v"(v) : "v"(addr) : "memory");
  return __builtin_amdgcn_readfirstlane(v);
}
__device__ __forceinline__ void lds_add(unsigned addr, unsigned n) {
  asm volatile("s_mov_b64 exec, 1\n\tds_add_u32 %0, %1\n\ts_mov_b64 exec, -1\n\ts_nop 3" : : "v"(addr), "v"(n) : "memory");
}
__device__ __forceinline__ void poll_ge(unsigned addr, unsigned need) {
  unsigned v, s, n2, cnt = SPIN_CAP;
  asm volatile("1:\n\tds_read_b32 %0, %4\n\ts_sub_u32 %3, %3, 1\n\ts_cselect_b32 %2, 0, %5\n\ts_waitcnt lgkmcnt(0)\n\t"
               "v_readfirstlane_b32 %1, %0\n\ts_cmp_lt_u32 %1, %2\n\ts_cbranch_scc1 1b"
               : "=&v"(v), "=&s"(s), "=&s"(n2), "+s"(cnt) : "v"(addr), "s"(need) : "memory", "scc");
}
__device__ __forceinline__ void poll_ge_sleep(unsigned addr, unsigned need) {
  need = __builtin_amdgcn_readfirstlane(need);
  unsigned v, s, n2, cnt = SPIN_CAP;
  asm volatile("s_branch 2f\n1:\n\ts_sleep " STR(SL_S) "\n2:\n\tds_read_b32 %0, %4\n\ts_sub_u32 %3, %3, 1\n\ts_cselect_b32 %2, 0, %5\n\ts_waitcnt lgkmcnt(0)\n\t"
               "v_readfirstlane_b32 %1, %0\n\ts_cmp_lt_u32 %1, %2\n\ts_cbranch_scc1 1b"
               : "=&v"(v), "=&s"(s), "=&s"(n2), "+s"(cnt) : "v"(addr), "s"(need) : "memory", "scc");
}
__device__ __forceinline__ bf16x8 img_ld(lds_ptr L0, int off, int kk, int lane) { return *(LDS_AS(bf16x8)*)(L0 + off + kk * 1024 + lane * 16); }
__device__ __forceinline__ void img_st(lds_ptr L0, int off, int kk, int lane, bf16x8 v) { *(LDS_AS(bf16x8)*)(L0 + off + kk * 1024 + lane * 16) = v; }

template <int N> __device__ __forceinline__ void wait_vm() { asm volatile("s_waitcnt vmcnt(%0)" :: "i"(N) : "memory"); }
constexpr int A_IMG = ((TPW + 1 - NPK) % NSLOT) * SLOT, RED_OFF = ((TPW - NPK) % NSLOT) * SLOT;
__device__ __forceinline__ unsigned line_tag(unsigned epoch, unsigned i) {
  unsigned x = (epoch + 1u + i * 0x9E3779B9u) * 0x85EBCA6Bu;
  x ^= x >> 15; x *= 0xC2B2AE35u;
  return x ^ (x >> 13);
}
__device__ __forceinline__ unsigned epoch_load(const char* box) {
  unsigned e;
  asm volatile("global_load_dword %0, %1, %2 sc1\n\ts_waitcnt vmcnt(0)" : "=v"(e) : "v"(EPOCH_OFF), "s"(box) : "memory");
  return __builtin_amdgcn_readfirstlane(e);
}
__device__ __forceinline__ int tr_off(int row, int col4) {
  const int ch = col4 >> 1;
  return row * ROWB + (((ch & ~7) | ((ch & 7) ^ swz(row & 15))) << 4) + 8 * (col4 & 1);
}
#define PIN3(a, i) "+v"(a[i]), "+v"(a[i + 1]), "+v"(a[i + 2])

#define DMA1(d, b, off, pol) "global_load_lds_dwordx4 %[" #d "], %[" #b "] offset:" #off pol "\n\t"
__device__ __forceinline__ void issue_rows16(const char* a0, unsigned dst, const unsigned (&d)[3]) {
  unsigned keep;
  asm volatile("s_mov_b32 %[keep], m0\n\ts_mov_b32 m0, %[dst]\n\ts_nop 0\n\t"
               DMA1(d0, a, 0, "") DMA1(d1, a, 1024, "") DMA1(d2, a, 2048, "") DMA1(d0, a, 3072, "")
               "s_add_u32 m0, %[dst], 0x1000\n\ts_nop 0\n\t" DMA1(d1, b, 0, "") DMA1(d2, b, 1024, "") "s_mov_b32 m0, %[keep]"
               : [keep] "=&s"(keep) : [dst] "s"(dst), [a] "s"(a0), [b] "s"(a0 + 4096), [d0] "v"(d[0]), [d1] "v"(d[1]), [d2] "v"(d[2]) : "memory", "scc");
}
#define LD_VM "s_getreg_b32 s43, hwreg(HW_REG_IB_STS, 22, 2)\n\ts_getreg_b32 s42, hwreg(HW_REG_IB_STS, 0, 4)\n\ts_lshl4_add_u32 s42, s43, s42\n\t"
#define LD_GATE(n, X) \
  #n "0:\n\t" LD_VM "s_cmp_le_i32 s42, s46\n\ts_cbranch_scc1 " #n "2f\n" \
  #n "1:\n\t" X "s_cmp_gt_u32 s42, %[win]\n\ts_cbranch_scc1 " #n "5f\n\ts_cmp_ge_u32 s45, s44\n\ts_cbranch_scc1 " #n "7f\n" \
  #n "6:\n\ts_memrealtime s[40:41]\n\ts_sleep " STR(SL_L) "\n\ts_waitcnt lgkmcnt(0)\n\ts_sub_u32 s45, s40, %[t0]\n\ts_lshl_b32 s45, s45, 4\n\ts_cmp_lt_u32 s45, s44\n\ts_cbranch_scc1 " #n "6b\n" \
  #n "7:\n\t" LD_VM
#define LD_NEXT \
  "s_cmp_gt_u32 s42, %[ahi]\n\ts_cselect_b32 s42, %[amg], 0\n\ts_sub_u32 s43, s45, s44\n\ts_cmp_lt_u32 s43, s42\n\ts_cselect_b32 s42, %[p3] + %[adl], s39\n\t" \
  "s_add_u32 s44, s44, s42\n\ts_add_u32 s46, s46, 5\n\t"
#define LD_PUB0(n, a, b, c) \
  #n "2:\n\tv_add_u32 %[pub], 2, %[pub]\n\ts_cmp_ge_u32 s46, 30\n\ts_cbranch_scc1 " #n "4f\n\ts_cmp_ge_u32 s46, 15\n\ts_cbranch_scc1 " #n "3f\n\t" \
  "s_waitcnt vmcnt(" #a ")\n" #n "3:\n\ts_waitcnt vmcnt(" #b ")\n" #n "4:\n\ts_waitcnt vmcnt(" #c ")\n\tds_write_b32 %[af], %[pub] ; NOTRACK\n\t" \
  "s_sub_u32 s46, s46, 15\n\ts_branch " #n "1b\n"
#define LD_PUB(n, a, b, c) LD_PUB0(n, a, b, c) \
  #n "5:\n\ts_sleep 1\n\ts_sub_i32 s47, s47, 1\n\ts_cmp_gt_i32 s47, 0\n\ts_cbranch_scc1 " #n "0b\n\ts_waitcnt vmcnt(%[win])\n\ts_branch " #n "6b\n"
#define LD1(d, b, off) "global_load_lds_dwordx4 %[" #d "], s[" #b "] offset:" #off " nt\n\t"

#define PV_REQ(P, U, M, C, CA) \
  "v_lshl_add_u32 v150, s44, 3, %[apk]\n\tv_lshl_add_u32 v151, s44, 1, %[apu]\n\tv_add_u32 v152, s44, %[apm]\n\t" \
  "ds_read_b32 v" #C ", v" #CA "\n\tds_read_b128 v[" P "], v150\n\tds_read_b64 v[" M "], v152\n\tds_read_b128 v[" U "], v151\n\t"
#define PV_TR(d0, d1, a, o) "ds_read_b64_tr_b16 v[" #d0 ":" #d1 "], v" #a " offset:" #o "\n\t"
#define PV_FETCH(y0, y0b, a0, a0b, b0, b0b, y1, y1b, a1, a1b, b1, b1b, y2, y2b, a2, a2b, b2, b2b) \
  "v_add_u32 v153, s45, %[ox]\n\tv_add_u32 v154, s45, %[oy]\n\t" \
  PV_TR(a0, a0b, 153, 0) PV_TR(b0, b0b, 153, 6144) PV_TR(a1, a1b, 153, 128) PV_TR(b1, b1b, 153, 6272) PV_TR(a2, a2b, 153, 256) PV_TR(b2, b2b, 153, 6400) \
  PV_TR(y0, y0b, 154, 0) PV_TR(y1, y1b, 154, 128) PV_TR(y2, y2b, 154, 256) \
  "s_add_u32 s45, s45, %[slot]\n\ts_cmp_eq_u32 s45, %[ring]\n\ts_cselect_b32 s45, 0, s45\n\t"
#define PV_FETCH_A PV_FETCH(90, 91, 92, 93, 94, 95, 96, 97, 98, 99, 100, 101, 102, 103, 104, 105, 106, 107)
#define PV_FETCH_B PV_FETCH(108, 109, 110, 111, 112, 113, 114, 115, 116, 117, 118, 119, 120, 121, 122, 123, 124, 125)
#define PV_MF(d, a, p) "v_mfma_f32_16x16x32_bf16 v[" d "], v[" a "], v[" p "], v[" d "]\n\t"
#define PV_MOV(d, s) "v_mov_b32 v" #d ", v" #s "\n\t"
#define PV_WAITF(n, CA)   \
  #n ":\n\tds_read_b32 v155, v" #CA "\n\ts_sub_i32 s47, s47, 1\n\ts_cmp_lt_i32 s47, 0\n\ts_cbranch_scc1 " #n "9f\n\ts_waitcnt lgkmcnt(0)\n\t" \
  "v_cmp_lt_u32 vcc, v155, %[need]\n\ts_and_b64 vcc, vcc, 2\n\ts_cbranch_scc0 " #n "9f\n\ts_sleep " STR(SL_P) "\n\ts_branch " #n "b\n" #n "9:\n\ts_waitcnt lgkmcnt(0)\n\t"
#define PV_STEP(n, P, U, M, m0, m1, C, CA, P2, U2, M2, C2, CA2, X0, X1, X2, Y0, Y1, Y2, MV0, MV1, MV2, HOOK) \
  "s_waitcnt lgkmcnt(10)\n\t" \
  "v_cmp_lt_u32 vcc, v" #C ", %[need]\n\tv_cmp_neq_f32 s[48:49], v" #m0 ", v88\n\tv_cmp_neq_f32 s[50:51], v" #m1 ", v88\n\t" \
  "v_add_u32 v" #CA2 ", 4, v" #CA "\n\ts_or_b64 s[48:49], s[48:49], vcc\n\tv_cmp_eq_u32 s[52:53], v" #CA2 ", %[lim]\n\t" \
  "s_or_b64 s[48:49], s[48:49], s[50:51]\n\ts_cbranch_scc1 " #n "1f\n" \
  #n "0:\n\ts_add_u32 s44, s44, 128\n\ts_cmp_eq_u32 s44, %[pring]\n\ts_cselect_b32 s44, 0, s44\n\t" \
  "v_cndmask_b32 v" #CA2 ", v" #CA2 ", %[base], s[52:53]\n\t" \
  PV_MF("84:87", U, P) PV_MF("72:75", X0, P) PV_MF("76:79", X1, P) MV0 PV_MF("80:83", X2, P) MV1 \
  PV_MF("72:75", Y0, P) MV2 HOOK PV_REQ(P2, U2, M2, C2, CA2) PV_MF("76:79", Y1, P) PV_MF("80:83", Y2, P)
#define PV_REL(CA) \
  "s_mov_b64 exec, 3\n\tds_add_u32 v" #CA ", %[one] offset:64\n\ts_mov_b64 exec, -1\n\tv_add_u32 %[need], 2, %[need]\n\ts_add_u32 s46, s46, 1\n\t"
#define PV_SC1(p) "v_lshlrev_b32 v151, 16, v" #p "\n\tv_and_b32 v152, 0xffff0000, v" #p "\n\tv_mul_f32 v151, v151, v150\n\tv_mul_f32 v152, v152, v150\n\t" \
                  "v_cvt_pk_bf16_f32 v" #p ", v151, v152\n\t"
#define PV_MUL4(a, b, c, d) "v_mul_f32 v" #a ", v" #a ", v150\n\tv_mul_f32 v" #b ", v" #b ", v150\n\tv_mul_f32 v" #c ", v" #c ", v150\n\tv_mul_f32 v" #d ", v" #d ", v150\n\t"
#define PV_SLOW(n, P, U, M, m0, m1, C, CA, p0, p1, p2, p3) \
  #n "1:\n\ts_bitcmp0_b32 vcc_lo, 0\n\ts_cbranch_scc1 " #n "4f\n\ts_cmp_gt_u32 s46, %[tight]\n\ts_cbranch_scc1 " #n "3f\n" \
  #n "2:\n\ts_sleep " STR(SL_P) "\n\tds_read_b32 v" #C ", v" #CA "\n\ts_sub_i32 s47, s47, 1\n\ts_cmp_lt_i32 s47, 0\n\ts_cbranch_scc1 " #n "3f\n\ts_waitcnt lgkmcnt(0)\n\t" \
  "v_cmp_lt_u32 vcc, v" #C ", %[need]\n\ts_and_b64 vcc, vcc, 1\n\ts_cbranch_scc1 " #n "2b\n" \
  #n "3:\n\t" PV_REQ(P, U, M, C, CA) "s_sub_i32 s47, s47, 1\n\ts_cmp_lt_i32 s47, 0\n\ts_cselect_b32 s54, 0, 1\n\ts_waitcnt lgkmcnt(0)\n\t" \
  "v_cmp_lt_u32 vcc, v" #C ", %[need]\n\tv_cmp_neq_f32 s[48:49], v" #m0 ", v88\n\tv_cmp_neq_f32 s[50:51], v" #m1 ", v88\n\t" \
  "s_and_b32 s54, s54, vcc_lo\n\ts_cbranch_scc1 " #n "3b\n\ts_or_b64 s[48:49], s[48:49], s[50:51]\n\ts_cbranch_scc0 " #n "0b\n\ts_branch " #n "5f\n" \
  #n "4:\n\tv_cmp_neq_f32 s[48:49], v" #m0 ", v88\n\ts_or_b64 s[48:49], s[48:49], s[50:51]\n\ts_cbranch_scc0 " #n "0b\n" \
  #n "5:\n\tv_mov_b32 v150, v88\n\tv_max3_f32 v88, v88, v" #m0 ", v" #m1 "\n\tv_sub_f32 v150, v150, v88\n\tv_exp_f32 v150, v150\n\ts_nop 1\n\t" \
  PV_MUL4(72, 73, 74, 75) PV_MUL4(76, 77, 78, 79) PV_MUL4(80, 81, 82, 83) PV_MUL4(84, 85, 86, 87) \
  "v_sub_f32 v150, v" #m0 ", v88\n\tv_exp_f32 v150, v150\n\ts_nop 1\n\t" PV_SC1(p0) PV_SC1(p1) \
  "v_sub_f32 v150, v" #m1 ", v88\n\tv_exp_f32 v150, v150\n\ts_nop 1\n\t" PV_SC1(p2) PV_SC1(p3) \
  "s_branch " #n "0b\n"
#define PV_W2(a0, a1, b0, b1, c0, c1) \
  "global_load_dwordx4 v[" #a0 ":" #a1 "], v150, %[w2] sc1\n\tglobal_load_dwordx4 v[" #b0 ":" #b1 "], v150, %[w2] offset:1024 sc1\n\t" \
  "global_load_dwordx4 v[" #c0 ":" #c1 "], v150, %[w2] offset:2048 sc1\n\tv_add_u32 v150, 0xc00, v150\n\ts_sub_u32 s54, s54, 1\n\ts_cbranch_scc1 30b\n\t"

#define PV_NRM(a, b, c, d, o)   \
  "v_pk_fma_f32 v[" #a ":" #b "], v[" #a ":" #b "], v[150:151], v[150:151] op_sel:[0,0,1] op_sel_hi:[1,0,1]\n\t" \
  "v_pk_fma_f32 v[" #c ":" #d "], v[" #c ":" #d "], v[150:151], v[150:151] op_sel:[0,0,1] op_sel_hi:[1,0,1]\n\t" \
  "v_cvt_pk_bf16_f32 v" #a ", v" #a ", v" #b "\n\tv_cvt_pk_bf16_f32 v" #b ", v" #c ", v" #d "\n\tds_write_b64 v152, v[" #a ":" #b "] offset:" #o "\n\t"
#define PV_G(n, a, w0, w1, w2, c0, c1, c2) "s_waitcnt lgkmcnt(" #n ")\n\tv_mfma_f32_16x16x32_bf16 v[72:75], v[" a "], v[" w0 "], " c0 "\n\t" \
  "v_mfma_f32_16x16x32_bf16 v[76:79], v[" a "], v[" w1 "], " c1 "\n\tv_mfma_f32_16x16x32_bf16 v[80:83], v[" a "], v[" w2 "], " c2 "\n\t"
#define PV_Z4(a, b, c, d) "v_mov_b32 v" #a ", 0\n\tv_mov_b32 v" #b ", 0\n\tv_mov_b32 v" #c ", 0\n\tv_mov_b32 v" #d ", 0\n\t"
#define PV_T(n)

using u32x8 = __attribute__((ext_vector_type(8))) unsigned;
struct SCtx {
  lds_ptr L0;
  unsigned ct, apk, apm, api, amr;
  int o_rb, o_yb, ev;
  u32x8 q[3];
  unsigned tq;
};
__device__ __forceinline__ bf16x8 s_qf(const SCtx& c, int j) {
  const u32x8& v = c.q[j >> 1];
  const int o = 4 * (j & 1);
  return __builtin_bit_cast(bf16x8, u32x4{v[o], v[o + 1], v[o + 2], v[o + 3]});
}
#define S_M(d, a, b) "v_mfma_f32_16x16x32_bf16 v[" d "], v[" a "], v[" b "], v[" d "]\n\t"
#define S_M0(d, a, b) "v_mfma_f32_16x16x32_bf16 v[" d "], v[" a "], v[" b "], 0\n\t"
#define S_R(d, a, o) "ds_read_b128 v[" d "], v" #a " offset:" #o "\n\t"
#define S_W(n) "s_waitcnt lgkmcnt(" #n ")\n\t"
#define S_D " row_mask:0xf bank_mask:0xf\n\t"
#define S_3(x, y) S_M("100:103", x, y) S_M("104:107", x, x) S_M("96:99", y, y)
#define S_SQ(g, h) "v_fmac_f32 v" #g ", 2.0, v" #h "\n\t"
#define S_SQD(g, h) "v_fmac_f32_dpp v" #g ", v" #h ", %[two] quad_perm:[0,0,2,2]" S_D
#define S_U(u, h, y) "v_add_f32_dpp v" #u ", v" #h ", v" #y " quad_perm:[1,1,3,3]" S_D
#define S_C(d, a, b, m) "v_cndmask_b32 v" #d ", v" #a ", v" #b ", %[" #m "]\n\t"
#define S_B(d, n) "v_mov_b32_dpp v" #d ", v157 row_newbcast:" #n S_D
#define S_BASE(d, u, t) "v_pk_fma_f32 v[" d "], v[" u "], v[116:117], v[" t "] op_sel:[0,1,0] op_sel_hi:[1,1,0]\n\t"
#define S_LG(d, r) "v_pk_fma_f32 v[" d "], v[" d "], v[" r "], %[toff] op_sel_hi:[1,1,0]\n\t"
#define S_SUB(p, d) "v_pk_add_f32 v[" p "], v[" d "], v[150:151] op_sel_hi:[1,0] neg_lo:[0,1] neg_hi:[0,1]\n\t"
#define S_E(p) "v_exp_f32 v" #p ", v" #p "\n\t"
#define S_MUL(p, r) "v_pk_mul_f32 v[" p "], v[" p "], v[" r "]\n\t"
#define S_POS   \
  "s_mul_i32 s50, s42, 26\n\ts_lshr_b32 s50, s50, 8\n\ts_mul_i32 s50, s50, -10\n\ts_add_u32 s50, s50, s42\n\ts_mul_i32 s45, s50, %[slot]\n\ts_mov_b32 s43, s45\n\t"
#define S_POLL(n, a, o, need, back, far) \
  #n ":\n\tds_read_b32 v152, v" #a " offset:" #o "\n\ts_sub_i32 s47, s47, 1\n\ts_cmp_lt_i32 s47, 0\n\ts_waitcnt lgkmcnt(0)\n\ts_cbranch_scc1 " back "\n\t" \
  "v_readfirstlane_b32 s40, v152\n\ts_cmp_ge_u32 s40, " need "\n\ts_cbranch_scc1 " back "\n\ts_sleep " STR(SL_S) "\n\t" \
  "s_add_u32 s40, s40, " far "\n\ts_cmp_ge_u32 s40, " need "\n\ts_cbranch_scc1 " #n "b\n\ts_sleep " STR(SL_FAR) "\n\ts_branch " #n "b\n"
__device__ __forceinline__ void s_items(SCtx& c, int k0, int late) {
  unsigned long long tq;
  asm volatile("s_mov_b32 s47, %[cap]\n\ts_mov_b32 s49, 0\n\ts_mov_b32 s42, %[k0]\n\ts_cmp_lg_u32 %[late], 0\n\ts_cbranch_scc1 7f\n"
               "1:\n\ts_cmp_ge_u32 s42, %[end]\n\ts_cbranch_scc1 9f\n\t"
               S_POS
               "s_mul_i32 s48, s42, 43\n\ts_lshr_b32 s48, s48, 8\n\ts_mul_i32 s46, s48, -6\n\ts_add_u32 s46, s46, s42\n\ts_lshl2_add_u32 s48, s48, %[ns]\n\t"
               "v_add_u32 v144, s45, %[orb]\n\tv_mad_u32_u24 v145, s43, %[ev], %[oyb]\n\ts_lshl2_add_u32 s40, s46, %[pfl]\n\ts_lshl2_add_u32 s41, s50, %[full]\n\t"
               "v_xor_b32 v146, 64, v144\n\tv_xor_b32 v147, 64, v145\n\tv_mov_b32 v148, s40\n\tv_mov_b32 v149, s41\n\t"
               "s_lshl1_add_u32 s44, s42, 2\n\ts_lshl_b32 s46, s46, 7\n\ts_mov_b64 vcc, %[dg]\n\ts_cmp_lt_u32 s49, s44\n\ts_cbranch_scc1 5f\n"
               "2:\n\tds_read_b32 v150, %[amr]\n\tds_read_b32 v151, v148 offset:64\n\t"
               S_R("24:27", 145, 0) S_R("48:51", 144, 0) S_R("28:31", 147, 0) S_R("52:55", 146, 0) S_R("32:35", 145, 128) S_R("56:59", 144, 128)
               S_R("36:39", 147, 128) S_R("60:63", 146, 128) S_R("40:43", 145, 256) S_R("64:67", 144, 256) S_R("44:47", 147, 256) S_R("68:71", 146, 256)
               S_W(10) S_M0("100:103", "48:51", "24:27") S_M0("104:107", "48:51", "48:51") S_R("72:75", 144, 6144) S_R("76:79", 146, 6144) S_M0("96:99", "24:27", "24:27")
               S_W(10) S_M("100:103", "52:55", "28:31") S_M("104:107", "52:55", "52:55") S_R("80:83", 144, 6272) S_R("84:87", 146, 6272) S_M("96:99", "28:31", "28:31")
               S_W(10) S_M("100:103", "56:59", "32:35") S_M("104:107", "56:59", "56:59") S_R("88:91", 144, 6400) S_R("92:95", 146, 6400) S_M("96:99", "32:35", "32:35")
               "s_mov_b64 exec, 1\n\tds_add_u32 v149, %[four] offset:64\n\ts_mov_b64 exec, -1\n\tv_readfirstlane_b32 s51, v151\n\ts_setprio 1\n\ts_nop 0\n\t"
               S_W(11) S_3("60:63", "36:39") S_W(9) S_3("64:67", "40:43") S_W(7) S_3("68:71", "44:47")
               S_W(6) S_M0("108:111", "72:75", "24:27") S_M0("112:115", "72:75", "72:75")
               S_W(5) S_M("108:111", "76:79", "28:31") S_M("112:115", "76:79", "76:79")
               S_W(4) S_M("108:111", "80:83", "32:35")
               "v_mov_b32_dpp v152, v96 quad_perm:[1,1,3,3]" S_D "v_mov_b32_dpp v153, v98 quad_perm:[1,1,3,3]" S_D
               S_M("112:115", "80:83", "80:83") S_C(154, 96, 98, r2) S_SQ(104, 100)
               S_W(3) S_M("108:111", "84:87", "36:39") S_SQD(105, 101) S_SQ(106, 102)
               S_M("112:115", "84:87", "84:87") S_SQD(107, 103) "v_add_f32 v154, %[eps], v154\n\t"
               S_W(2) S_M("108:111", "88:91", "40:43") S_U(128, 100, 152) S_U(129, 101, 152)
               S_M("112:115", "88:91", "88:91") S_U(130, 102, 153) S_U(131, 103, 153)
               S_W(1) S_M("108:111", "92:95", "44:47") S_C(104, 104, 105, r1) S_C(106, 106, 107, r1)
               S_M("112:115", "92:95", "92:95") S_C(155, 128, 129, r1) S_C(156, 130, 131, r1)
               "s_cmp_lt_u32 s42, %[early]\n\ts_cbranch_scc1 7f\n"
               "3:\n\t" S_M0("116:119", "24:27", "0:3") S_C(104, 104, 106, r2) S_C(155, 155, 156, r2)
               S_M("116:119", "28:31", "4:7") "v_add_f32_dpp v104, v154, v104 quad_perm:[0,0,2,2]" S_D S_U(132, 108, 152)
               S_M("116:119", "32:35", "8:11") S_U(133, 109, 152) S_U(134, 110, 153)
               S_M("116:119", "36:39", "12:15") S_U(135, 111, 153) S_SQ(112, 108)
               S_M("116:119", "40:43", "16:19") S_SQD(113, 109) S_SQ(114, 110)
               S_M("116:119", "44:47", "20:23") S_SQD(115, 111) S_C(156, 132, 133, r1) S_C(157, 134, 135, r1)
               S_C(112, 112, 113, r1) S_C(114, 114, 115, r1) S_C(156, 156, 157, r2) S_C(112, 112, 114, r2)
               "v_add_f32_dpp v112, v154, v112 quad_perm:[0,0,2,2]" S_D
               "v_mul_f32 v117, %[nic], v117\n\tv_cndmask_b32_dpp v155, v156, v155, vcc row_ror:4" S_D
               S_BASE("120:121", "128:129", "116:117") S_BASE("122:123", "130:131", "118:119")
               "v_cndmask_b32_dpp v104, v112, v104, vcc row_ror:4" S_D "v_mul_f32 v156, %[nic], v155\n\t"
               S_M("120:123", "48:51", "0:3") S_BASE("124:125", "132:133", "116:117") S_BASE("126:127", "134:135", "118:119")
               S_M("120:123", "52:55", "4:7") "v_fmac_f32 v104, v156, v155\n\tv_max_f32 v104, %[eps], v104\n\t"
               S_M("120:123", "56:59", "8:11") "v_rsq_f32 v157, v104\n\t"
               S_M("120:123", "60:63", "12:15") "v_mul_f32 v104, v104, v157\n\tv_mov_b32_dpp v157, v157 row_ror:12 row_mask:0x2 bank_mask:0xf\n\t"
               S_M("120:123", "64:67", "16:19") "v_cvt_pk_bf16_f32 v105, v155, v104\n\tv_mov_b32_dpp v157, v157 row_ror:8 row_mask:0x4 bank_mask:0xf\n\t"
               S_M("120:123", "68:71", "20:23") "v_lshlrev_b32 v106, 16, v105\n\tv_mov_b32_dpp v157, v157 row_ror:4 row_mask:0x8 bank_mask:0xf\n\t"
               S_M("124:127", "72:75", "0:3") "v_and_b32 v107, 0xffff0000, v105\n\t" S_B(136, 0)
               S_M("124:127", "76:79", "4:7") S_B(137, 1) S_B(138, 2)
               S_M("124:127", "80:83", "8:11") S_B(139, 3) S_LG("120:121", "136:137")
               S_M("124:127", "84:87", "12:15") S_LG("122:123", "138:139") S_SUB("128:129", "120:121")
               S_M("124:127", "88:91", "16:19") S_SUB("130:131", "122:123") S_E(128)
               S_M("124:127", "92:95", "20:23") S_E(129) S_E(130) S_E(131)
               S_B(140, 4) S_B(141, 5) S_B(142, 6) S_B(143, 7) "v_sub_f32 v106, v155, v106\n\t"
               S_LG("124:125", "140:141") S_LG("126:127", "142:143") "v_sub_f32 v107, v104, v107\n\tv_max3_f32 v152, v120, v121, v122\n\t"
               S_SUB("132:133", "124:125") S_SUB("134:135", "126:127") "v_max3_f32 v153, v123, v124, v125\n\t"
               S_E(132) S_E(133) S_E(134) S_E(135)
               "v_max3_f32 v152, v152, v126, v127\n\tv_add_f32 v154, %[thr], v150\n\tv_max_f32 v152, v152, v153\n\tv_cvt_pk_bf16_f32 v106, v106, v107\n\t"
               "v_lshl_add_u32 v144, s46, 3, %[apk]\n\tv_cmp_gt_f32 vcc, v152, v154\n\tv_add_u32 v145, s46, %[apm]\n\tv_lshl_add_u32 v146, s46, 1, %[api]\n\t"
               "s_cbranch_vccnz 8f\n"
               "4:\n\t" S_MUL("128:129", "136:137") S_MUL("130:131", "138:139") S_MUL("132:133", "140:141") S_MUL("134:135", "142:143")
               "v_cvt_pk_bf16_f32 v128, v128, v129\n\tv_cvt_pk_bf16_f32 v129, v130, v131\n\tv_cvt_pk_bf16_f32 v130, v132, v133\n\tv_cvt_pk_bf16_f32 v131, v134, v135\n\t"
               "s_cmp_lt_u32 s51, s48\n\ts_cbranch_scc1 6f\n"
               "10:\n\tds_read_b32 v151, v149\n\tds_write_b128 v144, v[128:131]\n\tds_write2_b32 v145, v150, v150 offset0:0 offset1:1\n\ts_mov_b64 exec, %[dg2]\n\t"
               "ds_write_b16 v146, v105\n\tds_write_b16 v146, v106 offset:16\n\tds_write_b16_d16_hi v146, v105 offset:32\n\tds_write_b16_d16_hi v146, v106 offset:48\n\t"
               "s_mov_b64 exec, 1\n\tds_add_u32 v148, %[inc]\n\ts_mov_b64 exec, -1\n\ts_setprio 0\n\ts_add_u32 s42, s42, %[ns]\n\t"
               S_W(7) "v_readfirstlane_b32 s49, v151\n\ts_branch 1b\n"
               S_POLL(5, 149, 0, "s44", "2b", "2")
               S_POLL(6, 148, 64, "s48", "10b", "64")
               "7:\n\tv_mov_b32 v144, %[qi]\n\tv_mov_b32 v145, v152\n\tv_mov_b32 v146, 1\n"
               S_POLL(70, 144, 0, "%[nq]", "71f", "64")
               "71:\n\tv_mov_b32 v152, v145\n\tds_read_b128 v[0:3], %[apk]\n\tds_read_b128 v[4:7], %[apk] offset:1024\n\tds_read_b128 v[8:11], %[apk] offset:2048\n\t"
               "ds_read_b128 v[12:15], %[apk] offset:3072\n\tds_read_b128 v[16:19], %[apk] offset:4096\n\tds_read_b128 v[20:23], %[apk] offset:5120\n\t"
               "v_subrev_u32 v144, %[mc], %[amr]\n\tds_read_b32 v150, %[amr]\n\ts_mov_b64 exec, 63\n\tds_add_u32 v144, v146\n\ts_mov_b64 exec, -1\n\t"
               "s_cmp_lg_u32 %[late], 0\n\ts_waitcnt lgkmcnt(0)\n\ts_nop 1\n\ts_cbranch_scc1 1b\n\ts_branch 3b\n"
               "8:\n\tv_mov_b32 v153, v152\n\ts_nop 1\n\tv_permlane16_swap_b32 v152, v153\n\tv_max_f32 v152, v152, v153\n\tv_mov_b32 v153, v152\n\ts_nop 1\n\t"
               "v_permlane32_swap_b32 v152, v153\n\tv_max_f32 v152, v152, v153\n\tv_mul_f32 v152, 0x3d000000, v152\n\tv_rndne_f32 v152, v152\n\t"
               "v_mul_f32 v152, 0x42000000, v152\n\tv_max_f32 v150, v150, v152\n\tds_max_f32 %[amr], v150\n\t"
               S_SUB("128:129", "120:121") S_SUB("130:131", "122:123") S_SUB("132:133", "124:125") S_SUB("134:135", "126:127")
               S_E(128) S_E(129) S_E(130) S_E(131) S_E(132) S_E(133) S_E(134) S_E(135) "s_branch 4b\n"
               "9:"
               : "=&{v[0:7]}"(c.q[0]), "=&{v[8:15]}"(c.q[1]), "=&{v[16:23]}"(c.q[2]), "=&{s[52:53]}"(tq)
               : [orb] "v"(c.o_rb), [oyb] "v"(c.o_yb), [ev] "v"(c.ev), [apk] "v"(c.apk), [apm] "v"(c.apm), [api] "v"(c.api), [amr] "v"(c.amr),
                 [two] "v"(2.f), [four] "v"(4u), [inc] "v"(2u * NPK), [k0] "s"(k0), [late] "s"(__builtin_amdgcn_readfirstlane(late)), [toff] "s"(f32x2{TOFF, TOFF}),
                 [r1] "s"(0xAAAAAAAAAAAAAAAAull), [r2] "s"(0xCCCCCCCCCCCCCCCCull), [dg] "s"(0xF0000F0000F0000Full), [dg2] "s"(0xF00FFF000FF000FFull),
                 [cap] "i"(SPIN_CAP), [end] "i"(TPW - NH1), [ns] "i"(NS), [pfl] "i"(CT_OFF + C_PFL), [full] "i"(CT_OFF + C_FULL), [early] "i"(NEARLY),
                 [qi] "i"(CT_OFF + C_QI), [nq] "i"(NS - NEARLY), [mc] "i"(C_MREF - C_CONS), [slot] "i"(SLOT),
                 [thr] "i"(__builtin_bit_cast(unsigned, THR)), [eps] "i"(__builtin_bit_cast(unsigned, EPS * CC)), [nic] "i"(__builtin_bit_cast(unsigned, -1.f / CC))
               : "memory", "scc", "vcc", "s40", "s41", "s42", "s43", "s44", "s45", "s46", "s47", "s48", "s49", "s50", "s51",
                 "v24", "v25", "v26", "v27", "v28", "v29", "v30", "v31", "v32", "v33", "v34", "v35", "v36", "v37", "v38", "v39", "v40", "v41", "v42", "v43", "v44", "v45", "v46", "v47", "v48", "v49", "v50", "v51", "v52", "v53", "v54", "v55", "v56", "v57", "v58", "v59", "v60", "v61", "v62", "v63", "v64", "v65", "v66", "v67", "v68", "v69", "v70", "v71", "v72", "v73", "v74", "v75", "v76", "v77", "v78", "v79", "v80", "v81", "v82", "v83", "v84", "v85", "v86", "v87", "v88", "v89", "v90", "v91", "v92", "v93", "v94", "v95", "v96", "v97", "v98", "v99", "v100", "v101", "v102", "v103", "v104", "v105", "v106", "v107", "v108", "v109", "v110", "v111", "v112", "v113", "v114", "v115", "v116", "v117", "v118", "v119", "v120", "v121", "v122", "v123", "v124", "v125", "v126", "v127", "v128", "v129", "v130", "v131", "v132", "v133", "v134", "v135", "v136", "v137", "v138", "v139", "v140", "v141", "v142", "v143", "v144", "v145", "v146", "v147", "v148", "v149", "v150", "v151", "v152", "v153", "v154", "v155", "v156", "v157");
  c.tq = (unsigned)tq;
}

__device__ __forceinline__ void s_half(SCtx& c, int k, int h) {
  const int s = k % NSLOT, e = k % NPK;
  const int sb_ = s * SLOT;
  int yba = c.ev * sb_ + c.o_yb;
  float hc = 1.f - 0.5f * c.ev;
  unsigned xa = (unsigned)(size_t)c.L0 + sb_ + h * 6144 + c.o_rb, xb = xa ^ 64, ai = c.api + e * 256 + h * 8;
  unsigned ap = c.apk + e * 1024 + h * 8, am = c.apm + e * 128 + h * 4, af = c.ct + C_PFL + e * 4, inc = NPK;
  float two = 2.f;
  f32x4 G = {EPS * CC, EPS * CC, EPS * CC, EPS * CC};
  poll_ge_sleep(c.ct + C_CONS + e * 4, (unsigned)(NE * (k / NPK) + NS));
  asm volatile("" : "+v"(yba), "+v"(hc), "+v"(xa), "+v"(xb), "+v"(ai), "+v"(ap), "+v"(am), "+v"(af), "+v"(inc), "+v"(two), "+{v[104:107]}"(G));
  poll_ge_sleep(c.ct + C_FULL, (unsigned)(2 * k + 1));
  __builtin_amdgcn_s_setprio(1);
  bf16x8 yb[6];
#pragma unroll
  for (int j = 0; j < 6; ++j) yb[j] = *(LDS_AS(bf16x8)*)(c.L0 + ((j & 1) ? (yba ^ 64) : yba) + (j >> 1) * 128);
  f32x4 Hx = {0.f, 0.f, 0.f, 0.f}, S = Hx;
#pragma unroll
  for (int j = 0; j < 6; ++j) { Hx = mfma(yb[j], yb[j], Hx); S = mfma(yb[j], s_qf(c, j), S); }
  const float nsq = S[1] * (-1.f / CC);
  Hx = f32x4{Hx[0] * hc, Hx[0] * hc, Hx[2] * hc, Hx[2] * hc};
  S = f32x4{S[0], S[0], S[2], S[2]};
  float mref, mt;
  f32x4 rs;
  unsigned long long raise;
#define SH_M(d, a, b) "v_mfma_f32_16x16x32_bf16 v[" d "], v[" a "], " b ", v[" d "]\n\t"
#define SH_X(n, a, y) "s_waitcnt lgkmcnt(" #n ")\n\t" SH_M("100:103", a, "%[" #y "]") SH_M("104:107", a, "v[" a "]")
#define SH_D " row_mask:0xf bank_mask:0xf\n\t"
  __builtin_amdgcn_s_setprio(0);
  if (HB == 0 || (HB == 1 && k < TPW - 1)) poll_ge_sleep(c.ct + C_FULL, (unsigned)(2 * k + 2));
  if (HB == 2) { if (k < TPW - 1) poll_ge_sleep(c.ct + C_FULL, (unsigned)(2 * k + 2)); else poll_ge(c.ct + C_FULL, (unsigned)(2 * TPW)); }
  asm volatile("s_mov_b32 s42, %[cap]\n"
               "1:\n\tds_read_b32 v136, %[afl]\n\tds_read_b128 v[112:115], %[xa]\n\tds_read_b128 v[116:119], %[xb]\n\tds_read_b128 v[120:123], %[xa] offset:128\n\t"
               "ds_read_b128 v[124:127], %[xb] offset:128\n\tds_read_b128 v[128:131], %[xa] offset:256\n\tds_read_b128 v[132:135], %[xb] offset:256\n\t"
               "ds_read_b32 %[mr], %[amr]\n\ts_sub_u32 s42, s42, 1\n\ts_cselect_b32 s41, 0, %[need]\n\ts_waitcnt lgkmcnt(7)\n\t"
               "v_readfirstlane_b32 s40, v136\n\ts_cmp_lt_u32 s40, s41\n\ts_cbranch_scc1 1b\n\ts_setprio 3\n\t"
               SH_X(6, "112:115", y0) SH_X(5, "116:119", y1) SH_X(4, "120:123", y2) SH_X(3, "124:127", y3) SH_X(2, "128:131", y4) SH_X(1, "132:135", y5)
               SH_M("108:111", "112:115", "%[q0]") SH_M("108:111", "116:119", "%[q1]")
               "s_waitcnt lgkmcnt(0)\n\tv_add_f32 v137, %[thr], %[mr]\n\tds_write_b32 %[am], %[mr]\n\ts_nop 1\n\t"
               "v_mov_b32_dpp v140, v100 quad_perm:[1,1,3,3]" SH_D "v_mov_b32_dpp v141, v101 quad_perm:[1,1,3,3]" SH_D
               "v_mov_b32_dpp v142, v102 quad_perm:[1,1,3,3]" SH_D "v_mov_b32_dpp v143, v103 quad_perm:[1,1,3,3]" SH_D
               SH_M("108:111", "120:123", "%[q2]")
               "v_fmac_f32 v104, 2.0, v100\n\tv_fmac_f32_dpp v105, v101, %[two] quad_perm:[0,0,2,2]" SH_D
               "v_fmac_f32 v106, 2.0, v102\n\tv_fmac_f32_dpp v107, v103, %[two] quad_perm:[0,0,2,2]" SH_D
               "v_cndmask_b32 v138, v140, v141, %[r1]\n\tv_cndmask_b32 v139, v142, v143, %[r1]\n\t"
               SH_M("108:111", "124:127", "%[q3]")
               "v_cndmask_b32 v104, v104, v105, %[r1]\n\tv_cndmask_b32 v106, v106, v107, %[r1]\n\tv_cndmask_b32 v138, v138, v139, %[r2]\n\t"
               "v_cndmask_b32 v104, v104, v106, %[r2]\n\tv_mul_f32 v139, %[nic], v138\n\t"
               SH_M("108:111", "128:131", "%[q4]")
               "v_fmac_f32 v104, v139, v138\n\tv_max_f32 v104, %[eps], v104\n\tv_rsq_f32 v105, v104\n\t"
               SH_M("108:111", "132:135", "%[q5]")
               "v_mul_f32 v104, v104, v105\n\t"
               "v_mov_b32_dpp v105, v105 row_ror:12 row_mask:0x2 bank_mask:0xf\n\tv_cvt_pk_bf16_f32 v106, v138, v104\n\ts_nop 0\n\t"
               "v_mov_b32_dpp v105, v105 row_ror:8 row_mask:0x4 bank_mask:0xf\n\tv_lshlrev_b32 v107, 16, v106\n\tv_and_b32 v139, 0xffff0000, v106\n\t"
               "v_mov_b32_dpp v105, v105 row_ror:4 row_mask:0x8 bank_mask:0xf\n\t"
               "v_fmac_f32 v108, v140, %[nsq]\n\tv_fmac_f32 v109, v141, %[nsq]\n\tv_fmac_f32 v110, v142, %[nsq]\n\tv_fmac_f32 v111, v143, %[nsq]\n\t"
               "v_mov_b32_dpp v100, v105 row_newbcast:0" SH_D "v_mov_b32_dpp v101, v105 row_newbcast:1" SH_D
               "v_mov_b32_dpp v102, v105 row_newbcast:2" SH_D "v_mov_b32_dpp v103, v105 row_newbcast:3" SH_D
               "v_fmaak_f32 v108, v108, v100, %[toff]\n\tv_fmaak_f32 v109, v109, v101, %[toff]\n\tv_fmaak_f32 v110, v110, v102, %[toff]\n\tv_fmaak_f32 v111, v111, v103, %[toff]\n\t"
               "v_sub_f32 v140, v108, %[mr]\n\tv_sub_f32 v141, v109, %[mr]\n\tv_sub_f32 v142, v110, %[mr]\n\tv_sub_f32 v143, v111, %[mr]\n\t"
               "v_exp_f32 v140, v140\n\tv_exp_f32 v141, v141\n\tv_exp_f32 v142, v142\n\tv_exp_f32 v143, v143\n\t"
               "v_max3_f32 %[mt], v108, v109, v110\n\tv_sub_f32 v138, v138, v107\n\tv_sub_f32 v104, v104, v139\n\tv_max_f32 %[mt], %[mt], v111\n\t"
               "v_pk_mul_f32 v[140:141], v[140:141], v[100:101]\n\tv_pk_mul_f32 v[142:143], v[142:143], v[102:103]\n\t"
               "v_cmp_gt_f32 %[ra], %[mt], v137\n\tv_cvt_pk_bf16_f32 v107, v138, v104\n\t"
               "v_cvt_pk_bf16_f32 v140, v140, v141\n\tv_cvt_pk_bf16_f32 v141, v142, v143\n\t"
               "ds_write_b64 %[ap], v[140:141]\n\ts_mov_b64 exec, %[dg]\n\t"
               "ds_write_b16 %[ai], v106\n\tds_write_b16 %[ai], v107 offset:16\n\tds_write_b16_d16_hi %[ai], v106 offset:32\n\tds_write_b16_d16_hi %[ai], v107 offset:48\n\t"
               "s_cmp_eq_u64 %[ra], 0\n\ts_cselect_b64 exec, 1, 0\n\tds_add_u32 %[af], %[inc]\n\ts_mov_b64 exec, -1\n\ts_setprio 0"
               : "+{v[100:103]}"(Hx), "+{v[104:107]}"(G), "+{v[108:111]}"(S), [mr] "=&v"(mref), [mt] "=&v"(mt), [ra] "=&s"(raise)
               : [y0] "v"(yb[0]), [y1] "v"(yb[1]), [y2] "v"(yb[2]), [y3] "v"(yb[3]), [y4] "v"(yb[4]), [y5] "v"(yb[5]),
                 [q0] "v"(s_qf(c, 0)), [q1] "v"(s_qf(c, 1)), [q2] "v"(s_qf(c, 2)), [q3] "v"(s_qf(c, 3)), [q4] "v"(s_qf(c, 4)), [q5] "v"(s_qf(c, 5)),
                 [afl] "v"(c.ct + C_FULL), [xa] "v"(xa), [xb] "v"(xb), [amr] "v"(c.amr), [am] "v"(am), [ap] "v"(ap), [ai] "v"(ai),
                 [af] "v"(af), [inc] "v"(inc), [nsq] "v"(nsq), [two] "v"(two),
                 [need] "s"(__builtin_amdgcn_readfirstlane(2 * k + 2)), [r1] "s"(0xAAAAAAAAAAAAAAAAull), [r2] "s"(0xCCCCCCCCCCCCCCCCull),
                 [dg] "s"(0xF0000F0000F0000Full), [cap] "i"(SPIN_CAP), [thr] "i"(__builtin_bit_cast(unsigned, THR)), [toff] "i"(__builtin_bit_cast(unsigned, TOFF)),
                 [eps] "i"(__builtin_bit_cast(unsigned, EPS * CC)), [nic] "i"(__builtin_bit_cast(unsigned, -1.f / CC))
               : "memory", "scc", "s40", "s41", "s42", "v112", "v113", "v114", "v115", "v116", "v117", "v118", "v119", "v120", "v121", "v122", "v123", "v124", "v125",
                 "v126", "v127", "v128", "v129", "v130", "v131", "v132", "v133", "v134", "v135", "v136", "v137", "v138", "v139", "v140", "v141", "v142", "v143");
  if (__builtin_expect(raise != 0ull, 0)) {
    rs = Hx;
    mref = fmaxf(mref, 32.f * __builtin_rintf(xrow16_max(mt) * (1.f / 32.f)));
    float pp[4];
#pragma unroll
    for (int i = 0; i < 4; ++i) pp[i] = __builtin_amdgcn_exp2f(S[i] - mref) * rs[i];
    asm volatile("ds_max_f32 %6, %3\n\tds_write_b32 %2, %3\n\tds_write_b64 %0, %1\n\ts_mov_b64 exec, 1\n\tds_add_u32 %4, %5\n\ts_mov_b64 exec, -1"
                 : : "v"(ap), "v"(u32x2{cvtpk(pp[0], pp[1]), cvtpk(pp[2], pp[3])}), "v"(am), "v"(mref), "v"(af), "v"((unsigned)NPK), "v"(c.amr) : "memory");
  }
}

using u32x16 = __attribute__((ext_vector_type(16))) unsigned;
typedef __attribute__((address_space(1))) unsigned short gus;
extern "C" __global__ __launch_bounds__(768, 3) void dino_main(const void*, const void*, const void*, const void*, const void*,
                                                               const void*, const void*, void*, void*) {
  u32x16 ka; u32x2 kb; unsigned long long tent64;
  __shared__ __attribute__((aligned(16))) char lds[LDS_BYTES];
  const int wave = __builtin_amdgcn_readfirstlane((int)threadIdx.x >> 6);
  const bool helper = blockIdx.x < 8;
  lds_ptr L0 = (lds_ptr)lds;
  const unsigned lbase = (unsigned)(size_t)L0;
  const unsigned ct = lbase + CT_OFF;
#define KARG(v, i) ((char*)(((unsigned long long)v[2 * (i) + 1] << 32) | v[2 * (i)]))
#define q KARG(ka, 0)
#define f0 KARG(ka, 1)
#define f1 KARG(ka, 2)
#define wq KARG(ka, 3)
#define wk KARG(ka, 4)
#define wv KARG(ka, 5)
#define wo KARG(ka, 6)
#define box KARG(ka, 7)
#define tent ((unsigned)tent64)
#define LANE_VARS int lane; asm volatile("v_mbcnt_lo_u32_b32 %0, -1, 0\n\tv_mbcnt_hi_u32_b32 %0, -1, %0" : "=v"(lane)); \
  const int r = lane & 15, g = lane >> 4; (void)r; (void)g;
#define DOFF_VARS unsigned doff[3];   \
  _Pragma("unroll") for (int i = 0; i < 3; ++i) doff[i] = (unsigned)(lane ^ ((swz_tbl(i) >> ((lane >> 3) * 4)) & 7)) << 4;
#define BARRIER() do { asm volatile("s_waitcnt lgkmcnt(0)" ::: "memory"); __builtin_amdgcn_s_barrier(); asm volatile("" ::: "memory"); } while (0)

  {
    unsigned doff[3];
    static_assert(swz_tbl(0) == swz_tbl(3) && swz_tbl(1) == swz_tbl(4) && swz_tbl(2) == swz_tbl(5), "");
    unsigned l16, db;
#define PD(d, b, off, pol) "global_load_lds_dwordx4 %[" #d "], s[" #b "] offset:" #off pol "\n\t"
    asm volatile("s_load_dwordx16 s[64:79], %[kp], 0x0\n\ts_load_dwordx2 s[80:81], %[kp], 0x40\n\ts_memrealtime s[82:83]\n\t"
                 "v_mbcnt_lo_u32_b32 %[l16], -1, 0\n\tv_mbcnt_hi_u32_b32 %[l16], -1, %[l16]\n\t"
                 "s_lshr_b32 s84, %[b], 3\n\ts_and_b32 s85, %[b], 7\n\t"
                 "v_lshrrev_b32 %[db], 1, %[l16]\n\tv_and_b32 %[db], -4, %[db]\n\t"
                 "s_mov_b32 s86, %[t0]\n\ts_mov_b32 s87, %[t1]\n\ts_mov_b32 s88, %[t2]\n\t"
                 "v_bfe_u32 %[d0], s86, %[db], 3\n\tv_bfe_u32 %[d1], s87, %[db], 3\n\tv_bfe_u32 %[d2], s88, %[db], 3\n\t"
                 "v_xor_b32 %[d0], %[d0], %[l16]\n\tv_xor_b32 %[d1], %[d1], %[l16]\n\tv_xor_b32 %[d2], %[d2], %[l16]\n\t"
                 "v_lshlrev_b32 %[d0], 4, %[d0]\n\tv_lshlrev_b32 %[d1], 4, %[d1]\n\tv_lshlrev_b32 %[d2], 4, %[d2]\n\t"
                 "v_lshlrev_b32 %[l16], 4, %[l16]\n\t"
                 "s_cmp_eq_u32 s84, 0\n\ts_cbranch_scc1 4f\n\t"
                 "s_cmp_lg_u32 %[w], 0\n\ts_cbranch_scc1 7f\n\t"
                 "s_setprio 3\n\tv_lshlrev_b32 %[db], 3, %[l16]\n\ts_mov_b32 m0, %[junk]\n\ts_lshl_b32 s89, s84, 13\n\t"
                 "s_cmp_gt_u32 s84, 3\n\ts_cbranch_scc1 2f\n\t"
                 "s_getpc_b64 s[90:91]\n1:\n\ts_sub_u32 s86, s89, 8192\n\ts_min_u32 s86, s86, dino_end-1b-8192\n\t"
                 "s_add_u32 s90, s90, s86\n\ts_addc_u32 s91, s91, 0\n\tglobal_load_lds_dword %[db], s[90:91]\n"
                 "2:\n\ts_mul_i32 s86, s85, 98304\n\ts_mul_i32 s87, s84, 3072\n\ts_sub_u32 s87, s87, 3072\n\t"
                 "s_add_u32 s88, s86, s87\n\ts_lshl1_add_u32 s86, s86, s87\n\ts_lshl_b32 s86, s86, 1\n\t"
                 "s_mul_i32 s87, s85, 12288\n\ts_lshr_b32 s90, s89, 1\n\ts_add_u32 s87, s87, s90\n\ts_sub_u32 s87, s87, 13 * 4096\n\t"
                 "s_sub_u32 s89, s89, 4 * 8192\n\ts_cmp_gt_u32 s85, 5\n\ts_cselect_b32 s89, -1, s89\n\ts_cmp_ge_u32 s89, 11 * 8192\n\t"
                 "s_waitcnt lgkmcnt(0)\n\ts_cbranch_scc1 3f\n\t"
                 "s_cmp_lt_u32 s89, 9 * 8192\n\ts_cselect_b64 s[90:91], s[70:71], s[72:73]\n\ts_cselect_b32 s89, s89, s87\n\t"
                 "s_add_u32 s90, s90, s89\n\ts_addc_u32 s91, s91, 0\n\tglobal_load_lds_dword %[db], s[90:91]\n"
                 "3:\n\ts_mov_b32 m0, 0x3000\n\t"
                 "s_add_u32 s90, s68, s88\n\ts_addc_u32 s91, s69, 0\n\t"
                 "s_add_u32 s92, s66, s86\n\ts_addc_u32 s93, s67, 0\n\t"
                 PD(d0, 90:91, 0, " nt") PD(d1, 90:91, 1024, " nt") PD(d2, 90:91, 2048, " nt")
                 "s_mov_b32 m0, 0\n\ts_nop 0\n\t"
                 PD(d0, 92:93, 0, " nt") PD(d1, 92:93, 1024, " nt")
                 "s_sub_u32 s89, s84, 15\n\ts_cmp_gt_u32 s85, 5\n\ts_cselect_b32 s89, -1, s89\n\ts_cmp_ge_u32 s89, 11\n\ts_cbranch_scc1 9f\n\t"
                 "s_mov_b32 m0, %[junk]\n\ts_sub_u32 s87, s87, 11 * 4096\n\ts_lshl_b32 s89, s89, 13\n\ts_cmp_lt_u32 s89, 9 * 8192\n\t"
                 "s_cselect_b64 s[90:91], s[76:77], s[74:75]\n\ts_cselect_b32 s89, s89, s87\n\t"
                 "s_add_u32 s90, s90, s89\n\ts_addc_u32 s91, s91, 0\n\tglobal_load_lds_dword %[db], s[90:91]\n\t"
                 "s_branch 9f\n"
                 "7:\n\ts_cmp_eq_u32 %[w], 1\n\ts_cbranch_scc1 8f\n\ts_barrier\n\ts_branch 8f\n"
                 "4:\n\t"
                 "s_cmp_gt_u32 %[b], 5\n\ts_cbranch_scc1 8f\n\ts_cmp_gt_u32 %[w], 9\n\ts_cbranch_scc1 8f\n\t"
                 "s_mov_b32 m0, %[img]\n\ts_mul_i32 s86, %[w], 9216\n\ts_and_b32 s88, %[w], 1\n\ts_lshl1_add_u32 s89, %[b], s88\n\t"
                 "s_mul_i32 s88, s88, 6144\n\ts_mul_i32 s89, s89, 6144\n\ts_add_u32 s88, s88, %[hr]\n\t"
                 "s_cmp_lg_u32 %[w], 0\n\ts_waitcnt lgkmcnt(0)\n\ts_cbranch_scc1 5f\n\t"
                 PD(d0, 64:65, 0, "") PD(d1, 64:65, 1024, "") PD(d2, 64:65, 2048, "") PD(d0, 64:65, 3072, "")
                 "s_mov_b32 m0, %[img] + 4096\n\ts_add_u32 s90, s64, 0x1000\n\ts_addc_u32 s91, s65, 0\n\t"
                 PD(d1, 90:91, 0, "") PD(d2, 90:91, 1024, "")
                 "\n5:\n\ts_cmp_gt_u32 %[w], 7\n\ts_cselect_b64 s[92:93], s[72:73], s[70:71]\n\ts_cselect_b32 m0, s88, s86\n\ts_cselect_b32 s86, s89, s86\n\t"
                 "s_cselect_b32 s87, 2, 3\n\ts_add_u32 s92, s92, s86\n\ts_addc_u32 s93, s93, 0\n"
                 "6:\n\ts_sub_u32 s87, s87, 1\n\t"
                 PD(d0, 92:93, 0, "") PD(d1, 92:93, 1024, "") PD(d2, 92:93, 2048, "")
                 "s_add_u32 m0, m0, 3072\n\ts_add_u32 s92, s92, 3072\n\ts_addc_u32 s93, s93, 0\n\ts_cmp_lg_u32 s87, 0\n\ts_cbranch_scc1 6b"
                 "\n8:\n\ts_waitcnt lgkmcnt(0)\n9:"
                 : "={s[64:79]}"(ka), "={s[80:81]}"(kb), "={s[82:83]}"(tent64),
                   [d0] "=&v"(doff[0]), [d1] "=&v"(doff[1]), [d2] "=&v"(doff[2]), [l16] "=&v"(l16), [db] "=&v"(db)
                 : [kp] "s"(__builtin_amdgcn_kernarg_segment_ptr()), [b] "s"(blockIdx.x), [w] "s"(wave),
                   [t0] "i"(swz_tbl(0)), [t1] "i"(swz_tbl(1)), [t2] "i"(swz_tbl(2)), [junk] "i"(JUNK), [img] "i"(IMG_A), [hr] "i"(H_R)
                 : "memory", "scc", "vcc", "s84", "s85", "s86", "s87", "s88", "s89", "s90", "s91", "s92", "s93", "s94", "s95", "s96", "s97", "s98", "s99");
  }
  const unsigned t0 = (unsigned)__builtin_amdgcn_s_memrealtime() - T0BACK;

  if (__builtin_expect(blockIdx.x < 6, 0)) {
    LANE_VARS
    DOFF_VARS
    const int j = blockIdx.x;
    TNOW(ta0);
    TNOW(ta1);
    const int qq = (lane >> 2) & 3, p4 = lane & 3, e4 = wave & 3;
    int ra = H_R + r * ROWB + ((g ^ swz(r)) << 4);
    bf16x8 a[6], b[6], w[18];
    auto rows = [&](int t, bf16x8* d) {
#pragma unroll
      for (int kk = 0; kk < 6; ++kk) d[kk] = *(LDS_AS(bf16x8)*)(L0 + t * 6144 + ((kk & 1) ? (ra ^ 64) : ra) + (kk >> 1) * 128);
    };
#define TSET(v)
    int o[3];
#pragma unroll
    for (int t = 0; t < 3; ++t) o[t] = tr_off(4 * g + qq, 4 * (3 * e4 + t) + p4);
    unsigned qo = lbase + H_Q + 3 * e4 * 512 + ((g >> 1) * 16 + r) * 16 + (g & 1) * 8, qa = lbase + IMG_A + tr_off(r, 8 * wave + g);
    if (wave == 11) {
      unsigned d;
      asm volatile("s_load_dword %0, %1, 0x40\n\ts_barrier\n\ts_barrier\n\ts_barrier\n\ts_waitcnt lgkmcnt(0)" : "=&s"(d) : "s"(box + EPOCH_OFF) : "memory");
    } else
    asm volatile("s_waitcnt vmcnt(9)\n\ts_barrier" : PIN3(o, 0), "+v"(qo), "+v"(qa) :: "memory");
    if (wave < 6 || wave == 10) {
      rows((IMG_A - H_R) / 6144, a);
      const u32x2 m0 = *(LDS_AS(u32x2)*)(size_t)qa, m1 = *(LDS_AS(u32x2)*)(size_t)(qa ^ 32);
      static_assert((IMG_A - H_R) % 6144 == 0, "");
      const bf16x8 ones = __builtin_bit_cast(bf16x8, u32x4{0x3f803f80u, 0x3f803f80u, 0x3f803f80u, 0x3f803f80u});
      f32x4 sm = {0.f, 0.f, 0.f, 0.f};
#pragma unroll
      for (int i = 0; i < 6; ++i) sm = mfma(ones, a[i], sm);
      const float mean = sm[0] * (1.f / CC);
      if (__builtin_expect(wave < 6, 1)) {
        img_st(L0, H_C, wave, lane, __builtin_bit_cast(bf16x8, u32x4{cvtpk(bflo(m0[0]) - mean, bfhi(m0[0]) - mean), cvtpk(bflo(m0[1]) - mean, bfhi(m0[1]) - mean),
                                                                   cvtpk(bflo(m1[0]) - mean, bfhi(m1[0]) - mean), cvtpk(bflo(m1[1]) - mean, bfhi(m1[1]) - mean)}));
      } else {
        asm volatile("s_barrier" : PIN3(a, 0), PIN3(a, 3) :: "memory");
        f32x2 v2 = {0.f, 0.f}, v3 = v2;
#pragma unroll
        for (int i = 0; i < 12; ++i) {
          const u32x4 d = __builtin_bit_cast(u32x4, a[i >> 1]);
          const unsigned d0 = d[2 * (i & 1)], d1 = d[2 * (i & 1) + 1];
          const f32x2 x0 = f32x2{bflo(d0), bfhi(d0)} - mean, x1 = f32x2{bflo(d1), bfhi(d1)} - mean;
          v2 += x0 * x0; v3 += x1 * x1;
        }
        v2 += v3;
        *(LDS_AS(float)*)(L0 + CT_OFF + r * 4) = LOG2E * __builtin_amdgcn_rsqf(xrow16_sum(v2[0] + v2[1]) * (1.f / CC) + EPS);
      }
      TSET(ta2);
    }
    if (wave < 4) {
      wait_vm<0>();
      TSET(ta3);
      BARRIER();
      TSET(th1);
#pragma unroll
      for (int kk = 0; kk < 6; ++kk) {
        b[kk] = img_ld(L0, H_C, kk, lane);
        w[kk] = mk8(tr_read(L0 + kk * 12288 + o[0]), tr_read(L0 + kk * 12288 + 6144 + o[0]));
        __builtin_amdgcn_sched_barrier(0);
      }
#pragma unroll
      for (int i = 6; i < 18; ++i) {
        w[i] = mk8(tr_read(L0 + (i % 6) * 12288 + o[i / 6]), tr_read(L0 + (i % 6) * 12288 + 6144 + o[i / 6]));
        __builtin_amdgcn_sched_barrier(0);
      }
#pragma unroll
      for (int t = 0; t < 3; ++t) {
        f32x4 D = {0.f, 0.f, 0.f, 0.f};
#pragma unroll
        for (int kk = 0; kk < 6; ++kk) D = mfma(w[6 * t + kk], b[kk], D);
        *(LDS_AS(u32x2)*)(size_t)(qo + t * 512) = u32x2{cvtpk(D[0], D[1]), cvtpk(D[2], D[3])};
      }
      TSET(ta4);
      BARRIER();
    } else if (wave == 8 || wave == 9) {
      unsigned so = lane * 16 + (2 * j + (wave & 1)) * 1024;
      unsigned epoch;
      asm volatile("global_load_dword %0, %1, %2 sc1\n\ts_barrier\n\ts_waitcnt vmcnt(0)" : "=v"(epoch) : "v"(EPOCH_OFF), "s"(box) : "memory");
      TSET(ta3);
      rows(wave & 1, a);
      unsigned ta = line_tag(epoch, 8), tb = line_tag(epoch, 9);
      asm volatile("" : PIN3(a, 0), PIN3(a, 3), "+v"(ta), "+v"(tb), "+v"(so));
      TSET(ta4);
      BARRIER();
      TSET(ta5);
#pragma unroll
      for (int kk = 0; kk < 6; ++kk) b[kk] = img_ld(L0, H_Q, kk, lane);
      const float sc = *(LDS_AS(float)*)(L0 + CT_OFF + r * 4);
      __builtin_amdgcn_sched_barrier(0);
      f32x4 D = {0.f, 0.f, 0.f, 0.f};
#pragma unroll
      for (int kk = 0; kk < 6; ++kk) D = mfma(a[kk], b[kk], D);
      D *= sc;
      const unsigned p0 = cvtpk(D[0], D[1]), p1 = cvtpk(D[2], D[3]);
      asm volatile("global_store_dwordx4 %0, %1, %2 sc1\n\ts_nop 1" : : "v"(so), "v"(u32x4{p0, p1, p0 ^ p1 ^ ta, tb}), "s"(box + QK_OFF) : "memory");
      TSET(th2);
    } else if (wave != 11) {
      if (wave != 10) asm volatile("s_waitcnt vmcnt(0) lgkmcnt(0)\n\ts_barrier" ::: "memory");
      BARRIER();
    }
    BARRIER();
    issue_rows16(wo + wave * 6144, lbase + wave * 6144, doff);
    if (wave < 2) issue_rows16(wv + (2 * j + wave) * 6144, lbase + H_R + wave * 6144, doff);
    const unsigned epoch = epoch_load(box);
    BARRIER();
    TSET(ta3);
    {
      const int o0 = tr_off(8 * g + qq, 4 * wave + p4), o1 = tr_off(8 * g + 4 + qq, 4 * wave + p4);
#pragma unroll
      for (int kk = 0; kk < 6; ++kk) b[kk] = mk8(tr_read(L0 + kk * 12288 + o0), tr_read(L0 + kk * 12288 + o1));
      f32x4 D[2];
#pragma unroll
      for (int h = 0; h < 2; ++h) {
        rows(h, a);
        D[h] = f32x4{0.f, 0.f, 0.f, 0.f};
#pragma unroll
        for (int kk = 0; kk < 6; ++kk) D[h] = mfma(a[kk], b[kk], D[h]);
      }
      const int e = wave / 3;
      asm volatile("global_store_dwordx4 %0, %1, %2 sc1\n\ts_waitcnt vmcnt(0)"
                   : : "v"(lane * 16 + (e * 15 + 3 * j + wave) * 1024), "v"(pack8(D[0] * SQRT_C, D[1] * SQRT_C)), "s"(box + W2_OFF) : "memory");
    }
    BARRIER();
    if (wave == 11)
      asm volatile("s_mov_b64 exec, 1\n\tglobal_store_dwordx2 %0, %1, %2 sc1\n\ts_mov_b64 exec, -1\n\ts_nop 1"
                   : : "v"(j * 128), "v"(u32x2{line_tag(epoch, 10), line_tag(epoch, 11)}), "s"(box + W2F_OFF) : "memory");
    TPUT(60 + wave * 5, ta0); TPUT(61 + wave * 5, ta1); TPUT(62 + wave * 5, ta2); TPUT(63 + wave * 5, ta3); TPUT(64 + wave * 5, ta4);
  }

  if (__builtin_expect(helper, 0) && wave == 6) {
    LANE_VARS
    unsigned d0, d1, d2;
    asm volatile("global_load_dword %0, %3, %4 sc1\n\tglobal_load_dword %1, %5, %6 sc1\n\tglobal_load_dword %2, %5, %7\n\ts_waitcnt vmcnt(0)"
                 : "=&v"(d0), "=&v"(d1), "=&v"(d2) : "v"((lane * NWG + blockIdx.x) * 128), "s"(box), "v"((lane & 1) * 6140), "s"(KARG(kb, 0)), "s"(q) : "memory");
  }
  int rslot = -1;
  if (__builtin_expect(wave == 0, 1)) {
    LANE_VARS
    DOFF_VARS
    if (helper) BARRIER();
    const unsigned afull = ct + C_FULL + r * 4;
    unsigned ts = 0;
    {
      const int ti = helper ? 1023 : (lane * 31 + (int)(blockIdx.x >> 3) - 1) & 1023;
      const int sp = ti >> 5, yy = sp * 8 + ((blockIdx.x + sp) & 7), xx = (ti + 5 * sp) & 31;
      unsigned tmp, pub = helper ? 2 * (TPW - 1) : 0, pre = 0, afr = ct + C_FREE + (helper ? ((TPW - 1) % NSLOT) * 4 : 0), e0, e1, e2, g0, g1, g2;
      asm volatile("s_mov_b32 s44, %[due0]\n\ts_mov_b32 s46, %[y0]\n\ts_mov_b32 s47, %[cap]\n\ts_mov_b32 s48, %[i0]\n\ts_mov_b32 s49, %[s0]\n\t"
                   "s_mov_b32 s51, 0\n\ts_mov_b32 s45, 0\n\ts_movk_i32 s39, %[p3s]\n"
                   "1:\n\tv_readlane_b32 s53, %[ob], s48\n\tv_readlane_b32 s52, %[oa], s48\n\ts_add_u32 m0, s49, 0x3000\n\tv_cmp_gt_u32 vcc, s51, %[pre]\n\t"
                   "v_add_u32 %[g0], s53, %[d0]\n\tv_add_u32 %[g1], s53, %[d1]\n\tv_add_u32 %[g2], s53, %[d2]\n\t"
                   "v_add_u32 %[e0], s52, %[d0]\n\tv_add_u32 %[e1], s52, %[d1]\n\tv_add_u32 %[e2], s52, %[d2]\n\t"
                   "s_cbranch_vccnz 5f\n\t"
                   "s_cmp_eq_u32 s48, 0\n\ts_cbranch_scc1 2f\n\ts_cmp_eq_u32 s48, 2\n\ts_cbranch_scc1 8f\n"
                   LD_GATE(1, "")
                   LD1(g0, 68:69, 0) LD1(g1, 68:69, 1024) LD1(g2, 68:69, 2048) "s_mov_b32 m0, s49\n\ts_nop 0\n\t" LD1(e0, 66:67, 0) LD1(e1, 66:67, 1024)
                   LD_NEXT
                   "\n2:\n\tv_add_u32 %[afr], 4, %[afr]\n\ts_cmp_eq_u32 s49, %[lasts]\n\ts_cbranch_scc1 7f\n"
                   "3:\n\tv_mov_b32 %[pre], 0\n\ts_add_u32 m0, s49, 0x800\n\tds_read_b32 %[pre], %[afr] ; NOTRACK\n"
                   LD_GATE(2, "")
                   LD1(e2, 56:57, 0) LD1(e0, 56:57, 1024) LD1(e1, 56:57, 2048) LD1(e2, 56:57, 3072) "s_add_u32 m0, s49, 0x1800\n\ts_nop 0\n\t" LD1(e0, 58:59, 0)
                   LD_NEXT
                   "\n29:\n\ts_add_u32 m0, s49, 0x1800\n\ts_add_u32 s48, s48, 1\n"
                   LD_GATE(3, "s_cmp_gt_u32 s48, %[tpw] - 3\n\ts_cbranch_scc1 38f\n39:\n\t")
                   LD1(e1, 58:59, 1024) LD1(e2, 58:59, 2048) LD1(e0, 58:59, 3072) "s_add_u32 m0, s49, 0x2800\n\ts_add_u32 s49, s49, %[slot]\n\t" LD1(e1, 60:61, 0) LD1(e2, 60:61, 1024)
                   LD_NEXT
                   "s_cmp_eq_u32 s49, %[ring]\n\ts_cselect_b32 s49, 0, s49\n\ts_cmp_gt_i32 s46, 30\n\ts_cbranch_scc1 6f\n"
                   "4:\n\ts_cmp_eq_u32 s48, %[pk]\n\ts_cmovk_i32 s39, %[p3]\n\ts_cmp_lg_u32 s48, %[tpw]\n\ts_cbranch_scc1 1b\n\ts_branch 9f\n"
                   LD_PUB(1, 0, 15, 30) LD_PUB(2, 5, 20, 35) LD_PUB(3, 10, 25, 40)
                   "38:\n\ts_cmp_eq_u32 s46, 10\n\ts_cselect_b32 s43, 23, 0\n\ts_cmp_lt_u32 s42, s43\n\ts_cbranch_scc0 39b\n\tv_add_u32 %[tmp], 1, %[pub]\n\ts_waitcnt vmcnt(22)\n\tds_write_b32 %[af], %[tmp] ; NOTRACK\n\ts_branch 39b\n"
                   "5:\n\ts_movk_i32 s40, 0x100\n"
                   "40:\n\tds_read_b32 %[pre], %[afr]\n\t" LD_VM
                   "s_sub_u32 s40, s40, 1\n\ts_cselect_b32 s42, 0, s42\n\ts_cselect_b32 s40, 0, s40\n\t"
                   "s_cmp_le_i32 s42, s46\n\ts_cbranch_scc1 42f\n"
                   "41:\n\ts_sub_i32 s47, s47, 1\n\ts_cmp_lt_i32 s47, 0\n\ts_waitcnt lgkmcnt(0)\n\ts_cbranch_scc1 10b\n\t"
                   "v_cmp_gt_u32 vcc, s51, %[pre]\n\ts_cbranch_vccnz 40b\n\ts_branch 10b\n"
                   LD_PUB0(4, 0, 15, 30)
                   "6:\n\ts_waitcnt vmcnt(45)\n\tv_add_u32 %[pub], 2, %[pub]\n\tds_write_b32 %[af], %[pub]\n\ts_sub_u32 s46, s46, 15\n\ts_branch 4b\n"
                   "7:\n\ts_waitcnt lgkmcnt(0)\n\ts_add_u32 s51, s51, 8\n\tv_subrev_u32 %[afr], %[frb], %[afr]\n\ts_branch 3b\n"
                   "8:\n\ts_barrier\n\ts_add_u32 s46, s46, 64\n\ts_branch 10b\n"
                   "9:\n\ts_cmp_lt_i32 s46, 30\n\ts_cbranch_scc1 91f\n\ts_waitcnt vmcnt(30)\n\tv_add_u32 %[pub], 2, %[pub]\n\tds_write_b32 %[af], %[pub]\n"
                   "91:\n\ts_cmp_lt_i32 s46, 15\n\ts_cbranch_scc1 92f\n\tv_add_u32 %[tmp], 1, %[pub]\n\ts_waitcnt vmcnt(27)\n\tds_write_b32 %[af], %[tmp]\n"
                   "92:\n\ts_waitcnt lgkmcnt(0)"
                   : [pub] "+v"(pub), [pre] "+v"(pre), [afr] "+v"(afr), [ts] "+v"(ts), [tmp] "=&v"(tmp),
                     [e0] "=&v"(e0), [e1] "=&v"(e1), [e2] "=&v"(e2), [g0] "=&v"(g0), [g1] "=&v"(g1), [g2] "=&v"(g2)
                   : "{s[64:79]}"(ka), "{s[56:57]}"(f0 + 0x800), "{s[58:59]}"(f0 + 0x30000), "{s[60:61]}"(f0 + 0x31000), [t0] "s"(t0), [af] "v"(afull),
                     [d0] "v"(doff[0]), [d1] "v"(doff[1]), [d2] "v"(doff[2]), [oa] "v"(yy * 393216 + xx * 6144), [ob] "v"(yy * 98304 + xx * 3072),
                     [due0] "s"(helper ? HDUE * (PACE16 / 3) : PRE5 * (PACES / 3)), [y0] "s"(helper ? -15 : -79 + 5 * PRE5), [i0] "s"(helper ? TPW - 1 : 0),
                     [s0] "s"(helper ? ((TPW - 1) % NSLOT) * SLOT : 0),
                     [cap] "i"(SPIN_CAP), [win] "i"(PWIN), [ahi] "s"(((blockIdx.x >> 3) & 3) == 1 ? AHIR : AHI), [adl] "i"(ADL), [amg] "i"(AMG), [p3] "i"(PACE16 / 3), [p3s] "i"(PACES / 3), [pk] "i"(PK1), [slot] "i"(SLOT),
                     [ring] "i"(NSLOT * SLOT), [frb] "i"(NSLOT * 4), [lasts] "i"((NSLOT - 1) * SLOT), [tpw] "i"(TPW)
                   : "memory", "scc", "vcc", "s39", "s40", "s41", "s42", "s43", "s44", "s45", "s46", "s47", "s48", "s49", "s51", "s52", "s53", "s62", "s63");
    }
    lds_st32(ct + C_ISS, 1u);
#define CLR(b) "global_store_dwordx4 %0, %1, %" #b " sc1\n\t"
    auto clear = [&]() {
      asm volatile(CLR(2) CLR(3) CLR(4) CLR(5) CLR(6) CLR(7) CLR(8) CLR(9) "s_nop 1"
                 : : "v"(((lane >> 3) * NWG + blockIdx.x) * 128 + (lane & 7) * 16), "v"(u32x4{0u, 0u, 0u, 0u}), "s"(box), "s"(box + 1 * 262144), "s"(box + 2 * 262144),
                     "s"(box + 3 * 262144), "s"(box + 4 * 262144), "s"(box + 5 * 262144), "s"(box + 6 * 262144), "s"(box + 7 * 262144) : "memory");
    };
    if (PRECLR == 1) clear();
    wait_vm<PRECLR == 1 ? 23 : 15>();
    lds_st32(afull, (unsigned)(2 * TPW - 2));
    if (PRECLR == 3) clear();
    wait_vm<(PRECLR == 1 || PRECLR == 3) ? 20 : 12>();
    lds_st32(afull, (unsigned)(2 * TPW - 1));
    wait_vm<(PRECLR == 1 || PRECLR == 3) ? 8 : 0>();
    lds_st32(afull, ~0u);
    TNOW(tl);
    if (PRECLR == 2) clear();
    if (PRECLR == 4) { unsigned d; asm volatile("global_load_dword %0, %1, %2 sc1\n\ts_waitcnt vmcnt(0)" : "=v"(d) : "v"(blockIdx.x * 128), "s"(box) : "memory"); }
    __builtin_amdgcn_s_setprio(0);
    wait_vm<0>();
    TNOW(tlx);
    if (PRECLR != 5) lds_add(ct + C_X1, 1u);
    rslot = 7;
  } else {
    if (wave == 1) {
      LANE_VARS
    {
      const unsigned o = lane * 8;
      unsigned z = (o >= C_MREF && o < C_MREF + 64) ? 0xff800000u : 0u;
      if (helper && o >= C_CONS && o < C_MREF) z = NE * ((TPW - 1) / NPK);
      u32x2 zz = {z, z};
      if (o >= C_PFL && o < C_CONS) zz = u32x2{(o - C_PFL) / 2 + 2, (o - C_PFL) / 2 + 4} + (helper ? 2u * NPK * ((TPW - 1) / NPK) : 0u);
      *(LDS_AS(u32x2)*)(L0 + CT_OFF + o) = zz;
      *(LDS_AS(u32x2)*)(L0 + ONES_OFF + (lane < 48 ? o : 0)) = u32x2{0x3f803f80u, 0x3f803f80u};
    }
    }
    if (helper || wave == 1) BARRIER();
    if (wave <= NE) {
      LANE_VARS
      const int ej = wave - 1;
      const int qq = (lane >> 2) & 3, p4 = lane & 3;
      const unsigned apk = lbase + IMG_A + lane * 16;
      const unsigned epoch = epoch_load(box);
      __builtin_amdgcn_s_setprio(2);
      if (helper) { while ((unsigned)__builtin_amdgcn_s_memrealtime() - tent < RELAYT) __builtin_amdgcn_s_sleep(8); }
      unsigned long long tpv, tp7, tx1;
      {
        static_assert((TPW & 1) == 1 && C_CONS == C_PFL + 64 && C_FREE == C_FULL + 64, "");
        const int k0 = helper ? TPW - 1 : 0;
        unsigned need = 2 * k0 + 2;
        asm volatile("s_mov_b32 s46, 0\n\ts_mov_b64 exec, 0\n\ts_nop 3\n\ts_branch 99f\n"
                     "2:\n\ts_mov_b64 exec, -1\n\ts_nop 7\n\t" PV_Z4(72, 73, 74, 75) PV_Z4(76, 77, 78, 79) PV_Z4(80, 81, 82, 83) PV_Z4(84, 85, 86, 87)
                     "v_mov_b32 v88, 0xff800000\n\t"
                     "s_mov_b32 s46, %[k0]\n\ts_mov_b32 s45, %[s0]\n\ts_mov_b32 s44, %[e0]\n\ts_mov_b32 s47, %[cap]\n\t"
                     "s_cmp_lg_u32 s46, 0\n\ts_cbranch_scc1 32f\n"
                     "3:\n\tv_mov_b32 v148, %[ca]\n\t" PV_WAITF(4, 148) PV_FETCH_A
                     "s_mov_b64 exec, 2\n\tds_add_u32 v148, %[one] offset:64\n\ts_mov_b64 exec, -2\n\t"
                     "v_add_u32 %[need], 2, %[need]\n\tv_add_u32 v148, 4, v148\n\ts_mov_b64 exec, -1\n\t"
                     PV_WAITF(5, 148)
                     "v_mov_b32 v149, v148\n\ts_mov_b64 exec, -2\n\tv_add_u32 %[need], 2, %[need]\n\tv_add_u32 v148, 4, v148\n\t"
                     "s_mov_b64 exec, 1\n\tv_add_u32 %[need], %[npk2], %[need]\n\ts_mov_b64 exec, -1\n\t"
                     PV_REQ("126:129", "130:133", "134:135", 136, 148) PV_FETCH_B
                     "s_mov_b64 exec, 2\n\tds_add_u32 v149, %[one] offset:64\n\ts_mov_b64 exec, -1\n"
                     "6:\n\t"
                     PV_STEP(1, "126:129", "130:133", "134:135", 134, 135, 136, 148, "138:141", "142:145", "146:147", 137, 149,
                             "92:95", "98:101", "104:107", "90:93", "96:99", "102:105",
                             PV_MOV(92, 90) PV_MOV(93, 91), PV_MOV(98, 96) PV_MOV(99, 97), PV_MOV(104, 102) PV_MOV(105, 103),
                             "s_cmp_eq_u32 s46, %[last]\n\ts_cbranch_scc1 99f\n\t")
                     "s_cbranch_vccnz 7f\n"
                     "16:\n\t" PV_FETCH_A PV_REL(148)
                     "s_lshl1_add_u32 s54, s46, -2\n\ts_lshr_b64 s[54:55], %[sch], s54\n\ts_and_b32 s54, s54, 15\n\ts_cbranch_scc1 31f\n"
                     "30:\n\t"
                     PV_STEP(2, "138:141", "142:145", "146:147", 146, 147, 137, 149, "126:129", "130:133", "134:135", 136, 148,
                             "110:113", "116:119", "122:125", "108:111", "114:117", "120:123",
                             PV_MOV(110, 108) PV_MOV(111, 109), PV_MOV(116, 114) PV_MOV(117, 115), PV_MOV(122, 120) PV_MOV(123, 121),
                             "s_cmp_eq_u32 s46, %[last] - 1\n\ts_cbranch_scc1 98f\n\t")
                     "s_cbranch_vccnz 8f\n"
                     "26:\n\t" PV_FETCH_B PV_REL(149)
                     "s_branch 6b\n"
                     PV_WAITF(7, 148) "s_branch 16b\n"
                     PV_WAITF(8, 149) "s_branch 26b\n"
                     "98:\n\t" PV_MF("76:79", "114:117", "138:141") PV_MF("80:83", "120:123", "138:141")
                     "v_add_u32 %[need], 2, %[need]\n\ts_add_u32 s46, s46, 1\n\ts_branch 13f\n"
                     PV_SLOW(1, "126:129", "130:133", "134:135", 134, 135, 136, 148, 126, 127, 128, 129)
                     PV_SLOW(2, "138:141", "142:145", "146:147", 146, 147, 137, 149, 138, 139, 140, 141)
                     "31:\n\ts_cmp_lg_u32 s54, 8\n\ts_cbranch_scc1 34f\n"
                     "32:\n\tv_mbcnt_lo_u32_b32 v152, 31, 0\n\tv_lshlrev_b32 v152, 7, v152\n\ts_mov_b32 s55, %[gcap]\n"
                     "33:\n\tglobal_load_dwordx2 v[150:151], v152, %[fl] sc1\n\ts_sub_u32 s55, s55, 1\n\ts_cselect_b32 s56, 0, -1\n\ts_waitcnt vmcnt(0) lgkmcnt(0)\n\t"
                     "v_cmp_ne_u64 s[48:49], %[tag], v[150:151]\n\ts_sleep 2\n\ts_and_b32 s48, s48, s56\n\ts_cbranch_scc1 33b\n\t"
                     "s_mov_b32 s57, 0\n\tv_mov_b32 v150, %[apk]\n\ts_cmp_lg_u32 s46, %[last]\n\ts_cbranch_scc1 30b\n\ts_mov_b32 s54, 7\n\ts_branch 38f\n"
                     "34:\n\ts_cmp_lg_u32 s46, %[last] - 3\n\ts_cbranch_scc1 36f\n\tv_mov_b32 v151, %[iss]\n"
                     "35:\n\tds_read_b32 v150, v151\n\ts_sub_i32 s47, s47, 1\n\ts_cmp_lt_i32 s47, 0\n\ts_cselect_b32 s55, 0, 1\n\ts_waitcnt lgkmcnt(0)\n\t"
                     "v_readfirstlane_b32 s56, v150\n\ts_cmp_lt_u32 s56, s55\n\ts_cbranch_scc1 35b\n"
                     "36:\n\ts_mul_i32 s55, s57, 3072\n\tv_add_u32 v150, s55, %[apk]\n\ts_getpc_b64 s[48:49]\n"
                     "37:\n\ts_mul_i32 s55, s57, 40\n\ts_add_u32 s55, s55, 38f-37b\n\ts_add_u32 s48, s48, s55\n\ts_addc_u32 s49, s49, 0\n\t"
                     "s_add_u32 s57, s57, s54\n\ts_sub_u32 s54, s54, 1\n\ts_setpc_b64 s[48:49]\n"
                     "38:\n\t" PV_W2(0, 3, 4, 7, 8, 11) PV_W2(12, 15, 16, 19, 20, 23) PV_W2(24, 27, 28, 31, 32, 35) PV_W2(36, 39, 40, 43, 44, 47)
                     PV_W2(48, 51, 52, 55, 56, 59) PV_W2(60, 63, 64, 67, 68, 71)
                     "s_waitcnt lgkmcnt(0)\n\ts_branch 3b\n"
                     "99:\n\t" PV_MF("76:79", "96:99", "126:129") PV_MF("80:83", "102:105", "126:129") PV_T(58)
                     "v_add_f32 v153, v86, v87\n\tv_add_f32 v151, v84, v85\n\tv_rcp_f32 v150, v153\n\tv_mul_f32 v151, %[nic], v151\n\t"
                     "v_add_u32 v152, %[aw], %[apk]\n\tv_mul_f32 v151, v151, v150\n\t"
                     PV_NRM(72, 73, 74, 75, 0) PV_NRM(76, 77, 78, 79, 2048) PV_NRM(80, 81, 82, 83, 4096)
                     "v_mov_b32 v154, %[x1]\n\ts_and_b64 exec, exec, 1\n\tds_add_u32 v154, %[one]\n\ts_cselect_b64 exec, -1, 0\n\ts_cselect_b32 s55, %[x1n], 0\n\t" PV_T(60)
                     "v_log_f32 v153, v153\n\tv_subrev_f32 v88, %[toff], v88\n\tv_lshrrev_b32 v151, 1, %[apm]\n\tv_add_f32 v88, v88, v153\n\tv_add_u32 v151, %[lwo], v151\n\t"
                     "v_mbcnt_lo_u32_b32 v155, -1, 0\n\tv_mbcnt_hi_u32_b32 v155, -1, v155\n\tds_write_b32 v151, v88\n\t"
                     "v_and_b32 v151, 1, v155\n\tv_and_b32 v155, 48, v155\n\tv_lshl_add_u32 v155, v151, 2, v155\n\tv_add_u32 v155, %[lw], v155\n\t"
                     "ds_read2_b32 v[114:115], v155 offset1:2\n\tv_add_u32 v152, %[ar], %[apk]\n"
                     "97:\n\tds_read_b32 v150, v154\n\tds_read_b128 v[90:93], v152\n\tds_read_b128 v[94:97], v152 offset:1024\n\tds_read_b128 v[98:101], v152 offset:2048\n\t"
                     "ds_read_b128 v[102:105], v152 offset:3072\n\tds_read_b128 v[106:109], v152 offset:4096\n\tds_read_b128 v[110:113], v152 offset:5120\n\t"
                     "s_sub_i32 s47, s47, 1\n\ts_cmp_lt_i32 s47, 0\n\ts_cselect_b32 s55, 0, s55\n\ts_waitcnt vmcnt(0) lgkmcnt(6)\n\t"
                     "v_readfirstlane_b32 s56, v150\n\ts_cmp_lt_u32 s56, s55\n\ts_cbranch_scc1 97b\n\ts_cmp_eq_u32 s46, 0\n\t"
                     PV_G(5, "90:93", "0:3", "4:7", "8:11", "0", "0", "0")
                     PV_G(4, "94:97", "12:15", "16:19", "20:23", "v[72:75]", "v[76:79]", "v[80:83]")
                     PV_G(3, "98:101", "24:27", "28:31", "32:35", "v[72:75]", "v[76:79]", "v[80:83]")
                     PV_G(2, "102:105", "36:39", "40:43", "44:47", "v[72:75]", "v[76:79]", "v[80:83]")
                     PV_G(1, "106:109", "48:51", "52:55", "56:59", "v[72:75]", "v[76:79]", "v[80:83]")
                     PV_G(0, "110:113", "60:63", "64:67", "68:71", "v[72:75]", "v[76:79]", "v[80:83]")
                     "v_xor_b32 v114, v114, %[ltag]\n\tv_xor_b32 v115, v115, %[ltag]\n\ts_nop 3\n\t"
                     "v_cvt_pk_bf16_f32 v116, v72, v73\n\tv_cvt_pk_bf16_f32 v120, v74, v75\n\tv_cvt_pk_bf16_f32 v117, v76, v77\n\tv_cvt_pk_bf16_f32 v121, v78, v79\n\t"
                     "v_cvt_pk_bf16_f32 v118, v80, v81\n\tv_bitop3_b32 v119, v116, v117, v114 bitop3:0x96\n\tv_cvt_pk_bf16_f32 v122, v82, v83\n\t"
                     "v_bitop3_b32 v123, v120, v121, v115 bitop3:0x96\n\tv_xor_b32 v119, v119, v118\n\tv_xor_b32 v123, v123, v122\n\t"
                     "s_cbranch_scc1 2b\n\t"
                     "global_store_dwordx4 %[so], v[116:119], %[bx]" SPOL "\n\tglobal_store_dwordx4 %[so], v[120:123], %[bx2]" SPOL "\n\t"
                     PV_T(62) "s_waitcnt lgkmcnt(0)\n\ts_nop 0"
                     : [need] "+v"(need), "={s[58:59]}"(tpv), "={s[60:61]}"(tp7), "={s[62:63]}"(tx1)
                     : [so] "v"(((ej * 16 + (lane >> 3)) * NWG + blockIdx.x) * 128 + (lane & 7) * 16), [ltag] "v"(line_tag(epoch, lane & 7)),
                       [bx] "s"(box), [bx2] "s"(box + 8 * NWG * 128), [aw] "s"(A_IMG - IMG_A + (ej >> 1) * 1024 + (ej & 1) * 8), [ar] "i"(A_IMG - IMG_A),
                       [nic] "i"(__builtin_bit_cast(unsigned, -1.f / CC)), [toff] "i"(__builtin_bit_cast(unsigned, TOFF)),
                       [lwo] "i"(CT_OFF + C_LW - PKM / 2), [lw] "i"(CT_OFF + C_LW), [x1] "i"(CT_OFF + C_X1), [x1n] "i"(X1N),
                       [apk] "v"(apk), [apm] "v"(lbase + PKM + r * 8), [apu] "v"(lbase + PKU + g * 64 + (r & 3) * 16),
                       [ox] "v"(lbase + tr_off(4 * g + qq, 4 * ej + p4)), [oy] "v"(lbase + 32 * ROWB + tr_off(2 * g + (qq >> 1), 4 * ej + p4)),
                       [ca] "v"(ct + (lane == 0 ? C_PFL + (k0 % NPK) * 4 : C_FULL + (k0 % NSLOT) * 4)),
                       [lim] "v"(ct + (lane == 0 ? C_PFL + NPK * 4 : C_FULL + NSLOT * 4)), [base] "v"(ct + (lane == 0 ? C_PFL : C_FULL)), [one] "v"(1u),
                       [k0] "s"(k0), [s0] "s"(helper ? ((TPW - 1) % NSLOT) * SLOT : 0), [e0] "s"(helper ? ((TPW - 1) % NPK) * 128 : 0),
                       [w2] "s"(box + W2_OFF + ej * 18432 - (lbase + IMG_A)), [fl] "s"(box + W2F_OFF),
                       [tag] "s"(u32x2{line_tag(epoch, 10), line_tag(epoch, 11)}), [gcap] "i"(GSPIN_CAP), [iss] "i"(CT_OFF + C_ISS), [cap] "i"(SPIN_CAP), [slot] "i"(SLOT), [ring] "i"(NSLOT * SLOT), [pring] "i"(NPK * 128),
                       [npk2] "i"(2 * NPK), [last] "i"(TPW - 1), [tight] "i"(TPW - 5),  [sch] "s"(W2SCH)
                     : "memory", "scc", "vcc", "s44", "s45", "s46", "s47", "s48", "s49", "s50", "s51", "s52", "s53", "s54", "s55", "s56", "s57",
                       "v0", "v1", "v2", "v3", "v4", "v5", "v6", "v7", "v8", "v9", "v10", "v11", "v12", "v13", "v14", "v15", "v16", "v17", "v18", "v19", "v20", "v21", "v22", "v23", "v24", "v25", "v26", "v27", "v28", "v29", "v30", "v31", "v32", "v33", "v34", "v35", "v36", "v37", "v38", "v39", "v40", "v41", "v42", "v43", "v44", "v45", "v46", "v47", "v48", "v49", "v50", "v51", "v52", "v53", "v54", "v55", "v56", "v57", "v58", "v59", "v60", "v61", "v62", "v63", "v64", "v65", "v66", "v67", "v68", "v69", "v70", "v71", "v72", "v73", "v74", "v75", "v76", "v77", "v78", "v79", "v80", "v81", "v82", "v83", "v84", "v85", "v86", "v87", "v88",
                       "v90", "v91", "v92", "v93", "v94", "v95", "v96", "v97", "v98", "v99", "v100", "v101", "v102", "v103", "v104", "v105", "v106", "v107",
                       "v108", "v109", "v110", "v111", "v112", "v113", "v114", "v115", "v116", "v117", "v118", "v119", "v120", "v121", "v122", "v123",
                       "v124", "v125", "v126", "v127", "v128", "v129", "v130", "v131", "v132", "v133", "v134", "v135", "v136", "v137", "v138", "v139",
                       "v140", "v141", "v142", "v143", "v144", "v145", "v146", "v147", "v148", "v149", "v150", "v151", "v152", "v153", "v154", "v155");
      }
    } else {
      LANE_VARS
      const int c = wave - 1 - NE;
      SCtx sc;
      sc.L0 = L0; sc.ct = ct; sc.apk = lbase + IMG_A + lane * 16; sc.apm = lbase + PKM + r * 8;
      sc.api = lbase + PKU + g * 64 + (r & 3) * 2 + ((r >> 2) == g ? 0 : 8); sc.amr = ct + C_MREF + r * 4;
      sc.o_rb = r * ROWB + ((g ^ swz(r)) << 4);
      sc.ev = (r & 1) ^ 1;
      sc.o_yb = (r & 1) ? ONES_OFF + (g << 4) : (32 + (r >> 1)) * ROWB + ((g ^ swz(r >> 1)) << 4);
      TNOW(tq0);
      if (c >= NEARLY) {
        static_assert(NS - NEARLY == 3, "");
        const int u = c - NEARLY;
        const unsigned epoch = epoch_load(box);
        const unsigned ta = line_tag(epoch, 8), tb = line_tag(epoch, 9);
        while ((unsigned)__builtin_amdgcn_s_memrealtime() - tent < QKWAIT) __builtin_amdgcn_s_sleep(4);
        u32x4 v[4];
#pragma unroll 1
        for (int sp = 0; sp < GSPIN_CAP; ++sp) {
          asm volatile("global_load_dwordx4 %0, %4, %5 sc1\n\tglobal_load_dwordx4 %1, %4, %5 offset:1024 sc1\n\tglobal_load_dwordx4 %2, %4, %5 offset:2048 sc1\n\t"
                       "global_load_dwordx4 %3, %4, %5 offset:3072 sc1\n\ts_waitcnt vmcnt(0)"
                       : "=&v"(v[0]), "=&v"(v[1]), "=&v"(v[2]), "=&v"(v[3]) : "v"(lane * 16), "s"(box + QK_OFF + u * 4096) : "memory");
          unsigned bad = 0;
#pragma unroll
          for (int t = 0; t < 4; ++t) bad |= (v[t][0] ^ v[t][1] ^ v[t][2] ^ ta) | (v[t][3] ^ tb);
          if (!__any(bad != 0u)) break;
        }
        TSET(tqa);
#pragma unroll
        for (int t = 0; t < 4; ++t)
          *(LDS_AS(u32x2)*)(L0 + IMG_A + (4 * u + t) * 512 + ((g >> 1) * 16 + r) * 16 + (g & 1) * 8) = u32x2{v[t][0], v[t][1]};
        asm volatile("" ::: "memory");
        lds_add(ct + C_QI, 1u);
        lds_st32(ct + C_EP, epoch);
      }
      static_assert(NE == 4 && NPK == 6 && NSLOT == 10, "");
      s_items(sc, helper ? TPW : c, helper || c >= NEARLY);
      const int hi = (HMAP >> (4 * c)) & 15;
      if (hi < 6 && (!helper || hi >= 4)) s_half(sc, TPW - NH1 + (hi >> 1), hi & 1);
      TNOW(tse);
      rslot = c;
    }
  }

  if (rslot >= 0 && ((blockIdx.x >> 3) & 3) == 1) {
    LANE_VARS
    __builtin_amdgcn_s_setprio(0);
    const int rid = ((blockIdx.x >> 5) << 3) | (blockIdx.x & 7);
    const int qa = ((rid >> 1) & 3) * 4 + ((rid >> 3) & 1) * 2;
    const char* src = box + (size_t)(rid * NWG + rslot * 32) * 128;
    const int e = lane & 7;
    const int oidx = (qa + (e & 1)) * CC + 48 * (rid >> 4) + 16 * (e < 6 ? e >> 1 : 0) + 8 * (rid & 1) + (lane >> 3);
    const unsigned short qv = ((const gus*)q)[oidx];
    u32x4 am;
#pragma unroll
    for (int d = 0; d < 4; ++d) am[d] = (e >> 1) == d ? ((e & 1) ? 0xffff0000u : 0x0000ffffu) : 0u;
    const unsigned epoch = *(LDS_AS(unsigned)*)(L0 + CT_OFF + C_EP);
    const unsigned arf = lbase + RED_OFF + 4096 + (lane & 1) * 4;
    poll_ge_sleep(ct + C_FREE + ((TPW - NPK) % NSLOT) * 4, 8u * ((TPW - NPK) / NSLOT + 1));
    lds_st32(arf + rslot * 8, 0u);
    unsigned long long tr;
#define R_LD(d, m, o) "s_mov_b64 exec, s[" m "]\n\tglobal_load_dwordx4 v[" d "], %[vo], %[src] offset:" #o " sc1\n\t"
#define R_W(l, a0, a1, a2, a3) "v_cvt_pk_bf16_f32 v" #a3 ", v" #a3 ", v" #a3 "\n\tv_and_b32 v" #a0 ", v60, v" #a3 "\n\tv_and_b32 v" #a1 ", v61, v" #a3 "\n\t" \
                               "v_and_b32 v" #a2 ", v62, v" #a3 "\n\tv_and_b32 v" #a3 ", v63, v" #a3 "\n\t"
#define R_G(n, w, m, l, d0, d1, d2, d3, a0, a1, a2, a3, c) \
  "s_mov_b64 exec, s[" m "]\n\ts_cbranch_execz " #n "f\n\ts_waitcnt vmcnt(" #w ")\n\tv_bitop3_b32 v" #l ", v" #d0 ", v" #d1 ", v" #d2 " bitop3:0x96\n\t" \
  "v_bitop3_b32 v" #l ", v" #l ", v" #d3 ", %[tag] bitop3:0x96\n\tv_mov_b32 v" #d3 ", s49\n\ts_mov_b64 exec, -1\n\tv_exp_f32 v" #a3 ", v" #l "\n\t" \
  "v_xor_b32_dpp v59, v" #l ", v" #l " quad_perm:[2,3,0,1] row_mask:0xf bank_mask:0x5\n\tv_xor_b32_dpp v59, v" #l ", v" #l " row_shr:4 row_mask:0xf bank_mask:0xa\n\t" \
  R_W(l, a0, a1, a2, a3) "v_cmp_ne_u32 s[" m "], 0, v59\n\ts_nop 0\n" \
  "1" #n ":\n\tv_mfma_f32_16x16x32_bf16 v[36:39], v[" #a0 ":" #a3 "], v[" #d0 ":" #d3 "], " c "\n\t"
#define R_X(n) #n ":\n\ts_mov_b64 exec, -1\n\ts_nop 2\n\ts_branch 1" #n "b\n"
#define R_F(l, d0, d3, a0, a1, a2, a3, c) "v_sub_f32 v" #a3 ", v" #l ", v56\n\tv_exp_f32 v" #a3 ", v" #a3 "\n\ts_nop 0\n\t" R_W(l, a0, a1, a2, a3) \
  "s_nop 1\n\tv_mfma_f32_16x16x32_bf16 v[36:39], v[" #a0 ":" #a3 "], v[" #d0 ":" #d3 "], " c "\n\t"
#define R_RD(d, a, b) "ds_read2st64_b32 v[" d "], %[ard] offset0:" #a " offset1:" #b "\n\t"
#define R_A(d, a) "v_pk_add_f32 v[" d "], v[" d "], v[" a "]\n\t"
#define R_S(x, f) "v_sub_f32 v" #f ", v" #f ", v56\n\tv_exp_f32 v" #f ", v" #f "\n\ts_nop 0\n\tv_mul_f32 v[" #x "], v[" #x "], v" #f "\n\tv_mul_f32 v[" #x " + 1], v[" #x " + 1], v" #f "\n\t"
    asm volatile("s_mov_b64 s[52:53], 0\n\ts_mov_b64 s[40:41], -1\n\ts_mov_b64 s[42:43], -1\n\ts_mov_b64 s[44:45], -1\n\ts_mov_b64 s[46:47], -1\n\ts_mov_b32 s48, %[cap]\n\ts_mov_b32 s49, 0x3f803f80\n\t"
                 "v_mov_b32 v57, 1\n\tv_mov_b32 v58, %[x1]\n"
                 "20:\n\ts_sleep 1\n\tds_read_b32 v56, v58\n\ts_sub_u32 s48, s48, 1\n\ts_cselect_b32 s50, 0, %[x1n]\n\ts_waitcnt lgkmcnt(0)\n\t"
                 "v_readfirstlane_b32 s51, v56\n\ts_cmp_lt_u32 s51, s50\n\ts_cbranch_scc1 20b\n\tv_mov_b32 v58, %[red]\n\ts_mov_b32 s48, %[gcap]\n\ts_sleep %[rs]\n"
                 "21:\n\t" R_LD("0:3", "40:41", 0) R_LD("4:7", "42:43", 1024) R_LD("8:11", "44:45", 2048) R_LD("12:15", "46:47", 3072)
                 "s_cmp_eq_u32 s48, %[gcap]\n\ts_cbranch_scc1 24f\n\ts_waitcnt vmcnt(0)\n24:\n\t"
                 R_G(1, 3, "40:41", 32, 0, 1, 2, 3, 16, 17, 18, 19, "0") R_G(2, 2, "42:43", 33, 4, 5, 6, 7, 20, 21, 22, 23, "v[36:39]")
                 R_G(3, 1, "44:45", 34, 8, 9, 10, 11, 24, 25, 26, 27, "v[36:39]") R_G(4, 0, "46:47", 35, 12, 13, 14, 15, 28, 29, 30, 31, "v[36:39]")
                 "v_max3_f32 v56, v32, v33, v34\n\ts_sub_u32 s48, s48, 1\n\ts_cbranch_scc1 22f\n\ts_or_b64 s[50:51], s[40:41], s[42:43]\n\tv_max_f32 v56, v56, v35\n\t"
                 "s_or_b64 s[50:51], s[50:51], s[44:45]\n\ts_or_b64 s[50:51], s[50:51], s[46:47]\n\tv_cmp_nlt_f32 vcc, |v56|, %[lim]\n\ts_cbranch_scc1 21b\n\t"
                 "s_cbranch_vccnz 25f\n"
                 "22:\n\ts_mov_b64 exec, %[wm]\n\tds_write_b128 %[awr], v[36:39]\n\ts_mov_b64 exec, 1\n\tds_add_rtn_u32 v59, v58, v57\n\ts_mov_b64 exec, -1\n\t"
                 R_RD("40:41", 0, 1) R_RD("42:43", 2, 3) R_RD("44:45", 4, 5) R_RD("46:47", 6, 7) R_RD("48:49", 8, 9) R_RD("50:51", 10, 11) R_RD("52:53", 12, 13) R_RD("54:55", 14, 15)
                 "s_waitcnt lgkmcnt(8)\n\tv_add_u32 v59, v59, v57\n\ts_nop 0\n\tv_readfirstlane_b32 s50, v59\n\ts_cmp_lg_u32 s50, 8\n\ts_cbranch_scc1 26f\n"
                 "23:\n\ts_waitcnt lgkmcnt(6)\n\t" R_A("40:41", "42:43") "s_waitcnt lgkmcnt(4)\n\t" R_A("44:45", "46:47") "s_waitcnt lgkmcnt(2)\n\t" R_A("48:49", "50:51")
                 R_A("40:41", "44:45") "s_waitcnt lgkmcnt(0)\n\t" R_A("52:53", "54:55") R_A("48:49", "52:53") R_A("40:41", "48:49")
                 "v_add_f32 v40, v40, v41\n\ts_nop 1\n\tv_mov_b32_dpp v41, v40 quad_perm:[2,3,2,3] row_mask:0xf bank_mask:0xf\n\ts_nop 1\n\t"
                 "v_mov_b32_dpp v41, v41 row_shl:4 row_mask:0xf bank_mask:0x5\n\tv_rcp_f32 v41, v41\n\ts_mov_b64 exec, %[om]\n\tv_mul_f32 v40, v40, v41\n\t"
                 "v_cvt_pk_bf16_f32 v40, v40, v40\n\tv_lshlrev_b32 v40, 16, v40\n\tv_add_f32 v40, v40, %[qf]\n\tv_cvt_pk_bf16_f32 v40, v40, v40\n\t"
                 "global_store_short %[oo], v40, %[out]\n\t"
                 "s_cmp_lg_u32 %[rid], 0\n\ts_cbranch_scc1 29f\n\ts_mov_b64 exec, 1\n\tv_mov_b32 v41, %[eo]\n\tglobal_store_dword v41, %[ep], %[bx] sc1\n\ts_branch 29f\n"
                 R_X(1) R_X(2) R_X(3) R_X(4)
                 "25:\n\tv_max_f32_dpp v56, v56, v56 row_ror:8 row_mask:0xf bank_mask:0xf\n\tv_mov_b32 v59, v56\n\ts_nop 1\n\tv_permlane16_swap_b32 v56, v59\n\tv_max_f32 v56, v56, v59\n\t"
                 "v_mov_b32 v59, v56\n\ts_nop 1\n\tv_permlane32_swap_b32 v56, v59\n\tv_max_f32 v56, v56, v59\n\tv_mov_b32 v57, 0x101\n\tv_add_u32 v59, %[rs8], %[arf]\n\tds_write_b32 v59, v56\n\t"
                 R_F(32, 0, 3, 16, 17, 18, 19, "0") R_F(33, 4, 7, 20, 21, 22, 23, "v[36:39]") R_F(34, 8, 11, 24, 25, 26, 27, "v[36:39]") R_F(35, 12, 15, 28, 29, 30, 31, "v[36:39]")
                 "s_nop 7\n\ts_branch 22b\n"
                 "26:\n\ts_waitcnt vmcnt(0)\n\ts_and_b32 s50, s50, 0xff\n\ts_cmp_lg_u32 s50, 8\n\ts_cbranch_scc1 29f\n\t"
                 "ds_read2_b32 v[0:1], %[arf] offset1:2\n\tds_read2_b32 v[2:3], %[arf] offset0:4 offset1:6\n\tds_read2_b32 v[4:5], %[arf] offset0:8 offset1:10\n\t"
                 "ds_read2_b32 v[6:7], %[arf] offset0:12 offset1:14\n\ts_waitcnt lgkmcnt(0)\n\tv_max3_f32 v56, v0, v1, v2\n\tv_max3_f32 v56, v56, v3, v4\n\t"
                 "v_max3_f32 v56, v56, v5, v6\n\tv_max_f32 v56, v56, v7\n\t"
                 R_S(40, 0) R_S(42, 1) R_S(44, 2) R_S(46, 3) R_S(48, 4) R_S(50, 5) R_S(52, 6) R_S(54, 7) "s_branch 23b\n"
                 "29:\n\ts_mov_b64 exec, -1\n\ts_waitcnt lgkmcnt(0)"
                 : "=&{s[52:53]}"(tr)
                 : "{v[60:63]}"(am), [vo] "v"(lane * 16), [src] "s"(src), [tag] "v"(line_tag(epoch, e)),
                   [awr] "v"(lbase + RED_OFF + (rslot * 2 + (r >> 3)) * 256 + (r & 7) * 32 + (g & 1) * 16), [ard] "v"(lbase + RED_OFF + lane * 4), [arf] "v"(arf),
                   [qf] "v"((unsigned)qv << 16), [oo] "v"(oidx * 2), [out] "s"(KARG(kb, 0)), [ep] "v"(epoch + 1u), [bx] "s"(box), [rid] "s"(rid), [rs8] "s"(rslot * 8),
                   [wm] "s"(0xff00ff0000ff00ffull), [om] "s"(0x3f3f3f3f3f3f3f3full), [lim] "s"(64.f),
                   [cap] "i"(SPIN_CAP), [gcap] "i"(GSPIN_CAP), [x1] "i"(CT_OFF + C_X1), [x1n] "i"(X1N), [red] "i"(CT_OFF + C_RED), [rs] "i"(RSLEEP), [eo] "i"(EPOCH_OFF)
                 : "memory", "scc", "vcc", "s40", "s41", "s42", "s43", "s44", "s45", "s46", "s47", "s48", "s49", "s50", "s51", "v0", "v1", "v2", "v3", "v4", "v5", "v6", "v7", "v8", "v9", "v10", "v11", "v12", "v13", "v14", "v15", "v16", "v17", "v18", "v19", "v20", "v21", "v22", "v23", "v24", "v25", "v26", "v27", "v28", "v29", "v30", "v31", "v32", "v33", "v34", "v35", "v36", "v37", "v38", "v39", "v40", "v41", "v42", "v43", "v44", "v45", "v46", "v47", "v48", "v49", "v50", "v51", "v52", "v53", "v54", "v55", "v56", "v57", "v58", "v59");
  }
}

#undef q
#undef f0
#undef f1
#undef wq
#undef wk
#undef wv
#undef wo
#undef box
extern "C" __global__ void dino_end() {}

extern "C" void kernel_launch(void* const* d_in, const int* in_sizes, int n_in,
                              void* d_out, int out_size, void* d_ws, size_t ws_size,
                              hipStream_t stream) {
  dino_main<<<NWG, 768, 0, stream>>>((const unsigned short*)d_in[0], (const char*)d_in[1], (const char*)d_in[2], (const char*)d_in[3],
                                     (const char*)d_in[4], (const char*)d_in[5], (const char*)d_in[6], (char*)d_ws + WSOFF,
                                     (unsigned short*)d_out);
}
```

```cpp
#include <hip/hip_runtime.h>
#include <hip/hip_bf16.h>
#include <stdint.h>

#define PROFOFF (380ll << 20)
#define TNOW(v) do {} while (0)
#define TPUT(id, v) do {} while (0)

using bf16x8  = __attribute__((ext_vector_type(8))) short;
using f32x4   = __attribute__((ext_vector_type(4))) float;
using f32x2   = __attribute__((ext_vector_type(2))) float;
using u32x4   = __attribute__((ext_vector_type(4))) unsigned;
using u32x2   = __attribute__((ext_vector_type(2))) unsigned;
using v4i16_t = __attribute__((ext_vector_type(4))) short;
typedef __attribute__((address_space(3))) char* lds_ptr;
#define LDS_AS(T) __attribute__((address_space(3))) T

constexpr int CC = 192, IMW = 512;
constexpr int NWG = 256, NS = 7, NE = 4;
constexpr int TPW = 33;
constexpr int ROWB = 384;
constexpr int SLOT = 40 * ROWB;
constexpr int NSLOT = 10;
#define RSTR 322
#define PSTR 1
constexpr int BOX_BYTES = (64 * RSTR + NWG * PSTR) * 128, EPOCH_OFF = BOX_BYTES, QK_OFF = EPOCH_OFF + 4096, W2F_OFF = QK_OFF + 12288, W2_OFF = W2F_OFF + 1024;
constexpr int GSPIN_CAP = 1 << 16;
#define PACE16 777
#define QKWAIT 300
#define NEARLY 4
#define PRECLR 4
#define RSLEEP 24
#define HDUE 93
#define PACES 810
#define PK1 14
#define AG 100
#define AHI 22
#define ADL 60
#define AMG 200
#define NORELAY
#define HMAP 0x4321085
#define HB 1
#define WSOFF ((ws_size - (4u << 20)) & ~(size_t)0x1fffff)
#define EPM 0
#define RSTART 1880
#define RMODE 1
#define CLRK 17
#define X1N (PRECLR == 5 ? 11u : 5u)
#define RPOL " sc1"
#define SPOL " sc1"
#define RGAP 0
#define RDRY 0
#define RWAIT 76
#define RMIN 30
#define PROBE 0xffffffffffffffffull
#define RELAYT 0
#define PRE5 1
#define SL_S 2
#define SL_FAR 12
#define SL_P 2
#define SL_L 1
#define STR_(x) #x
#define STR(x) STR_(x)
#define AHIR AHI
#define QKFAST 20
#define QKWAIT2 240
#define OPOL ""
#define T0BACK 7
#define W2SCH 0x0311180000000000ull
#define NH1 3
constexpr int NPK = 6;
constexpr float EPS = 1e-5f;
constexpr float SQRT_C = 13.856406460551018f;
constexpr float LOG2E = 1.4426950408889634f;
constexpr float THR = 20.0f;
constexpr float TOFF = 192.0f;
constexpr int SPIN_CAP = 1 << 22;

constexpr int IMG_A = NSLOT * SLOT;
constexpr int CT_OFF = IMG_A + 6144;
constexpr int PKM = CT_OFF + 512, PKU = PKM + NPK * 128;
constexpr int ONES_OFF = PKU + NPK * 256;
constexpr int JUNK = ONES_OFF + ROWB;
constexpr int LDS_BYTES = JUNK + 256;
constexpr int H_R = 192 * ROWB, H_Q = H_R + 32 * ROWB, H_C = H_Q + 6144;
#define PWIN 35
static_assert(LDS_BYTES <= 163840 && (PKU & 15) == 0 && (ONES_OFF & 127) == 0, "lds");
constexpr int C_FULL = 0, C_FREE = 64, C_EP = 128, C_RED = 144, C_ISS = 152, C_X1 = 156, C_QI = 160, C_PFL = 192, C_CONS = 256,
              C_MREF = 320, C_LW = 384;

typedef __bf16 bf16x2_t __attribute__((ext_vector_type(2)));
__device__ __forceinline__ unsigned cvtpk(float lo, float hi) {
  return __builtin_bit_cast(unsigned, __builtin_convertvector(f32x2{lo, hi}, bf16x2_t));
}
__device__ __forceinline__ float rbf(float x) { return __uint_as_float(cvtpk(x, x) << 16); }
__device__ __forceinline__ float bflo(unsigned p) { return __uint_as_float(p << 16); }
__device__ __forceinline__ float bfhi(unsigned p) { return __uint_as_float(p & 0xffff0000u); }
__device__ __forceinline__ bf16x8 pack8(const f32x4& a, const f32x4& b) {
  return __builtin_bit_cast(bf16x8, u32x4{cvtpk(a[0], a[1]), cvtpk(a[2], a[3]), cvtpk(b[0], b[1]), cvtpk(b[2], b[3])});
}
__device__ __forceinline__ float xrow16_max(float x) {
  auto s = __builtin_amdgcn_permlane16_swap(__float_as_uint(x), __float_as_uint(x), false, false);
  x = fmaxf(__uint_as_float(s[0]), __uint_as_float(s[1]));
  auto t = __builtin_amdgcn_permlane32_swap(__float_as_uint(x), __float_as_uint(x), false, false);
  return fmaxf(__uint_as_float(t[0]), __uint_as_float(t[1]));
}
__device__ __forceinline__ float xrow16_sum(float x) {
  auto s = __builtin_amdgcn_permlane16_swap(__float_as_uint(x), __float_as_uint(x), false, false);
  x = __uint_as_float(s[0]) + __uint_as_float(s[1]);
  auto t = __builtin_amdgcn_permlane32_swap(__float_as_uint(x), __float_as_uint(x), false, false);
  return __uint_as_float(t[0]) + __uint_as_float(t[1]);
}
__device__ __forceinline__ v4i16_t tr_read(lds_ptr p) {
  return __builtin_amdgcn_ds_read_tr16_b64_v4i16((LDS_AS(v4i16_t)*)p);
}
__device__ __forceinline__ f32x4 mfma(bf16x8 a, bf16x8 b, f32x4 c) {
  return __builtin_amdgcn_mfma_f32_16x16x32_bf16(a, b, c, 0, 0, 0);
}
__device__ __forceinline__ int swz(int p) { return (p & 6) | ((p >> 2) & 1); }
constexpr unsigned swz_tbl(int i) {
  unsigned t = 0;
  for (int j = 0; j < 8; ++j) { const int p = (8 * i + j) / 3; t |= (unsigned)((p & 6) | ((p >> 2) & 1)) << (4 * j); }
  return t;
}
__device__ __forceinline__ bf16x8 mk8(v4i16_t a, v4i16_t b) { return bf16x8{a[0], a[1], a[2], a[3], b[0], b[1], b[2], b[3]}; }

__device__ __forceinline__ void lds_st32(unsigned addr, unsigned v) {
  asm volatile("ds_write_b32 %0, %1" : : "v"(addr), "v"(v) : "memory");
}
__device__ __forceinline__ unsigned lds_ld32(unsigned addr) {
  unsigned v;
  asm volatile("ds_read_b32 %0, %1\n\ts_waitcnt lgkmcnt(0)" : "=v"(v) : "v"(addr) : "memory");
  return __builtin_amdgcn_readfirstlane(v);
}
__device__ __forceinline__ void lds_add(unsigned addr, unsigned n) {
  asm volatile("s_mov_b64 exec, 1\n\tds_add_u32 %0, %1\n\ts_mov_b64 exec, -1\n\ts_nop 3" : : "v"(addr), "v"(n) : "memory");
}
__device__ __forceinline__ void poll_ge(unsigned addr, unsigned need) {
  unsigned v, s, n2, cnt = SPIN_CAP;
  asm volatile("1:\n\tds_read_b32 %0, %4\n\ts_sub_u32 %3, %3, 1\n\ts_cselect_b32 %2, 0, %5\n\ts_waitcnt lgkmcnt(0)\n\t"
               "v_readfirstlane_b32 %1, %0\n\ts_cmp_lt_u32 %1, %2\n\ts_cbranch_scc1 1b"
               : "=&v"(v), "=&s"(s), "=&s"(n2), "+s"(cnt) : "v"(addr), "s"(need) : "memory", "scc");
}
__device__ __forceinline__ void poll_ge_sleep(unsigned addr, unsigned need) {
  need = __builtin_amdgcn_readfirstlane(need);
  unsigned v, s, n2, cnt = SPIN_CAP;
  asm volatile("s_branch 2f\n1:\n\ts_sleep " STR(SL_S) "\n2:\n\tds_read_b32 %0, %4\n\ts_sub_u32 %3, %3, 1\n\ts_cselect_b32 %2, 0, %5\n\ts_waitcnt lgkmcnt(0)\n\t"
               "v_readfirstlane_b32 %1, %0\n\ts_cmp_lt_u32 %1, %2\n\ts_cbranch_scc1 1b"
               : "=&v"(v), "=&s"(s), "=&s"(n2), "+s"(cnt) : "v"(addr), "s"(need) : "memory", "scc");
}
__device__ __forceinline__ bf16x8 img_ld(lds_ptr L0, int off, int kk, int lane) { return *(LDS_AS(bf16x8)*)(L0 + off + kk * 1024 + lane * 16); }
__device__ __forceinline__ void img_st(lds_ptr L0, int off, int kk, int lane, bf16x8 v) { *(LDS_AS(bf16x8)*)(L0 + off + kk * 1024 + lane * 16) = v; }

template <int N> __device__ __forceinline__ void wait_vm() { asm volatile("s_waitcnt vmcnt(%0)" :: "i"(N) : "memory"); }
constexpr int A_IMG = ((TPW + 1 - NPK) % NSLOT) * SLOT, RED_OFF = ((TPW - NPK) % NSLOT) * SLOT;
__device__ __forceinline__ unsigned line_tag(unsigned epoch, unsigned i) {
  unsigned x = (epoch + 1u + i * 0x9E3779B9u) * 0x85EBCA6Bu;
  x ^= x >> 15; x *= 0xC2B2AE35u;
  return x ^ (x >> 13);
}
__device__ __forceinline__ unsigned epoch_load(const char* box) {
  unsigned e;
  asm volatile("global_load_dword %0, %1, %2 sc1\n\ts_waitcnt vmcnt(0)" : "=v"(e) : "v"(EPOCH_OFF), "s"(box) : "memory");
  return __builtin_amdgcn_readfirstlane(e);
}
__device__ __forceinline__ int tr_off(int row, int col4) {
  const int ch = col4 >> 1;
  return row * ROWB + (((ch & ~7) | ((ch & 7) ^ swz(row & 15))) << 4) + 8 * (col4 & 1);
}
#define PIN3(a, i) "+v"(a[i]), "+v"(a[i + 1]), "+v"(a[i + 2])

#define DMA1(d, b, off, pol) "global_load_lds_dwordx4 %[" #d "], %[" #b "] offset:" #off pol "\n\t"
__device__ __forceinline__ void issue_rows16(const char* a0, unsigned dst, const unsigned (&d)[3]) {
  unsigned keep;
  asm volatile("s_mov_b32 %[keep], m0\n\ts_mov_b32 m0, %[dst]\n\ts_nop 0\n\t"
               DMA1(d0, a, 0, "") DMA1(d1, a, 1024, "") DMA1(d2, a, 2048, "") DMA1(d0, a, 3072, "")
               "s_add_u32 m0, %[dst], 0x1000\n\ts_nop 0\n\t" DMA1(d1, b, 0, "") DMA1(d2, b, 1024, "") "s_mov_b32 m0, %[keep]"
               : [keep] "=&s"(keep) : [dst] "s"(dst), [a] "s"(a0), [b] "s"(a0 + 4096), [d0] "v"(d[0]), [d1] "v"(d[1]), [d2] "v"(d[2]) : "memory", "scc");
}
#define LD_VM "s_getreg_b32 s43, hwreg(HW_REG_IB_STS, 22, 2)\n\ts_getreg_b32 s42, hwreg(HW_REG_IB_STS, 0, 4)\n\ts_lshl4_add_u32 s42, s43, s42\n\t"
#define LD_GATE(n, X) \
  #n "0:\n\t" LD_VM "s_cmp_le_i32 s42, s46\n\ts_cbranch_scc1 " #n "2f\n" \
  #n "1:\n\t" X "s_cmp_gt_u32 s42, %[win]\n\ts_cbranch_scc1 " #n "5f\n\ts_cmp_ge_u32 s45, s44\n\ts_cbranch_scc1 " #n "7f\n" \
  #n "6:\n\ts_memrealtime s[40:41]\n\ts_sleep " STR(SL_L) "\n\ts_waitcnt lgkmcnt(0)\n\ts_sub_u32 s45, s40, %[t0]\n\ts_lshl_b32 s45, s45, 4\n\ts_cmp_lt_u32 s45, s44\n\ts_cbranch_scc1 " #n "6b\n" \
  #n "7:\n\t" LD_VM
#define LD_NEXT \
  "s_cmp_gt_u32 s42, %[ahi]\n\ts_cselect_b32 s42, %[amg], 0\n\ts_sub_u32 s43, s45, s44\n\ts_cmp_lt_u32 s43, s42\n\ts_cselect_b32 s42, %[p3] + %[adl], s39\n\t" \
  "s_add_u32 s44, s44, s42\n\ts_add_u32 s46, s46, 5\n\t"
#define LD_PUB0(n, a, b, c) \
  #n "2:\n\tv_add_u32 %[pub], 2, %[pub]\n\ts_cmp_ge_u32 s46, 30\n\ts_cbranch_scc1 " #n "4f\n\ts_cmp_ge_u32 s46, 15\n\ts_cbranch_scc1 " #n "3f\n\t" \
  "s_waitcnt vmcnt(" #a ")\n" #n "3:\n\ts_waitcnt vmcnt(" #b ")\n" #n "4:\n\ts_waitcnt vmcnt(" #c ")\n\tds_write_b32 %[af], %[pub] ; NOTRACK\n\t" \
  "s_sub_u32 s46, s46, 15\n\ts_branch " #n "1b\n"
#define LD_PUB(n, a, b, c) LD_PUB0(n, a, b, c) \
  #n "5:\n\ts_sleep 1\n\ts_sub_i32 s47, s47, 1\n\ts_cmp_gt_i32 s47, 0\n\ts_cbranch_scc1 " #n "0b\n\ts_waitcnt vmcnt(%[win])\n\ts_branch " #n "6b\n"
#define LD1(d, b, off) "global_load_lds_dwordx4 %[" #d "], s[" #b "] offset:" #off " nt\n\t"

#define PV_REQ(P, U, M, C, CA) \
  "v_lshl_add_u32 v150, s44, 3, %[apk]\n\tv_lshl_add_u32 v151, s44, 1, %[apu]\n\tv_add_u32 v152, s44, %[apm]\n\t" \
  "ds_read_b32 v" #C ", v" #CA "\n\tds_read_b128 v[" P "], v150\n\tds_read_b64 v[" M "], v152\n\tds_read_b128 v[" U "], v151\n\t"
#define PV_TR(d0, d1, a, o) "ds_read_b64_tr_b16 v[" #d0 ":" #d1 "], v" #a " offset:" #o "\n\t"
#define PV_FETCH(y0, y0b, a0, a0b, b0, b0b, y1, y1b, a1, a1b, b1, b1b, y2, y2b, a2, a2b, b2, b2b) \
  "v_add_u32 v153, s45, %[ox]\n\tv_add_u32 v154, s45, %[oy]\n\t" \
  PV_TR(a0, a0b, 153, 0) PV_TR(b0, b0b, 153, 6144) PV_TR(a1, a1b, 153, 128) PV_TR(b1, b1b, 153, 6272) PV_TR(a2, a2b, 153, 256) PV_TR(b2, b2b, 153, 6400) \
  PV_TR(y0, y0b, 154, 0) PV_TR(y1, y1b, 154, 128) PV_TR(y2, y2b, 154, 256) \
  "s_add_u32 s45, s45, %[slot]\n\ts_cmp_eq_u32 s45, %[ring]\n\ts_cselect_b32 s45, 0, s45\n\t"
#define PV_FETCH_A PV_FETCH(90, 91, 92, 93, 94, 95, 96, 97, 98, 99, 100, 101, 102, 103, 104, 105, 106, 107)
#define PV_FETCH_B PV_FETCH(108, 109, 110, 111, 112, 113, 114, 115, 116, 117, 118, 119, 120, 121, 122, 123, 124, 125)
#define PV_MF(d, a, p) "v_mfma_f32_16x16x32_bf16 v[" d "], v[" a "], v[" p "], v[" d "]\n\t"
#define PV_MOV(d, s) "v_mov_b32 v" #d ", v" #s "\n\t"
#define PV_WAITF(n, CA)   \
  #n ":\n\tds_read_b32 v155, v" #CA "\n\ts_sub_i32 s47, s47, 1\n\ts_cmp_lt_i32 s47, 0\n\ts_cbranch_scc1 " #n "9f\n\ts_waitcnt lgkmcnt(0)\n\t" \
  "v_cmp_lt_u32 vcc, v155, %[need]\n\ts_and_b64 vcc, vcc, 2\n\ts_cbranch_scc0 " #n "9f\n\ts_sleep " STR(SL_P) "\n\ts_branch " #n "b\n" #n "9:\n\ts_waitcnt lgkmcnt(0)\n\t"
#define PV_STEP(n, P, U, M, m0, m1, C, CA, P2, U2, M2, C2, CA2, X0, X1, X2, Y0, Y1, Y2, MV0, MV1, MV2, HOOK) \
  "s_waitcnt lgkmcnt(10)\n\t" \
  "v_cmp_lt_u32 vcc, v" #C ", %[need]\n\tv_cmp_neq_f32 s[48:49], v" #m0 ", v88\n\tv_cmp_neq_f32 s[50:51], v" #m1 ", v88\n\t" \
  "v_add_u32 v" #CA2 ", 4, v" #CA "\n\ts_or_b64 s[48:49], s[48:49], vcc\n\tv_cmp_eq_u32 s[52:53], v" #CA2 ", %[lim]\n\t" \
  "s_or_b64 s[48:49], s[48:49], s[50:51]\n\ts_cbranch_scc1 " #n "1f\n" \
  #n "0:\n\ts_add_u32 s44, s44, 128\n\ts_cmp_eq_u32 s44, %[pring]\n\ts_cselect_b32 s44, 0, s44\n\t" \
  "v_cndmask_b32 v" #CA2 ", v" #CA2 ", %[base], s[52:53]\n\t" \
  PV_MF("84:87", U, P) PV_MF("72:75", X0, P) PV_MF("76:79", X1, P) MV0 PV_MF("80:83", X2, P) MV1 \
  PV_MF("72:75", Y0, P) MV2 HOOK PV_REQ(P2, U2, M2, C2, CA2) PV_MF("76:79", Y1, P) PV_MF("80:83", Y2, P)
#define PV_REL(CA) \
  "s_mov_b64 exec, 3\n\tds_add_u32 v" #CA ", %[one] offset:64\n\ts_mov_b64 exec, -1\n\tv_add_u32 %[need], 2, %[need]\n\ts_add_u32 s46, s46, 1\n\t"
#define PV_SC1(p) "v_lshlrev_b32 v151, 16, v" #p "\n\tv_and_b32 v152, 0xffff0000, v" #p "\n\tv_mul_f32 v151, v151, v150\n\tv_mul_f32 v152, v152, v150\n\t" \
                  "v_cvt_pk_bf16_f32 v" #p ", v151, v152\n\t"
#define PV_MUL4(a, b, c, d) "v_mul_f32 v" #a ", v" #a ", v150\n\tv_mul_f32 v" #b ", v" #b ", v150\n\tv_mul_f32 v" #c ", v" #c ", v150\n\tv_mul_f32 v" #d ", v" #d ", v150\n\t"
#define PV_SLOW(n, P, U, M, m0, m1, C, CA, p0, p1, p2, p3) \
  #n "1:\n\ts_bitcmp0_b32 vcc_lo, 0\n\ts_cbranch_scc1 " #n "4f\n\ts_cmp_gt_u32 s46, %[tight]\n\ts_cbranch_scc1 " #n "3f\n" \
  #n "2:\n\ts_sleep " STR(SL_P) "\n\tds_read_b32 v" #C ", v" #CA "\n\ts_sub_i32 s47, s47, 1\n\ts_cmp_lt_i32 s47, 0\n\ts_cbranch_scc1 " #n "3f\n\ts_waitcnt lgkmcnt(0)\n\t" \
  "v_cmp_lt_u32 vcc, v" #C ", %[need]\n\ts_and_b64 vcc, vcc, 1\n\ts_cbranch_scc1 " #n "2b\n" \
  #n "3:\n\t" PV_REQ(P, U, M, C, CA) "s_sub_i32 s47, s47, 1\n\ts_cmp_lt_i32 s47, 0\n\ts_cselect_b32 s54, 0, 1\n\ts_waitcnt lgkmcnt(0)\n\t" \
  "v_cmp_lt_u32 vcc, v" #C ", %[need]\n\tv_cmp_neq_f32 s[48:49], v" #m0 ", v88\n\tv_cmp_neq_f32 s[50:51], v" #m1 ", v88\n\t" \
  "s_and_b32 s54, s54, vcc_lo\n\ts_cbranch_scc1 " #n "3b\n\ts_or_b64 s[48:49], s[48:49], s[50:51]\n\ts_cbranch_scc0 " #n "0b\n\ts_branch " #n "5f\n" \
  #n "4:\n\tv_cmp_neq_f32 s[48:49], v" #m0 ", v88\n\ts_or_b64 s[48:49], s[48:49], s[50:51]\n\ts_cbranch_scc0 " #n "0b\n" \
  #n "5:\n\tv_mov_b32 v150, v88\n\tv_max3_f32 v88, v88, v" #m0 ", v" #m1 "\n\tv_sub_f32 v150, v150, v88\n\tv_exp_f32 v150, v150\n\ts_nop 1\n\t" \
  PV_MUL4(72, 73, 74, 75) PV_MUL4(76, 77, 78, 79) PV_MUL4(80, 81, 82, 83) PV_MUL4(84, 85, 86, 87) \
  "v_sub_f32 v150, v" #m0 ", v88\n\tv_exp_f32 v150, v150\n\ts_nop 1\n\t" PV_SC1(p0) PV_SC1(p1) \
  "v_sub_f32 v150, v" #m1 ", v88\n\tv_exp_f32 v150, v150\n\ts_nop 1\n\t" PV_SC1(p2) PV_SC1(p3) \
  "s_branch " #n "0b\n"
#define PV_W2(a0, a1, b0, b1, c0, c1) \
  "global_load_dwordx4 v[" #a0 ":" #a1 "], v150, %[w2] sc1\n\tglobal_load_dwordx4 v[" #b0 ":" #b1 "], v150, %[w2] offset:1024 sc1\n\t" \
  "global_load_dwordx4 v[" #c0 ":" #c1 "], v150, %[w2] offset:2048 sc1\n\tv_add_u32 v150, 0xc00, v150\n\ts_sub_u32 s54, s54, 1\n\ts_cbranch_scc1 30b\n\t"

#define PV_NRM(a, b, c, d, o)   \
  "v_pk_fma_f32 v[" #a ":" #b "], v[" #a ":" #b "], v[150:151], v[150:151] op_sel:[0,0,1] op_sel_hi:[1,0,1]\n\t" \
  "v_pk_fma_f32 v[" #c ":" #d "], v[" #c ":" #d "], v[150:151], v[150:151] op_sel:[0,0,1] op_sel_hi:[1,0,1]\n\t" \
  "v_cvt_pk_bf16_f32 v" #a ", v" #a ", v" #b "\n\tv_cvt_pk_bf16_f32 v" #b ", v" #c ", v" #d "\n\tds_write_b64 v152, v[" #a ":" #b "] offset:" #o "\n\t"
#define PV_G(n, a, w0, w1, w2, c0, c1, c2) "s_waitcnt lgkmcnt(" #n ")\n\tv_mfma_f32_16x16x32_bf16 v[72:75], v[" a "], v[" w0 "], " c0 "\n\t" \
  "v_mfma_f32_16x16x32_bf16 v[76:79], v[" a "], v[" w1 "], " c1 "\n\tv_mfma_f32_16x16x32_bf16 v[80:83], v[" a "], v[" w2 "], " c2 "\n\t"
#define PV_Z4(a, b, c, d) "v_mov_b32 v" #a ", 0\n\tv_mov_b32 v" #b ", 0\n\tv_mov_b32 v" #c ", 0\n\tv_mov_b32 v" #d ", 0\n\t"
#define PV_T(n)

using u32x8 = __attribute__((ext_vector_type(8))) unsigned;
struct SCtx {
  lds_ptr L0;
  unsigned ct, apk, apm, api, amr;
  int o_rb, o_yb, ev;
  u32x8 q[3];
  unsigned tq;
};
__device__ __forceinline__ bf16x8 s_qf(const SCtx& c, int j) {
  const u32x8& v = c.q[j >> 1];
  const int o = 4 * (j & 1);
  return __builtin_bit_cast(bf16x8, u32x4{v[o], v[o + 1], v[o + 2], v[o + 3]});
}
#define S_M(d, a, b) "v_mfma_f32_16x16x32_bf16 v[" d "], v[" a "], v[" b "], v[" d "]\n\t"
#define S_M0(d, a, b) "v_mfma_f32_16x16x32_bf16 v[" d "], v[" a "], v[" b "], 0\n\t"
#define S_R(d, a, o) "ds_read_b128 v[" d "], v" #a " offset:" #o "\n\t"
#define S_W(n) "s_waitcnt lgkmcnt(" #n ")\n\t"
#define S_D " row_mask:0xf bank_mask:0xf\n\t"
#define S_3(x, y) S_M("100:103", x, y) S_M("104:107", x, x) S_M("96:99", y, y)
#define S_SQ(g, h) "v_fmac_f32 v" #g ", 2.0, v" #h "\n\t"
#define S_SQD(g, h) "v_fmac_f32_dpp v" #g ", v" #h ", %[two] quad_perm:[0,0,2,2]" S_D
#define S_U(u, h, y) "v_add_f32_dpp v" #u ", v" #h ", v" #y " quad_perm:[1,1,3,3]" S_D
#define S_C(d, a, b, m) "v_cndmask_b32 v" #d ", v" #a ", v" #b ", %[" #m "]\n\t"
#define S_B(d, n) "v_mov_b32_dpp v" #d ", v157 row_newbcast:" #n S_D
#define S_BASE(d, u, t) "v_pk_fma_f32 v[" d "], v[" u "], v[116:117], v[" t "] op_sel:[0,1,0] op_sel_hi:[1,1,0]\n\t"
#define S_LG(d, r) "v_pk_fma_f32 v[" d "], v[" d "], v[" r "], %[toff] op_sel_hi:[1,1,0]\n\t"
#define S_SUB(p, d) "v_pk_add_f32 v[" p "], v[" d "], v[150:151] op_sel_hi:[1,0] neg_lo:[0,1] neg_hi:[0,1]\n\t"
#define S_E(p) "v_exp_f32 v" #p ", v" #p "\n\t"
#define S_MUL(p, r) "v_pk_mul_f32 v[" p "], v[" p "], v[" r "]\n\t"
#define S_POS   \
  "s_mul_i32 s50, s42, 26\n\ts_lshr_b32 s50, s50, 8\n\ts_mul_i32 s50, s50, -10\n\ts_add_u32 s50, s50, s42\n\ts_mul_i32 s45, s50, %[slot]\n\ts_mov_b32 s43, s45\n\t"
#define S_POLL(n, a, o, need, back, far) \
  #n ":\n\tds_read_b32 v152, v" #a " offset:" #o "\n\ts_sub_i32 s47, s47, 1\n\ts_cmp_lt_i32 s47, 0\n\ts_waitcnt lgkmcnt(0)\n\ts_cbranch_scc1 " back "\n\t" \
  "v_readfirstlane_b32 s40, v152\n\ts_cmp_ge_u32 s40, " need "\n\ts_cbranch_scc1 " back "\n\ts_sleep " STR(SL_S) "\n\t" \
  "s_add_u32 s40, s40, " far "\n\ts_cmp_ge_u32 s40, " need "\n\ts_cbranch_scc1 " #n "b\n\ts_sleep " STR(SL_FAR) "\n\ts_branch " #n "b\n"
__device__ __forceinline__ void s_items(SCtx& c, int k0, int late) {
  unsigned long long tq;
  asm volatile("s_mov_b32 s47, %[cap]\n\ts_mov_b32 s49, 0\n\ts_mov_b32 s42, %[k0]\n\ts_cmp_lg_u32 %[late], 0\n\ts_cbranch_scc1 7f\n"
               "1:\n\ts_cmp_ge_u32 s42, %[end]\n\ts_cbranch_scc1 9f\n\t"
               S_POS
               "s_mul_i32 s48, s42, 43\n\ts_lshr_b32 s48, s48, 8\n\ts_mul_i32 s46, s48, -6\n\ts_add_u32 s46, s46, s42\n\ts_lshl2_add_u32 s48, s48, %[ns]\n\t"
               "v_add_u32 v144, s45, %[orb]\n\tv_mad_u32_u24 v145, s43, %[ev], %[oyb]\n\ts_lshl2_add_u32 s40, s46, %[pfl]\n\ts_lshl2_add_u32 s41, s50, %[full]\n\t"
               "v_xor_b32 v146, 64, v144\n\tv_xor_b32 v147, 64, v145\n\tv_mov_b32 v148, s40\n\tv_mov_b32 v149, s41\n\t"
               "s_lshl1_add_u32 s44, s42, 2\n\ts_lshl_b32 s46, s46, 7\n\ts_mov_b64 vcc, %[dg]\n\ts_cmp_lt_u32 s49, s44\n\ts_cbranch_scc1 5f\n"
               "2:\n\tds_read_b32 v150, %[amr]\n\tds_read_b32 v151, v148 offset:64\n\t"
               S_R("24:27", 145, 0) S_R("48:51", 144, 0) S_R("28:31", 147, 0) S_R("52:55", 146, 0) S_R("32:35", 145, 128) S_R("56:59", 144, 128)
               S_R("36:39", 147, 128) S_R("60:63", 146, 128) S_R("40:43", 145, 256) S_R("64:67", 144, 256) S_R("44:47", 147, 256) S_R("68:71", 146, 256)
               S_W(10) S_M0("100:103", "48:51", "24:27") S_M0("104:107", "48:51", "48:51") S_R("72:75", 144, 6144) S_R("76:79", 146, 6144) S_M0("96:99", "24:27", "24:27")
               S_W(10) S_M("100:103", "52:55", "28:31") S_M("104:107", "52:55", "52:55") S_R("80:83", 144, 6272) S_R("84:87", 146, 6272) S_M("96:99", "28:31", "28:31")
               S_W(10) S_M("100:103", "56:59", "32:35") S_M("104:107", "56:59", "56:59") S_R("88:91", 144, 6400) S_R("92:95", 146, 6400) S_M("96:99", "32:35", "32:35")
               "s_mov_b64 exec, 1\n\tds_add_u32 v149, %[four] offset:64\n\ts_mov_b64 exec, -1\n\tv_readfirstlane_b32 s51, v151\n\ts_setprio 1\n\ts_nop 0\n\t"
               S_W(11) S_3("60:63", "36:39") S_W(9) S_3("64:67", "40:43") S_W(7) S_3("68:71", "44:47")
               S_W(6) S_M0("108:111", "72:75", "24:27") S_M0("112:115", "72:75", "72:75")
               S_W(5) S_M("108:111", "76:79", "28:31") S_M("112:115", "76:79", "76:79")
               S_W(4) S_M("108:111", "80:83", "32:35")
               "v_mov_b32_dpp v152, v96 quad_perm:[1,1,3,3]" S_D "v_mov_b32_dpp v153, v98 quad_perm:[1,1,3,3]" S_D
               S_M("112:115", "80:83", "80:83") S_C(154, 96, 98, r2) S_SQ(104, 100)
               S_W(3) S_M("108:111", "84:87", "36:39") S_SQD(105, 101) S_SQ(106, 102)
               S_M("112:115", "84:87", "84:87") S_SQD(107, 103) "v_add_f32 v154, %[eps], v154\n\t"
               S_W(2) S_M("108:111", "88:91", "40:43") S_U(128, 100, 152) S_U(129, 101, 152)
               S_M("112:115", "88:91", "88:91") S_U(130, 102, 153) S_U(131, 103, 153)
               S_W(1) S_M("108:111", "92:95", "44:47") S_C(104, 104, 105, r1) S_C(106, 106, 107, r1)
               S_M("112:115", "92:95", "92:95") S_C(155, 128, 129, r1) S_C(156, 130, 131, r1)
               "s_cmp_lt_u32 s42, %[early]\n\ts_cbranch_scc1 7f\n"
               "3:\n\t" S_M0("116:119", "24:27", "0:3") S_C(104, 104, 106, r2) S_C(155, 155, 156, r2)
               S_M("116:119", "28:31", "4:7") "v_add_f32_dpp v104, v154, v104 quad_perm:[0,0,2,2]" S_D S_U(132, 108, 152)
               S_M("116:119", "32:35", "8:11") S_U(133, 109, 152) S_U(134, 110, 153)
               S_M("116:119", "36:39", "12:15") S_U(135, 111, 153) S_SQ(112, 108)
               S_M("116:119", "40:43", "16:19") S_SQD(113, 109) S_SQ(114, 110)
               S_M("116:119", "44:47", "20:23") S_SQD(115, 111) S_C(156, 132, 133, r1) S_C(157, 134, 135, r1)
               S_C(112, 112, 113, r1) S_C(114, 114, 115, r1) S_C(156, 156, 157, r2) S_C(112, 112, 114, r2)
               "v_add_f32_dpp v112, v154, v112 quad_perm:[0,0,2,2]" S_D
               "v_mul_f32 v117, %[nic], v117\n\tv_cndmask_b32_dpp v155, v156, v155, vcc row_ror:4" S_D
               S_BASE("120:121", "128:129", "116:117") S_BASE("122:123", "130:131", "118:119")
               "v_cndmask_b32_dpp v104, v112, v104, vcc row_ror:4" S_D "v_mul_f32 v156, %[nic], v155\n\t"
               S_M("120:123", "48:51", "0:3") S_BASE("124:125", "132:133", "116:117") S_BASE("126:127", "134:135", "118:119")
               S_M("120:123", "52:55", "4:7") "v_fmac_f32 v104, v156, v155\n\tv_max_f32 v104, %[eps], v104\n\t"
               S_M("120:123", "56:59", "8:11") "v_rsq_f32 v157, v104\n\t"
               S_M("120:123", "60:63", "12:15") "v_mul_f32 v104, v104, v157\n\tv_mov_b32_dpp v157, v157 row_ror:12 row_mask:0x2 bank_mask:0xf\n\t"
               S_M("120:123", "64:67", "16:19") "v_cvt_pk_bf16_f32 v105, v155, v104\n\tv_mov_b32_dpp v157, v157 row_ror:8 row_mask:0x4 bank_mask:0xf\n\t"
               S_M("120:123", "68:71", "20:23") "v_lshlrev_b32 v106, 16, v105\n\tv_mov_b32_dpp v157, v157 row_ror:4 row_mask:0x8 bank_mask:0xf\n\t"
               S_M("124:127", "72:75", "0:3") "v_and_b32 v107, 0xffff0000, v105\n\t" S_B(136, 0)
               S_M("124:127", "76:79", "4:7") S_B(137, 1) S_B(138, 2)
               S_M("124:127", "80:83", "8:11") S_B(139, 3) S_LG("120:121", "136:137")
               S_M("124:127", "84:87", "12:15") S_LG("122:123", "138:139") S_SUB("128:129", "120:121")
               S_M("124:127", "88:91", "16:19") S_SUB("130:131", "122:123") S_E(128)
               S_M("124:127", "92:95", "20:23") S_E(129) S_E(130) S_E(131)
               S_B(140, 4) S_B(141, 5) S_B(142, 6) S_B(143, 7) "v_sub_f32 v106, v155, v106\n\t"
               S_LG("124:125", "140:141") S_LG("126:127", "142:143") "v_sub_f32 v107, v104, v107\n\tv_max3_f32 v152, v120, v121, v122\n\t"
               S_SUB("132:133", "124:125") S_SUB("134:135", "126:127") "v_max3_f32 v153, v123, v124, v125\n\t"
               S_E(132) S_E(133) S_E(134) S_E(135)
               "v_max3_f32 v152, v152, v126, v127\n\tv_add_f32 v154, %[thr], v150\n\tv_max_f32 v152, v152, v153\n\tv_cvt_pk_bf16_f32 v106, v106, v107\n\t"
               "v_lshl_add_u32 v144, s46, 3, %[apk]\n\tv_cmp_gt_f32 vcc, v152, v154\n\tv_add_u32 v145, s46, %[apm]\n\tv_lshl_add_u32 v146, s46, 1, %[api]\n\t"
               "s_cbranch_vccnz 8f\n"
               "4:\n\t" S_MUL("128:129", "136:137") S_MUL("130:131", "138:139") S_MUL("132:133", "140:141") S_MUL("134:135", "142:143")
               "v_cvt_pk_bf16_f32 v128, v128, v129\n\tv_cvt_pk_bf16_f32 v129, v130, v131\n\tv_cvt_pk_bf16_f32 v130, v132, v133\n\tv_cvt_pk_bf16_f32 v131, v134, v135\n\t"
               "s_cmp_lt_u32 s51, s48\n\ts_cbranch_scc1 6f\n"
               "10:\n\tds_read_b32 v151, v149\n\tds_write_b128 v144, v[128:131]\n\tds_write2_b32 v145, v150, v150 offset0:0 offset1:1\n\ts_mov_b64 exec, %[dg2]\n\t"
               "ds_write_b16 v146, v105\n\tds_write_b16 v146, v106 offset:16\n\tds_write_b16_d16_hi v146, v105 offset:32\n\tds_write_b16_d16_hi v146, v106 offset:48\n\t"
               "s_mov_b64 exec, 1\n\tds_add_u32 v148, %[inc]\n\ts_mov_b64 exec, -1\n\ts_setprio 0\n\ts_add_u32 s42, s42, %[ns]\n\t"
               S_W(7) "v_readfirstlane_b32 s49, v151\n\ts_branch 1b\n"
               S_POLL(5, 149, 0, "s44", "2b", "2")
               S_POLL(6, 148, 64, "s48", "10b", "64")
               "7:\n\tv_mov_b32 v144, %[qi]\n\tv_mov_b32 v145, v152\n\tv_mov_b32 v146, 1\n"
               S_POLL(70, 144, 0, "%[nq]", "71f", "64")
               "71:\n\tv_mov_b32 v152, v145\n\tds_read_b128 v[0:3], %[apk]\n\tds_read_b128 v[4:7], %[apk] offset:1024\n\tds_read_b128 v[8:11], %[apk] offset:2048\n\t"
               "ds_read_b128 v[12:15], %[apk] offset:3072\n\tds_read_b128 v[16:19], %[apk] offset:4096\n\tds_read_b128 v[20:23], %[apk] offset:5120\n\t"
               "v_subrev_u32 v144, %[mc], %[amr]\n\tds_read_b32 v150, %[amr]\n\ts_mov_b64 exec, 63\n\tds_add_u32 v144, v146\n\ts_mov_b64 exec, -1\n\t"
               "s_cmp_lg_u32 %[late], 0\n\ts_waitcnt lgkmcnt(0)\n\ts_nop 1\n\ts_cbranch_scc1 1b\n\ts_branch 3b\n"
               "8:\n\tv_mov_b32 v153, v152\n\ts_nop 1\n\tv_permlane16_swap_b32 v152, v153\n\tv_max_f32 v152, v152, v153\n\tv_mov_b32 v153, v152\n\ts_nop 1\n\t"
               "v_permlane32_swap_b32 v152, v153\n\tv_max_f32 v152, v152, v153\n\tv_mul_f32 v152, 0x3d000000, v152\n\tv_rndne_f32 v152, v152\n\t"
               "v_mul_f32 v152, 0x42000000, v152\n\tv_max_f32 v150, v150, v152\n\tds_max_f32 %[amr], v150\n\t"
               S_SUB("128:129", "120:121") S_SUB("130:131", "122:123") S_SUB("132:133", "124:125") S_SUB("134:135", "126:127")
               S_E(128) S_E(129) S_E(130) S_E(131) S_E(132) S_E(133) S_E(134) S_E(135) "s_branch 4b\n"
               "9:"
               : "=&{v[0:7]}"(c.q[0]), "=&{v[8:15]}"(c.q[1]), "=&{v[16:23]}"(c.q[2]), "=&{s[52:53]}"(tq)
               : [orb] "v"(c.o_rb), [oyb] "v"(c.o_yb), [ev] "v"(c.ev), [apk] "v"(c.apk), [apm] "v"(c.apm), [api] "v"(c.api), [amr] "v"(c.amr),
                 [two] "v"(2.f), [four] "v"(4u), [inc] "v"(2u * NPK), [k0] "s"(k0), [late] "s"(__builtin_amdgcn_readfirstlane(late)), [toff] "s"(f32x2{TOFF, TOFF}),
                 [r1] "s"(0xAAAAAAAAAAAAAAAAull), [r2] "s"(0xCCCCCCCCCCCCCCCCull), [dg] "s"(0xF0000F0000F0000Full), [dg2] "s"(0xF00FFF000FF000FFull),
                 [cap] "i"(SPIN_CAP), [end] "i"(TPW - NH1), [ns] "i"(NS), [pfl] "i"(CT_OFF + C_PFL), [full] "i"(CT_OFF + C_FULL), [early] "i"(NEARLY),
                 [qi] "i"(CT_OFF + C_QI), [nq] "i"(NS - NEARLY), [mc] "i"(C_MREF - C_CONS), [slot] "i"(SLOT),
                 [thr] "i"(__builtin_bit_cast(unsigned, THR)), [eps] "i"(__builtin_bit_cast(unsigned, EPS * CC)), [nic] "i"(__builtin_bit_cast(unsigned, -1.f / CC))
               : "memory", "scc", "vcc", "s40", "s41", "s42", "s43", "s44", "s45", "s46", "s47", "s48", "s49", "s50", "s51",
                 "v24", "v25", "v26", "v27", "v28", "v29", "v30", "v31", "v32", "v33", "v34", "v35", "v36", "v37", "v38", "v39", "v40", "v41", "v42", "v43", "v44", "v45", "v46", "v47", "v48", "v49", "v50", "v51", "v52", "v53", "v54", "v55", "v56", "v57", "v58", "v59", "v60", "v61", "v62", "v63", "v64", "v65", "v66", "v67", "v68", "v69", "v70", "v71", "v72", "v73", "v74", "v75", "v76", "v77", "v78", "v79", "v80", "v81", "v82", "v83", "v84", "v85", "v86", "v87", "v88", "v89", "v90", "v91", "v92", "v93", "v94", "v95", "v96", "v97", "v98", "v99", "v100", "v101", "v102", "v103", "v104", "v105", "v106", "v107", "v108", "v109", "v110", "v111", "v112", "v113", "v114", "v115", "v116", "v117", "v118", "v119", "v120", "v121", "v122", "v123", "v124", "v125", "v126", "v127", "v128", "v129", "v130", "v131", "v132", "v133", "v134", "v135", "v136", "v137", "v138", "v139", "v140", "v141", "v142", "v143", "v144", "v145", "v146", "v147", "v148", "v149", "v150", "v151", "v152", "v153", "v154", "v155", "v156", "v157");
  c.tq = (unsigned)tq;
}

__device__ __forceinline__ void s_half(SCtx& c, int k, int h) {
  const int s = k % NSLOT, e = k % NPK;
  const int sb_ = s * SLOT;
  int yba = c.ev * sb_ + c.o_yb;
  float hc = 1.f - 0.5f * c.ev;
  unsigned xa = (unsigned)(size_t)c.L0 + sb_ + h * 6144 + c.o_rb, xb = xa ^ 64, ai = c.api + e * 256 + h * 8;
  unsigned ap = c.apk + e * 1024 + h * 8, am = c.apm + e * 128 + h * 4, af = c.ct + C_PFL + e * 4, inc = NPK;
  float two = 2.f;
  f32x4 G = {EPS * CC, EPS * CC, EPS * CC, EPS * CC};
  poll_ge_sleep(c.ct + C_CONS + e * 4, (unsigned)(NE * (k / NPK) + NS));
  asm volatile("" : "+v"(yba), "+v"(hc), "+v"(xa), "+v"(xb), "+v"(ai), "+v"(ap), "+v"(am), "+v"(af), "+v"(inc), "+v"(two), "+{v[104:107]}"(G));
  poll_ge_sleep(c.ct + C_FULL, (unsigned)(2 * k + 1));
  __builtin_amdgcn_s_setprio(1);
  bf16x8 yb[6];
#pragma unroll
  for (int j = 0; j < 6; ++j) yb[j] = *(LDS_AS(bf16x8)*)(c.L0 + ((j & 1) ? (yba ^ 64) : yba) + (j >> 1) * 128);
  f32x4 Hx = {0.f, 0.f, 0.f, 0.f}, S = Hx;
#pragma unroll
  for (int j = 0; j < 6; ++j) { Hx = mfma(yb[j], yb[j], Hx); S = mfma(yb[j], s_qf(c, j), S); }
  const float nsq = S[1] * (-1.f / CC);
  Hx = f32x4{Hx[0] * hc, Hx[0] * hc, Hx[2] * hc, Hx[2] * hc};
  S = f32x4{S[0], S[0], S[2], S[2]};
  float mref, mt;
  f32x4 rs;
  unsigned long long raise;
#define SH_M(d, a, b) "v_mfma_f32_16x16x32_bf16 v[" d "], v[" a "], " b ", v[" d "]\n\t"
#define SH_X(n, a, y) "s_waitcnt lgkmcnt(" #n ")\n\t" SH_M("100:103", a, "%[" #y "]") SH_M("104:107", a, "v[" a "]")
#define SH_D " row_mask:0xf bank_mask:0xf\n\t"
  __builtin_amdgcn_s_setprio(0);
  if (HB == 0 || (HB == 1 && k < TPW - 1)) poll_ge_sleep(c.ct + C_FULL, (unsigned)(2 * k + 2));
  if (HB == 2) { if (k < TPW - 1) poll_ge_sleep(c.ct + C_FULL, (unsigned)(2 * k + 2)); else poll_ge(c.ct + C_FULL, (unsigned)(2 * TPW)); }
  asm volatile("s_mov_b32 s42, %[cap]\n"
               "1:\n\tds_read_b32 v136, %[afl]\n\tds_read_b128 v[112:115], %[xa]\n\tds_read_b128 v[116:119], %[xb]\n\tds_read_b128 v[120:123], %[xa] offset:128\n\t"
               "ds_read_b128 v[124:127], %[xb] offset:128\n\tds_read_b128 v[128:131], %[xa] offset:256\n\tds_read_b128 v[132:135], %[xb] offset:256\n\t"
               "ds_read_b32 %[mr], %[amr]\n\ts_sub_u32 s42, s42, 1\n\ts_cselect_b32 s41, 0, %[need]\n\ts_waitcnt lgkmcnt(7)\n\t"
               "v_readfirstlane_b32 s40, v136\n\ts_cmp_lt_u32 s40, s41\n\ts_cbranch_scc1 1b\n\ts_setprio 3\n\t"
               SH_X(6, "112:115", y0) SH_X(5, "116:119", y1) SH_X(4, "120:123", y2) SH_X(3, "124:127", y3) SH_X(2, "128:131", y4) SH_X(1, "132:135", y5)
               SH_M("108:111", "112:115", "%[q0]") SH_M("108:111", "116:119", "%[q1]")
               "s_waitcnt lgkmcnt(0)\n\tv_add_f32 v137, %[thr], %[mr]\n\tds_write_b32 %[am], %[mr]\n\ts_nop 1\n\t"
               "v_mov_b32_dpp v140, v100 quad_perm:[1,1,3,3]" SH_D "v_mov_b32_dpp v141, v101 quad_perm:[1,1,3,3]" SH_D
               "v_mov_b32_dpp v142, v102 quad_perm:[1,1,3,3]" SH_D "v_mov_b32_dpp v143, v103 quad_perm:[1,1,3,3]" SH_D
               SH_M("108:111", "120:123", "%[q2]")
               "v_fmac_f32 v104, 2.0, v100\n\tv_fmac_f32_dpp v105, v101, %[two] quad_perm:[0,0,2,2]" SH_D
               "v_fmac_f32 v106, 2.0, v102\n\tv_fmac_f32_dpp v107, v103, %[two] quad_perm:[0,0,2,2]" SH_D
               "v_cndmask_b32 v138, v140, v141, %[r1]\n\tv_cndmask_b32 v139, v142, v143, %[r1]\n\t"
               SH_M("108:111", "124:127", "%[q3]")
               "v_cndmask_b32 v104, v104, v105, %[r1]\n\tv_cndmask_b32 v106, v106, v107, %[r1]\n\tv_cndmask_b32 v138, v138, v139, %[r2]\n\t"
               "v_cndmask_b32 v104, v104, v106, %[r2]\n\tv_mul_f32 v139, %[nic], v138\n\t"
               SH_M("108:111", "128:131", "%[q4]")
               "v_fmac_f32 v104, v139, v138\n\tv_max_f32 v104, %[eps], v104\n\tv_rsq_f32 v105, v104\n\t"
               SH_M("108:111", "132:135", "%[q5]")
               "v_mul_f32 v104, v104, v105\n\t"
               "v_mov_b32_dpp v105, v105 row_ror:12 row_mask:0x2 bank_mask:0xf\n\tv_cvt_pk_bf16_f32 v106, v138, v104\n\ts_nop 0\n\t"
               "v_mov_b32_dpp v105, v105 row_ror:8 row_mask:0x4 bank_mask:0xf\n\tv_lshlrev_b32 v107, 16, v106\n\tv_and_b32 v139, 0xffff0000, v106\n\t"
               "v_mov_b32_dpp v105, v105 row_ror:4 row_mask:0x8 bank_mask:0xf\n\t"
               "v_fmac_f32 v108, v140, %[nsq]\n\tv_fmac_f32 v109, v141, %[nsq]\n\tv_fmac_f32 v110, v142, %[nsq]\n\tv_fmac_f32 v111, v143, %[nsq]\n\t"
               "v_mov_b32_dpp v100, v105 row_newbcast:0" SH_D "v_mov_b32_dpp v101, v105 row_newbcast:1" SH_D
               "v_mov_b32_dpp v102, v105 row_newbcast:2" SH_D "v_mov_b32_dpp v103, v105 row_newbcast:3" SH_D
               "v_fmaak_f32 v108, v108, v100, %[toff]\n\tv_fmaak_f32 v109, v109, v101, %[toff]\n\tv_fmaak_f32 v110, v110, v102, %[toff]\n\tv_fmaak_f32 v111, v111, v103, %[toff]\n\t"
               "v_sub_f32 v140, v108, %[mr]\n\tv_sub_f32 v141, v109, %[mr]\n\tv_sub_f32 v142, v110, %[mr]\n\tv_sub_f32 v143, v111, %[mr]\n\t"
               "v_exp_f32 v140, v140\n\tv_exp_f32 v141, v141\n\tv_exp_f32 v142, v142\n\tv_exp_f32 v143, v143\n\t"
               "v_max3_f32 %[mt], v108, v109, v110\n\tv_sub_f32 v138, v138, v107\n\tv_sub_f32 v104, v104, v139\n\tv_max_f32 %[mt], %[mt], v111\n\t"
               "v_pk_mul_f32 v[140:141], v[140:141], v[100:101]\n\tv_pk_mul_f32 v[142:143], v[142:143], v[102:103]\n\t"
               "v_cmp_gt_f32 %[ra], %[mt], v137\n\tv_cvt_pk_bf16_f32 v107, v138, v104\n\t"
               "v_cvt_pk_bf16_f32 v140, v140, v141\n\tv_cvt_pk_bf16_f32 v141, v142, v143\n\t"
               "ds_write_b64 %[ap], v[140:141]\n\ts_mov_b64 exec, %[dg]\n\t"
               "ds_write_b16 %[ai], v106\n\tds_write_b16 %[ai], v107 offset:16\n\tds_write_b16_d16_hi %[ai], v106 offset:32\n\tds_write_b16_d16_hi %[ai], v107 offset:48\n\t"
               "s_cmp_eq_u64 %[ra], 0\n\ts_cselect_b64 exec, 1, 0\n\tds_add_u32 %[af], %[inc]\n\ts_mov_b64 exec, -1\n\ts_setprio 0"
               : "+{v[100:103]}"(Hx), "+{v[104:107]}"(G), "+{v[108:111]}"(S), [mr] "=&v"(mref), [mt] "=&v"(mt), [ra] "=&s"(raise)
               : [y0] "v"(yb[0]), [y1] "v"(yb[1]), [y2] "v"(yb[2]), [y3] "v"(yb[3]), [y4] "v"(yb[4]), [y5] "v"(yb[5]),
                 [q0] "v"(s_qf(c, 0)), [q1] "v"(s_qf(c, 1)), [q2] "v"(s_qf(c, 2)), [q3] "v"(s_qf(c, 3)), [q4] "v"(s_qf(c, 4)), [q5] "v"(s_qf(c, 5)),
                 [afl] "v"(c.ct + C_FULL), [xa] "v"(xa), [xb] "v"(xb), [amr] "v"(c.amr), [am] "v"(am), [ap] "v"(ap), [ai] "v"(ai),
                 [af] "v"(af), [inc] "v"(inc), [nsq] "v"(nsq), [two] "v"(two),
                 [need] "s"(__builtin_amdgcn_readfirstlane(2 * k + 2)), [r1] "s"(0xAAAAAAAAAAAAAAAAull), [r2] "s"(0xCCCCCCCCCCCCCCCCull),
                 [dg] "s"(0xF0000F0000F0000Full), [cap] "i"(SPIN_CAP), [thr] "i"(__builtin_bit_cast(unsigned, THR)), [toff] "i"(__builtin_bit_cast(unsigned, TOFF)),
                 [eps] "i"(__builtin_bit_cast(unsigned, EPS * CC)), [nic] "i"(__builtin_bit_cast(unsigned, -1.f / CC))
               : "memory", "scc", "s40", "s41", "s42", "v112", "v113", "v114", "v115", "v116", "v117", "v118", "v119", "v120", "v121", "v122", "v123", "v124", "v125",
                 "v126", "v127", "v128", "v129", "v130", "v131", "v132", "v133", "v134", "v135", "v136", "v137", "v138", "v139", "v140", "v141", "v142", "v143");
  if (__builtin_expect(raise != 0ull, 0)) {
    rs = Hx;
    mref = fmaxf(mref, 32.f * __builtin_rintf(xrow16_max(mt) * (1.f / 32.f)));
    float pp[4];
#pragma unroll
    for (int i = 0; i < 4; ++i) pp[i] = __builtin_amdgcn_exp2f(S[i] - mref) * rs[i];
    asm volatile("ds_max_f32 %6, %3\n\tds_write_b32 %2, %3\n\tds_write_b64 %0, %1\n\ts_mov_b64 exec, 1\n\tds_add_u32 %4, %5\n\ts_mov_b64 exec, -1"
                 : : "v"(ap), "v"(u32x2{cvtpk(pp[0], pp[1]), cvtpk(pp[2], pp[3])}), "v"(am), "v"(mref), "v"(af), "v"((unsigned)NPK), "v"(c.amr) : "memory");
  }
}

using u32x16 = __attribute__((ext_vector_type(16))) unsigned;
typedef __attribute__((address_space(1))) unsigned short gus;
extern "C" __global__ __launch_bounds__(768, 3) void dino_main(const void*, const void*, const void*, const void*, const void*,
                                                               const void*, const void*, void*, void*) {
  u32x16 ka; u32x2 kb; unsigned long long tent64;
  __shared__ __attribute__((aligned(16))) char lds[LDS_BYTES];
  const int wave = __builtin_amdgcn_readfirstlane((int)threadIdx.x >> 6);
  const bool helper = blockIdx.x < 8;
  lds_ptr L0 = (lds_ptr)lds;
  const unsigned lbase = (unsigned)(size_t)L0;
  const unsigned ct = lbase + CT_OFF;
#define KARG(v, i) ((char*)(((unsigned long long)v[2 * (i) + 1] << 32) | v[2 * (i)]))
#define q KARG(ka, 0)
#define f0 KARG(ka, 1)
#define f1 KARG(ka, 2)
#define wq KARG(ka, 3)
#define wk KARG(ka, 4)
#define wv KARG(ka, 5)
#define wo KARG(ka, 6)
#define box KARG(ka, 7)
#define tent ((unsigned)tent64)
#define LANE_VARS int lane; asm volatile("v_mbcnt_lo_u32_b32 %0, -1, 0\n\tv_mbcnt_hi_u32_b32 %0, -1, %0" : "=v"(lane)); \
  const int r = lane & 15, g = lane >> 4; (void)r; (void)g;
#define DOFF_VARS unsigned doff[3];   \
  _Pragma("unroll") for (int i = 0; i < 3; ++i) doff[i] = (unsigned)(lane ^ ((swz_tbl(i) >> ((lane >> 3) * 4)) & 7)) << 4;
#define BARRIER() do { asm volatile("s_waitcnt lgkmcnt(0)" ::: "memory"); __builtin_amdgcn_s_barrier(); asm volatile("" ::: "memory"); } while (0)

  {
    unsigned doff[3];
    static_assert(swz_tbl(0) == swz_tbl(3) && swz_tbl(1) == swz_tbl(4) && swz_tbl(2) == swz_tbl(5), "");
    unsigned l16, db;
#define PD(d, b, off, pol) "global_load_lds_dwordx4 %[" #d "], s[" #b "] offset:" #off pol "\n\t"
    asm volatile("s_load_dwordx16 s[64:79], %[kp], 0x0\n\ts_load_dwordx2 s[80:81], %[kp], 0x40\n\ts_memrealtime s[82:83]\n\t"
                 "v_mbcnt_lo_u32_b32 %[l16], -1, 0\n\tv_mbcnt_hi_u32_b32 %[l16], -1, %[l16]\n\t"
                 "s_lshr_b32 s84, %[b], 3\n\ts_and_b32 s85, %[b], 7\n\t"
                 "v_lshrrev_b32 %[db], 1, %[l16]\n\tv_and_b32 %[db], -4, %[db]\n\t"
                 "s_mov_b32 s86, %[t0]\n\ts_mov_b32 s87, %[t1]\n\ts_mov_b32 s88, %[t2]\n\t"
                 "v_bfe_u32 %[d0], s86, %[db], 3\n\tv_bfe_u32 %[d1], s87, %[db], 3\n\tv_bfe_u32 %[d2], s88, %[db], 3\n\t"
                 "v_xor_b32 %[d0], %[d0], %[l16]\n\tv_xor_b32 %[d1], %[d1], %[l16]\n\tv_xor_b32 %[d2], %[d2], %[l16]\n\t"
                 "v_lshlrev_b32 %[d0], 4, %[d0]\n\tv_lshlrev_b32 %[d1], 4, %[d1]\n\tv_lshlrev_b32 %[d2], 4, %[d2]\n\t"
                 "v_lshlrev_b32 %[l16], 4, %[l16]\n\t"
                 "s_cmp_eq_u32 s84, 0\n\ts_cbranch_scc1 4f\n\t"
                 "s_cmp_lg_u32 %[w], 0\n\ts_cbranch_scc1 7f\n\t"
                 "s_setprio 3\n\tv_lshlrev_b32 %[db], 3, %[l16]\n\ts_mov_b32 m0, %[junk]\n\ts_lshl_b32 s89, s84, 13\n\t"
                 "s_cmp_gt_u32 s84, 3\n\ts_cbranch_scc1 2f\n\t"
                 "s_getpc_b64 s[90:91]\n1:\n\ts_sub_u32 s86, s89, 8192\n\ts_min_u32 s86, s86, dino_end-1b-8192\n\t"
                 "s_add_u32 s90, s90, s86\n\ts_addc_u32 s91, s91, 0\n\tglobal_load_lds_dword %[db], s[90:91]\n"
                 "2:\n\ts_mul_i32 s86, s85, 98304\n\ts_mul_i32 s87, s84, 3072\n\ts_sub_u32 s87, s87, 3072\n\t"
                 "s_add_u32 s88, s86, s87\n\ts_lshl1_add_u32 s86, s86, s87\n\ts_lshl_b32 s86, s86, 1\n\t"
                 "s_mul_i32 s87, s85, 12288\n\ts_lshr_b32 s90, s89, 1\n\ts_add_u32 s87, s87, s90\n\ts_sub_u32 s87, s87, 13 * 4096\n\t"
                 "s_sub_u32 s89, s89, 4 * 8192\n\ts_cmp_gt_u32 s85, 5\n\ts_cselect_b32 s89, -1, s89\n\ts_cmp_ge_u32 s89, 11 * 8192\n\t"
                 "s_waitcnt lgkmcnt(0)\n\ts_cbranch_scc1 3f\n\t"
                 "s_cmp_lt_u32 s89, 9 * 8192\n\ts_cselect_b64 s[90:91], s[70:71], s[72:73]\n\ts_cselect_b32 s89, s89, s87\n\t"
                 "s_add_u32 s90, s90, s89\n\ts_addc_u32 s91, s91, 0\n\tglobal_load_lds_dword %[db], s[90:91]\n"
                 "3:\n\ts_mov_b32 m0, 0x3000\n\t"
                 "s_add_u32 s90, s68, s88\n\ts_addc_u32 s91, s69, 0\n\t"
                 "s_add_u32 s92, s66, s86\n\ts_addc_u32 s93, s67, 0\n\t"
                 PD(d0, 90:91, 0, " nt") PD(d1, 90:91, 1024, " nt") PD(d2, 90:91, 2048, " nt")
                 "s_mov_b32 m0, 0\n\ts_nop 0\n\t"
                 PD(d0, 92:93, 0, " nt") PD(d1, 92:93, 1024, " nt")
                 "s_sub_u32 s89, s84, 15\n\ts_cmp_gt_u32 s85, 5\n\ts_cselect_b32 s89, -1, s89\n\ts_cmp_ge_u32 s89, 11\n\ts_cbranch_scc1 9f\n\t"
                 "s_mov_b32 m0, %[junk]\n\ts_sub_u32 s87, s87, 11 * 4096\n\ts_lshl_b32 s89, s89, 13\n\ts_cmp_lt_u32 s89, 9 * 8192\n\t"
                 "s_cselect_b64 s[90:91], s[76:77], s[74:75]\n\ts_cselect_b32 s89, s89, s87\n\t"
                 "s_add_u32 s90, s90, s89\n\ts_addc_u32 s91, s91, 0\n\tglobal_load_lds_dword %[db], s[90:91]\n\t"
                 "s_branch 9f\n"
                 "7:\n\ts_cmp_eq_u32 %[w], 1\n\ts_cbranch_scc1 8f\n\ts_barrier\n\ts_branch 8f\n"
                 "4:\n\t"
                 "s_cmp_gt_u32 %[b], 5\n\ts_cbranch_scc1 8f\n\ts_cmp_gt_u32 %[w], 9\n\ts_cbranch_scc1 8f\n\t"
                 "s_mov_b32 m0, %[img]\n\ts_mul_i32 s86, %[w], 9216\n\ts_and_b32 s88, %[w], 1\n\ts_lshl1_add_u32 s89, %[b], s88\n\t"
                 "s_mul_i32 s88, s88, 6144\n\ts_mul_i32 s89, s89, 6144\n\ts_add_u32 s88, s88, %[hr]\n\t"
                 "s_cmp_lg_u32 %[w], 0\n\ts_waitcnt lgkmcnt(0)\n\ts_cbranch_scc1 5f\n\t"
                 PD(d0, 64:65, 0, "") PD(d1, 64:65, 1024, "") PD(d2, 64:65, 2048, "") PD(d0, 64:65, 3072, "")
                 "s_mov_b32 m0, %[img] + 4096\n\ts_add_u32 s90, s64, 0x1000\n\ts_addc_u32 s91, s65, 0\n\t"
                 PD(d1, 90:91, 0, "") PD(d2, 90:91, 1024, "")
                 "\n5:\n\ts_cmp_gt_u32 %[w], 7\n\ts_cselect_b64 s[92:93], s[72:73], s[70:71]\n\ts_cselect_b32 m0, s88, s86\n\ts_cselect_b32 s86, s89, s86\n\t"
                 "s_cselect_b32 s87, 2, 3\n\ts_add_u32 s92, s92, s86\n\ts_addc_u32 s93, s93, 0\n"
                 "6:\n\ts_sub_u32 s87, s87, 1\n\t"
                 PD(d0, 92:93, 0, "") PD(d1, 92:93, 1024, "") PD(d2, 92:93, 2048, "")
                 "s_add_u32 m0, m0, 3072\n\ts_add_u32 s92, s92, 3072\n\ts_addc_u32 s93, s93, 0\n\ts_cmp_lg_u32 s87, 0\n\ts_cbranch_scc1 6b"
                 "\n8:\n\ts_waitcnt lgkmcnt(0)\n9:"
                 : "={s[64:79]}"(ka), "={s[80:81]}"(kb), "={s[82:83]}"(tent64),
                   [d0] "=&v"(doff[0]), [d1] "=&v"(doff[1]), [d2] "=&v"(doff[2]), [l16] "=&v"(l16), [db] "=&v"(db)
                 : [kp] "s"(__builtin_amdgcn_kernarg_segment_ptr()), [b] "s"(blockIdx.x), [w] "s"(wave),
                   [t0] "i"(swz_tbl(0)), [t1] "i"(swz_tbl(1)), [t2] "i"(swz_tbl(2)), [junk] "i"(JUNK), [img] "i"(IMG_A), [hr] "i"(H_R)
                 : "memory", "scc", "vcc", "s84", "s85", "s86", "s87", "s88", "s89", "s90", "s91", "s92", "s93", "s94", "s95", "s96", "s97", "s98", "s99");
  }
  const unsigned t0 = (unsigned)__builtin_amdgcn_s_memrealtime() - T0BACK;

  if (__builtin_expect(blockIdx.x < 6, 0)) {
    LANE_VARS
    DOFF_VARS
    const int j = blockIdx.x;
    TNOW(ta0);
    TNOW(ta1);
    const int qq = (lane >> 2) & 3, p4 = lane & 3, e4 = wave & 3;
    int ra = H_R + r * ROWB + ((g ^ swz(r)) << 4);
    bf16x8 a[6], b[6], w[18];
    auto rows = [&](int t, bf16x8* d) {
#pragma unroll
      for (int kk = 0; kk < 6; ++kk) d[kk] = *(LDS_AS(bf16x8)*)(L0 + t * 6144 + ((kk & 1) ? (ra ^ 64) : ra) + (kk >> 1) * 128);
    };
#define TSET(v)
    int o[3];
#pragma unroll
    for (int t = 0; t < 3; ++t) o[t] = tr_off(4 * g + qq, 4 * (3 * e4 + t) + p4);
    unsigned qo = lbase + H_Q + 3 * e4 * 512 + ((g >> 1) * 16 + r) * 16 + (g & 1) * 8, qa = lbase + IMG_A + tr_off(r, 8 * wave + g);
    if (wave == 11) {
      unsigned d;
      asm volatile("s_load_dword %0, %1, 0x40\n\ts_barrier\n\ts_barrier\n\ts_barrier\n\ts_waitcnt lgkmcnt(0)" : "=&s"(d) : "s"(box + EPOCH_OFF) : "memory");
    } else
    asm volatile("s_waitcnt vmcnt(9)\n\ts_barrier" : PIN3(o, 0), "+v"(qo), "+v"(qa) :: "memory");
    if (wave < 6 || wave == 10) {
      rows((IMG_A - H_R) / 6144, a);
      const u32x2 m0 = *(LDS_AS(u32x2)*)(size_t)qa, m1 = *(LDS_AS(u32x2)*)(size_t)(qa ^ 32);
      static_assert((IMG_A - H_R) % 6144 == 0, "");
      const bf16x8 ones = __builtin_bit_cast(bf16x8, u32x4{0x3f803f80u, 0x3f803f80u, 0x3f803f80u, 0x3f803f80u});
      f32x4 sm = {0.f, 0.f, 0.f, 0.f};
#pragma unroll
      for (int i = 0; i < 6; ++i) sm = mfma(ones, a[i], sm);
      const float mean = sm[0] * (1.f / CC);
      if (__builtin_expect(wave < 6, 1)) {
        img_st(L0, H_C, wave, lane, __builtin_bit_cast(bf16x8, u32x4{cvtpk(bflo(m0[0]) - mean, bfhi(m0[0]) - mean), cvtpk(bflo(m0[1]) - mean, bfhi(m0[1]) - mean),
                                                                   cvtpk(bflo(m1[0]) - mean, bfhi(m1[0]) - mean), cvtpk(bflo(m1[1]) - mean, bfhi(m1[1]) - mean)}));
      } else {
        asm volatile("s_barrier" : PIN3(a, 0), PIN3(a, 3) :: "memory");
        f32x2 v2 = {0.f, 0.f}, v3 = v2;
#pragma unroll
        for (int i = 0; i < 12; ++i) {
          const u32x4 d = __builtin_bit_cast(u32x4, a[i >> 1]);
          const unsigned d0 = d[2 * (i & 1)], d1 = d[2 * (i & 1) + 1];
          const f32x2 x0 = f32x2{bflo(d0), bfhi(d0)} - mean, x1 = f32x2{bflo(d1), bfhi(d1)} - mean;
          v2 += x0 * x0; v3 += x1 * x1;
        }
        v2 += v3;
        *(LDS_AS(float)*)(L0 + CT_OFF + r * 4) = LOG2E * __builtin_amdgcn_rsqf(xrow16_sum(v2[0] + v2[1]) * (1.f / CC) + EPS);
      }
      TSET(ta2);
    }
    if (wave < 4) {
      wait_vm<0>();
      TSET(ta3);
      BARRIER();
      TSET(th1);
#pragma unroll
      for (int kk = 0; kk < 6; ++kk) {
        b[kk] = img_ld(L0, H_C, kk, lane);
        w[kk] = mk8(tr_read(L0 + kk * 12288 + o[0]), tr_read(L0 + kk * 12288 + 6144 + o[0]));
        __builtin_amdgcn_sched_barrier(0);
      }
#pragma unroll
      for (int i = 6; i < 18; ++i) {
        w[i] = mk8(tr_read(L0 + (i % 6) * 12288 + o[i / 6]), tr_read(L0 + (i % 6) * 12288 + 6144 + o[i / 6]));
        __builtin_amdgcn_sched_barrier(0);
      }
#pragma unroll
      for (int t = 0; t < 3; ++t) {
        f32x4 D = {0.f, 0.f, 0.f, 0.f};
#pragma unroll
        for (int kk = 0; kk < 6; ++kk) D = mfma(w[6 * t + kk], b[kk], D);
        *(LDS_AS(u32x2)*)(size_t)(qo + t * 512) = u32x2{cvtpk(D[0], D[1]), cvtpk(D[2], D[3])};
      }
      TSET(ta4);
      BARRIER();
    } else if (wave == 8 || wave == 9) {
      unsigned so = lane * 16 + (2 * j + (wave & 1)) * 1024;
      unsigned epoch;
      asm volatile("global_load_dword %0, %1, %2 sc1\n\ts_barrier\n\ts_waitcnt vmcnt(0)" : "=v"(epoch) : "v"(EPOCH_OFF), "s"(box) : "memory");
      TSET(ta3);
      rows(wave & 1, a);
      unsigned ta = line_tag(epoch, 8), tb = line_tag(epoch, 9);
      asm volatile("" : PIN3(a, 0), PIN3(a, 3), "+v"(ta), "+v"(tb), "+v"(so));
      TSET(ta4);
      BARRIER();
      TSET(ta5);
#pragma unroll
      for (int kk = 0; kk < 6; ++kk) b[kk] = img_ld(L0, H_Q, kk, lane);
      const float sc = *(LDS_AS(float)*)(L0 + CT_OFF + r * 4);
      __builtin_amdgcn_sched_barrier(0);
      f32x4 D = {0.f, 0.f, 0.f, 0.f};
#pragma unroll
      for (int kk = 0; kk < 6; ++kk) D = mfma(a[kk], b[kk], D);
      D *= sc;
      const unsigned p0 = cvtpk(D[0], D[1]), p1 = cvtpk(D[2], D[3]);
      asm volatile("global_store_dwordx4 %0, %1, %2 sc1\n\ts_nop 1" : : "v"(so), "v"(u32x4{p0, p1, p0 ^ p1 ^ ta, tb}), "s"(box + QK_OFF) : "memory");
      TSET(th2);
    } else if (wave != 11) {
      if (wave != 10) asm volatile("s_waitcnt vmcnt(0) lgkmcnt(0)\n\ts_barrier" ::: "memory");
      BARRIER();
    }
    BARRIER();
    issue_rows16(wo + wave * 6144, lbase + wave * 6144, doff);
    if (wave < 2) issue_rows16(wv + (2 * j + wave) * 6144, lbase + H_R + wave * 6144, doff);
    const unsigned epoch = epoch_load(box);
    BARRIER();
    TSET(ta3);
    {
      const int o0 = tr_off(8 * g + qq, 4 * wave + p4), o1 = tr_off(8 * g + 4 + qq, 4 * wave + p4);
#pragma unroll
      for (int kk = 0; kk < 6; ++kk) b[kk] = mk8(tr_read(L0 + kk * 12288 + o0), tr_read(L0 + kk * 12288 + o1));
      f32x4 D[2];
#pragma unroll
      for (int h = 0; h < 2; ++h) {
        rows(h, a);
        D[h] = f32x4{0.f, 0.f, 0.f, 0.f};
#pragma unroll
        for (int kk = 0; kk < 6; ++kk) D[h] = mfma(a[kk], b[kk], D[h]);
      }
      const int e = wave / 3;
      asm volatile("global_store_dwordx4 %0, %1, %2 sc1\n\ts_waitcnt vmcnt(0)"
                   : : "v"(lane * 16 + (e * 15 + 3 * j + wave) * 1024), "v"(pack8(D[0] * SQRT_C, D[1] * SQRT_C)), "s"(box + W2_OFF) : "memory");
    }
    BARRIER();
    if (wave == 11)
      asm volatile("s_mov_b64 exec, 1\n\tglobal_store_dwordx2 %0, %1, %2 sc1\n\ts_mov_b64 exec, -1\n\ts_nop 1"
                   : : "v"(j * 128), "v"(u32x2{line_tag(epoch, 10), line_tag(epoch, 11)}), "s"(box + W2F_OFF) : "memory");
    TPUT(60 + wave * 5, ta0); TPUT(61 + wave * 5, ta1); TPUT(62 + wave * 5, ta2); TPUT(63 + wave * 5, ta3); TPUT(64 + wave * 5, ta4);
  }

  int rslot = -1;
  if (__builtin_expect(wave == 0, 1)) {
    LANE_VARS
    DOFF_VARS
    if (helper) BARRIER();
    const unsigned afull = ct + C_FULL + r * 4;
    unsigned ts = 0;
    {
      const int ti = helper ? 1023 : (lane * 31 + (int)(blockIdx.x >> 3) - 1) & 1023;
      const int sp = ti >> 5, yy = sp * 8 + ((blockIdx.x + sp) & 7), xx = (ti + 5 * sp) & 31;
      unsigned tmp, pub = helper ? 2 * (TPW - 1) : 0, pre = 0, afr = ct + C_FREE + (helper ? ((TPW - 1) % NSLOT) * 4 : 0), e0, e1, e2, g0, g1, g2;
      asm volatile("s_mov_b32 s44, %[due0]\n\ts_mov_b32 s46, %[y0]\n\ts_mov_b32 s47, %[cap]\n\ts_mov_b32 s48, %[i0]\n\ts_mov_b32 s49, %[s0]\n\t"
                   "s_mov_b32 s51, 0\n\ts_mov_b32 s45, 0\n\ts_movk_i32 s39, %[p3s]\n"
                   "1:\n\tv_readlane_b32 s53, %[ob], s48\n\tv_readlane_b32 s52, %[oa], s48\n\ts_add_u32 m0, s49, 0x3000\n\tv_cmp_gt_u32 vcc, s51, %[pre]\n\t"
                   "v_add_u32 %[g0], s53, %[d0]\n\tv_add_u32 %[g1], s53, %[d1]\n\tv_add_u32 %[g2], s53, %[d2]\n\t"
                   "v_add_u32 %[e0], s52, %[d0]\n\tv_add_u32 %[e1], s52, %[d1]\n\tv_add_u32 %[e2], s52, %[d2]\n\t"
                   "s_cbranch_vccnz 5f\n\t"
                   "s_cmp_eq_u32 s48, 0\n\ts_cbranch_scc1 2f\n\ts_cmp_eq_u32 s48, 2\n\ts_cbranch_scc1 8f\n"
                   LD_GATE(1, "")
                   LD1(g0, 68:69, 0) LD1(g1, 68:69, 1024) LD1(g2, 68:69, 2048) "s_mov_b32 m0, s49\n\ts_nop 0\n\t" LD1(e0, 66:67, 0) LD1(e1, 66:67, 1024)
                   LD_NEXT
                   "\n2:\n\tv_add_u32 %[afr], 4, %[afr]\n\ts_cmp_eq_u32 s49, %[lasts]\n\ts_cbranch_scc1 7f\n"
                   "3:\n\tv_mov_b32 %[pre], 0\n\ts_add_u32 m0, s49, 0x800\n\tds_read_b32 %[pre], %[afr] ; NOTRACK\n"
                   LD_GATE(2, "")
                   LD1(e2, 56:57, 0) LD1(e0, 56:57, 1024) LD1(e1, 56:57, 2048) LD1(e2, 56:57, 3072) "s_add_u32 m0, s49, 0x1800\n\ts_nop 0\n\t" LD1(e0, 58:59, 0)
                   LD_NEXT
                   "\n29:\n\ts_add_u32 m0, s49, 0x1800\n\ts_add_u32 s48, s48, 1\n"
                   LD_GATE(3, "s_cmp_gt_u32 s48, %[tpw] - 3\n\ts_cbranch_scc1 38f\n39:\n\t")
                   LD1(e1, 58:59, 1024) LD1(e2, 58:59, 2048) LD1(e0, 58:59, 3072) "s_add_u32 m0, s49, 0x2800\n\ts_add_u32 s49, s49, %[slot]\n\t" LD1(e1, 60:61, 0) LD1(e2, 60:61, 1024)
                   LD_NEXT
                   "s_cmp_eq_u32 s49, %[ring]\n\ts_cselect_b32 s49, 0, s49\n\ts_cmp_gt_i32 s46, 30\n\ts_cbranch_scc1 6f\n"
                   "4:\n\ts_cmp_eq_u32 s48, %[pk]\n\ts_cmovk_i32 s39, %[p3]\n\ts_cmp_lg_u32 s48, %[tpw]\n\ts_cbranch_scc1 1b\n\ts_branch 9f\n"
                   LD_PUB(1, 0, 15, 30) LD_PUB(2, 5, 20, 35) LD_PUB(3, 10, 25, 40)
                   "38:\n\ts_cmp_eq_u32 s46, 10\n\ts_cselect_b32 s43, 23, 0\n\ts_cmp_lt_u32 s42, s43\n\ts_cbranch_scc0 39b\n\tv_add_u32 %[tmp], 1, %[pub]\n\ts_waitcnt vmcnt(22)\n\tds_write_b32 %[af], %[tmp] ; NOTRACK\n\ts_branch 39b\n"
                   "5:\n\ts_movk_i32 s40, 0x100\n"
                   "40:\n\tds_read_b32 %[pre], %[afr]\n\t" LD_VM
                   "s_sub_u32 s40, s40, 1\n\ts_cselect_b32 s42, 0, s42\n\ts_cselect_b32 s40, 0, s40\n\t"
                   "s_cmp_le_i32 s42, s46\n\ts_cbranch_scc1 42f\n"
                   "41:\n\ts_sub_i32 s47, s47, 1\n\ts_cmp_lt_i32 s47, 0\n\ts_waitcnt lgkmcnt(0)\n\ts_cbranch_scc1 10b\n\t"
                   "v_cmp_gt_u32 vcc, s51, %[pre]\n\ts_cbranch_vccnz 40b\n\ts_branch 10b\n"
                   LD_PUB0(4, 0, 15, 30)
                   "6:\n\ts_waitcnt vmcnt(45)\n\tv_add_u32 %[pub], 2, %[pub]\n\tds_write_b32 %[af], %[pub]\n\ts_sub_u32 s46, s46, 15\n\ts_branch 4b\n"
                   "7:\n\ts_waitcnt lgkmcnt(0)\n\ts_add_u32 s51, s51, 8\n\tv_subrev_u32 %[afr], %[frb], %[afr]\n\ts_branch 3b\n"
                   "8:\n\ts_barrier\n\ts_add_u32 s46, s46, 64\n\ts_branch 10b\n"
                   "9:\n\ts_cmp_lt_i32 s46, 30\n\ts_cbranch_scc1 91f\n\ts_waitcnt vmcnt(30)\n\tv_add_u32 %[pub], 2, %[pub]\n\tds_write_b32 %[af], %[pub]\n"
                   "91:\n\ts_cmp_lt_i32 s46, 15\n\ts_cbranch_scc1 92f\n\tv_add_u32 %[tmp], 1, %[pub]\n\ts_waitcnt vmcnt(27)\n\tds_write_b32 %[af], %[tmp]\n"
                   "92:\n\ts_waitcnt lgkmcnt(0)"
                   : [pub] "+v"(pub), [pre] "+v"(pre), [afr] "+v"(afr), [ts] "+v"(ts), [tmp] "=&v"(tmp),
                     [e0] "=&v"(e0), [e1] "=&v"(e1), [e2] "=&v"(e2), [g0] "=&v"(g0), [g1] "=&v"(g1), [g2] "=&v"(g2)
                   : "{s[64:79]}"(ka), "{s[56:57]}"(f0 + 0x800), "{s[58:59]}"(f0 + 0x30000), "{s[60:61]}"(f0 + 0x31000), [t0] "s"(t0), [af] "v"(afull),
                     [d0] "v"(doff[0]), [d1] "v"(doff[1]), [d2] "v"(doff[2]), [oa] "v"(yy * 393216 + xx * 6144), [ob] "v"(yy * 98304 + xx * 3072),
                     [due0] "s"(helper ? HDUE * (PACE16 / 3) : PRE5 * (PACES / 3)), [y0] "s"(helper ? -15 : -79 + 5 * PRE5), [i0] "s"(helper ? TPW - 1 : 0),
                     [s0] "s"(helper ? ((TPW - 1) % NSLOT) * SLOT : 0),
                     [cap] "i"(SPIN_CAP), [win] "i"(PWIN), [ahi] "s"(((blockIdx.x >> 3) & 3) == 1 ? AHIR : AHI), [adl] "i"(ADL), [amg] "i"(AMG), [p3] "i"(PACE16 / 3), [p3s] "i"(PACES / 3), [pk] "i"(PK1), [slot] "i"(SLOT),
                     [ring] "i"(NSLOT * SLOT), [frb] "i"(NSLOT * 4), [lasts] "i"((NSLOT - 1) * SLOT), [tpw] "i"(TPW)
                   : "memory", "scc", "vcc", "s39", "s40", "s41", "s42", "s43", "s44", "s45", "s46", "s47", "s48", "s49", "s51", "s52", "s53", "s62", "s63");
    }
    lds_st32(ct + C_ISS, 1u);
#define CLR(b) "global_store_dwordx4 %0, %1, %" #b " sc1\n\t"
    auto clear = [&]() {
      asm volatile(CLR(2) CLR(3) CLR(4) CLR(5) CLR(6) CLR(7) CLR(8) CLR(9) "s_nop 1"
                 : : "v"(((lane >> 3) * NWG + blockIdx.x) * 128 + (lane & 7) * 16), "v"(u32x4{0u, 0u, 0u, 0u}), "s"(box), "s"(box + 1 * 262144), "s"(box + 2 * 262144),
                     "s"(box + 3 * 262144), "s"(box + 4 * 262144), "s"(box + 5 * 262144), "s"(box + 6 * 262144), "s"(box + 7 * 262144) : "memory");
    };
    if (PRECLR == 1) clear();
    wait_vm<PRECLR == 1 ? 23 : 15>();
    lds_st32(afull, (unsigned)(2 * TPW - 2));
    if (PRECLR == 3) clear();
    wait_vm<(PRECLR == 1 || PRECLR == 3) ? 20 : 12>();
    lds_st32(afull, (unsigned)(2 * TPW - 1));
    wait_vm<(PRECLR == 1 || PRECLR == 3) ? 8 : 0>();
    lds_st32(afull, ~0u);
    TNOW(tl);
    if (PRECLR == 2) clear();
    if (PRECLR == 4) { unsigned d; asm volatile("global_load_dword %0, %1, %2 sc1\n\ts_waitcnt vmcnt(0)" : "=v"(d) : "v"(blockIdx.x * 128), "s"(box) : "memory"); }
    __builtin_amdgcn_s_setprio(0);
    wait_vm<0>();
    TNOW(tlx);
    if (PRECLR != 5) lds_add(ct + C_X1, 1u);
    rslot = 7;
  } else {
    if (wave == 1) {
      LANE_VARS
    {
      const unsigned o = lane * 8;
      unsigned z = (o >= C_MREF && o < C_MREF + 64) ? 0xff800000u : 0u;
      if (helper && o >= C_CONS && o < C_MREF) z = NE * ((TPW - 1) / NPK);
      u32x2 zz = {z, z};
      if (o >= C_PFL && o < C_CONS) zz = u32x2{(o - C_PFL) / 2 + 2, (o - C_PFL) / 2 + 4} + (helper ? 2u * NPK * ((TPW - 1) / NPK) : 0u);
      if (o == C_EP) zz[1] = t0 + T0BACK - tent < QKFAST ? QKWAIT2 : QKWAIT;
      *(LDS_AS(u32x2)*)(L0 + CT_OFF + o) = zz;
      *(LDS_AS(u32x2)*)(L0 + ONES_OFF + (lane < 48 ? o : 0)) = u32x2{0x3f803f80u, 0x3f803f80u};
    }
    }
    if (helper || wave == 1) BARRIER();
    if (wave <= NE) {
      LANE_VARS
      const int ej = wave - 1;
      const int qq = (lane >> 2) & 3, p4 = lane & 3;
      const unsigned apk = lbase + IMG_A + lane * 16;
      const unsigned epoch = epoch_load(box);
      __builtin_amdgcn_s_setprio(2);
      if (helper) { while ((unsigned)__builtin_amdgcn_s_memrealtime() - tent < RELAYT) __builtin_amdgcn_s_sleep(8); }
      unsigned long long tpv, tp7, tx1;
      {
        static_assert((TPW & 1) == 1 && C_CONS == C_PFL + 64 && C_FREE == C_FULL + 64, "");
        const int k0 = helper ? TPW - 1 : 0;
        unsigned need = 2 * k0 + 2;
        asm volatile("s_mov_b32 s46, 0\n\ts_mov_b64 exec, 0\n\ts_nop 3\n\ts_branch 99f\n"
                     "2:\n\ts_mov_b64 exec, -1\n\ts_nop 7\n\t" PV_Z4(72, 73, 74, 75) PV_Z4(76, 77, 78, 79) PV_Z4(80, 81, 82, 83) PV_Z4(84, 85, 86, 87)
                     "v_mov_b32 v88, 0xff800000\n\t"
                     "s_mov_b32 s46, %[k0]\n\ts_mov_b32 s45, %[s0]\n\ts_mov_b32 s44, %[e0]\n\ts_mov_b32 s47, %[cap]\n\t"
                     "s_cmp_lg_u32 s46, 0\n\ts_cbranch_scc1 32f\n"
                     "3:\n\tv_mov_b32 v148, %[ca]\n\t" PV_WAITF(4, 148) PV_FETCH_A
                     "s_mov_b64 exec, 2\n\tds_add_u32 v148, %[one] offset:64\n\ts_mov_b64 exec, -2\n\t"
                     "v_add_u32 %[need], 2, %[need]\n\tv_add_u32 v148, 4, v148\n\ts_mov_b64 exec, -1\n\t"
                     PV_WAITF(5, 148)
                     "v_mov_b32 v149, v148\n\ts_mov_b64 exec, -2\n\tv_add_u32 %[need], 2, %[need]\n\tv_add_u32 v148, 4, v148\n\t"
                     "s_mov_b64 exec, 1\n\tv_add_u32 %[need], %[npk2], %[need]\n\ts_mov_b64 exec, -1\n\t"
                     PV_REQ("126:129", "130:133", "134:135", 136, 148) PV_FETCH_B
                     "s_mov_b64 exec, 2\n\tds_add_u32 v149, %[one] offset:64\n\ts_mov_b64 exec, -1\n"
                     "6:\n\t"
                     PV_STEP(1, "126:129", "130:133", "134:135", 134, 135, 136, 148, "138:141", "142:145", "146:147", 137, 149,
                             "92:95", "98:101", "104:107", "90:93", "96:99", "102:105",
                             PV_MOV(92, 90) PV_MOV(93, 91), PV_MOV(98, 96) PV_MOV(99, 97), PV_MOV(104, 102) PV_MOV(105, 103),
                             "s_cmp_eq_u32 s46, %[last]\n\ts_cbranch_scc1 99f\n\t")
                     "s_cbranch_vccnz 7f\n"
                     "16:\n\t" PV_FETCH_A PV_REL(148)
                     "s_lshl1_add_u32 s54, s46, -2\n\ts_lshr_b64 s[54:55], %[sch], s54\n\ts_and_b32 s54, s54, 15\n\ts_cbranch_scc1 31f\n"
                     "30:\n\t"
                     PV_STEP(2, "138:141", "142:145", "146:147", 146, 147, 137, 149, "126:129", "130:133", "134:135", 136, 148,
                             "110:113", "116:119", "122:125", "108:111", "114:117", "120:123",
                             PV_MOV(110, 108) PV_MOV(111, 109), PV_MOV(116, 114) PV_MOV(117, 115), PV_MOV(122, 120) PV_MOV(123, 121),
                             "s_cmp_eq_u32 s46, %[last] - 1\n\ts_cbranch_scc1 98f\n\t")
                     "s_cbranch_vccnz 8f\n"
                     "26:\n\t" PV_FETCH_B PV_REL(149)
                     "s_branch 6b\n"
                     PV_WAITF(7, 148) "s_branch 16b\n"
                     PV_WAITF(8, 149) "s_branch 26b\n"
                     "98:\n\t" PV_MF("76:79", "114:117", "138:141") PV_MF("80:83", "120:123", "138:141")
                     "v_add_u32 %[need], 2, %[need]\n\ts_add_u32 s46, s46, 1\n\ts_branch 13f\n"
                     PV_SLOW(1, "126:129", "130:133", "134:135", 134, 135, 136, 148, 126, 127, 128, 129)
                     PV_SLOW(2, "138:141", "142:145", "146:147", 146, 147, 137, 149, 138, 139, 140, 141)
                     "31:\n\ts_cmp_lg_u32 s54, 8\n\ts_cbranch_scc1 34f\n"
                     "32:\n\tv_mbcnt_lo_u32_b32 v152, 31, 0\n\tv_lshlrev_b32 v152, 7, v152\n\ts_mov_b32 s55, %[gcap]\n"
                     "33:\n\tglobal_load_dwordx2 v[150:151], v152, %[fl] sc1\n\ts_sub_u32 s55, s55, 1\n\ts_cselect_b32 s56, 0, -1\n\ts_waitcnt vmcnt(0) lgkmcnt(0)\n\t"
                     "v_cmp_ne_u64 s[48:49], %[tag], v[150:151]\n\ts_sleep 2\n\ts_and_b32 s48, s48, s56\n\ts_cbranch_scc1 33b\n\t"
                     "s_mov_b32 s57, 0\n\tv_mov_b32 v150, %[apk]\n\ts_cmp_lg_u32 s46, %[last]\n\ts_cbranch_scc1 30b\n\ts_mov_b32 s54, 7\n\ts_branch 38f\n"
                     "34:\n\ts_cmp_lg_u32 s46, %[last] - 3\n\ts_cbranch_scc1 36f\n\tv_mov_b32 v151, %[iss]\n"
                     "35:\n\tds_read_b32 v150, v151\n\ts_sub_i32 s47, s47, 1\n\ts_cmp_lt_i32 s47, 0\n\ts_cselect_b32 s55, 0, 1\n\ts_waitcnt lgkmcnt(0)\n\t"
                     "v_readfirstlane_b32 s56, v150\n\ts_cmp_lt_u32 s56, s55\n\ts_cbranch_scc1 35b\n"
                     "36:\n\ts_mul_i32 s55, s57, 3072\n\tv_add_u32 v150, s55, %[apk]\n\ts_getpc_b64 s[48:49]\n"
                     "37:\n\ts_mul_i32 s55, s57, 40\n\ts_add_u32 s55, s55, 38f-37b\n\ts_add_u32 s48, s48, s55\n\ts_addc_u32 s49, s49, 0\n\t"
                     "s_add_u32 s57, s57, s54\n\ts_sub_u32 s54, s54, 1\n\ts_setpc_b64 s[48:49]\n"
                     "38:\n\t" PV_W2(0, 3, 4, 7, 8, 11) PV_W2(12, 15, 16, 19, 20, 23) PV_W2(24, 27, 28, 31, 32, 35) PV_W2(36, 39, 40, 43, 44, 47)
                     PV_W2(48, 51, 52, 55, 56, 59) PV_W2(60, 63, 64, 67, 68, 71)
                     "s_waitcnt lgkmcnt(0)\n\ts_branch 3b\n"
                     "99:\n\t" PV_MF("76:79", "96:99", "126:129") PV_MF("80:83", "102:105", "126:129") PV_T(58)
                     "v_add_f32 v153, v86, v87\n\tv_add_f32 v151, v84, v85\n\tv_rcp_f32 v150, v153\n\tv_mul_f32 v151, %[nic], v151\n\t"
                     "v_add_u32 v152, %[aw], %[apk]\n\tv_mul_f32 v151, v151, v150\n\t"
                     PV_NRM(72, 73, 74, 75, 0) PV_NRM(76, 77, 78, 79, 2048) PV_NRM(80, 81, 82, 83, 4096)
                     "v_mov_b32 v154, %[x1]\n\ts_and_b64 exec, exec, 1\n\tds_add_u32 v154, %[one]\n\ts_cselect_b64 exec, -1, 0\n\ts_cselect_b32 s55, %[x1n], 0\n\t" PV_T(60)
                     "v_log_f32 v153, v153\n\tv_subrev_f32 v88, %[toff], v88\n\tv_lshrrev_b32 v151, 1, %[apm]\n\tv_add_f32 v88, v88, v153\n\tv_add_u32 v151, %[lwo], v151\n\t"
                     "v_mbcnt_lo_u32_b32 v155, -1, 0\n\tv_mbcnt_hi_u32_b32 v155, -1, v155\n\tds_write_b32 v151, v88\n\t"
                     "v_and_b32 v151, 1, v155\n\tv_and_b32 v155, 48, v155\n\tv_lshl_add_u32 v155, v151, 2, v155\n\tv_add_u32 v155, %[lw], v155\n\t"
                     "ds_read2_b32 v[114:115], v155 offset1:2\n\tv_add_u32 v152, %[ar], %[apk]\n"
                     "97:\n\tds_read_b32 v150, v154\n\tds_read_b128 v[90:93], v152\n\tds_read_b128 v[94:97], v152 offset:1024\n\tds_read_b128 v[98:101], v152 offset:2048\n\t"
                     "ds_read_b128 v[102:105], v152 offset:3072\n\tds_read_b128 v[106:109], v152 offset:4096\n\tds_read_b128 v[110:113], v152 offset:5120\n\t"
                     "s_sub_i32 s47, s47, 1\n\ts_cmp_lt_i32 s47, 0\n\ts_cselect_b32 s55, 0, s55\n\ts_waitcnt vmcnt(0) lgkmcnt(6)\n\t"
                     "v_readfirstlane_b32 s56, v150\n\ts_cmp_lt_u32 s56, s55\n\ts_cbranch_scc1 97b\n\ts_cmp_eq_u32 s46, 0\n\t"
                     PV_G(5, "90:93", "0:3", "4:7", "8:11", "0", "0", "0")
                     PV_G(4, "94:97", "12:15", "16:19", "20:23", "v[72:75]", "v[76:79]", "v[80:83]")
                     PV_G(3, "98:101", "24:27", "28:31", "32:35", "v[72:75]", "v[76:79]", "v[80:83]")
                     PV_G(2, "102:105", "36:39", "40:43", "44:47", "v[72:75]", "v[76:79]", "v[80:83]")
                     PV_G(1, "106:109", "48:51", "52:55", "56:59", "v[72:75]", "v[76:79]", "v[80:83]")
                     PV_G(0, "110:113", "60:63", "64:67", "68:71", "v[72:75]", "v[76:79]", "v[80:83]")
                     "v_xor_b32 v114, v114, %[ltag]\n\tv_xor_b32 v115, v115, %[ltag]\n\ts_nop 3\n\t"
                     "v_cvt_pk_bf16_f32 v116, v72, v73\n\tv_cvt_pk_bf16_f32 v120, v74, v75\n\tv_cvt_pk_bf16_f32 v117, v76, v77\n\tv_cvt_pk_bf16_f32 v121, v78, v79\n\t"
                     "v_cvt_pk_bf16_f32 v118, v80, v81\n\tv_bitop3_b32 v119, v116, v117, v114 bitop3:0x96\n\tv_cvt_pk_bf16_f32 v122, v82, v83\n\t"
                     "v_bitop3_b32 v123, v120, v121, v115 bitop3:0x96\n\tv_xor_b32 v119, v119, v118\n\tv_xor_b32 v123, v123, v122\n\t"
                     "s_cbranch_scc1 2b\n\t"
                     "global_store_dwordx4 %[so], v[116:119], %[bx]" SPOL "\n\tglobal_store_dwordx4 %[so], v[120:123], %[bx2]" SPOL "\n\t"
                     PV_T(62) "s_waitcnt lgkmcnt(0)\n\ts_nop 0"
                     : [need] "+v"(need), "={s[58:59]}"(tpv), "={s[60:61]}"(tp7), "={s[62:63]}"(tx1)
                     : [so] "v"(((ej * 16 + (lane >> 3)) * RSTR + blockIdx.x * PSTR) * 128 + (lane & 7) * 16), [ltag] "v"(line_tag(epoch, lane & 7)),
                       [bx] "s"(box), [bx2] "s"(box + 8 * RSTR * 128), [aw] "s"(A_IMG - IMG_A + (ej >> 1) * 1024 + (ej & 1) * 8), [ar] "i"(A_IMG - IMG_A),
                       [nic] "i"(__builtin_bit_cast(unsigned, -1.f / CC)), [toff] "i"(__builtin_bit_cast(unsigned, TOFF)),
                       [lwo] "i"(CT_OFF + C_LW - PKM / 2), [lw] "i"(CT_OFF + C_LW), [x1] "i"(CT_OFF + C_X1), [x1n] "i"(X1N),
                       [apk] "v"(apk), [apm] "v"(lbase + PKM + r * 8), [apu] "v"(lbase + PKU + g * 64 + (r & 3) * 16),
                       [ox] "v"(lbase + tr_off(4 * g + qq, 4 * ej + p4)), [oy] "v"(lbase + 32 * ROWB + tr_off(2 * g + (qq >> 1), 4 * ej + p4)),
                       [ca] "v"(ct + (lane == 0 ? C_PFL + (k0 % NPK) * 4 : C_FULL + (k0 % NSLOT) * 4)),
                       [lim] "v"(ct + (lane == 0 ? C_PFL + NPK * 4 : C_FULL + NSLOT * 4)), [base] "v"(ct + (lane == 0 ? C_PFL : C_FULL)), [one] "v"(1u),
                       [k0] "s"(k0), [s0] "s"(helper ? ((TPW - 1) % NSLOT) * SLOT : 0), [e0] "s"(helper ? ((TPW - 1) % NPK) * 128 : 0),
                       [w2] "s"(box + W2_OFF + ej * 18432 - (lbase + IMG_A)), [fl] "s"(box + W2F_OFF),
                       [tag] "s"(u32x2{line_tag(epoch, 10), line_tag(epoch, 11)}), [gcap] "i"(GSPIN_CAP), [iss] "i"(CT_OFF + C_ISS), [cap] "i"(SPIN_CAP), [slot] "i"(SLOT), [ring] "i"(NSLOT * SLOT), [pring] "i"(NPK * 128),
                       [npk2] "i"(2 * NPK), [last] "i"(TPW - 1), [tight] "i"(TPW - 5),  [sch] "s"(W2SCH)
                     : "memory", "scc", "vcc", "s44", "s45", "s46", "s47", "s48", "s49", "s50", "s51", "s52", "s53", "s54", "s55", "s56", "s57",
                       "v0", "v1", "v2", "v3", "v4", "v5", "v6", "v7", "v8", "v9", "v10", "v11", "v12", "v13", "v14", "v15", "v16", "v17", "v18", "v19", "v20", "v21", "v22", "v23", "v24", "v25", "v26", "v27", "v28", "v29", "v30", "v31", "v32", "v33", "v34", "v35", "v36", "v37", "v38", "v39", "v40", "v41", "v42", "v43", "v44", "v45", "v46", "v47", "v48", "v49", "v50", "v51", "v52", "v53", "v54", "v55", "v56", "v57", "v58", "v59", "v60", "v61", "v62", "v63", "v64", "v65", "v66", "v67", "v68", "v69", "v70", "v71", "v72", "v73", "v74", "v75", "v76", "v77", "v78", "v79", "v80", "v81", "v82", "v83", "v84", "v85", "v86", "v87", "v88",
                       "v90", "v91", "v92", "v93", "v94", "v95", "v96", "v97", "v98", "v99", "v100", "v101", "v102", "v103", "v104", "v105", "v106", "v107",
                       "v108", "v109", "v110", "v111", "v112", "v113", "v114", "v115", "v116", "v117", "v118", "v119", "v120", "v121", "v122", "v123",
                       "v124", "v125", "v126", "v127", "v128", "v129", "v130", "v131", "v132", "v133", "v134", "v135", "v136", "v137", "v138", "v139",
                       "v140", "v141", "v142", "v143", "v144", "v145", "v146", "v147", "v148", "v149", "v150", "v151", "v152", "v153", "v154", "v155");
      }
    } else {
      LANE_VARS
      const int c = wave - 1 - NE;
      SCtx sc;
      sc.L0 = L0; sc.ct = ct; sc.apk = lbase + IMG_A + lane * 16; sc.apm = lbase + PKM + r * 8;
      sc.api = lbase + PKU + g * 64 + (r & 3) * 2 + ((r >> 2) == g ? 0 : 8); sc.amr = ct + C_MREF + r * 4;
      sc.o_rb = r * ROWB + ((g ^ swz(r)) << 4);
      sc.ev = (r & 1) ^ 1;
      sc.o_yb = (r & 1) ? ONES_OFF + (g << 4) : (32 + (r >> 1)) * ROWB + ((g ^ swz(r >> 1)) << 4);
      TNOW(tq0);
      if (c >= NEARLY) {
        static_assert(NS - NEARLY == 3, "");
        const int u = c - NEARLY;
        const unsigned epoch = epoch_load(box);
        const unsigned ta = line_tag(epoch, 8), tb = line_tag(epoch, 9);
        const unsigned qw = lds_ld32(ct + C_EP + 4);
        while ((unsigned)__builtin_amdgcn_s_memrealtime() - tent < qw) __builtin_amdgcn_s_sleep(4);
        u32x4 v[4];
#pragma unroll 1
        for (int sp = 0; sp < GSPIN_CAP; ++sp) {
          asm volatile("global_load_dwordx4 %0, %4, %5 sc1\n\tglobal_load_dwordx4 %1, %4, %5 offset:1024 sc1\n\tglobal_load_dwordx4 %2, %4, %5 offset:2048 sc1\n\t"
                       "global_load_dwordx4 %3, %4, %5 offset:3072 sc1\n\ts_waitcnt vmcnt(0)"
                       : "=&v"(v[0]), "=&v"(v[1]), "=&v"(v[2]), "=&v"(v[3]) : "v"(lane * 16), "s"(box + QK_OFF + u * 4096) : "memory");
          unsigned bad = 0;
#pragma unroll
          for (int t = 0; t < 4; ++t) bad |= (v[t][0] ^ v[t][1] ^ v[t][2] ^ ta) | (v[t][3] ^ tb);
          if (!__any(bad != 0u)) break;
        }
        TSET(tqa);
#pragma unroll
        for (int t = 0; t < 4; ++t)
          *(LDS_AS(u32x2)*)(L0 + IMG_A + (4 * u + t) * 512 + ((g >> 1) * 16 + r) * 16 + (g & 1) * 8) = u32x2{v[t][0], v[t][1]};
        asm volatile("" ::: "memory");
        lds_add(ct + C_QI, 1u);
        lds_st32(ct + C_EP, epoch);
      }
      static_assert(NE == 4 && NPK == 6 && NSLOT == 10, "");
      s_items(sc, helper ? TPW : c, helper || c >= NEARLY);
      const int hi = (HMAP >> (4 * c)) & 15;
      if (hi < 6 && (!helper || hi >= 4)) s_half(sc, TPW - NH1 + (hi >> 1), hi & 1);
      TNOW(tse);
      rslot = c;
    }
  }

  if (rslot >= 0 && ((blockIdx.x >> 3) & 3) == 1) {
    LANE_VARS
    __builtin_amdgcn_s_setprio(0);
    const int rid = ((blockIdx.x >> 5) << 3) | (blockIdx.x & 7);
    const int qa = ((rid >> 1) & 3) * 4 + ((rid >> 3) & 1) * 2;
    const char* src = box + (size_t)(rid * RSTR + rslot * 32 * PSTR) * 128;
    const int e = lane & 7;
    const int oidx = (qa + (e & 1)) * CC + 48 * (rid >> 4) + 16 * (e < 6 ? e >> 1 : 0) + 8 * (rid & 1) + (lane >> 3);
    const unsigned short qv = ((const gus*)q)[oidx];
    u32x4 am;
#pragma unroll
    for (int d = 0; d < 4; ++d) am[d] = (e >> 1) == d ? ((e & 1) ? 0xffff0000u : 0x0000ffffu) : 0u;
    const unsigned epoch = *(LDS_AS(unsigned)*)(L0 + CT_OFF + C_EP);
    const unsigned arf = lbase + RED_OFF + 4096 + (lane & 1) * 4;
    poll_ge_sleep(ct + C_FREE + ((TPW - NPK) % NSLOT) * 4, 8u * ((TPW - NPK) / NSLOT + 1));
    lds_st32(arf + rslot * 8, 0u);
    unsigned long long tr;
#define R_LD(d, m, o) "s_mov_b64 exec, s[" m "]\n\tglobal_load_dwordx4 v[" d "], %[vo], %[" #o "] sc1\n\t"
#define R_W(l, a0, a1, a2, a3) "v_cvt_pk_bf16_f32 v" #a3 ", v" #a3 ", v" #a3 "\n\tv_and_b32 v" #a0 ", v60, v" #a3 "\n\tv_and_b32 v" #a1 ", v61, v" #a3 "\n\t" \
                               "v_and_b32 v" #a2 ", v62, v" #a3 "\n\tv_and_b32 v" #a3 ", v63, v" #a3 "\n\t"
#define R_G(n, w, m, l, d0, d1, d2, d3, a0, a1, a2, a3, c) \
  "s_mov_b64 exec, s[" m "]\n\ts_cbranch_execz " #n "f\n\ts_waitcnt vmcnt(" #w ")\n\tv_bitop3_b32 v" #l ", v" #d0 ", v" #d1 ", v" #d2 " bitop3:0x96\n\t" \
  "v_bitop3_b32 v" #l ", v" #l ", v" #d3 ", %[tag] bitop3:0x96\n\tv_mov_b32 v" #d3 ", s49\n\ts_mov_b64 exec, -1\n\tv_exp_f32 v" #a3 ", v" #l "\n\t" \
  "v_xor_b32_dpp v59, v" #l ", v" #l " quad_perm:[2,3,0,1] row_mask:0xf bank_mask:0x5\n\tv_xor_b32_dpp v59, v" #l ", v" #l " row_shr:4 row_mask:0xf bank_mask:0xa\n\t" \
  R_W(l, a0, a1, a2, a3) "v_cmp_ne_u32 s[" m "], 0, v59\n\ts_nop 0\n" \
  "1" #n ":\n\tv_mfma_f32_16x16x32_bf16 v[36:39], v[" #a0 ":" #a3 "], v[" #d0 ":" #d3 "], " c "\n\t"
#define R_X(n) #n ":\n\ts_mov_b64 exec, -1\n\ts_nop 2\n\ts_branch 1" #n "b\n"
#define R_F(l, d0, d3, a0, a1, a2, a3, c) "v_sub_f32 v" #a3 ", v" #l ", v56\n\tv_exp_f32 v" #a3 ", v" #a3 "\n\ts_nop 0\n\t" R_W(l, a0, a1, a2, a3) \
  "s_nop 1\n\tv_mfma_f32_16x16x32_bf16 v[36:39], v[" #a0 ":" #a3 "], v[" #d0 ":" #d3 "], " c "\n\t"
#define R_RD(d, a, b) "ds_read2st64_b32 v[" d "], %[ard] offset0:" #a " offset1:" #b "\n\t"
#define R_A(d, a) "v_pk_add_f32 v[" d "], v[" d "], v[" a "]\n\t"
#define R_S(x, f) "v_sub_f32 v" #f ", v" #f ", v56\n\tv_exp_f32 v" #f ", v" #f "\n\ts_nop 0\n\tv_mul_f32 v[" #x "], v[" #x "], v" #f "\n\tv_mul_f32 v[" #x " + 1], v[" #x " + 1], v" #f "\n\t"
    asm volatile("s_mov_b64 s[52:53], 0\n\ts_mov_b64 s[40:41], -1\n\ts_mov_b64 s[42:43], -1\n\ts_mov_b64 s[44:45], -1\n\ts_mov_b64 s[46:47], -1\n\ts_mov_b32 s48, %[cap]\n\ts_mov_b32 s49, 0x3f803f80\n\t"
                 "v_mov_b32 v57, 1\n\tv_mov_b32 v58, %[x1]\n"
                 "20:\n\ts_sleep 1\n\tds_read_b32 v56, v58\n\ts_sub_u32 s48, s48, 1\n\ts_cselect_b32 s50, 0, %[x1n]\n\ts_waitcnt lgkmcnt(0)\n\t"
                 "v_readfirstlane_b32 s51, v56\n\ts_cmp_lt_u32 s51, s50\n\ts_cbranch_scc1 20b\n\tv_mov_b32 v58, %[red]\n\ts_mov_b32 s48, %[gcap]\n\ts_sleep %[rs]\n"
                 "21:\n\t" R_LD("0:3", "40:41", src) R_LD("4:7", "42:43", sr1) R_LD("8:11", "44:45", sr2) R_LD("12:15", "46:47", sr3)
                 "s_cmp_eq_u32 s48, %[gcap]\n\ts_cbranch_scc1 24f\n\ts_waitcnt vmcnt(0)\n24:\n\t"
                 R_G(1, 3, "40:41", 32, 0, 1, 2, 3, 16, 17, 18, 19, "0") R_G(2, 2, "42:43", 33, 4, 5, 6, 7, 20, 21, 22, 23, "v[36:39]")
                 R_G(3, 1, "44:45", 34, 8, 9, 10, 11, 24, 25, 26, 27, "v[36:39]") R_G(4, 0, "46:47", 35, 12, 13, 14, 15, 28, 29, 30, 31, "v[36:39]")
                 "v_max3_f32 v56, v32, v33, v34\n\ts_sub_u32 s48, s48, 1\n\ts_cbranch_scc1 22f\n\ts_or_b64 s[50:51], s[40:41], s[42:43]\n\tv_max_f32 v56, v56, v35\n\t"
                 "s_or_b64 s[50:51], s[50:51], s[44:45]\n\ts_or_b64 s[50:51], s[50:51], s[46:47]\n\tv_cmp_nlt_f32 vcc, |v56|, %[lim]\n\ts_cbranch_scc1 21b\n\t"
                 "s_cbranch_vccnz 25f\n"
                 "22:\n\ts_mov_b64 exec, %[wm]\n\tds_write_b128 %[awr], v[36:39]\n\ts_mov_b64 exec, 1\n\tds_add_rtn_u32 v59, v58, v57\n\ts_mov_b64 exec, -1\n\t"
                 R_RD("40:41", 0, 1) R_RD("42:43", 2, 3) R_RD("44:45", 4, 5) R_RD("46:47", 6, 7) R_RD("48:49", 8, 9) R_RD("50:51", 10, 11) R_RD("52:53", 12, 13) R_RD("54:55", 14, 15)
                 "s_waitcnt lgkmcnt(8)\n\tv_add_u32 v59, v59, v57\n\ts_nop 0\n\tv_readfirstlane_b32 s50, v59\n\ts_cmp_lg_u32 s50, 8\n\ts_cbranch_scc1 26f\n"
                 "23:\n\ts_waitcnt lgkmcnt(6)\n\t" R_A("40:41", "42:43") "s_waitcnt lgkmcnt(4)\n\t" R_A("44:45", "46:47") "s_waitcnt lgkmcnt(2)\n\t" R_A("48:49", "50:51")
                 R_A("40:41", "44:45") "s_waitcnt lgkmcnt(0)\n\t" R_A("52:53", "54:55") R_A("48:49", "52:53") R_A("40:41", "48:49")
                 "v_add_f32 v40, v40, v41\n\ts_nop 1\n\tv_mov_b32_dpp v41, v40 quad_perm:[2,3,2,3] row_mask:0xf bank_mask:0xf\n\ts_nop 1\n\t"
                 "v_mov_b32_dpp v41, v41 row_shl:4 row_mask:0xf bank_mask:0x5\n\tv_rcp_f32 v41, v41\n\ts_mov_b64 exec, %[om]\n\tv_mul_f32 v40, v40, v41\n\t"
                 "v_cvt_pk_bf16_f32 v40, v40, v40\n\tv_lshlrev_b32 v40, 16, v40\n\tv_add_f32 v40, v40, %[qf]\n\tv_cvt_pk_bf16_f32 v40, v40, v40\n\t"
                 "global_store_short %[oo], v40, %[out]" OPOL "\n\t"
                 "s_cmp_lg_u32 %[rid], 0\n\ts_cbranch_scc1 29f\n\ts_mov_b64 exec, 1\n\tv_mov_b32 v41, %[eo]\n\tglobal_store_dword v41, %[ep], %[bx] sc1\n\ts_branch 29f\n"
                 R_X(1) R_X(2) R_X(3) R_X(4)
                 "25:\n\tv_max_f32_dpp v56, v56, v56 row_ror:8 row_mask:0xf bank_mask:0xf\n\tv_mov_b32 v59, v56\n\ts_nop 1\n\tv_permlane16_swap_b32 v56, v59\n\tv_max_f32 v56, v56, v59\n\t"
                 "v_mov_b32 v59, v56\n\ts_nop 1\n\tv_permlane32_swap_b32 v56, v59\n\tv_max_f32 v56, v56, v59\n\tv_mov_b32 v57, 0x101\n\tv_add_u32 v59, %[rs8], %[arf]\n\tds_write_b32 v59, v56\n\t"
                 R_F(32, 0, 3, 16, 17, 18, 19, "0") R_F(33, 4, 7, 20, 21, 22, 23, "v[36:39]") R_F(34, 8, 11, 24, 25, 26, 27, "v[36:39]") R_F(35, 12, 15, 28, 29, 30, 31, "v[36:39]")
                 "s_nop 7\n\ts_branch 22b\n"
                 "26:\n\ts_waitcnt vmcnt(0)\n\ts_and_b32 s50, s50, 0xff\n\ts_cmp_lg_u32 s50, 8\n\ts_cbranch_scc1 29f\n\t"
                 "ds_read2_b32 v[0:1], %[arf] offset1:2\n\tds_read2_b32 v[2:3], %[arf] offset0:4 offset1:6\n\tds_read2_b32 v[4:5], %[arf] offset0:8 offset1:10\n\t"
                 "ds_read2_b32 v[6:7], %[arf] offset0:12 offset1:14\n\ts_waitcnt lgkmcnt(0)\n\tv_max3_f32 v56, v0, v1, v2\n\tv_max3_f32 v56, v56, v3, v4\n\t"
                 "v_max3_f32 v56, v56, v5, v6\n\tv_max_f32 v56, v56, v7\n\t"
                 R_S(40, 0) R_S(42, 1) R_S(44, 2) R_S(46, 3) R_S(48, 4) R_S(50, 5) R_S(52, 6) R_S(54, 7) "s_branch 23b\n"
                 "29:\n\ts_mov_b64 exec, -1\n\ts_waitcnt lgkmcnt(0)"
                 : "=&{s[52:53]}"(tr)
                 : "{v[60:63]}"(am), [vo] "v"((lane >> 3) * (PSTR * 128) + e * 16), [src] "s"(src), [sr1] "s"(src + PSTR * 1024), [sr2] "s"(src + PSTR * 2048), [sr3] "s"(src + PSTR * 3072), [tag] "v"(line_tag(epoch, e)),
                   [awr] "v"(lbase + RED_OFF + (rslot * 2 + (r >> 3)) * 256 + (r & 7) * 32 + (g & 1) * 16), [ard] "v"(lbase + RED_OFF + lane * 4), [arf] "v"(arf),
                   [qf] "v"((unsigned)qv << 16), [oo] "v"(oidx * 2), [out] "s"(KARG(kb, 0)), [ep] "v"(epoch + 1u), [bx] "s"(box), [rid] "s"(rid), [rs8] "s"(rslot * 8),
                   [wm] "s"(0xff00ff0000ff00ffull), [om] "s"(0x3f3f3f3f3f3f3f3full), [lim] "s"(64.f),
                   [cap] "i"(SPIN_CAP), [gcap] "i"(GSPIN_CAP), [x1] "i"(CT_OFF + C_X1), [x1n] "i"(X1N), [red] "i"(CT_OFF + C_RED), [rs] "i"(RSLEEP), [eo] "i"(EPOCH_OFF)
                 : "memory", "scc", "vcc", "s40", "s41", "s42", "s43", "s44", "s45", "s46", "s47", "s48", "s49", "s50", "s51", "v0", "v1", "v2", "v3", "v4", "v5", "v6", "v7", "v8", "v9", "v10", "v11", "v12", "v13", "v14", "v15", "v16", "v17", "v18", "v19", "v20", "v21", "v22", "v23", "v24", "v25", "v26", "v27", "v28", "v29", "v30", "v31", "v32", "v33", "v34", "v35", "v36", "v37", "v38", "v39", "v40", "v41", "v42", "v43", "v44", "v45", "v46", "v47", "v48", "v49", "v50", "v51", "v52", "v53", "v54", "v55", "v56", "v57", "v58", "v59");
  }
}

#undef q
#undef f0
#undef f1
#undef wq
#undef wk
#undef wv
#undef wo
#undef box
extern "C" __global__ void dino_end() {}

extern "C" void kernel_launch(void* const* d_in, const int* in_sizes, int n_in,
                              void* d_out, int out_size, void* d_ws, size_t ws_size,
                              hipStream_t stream) {
  dino_main<<<NWG, 768, 0, stream>>>((const unsigned short*)d_in[0], (const char*)d_in[1], (const char*)d_in[2], (const char*)d_in[3],
                                     (const char*)d_in[4], (const char*)d_in[5], (const char*)d_in[6], (char*)d_ws + WSOFF,
                                     (unsigned short*)d_out);
}
```

```cpp
#include <hip/hip_runtime.h>
#include <hip/hip_bf16.h>
#include <stdint.h>


using bf16x8  = __attribute__((ext_vector_type(8))) short;
using f32x4   = __attribute__((ext_vector_type(4))) float;
using f32x2   = __attribute__((ext_vector_type(2))) float;
using u32x4   = __attribute__((ext_vector_type(4))) unsigned;
using u32x2   = __attribute__((ext_vector_type(2))) unsigned;
using v4i16_t = __attribute__((ext_vector_type(4))) short;
typedef __attribute__((address_space(3))) char* lds_ptr;
#define LDS_AS(T) __attribute__((address_space(3))) T

constexpr int CC = 192;
constexpr int NWG = 256, NS = 7, NE = 4;
constexpr int TPW = 33;
constexpr int ROWB = 384;
constexpr int SLOT = 40 * ROWB;
constexpr int NSLOT = 10;
constexpr int RSTR = 322;
constexpr int EPOCH_OFF = 64 * RSTR * 128, QK_OFF = EPOCH_OFF + 4096, W2F_OFF = QK_OFF + 12288, W2_OFF = W2F_OFF + 1024;
static_assert(W2_OFF + 72 * 1024 <= (4 << 20), "");
constexpr int GSPIN_CAP = 1 << 16;
#define PACE16 777
#define PACES 810
#define PK1 14
#define PWIN 35
#define AHI 22
#define ADL 60
#define AMG 200
#define T0BACK 7
#define HDUE 93
#define QKW0 200
#define QKW1 3
#define RSLEEP 24
#define W2SCH 0x0311180000000000ull
constexpr int NEARLY = 4;
constexpr int NH1 = 3;
constexpr int NPK = 6;
constexpr float EPS = 1e-5f;
constexpr float SQRT_C = 13.856406460551018f;
constexpr float LOG2E = 1.4426950408889634f;
constexpr float THR = 20.0f;
constexpr float TOFF = 192.0f;
constexpr int SPIN_CAP = 1 << 22;

constexpr int IMG_A = NSLOT * SLOT;
constexpr int CT_OFF = IMG_A + 6144;
constexpr int PKM = CT_OFF + 512, PKU = PKM + NPK * 128;
constexpr int ONES_OFF = PKU + NPK * 256;
constexpr int JUNK = ONES_OFF + ROWB;
constexpr int LDS_BYTES = JUNK + 256;
constexpr int H_R = 192 * ROWB, H_Q = H_R + 32 * ROWB, H_C = H_Q + 6144;
static_assert(LDS_BYTES <= 163840 && (PKU & 15) == 0 && (ONES_OFF & 127) == 0, "lds");
constexpr int C_FULL = 0, C_FREE = 64, C_EP = 128, C_RED = 144, C_ISS = 152, C_X1 = 156, C_QI = 160, C_PFL = 192, C_CONS = 256,
              C_MREF = 320, C_LW = 384;

typedef __bf16 bf16x2_t __attribute__((ext_vector_type(2)));
__device__ __forceinline__ unsigned cvtpk(float lo, float hi) {
  return __builtin_bit_cast(unsigned, __builtin_convertvector(f32x2{lo, hi}, bf16x2_t));
}
__device__ __forceinline__ float bflo(unsigned p) { return __uint_as_float(p << 16); }
__device__ __forceinline__ float bfhi(unsigned p) { return __uint_as_float(p & 0xffff0000u); }
__device__ __forceinline__ bf16x8 pack8(const f32x4& a, const f32x4& b) {
  return __builtin_bit_cast(bf16x8, u32x4{cvtpk(a[0], a[1]), cvtpk(a[2], a[3]), cvtpk(b[0], b[1]), cvtpk(b[2], b[3])});
}
__device__ __forceinline__ float xrow16_max(float x) {
  auto s = __builtin_amdgcn_permlane16_swap(__float_as_uint(x), __float_as_uint(x), false, false);
  x = fmaxf(__uint_as_float(s[0]), __uint_as_float(s[1]));
  auto t = __builtin_amdgcn_permlane32_swap(__float_as_uint(x), __float_as_uint(x), false, false);
  return fmaxf(__uint_as_float(t[0]), __uint_as_float(t[1]));
}
__device__ __forceinline__ float xrow16_sum(float x) {
  auto s = __builtin_amdgcn_permlane16_swap(__float_as_uint(x), __float_as_uint(x), false, false);
  x = __uint_as_float(s[0]) + __uint_as_float(s[1]);
  auto t = __builtin_amdgcn_permlane32_swap(__float_as_uint(x), __float_as_uint(x), false, false);
  return __uint_as_float(t[0]) + __uint_as_float(t[1]);
}
__device__ __forceinline__ v4i16_t tr_read(lds_ptr p) {
  return __builtin_amdgcn_ds_read_tr16_b64_v4i16((LDS_AS(v4i16_t)*)p);
}
__device__ __forceinline__ f32x4 mfma(bf16x8 a, bf16x8 b, f32x4 c) {
  return __builtin_amdgcn_mfma_f32_16x16x32_bf16(a, b, c, 0, 0, 0);
}
__device__ __forceinline__ int swz(int p) { return (p & 6) | ((p >> 2) & 1); }
constexpr unsigned swz_tbl(int i) {
  unsigned t = 0;
  for (int j = 0; j < 8; ++j) { const int p = (8 * i + j) / 3; t |= (unsigned)((p & 6) | ((p >> 2) & 1)) << (4 * j); }
  return t;
}
__device__ __forceinline__ bf16x8 mk8(v4i16_t a, v4i16_t b) { return bf16x8{a[0], a[1], a[2], a[3], b[0], b[1], b[2], b[3]}; }

__device__ __forceinline__ void lds_st32(unsigned addr, unsigned v) {
  asm volatile("ds_write_b32 %0, %1" : : "v"(addr), "v"(v) : "memory");
}
__device__ __forceinline__ unsigned lds_ld32(unsigned addr) {
  unsigned v;
  asm volatile("ds_read_b32 %0, %1\n\ts_waitcnt lgkmcnt(0)" : "=v"(v) : "v"(addr) : "memory");
  return __builtin_amdgcn_readfirstlane(v);
}
__device__ __forceinline__ void lds_add(unsigned addr, unsigned n) {
  asm volatile("s_mov_b64 exec, 1\n\tds_add_u32 %0, %1\n\ts_mov_b64 exec, -1\n\ts_nop 3" : : "v"(addr), "v"(n) : "memory");
}
__device__ __forceinline__ void poll_ge_sleep(unsigned addr, unsigned need) {
  need = __builtin_amdgcn_readfirstlane(need);
  unsigned v, s, n2, cnt = SPIN_CAP;
  asm volatile("s_branch 2f\n1:\n\ts_sleep 2\n2:\n\tds_read_b32 %0, %4\n\ts_sub_u32 %3, %3, 1\n\ts_cselect_b32 %2, 0, %5\n\ts_waitcnt lgkmcnt(0)\n\t"
               "v_readfirstlane_b32 %1, %0\n\ts_cmp_lt_u32 %1, %2\n\ts_cbranch_scc1 1b"
               : "=&v"(v), "=&s"(s), "=&s"(n2), "+s"(cnt) : "v"(addr), "s"(need) : "memory", "scc");
}
__device__ __forceinline__ bf16x8 img_ld(lds_ptr L0, int off, int kk, int lane) { return *(LDS_AS(bf16x8)*)(L0 + off + kk * 1024 + lane * 16); }
__device__ __forceinline__ void img_st(lds_ptr L0, int off, int kk, int lane, bf16x8 v) { *(LDS_AS(bf16x8)*)(L0 + off + kk * 1024 + lane * 16) = v; }

template <int N> __device__ __forceinline__ void wait_vm() { asm volatile("s_waitcnt vmcnt(%0)" :: "i"(N) : "memory"); }
constexpr int A_IMG = ((TPW + 1 - NPK) % NSLOT) * SLOT, RED_OFF = ((TPW - NPK) % NSLOT) * SLOT;
__device__ __forceinline__ unsigned line_tag(unsigned epoch, unsigned i) {
  unsigned x = (epoch + 1u + i * 0x9E3779B9u) * 0x85EBCA6Bu;
  x ^= x >> 15; x *= 0xC2B2AE35u;
  return x ^ (x >> 13);
}
__device__ __forceinline__ unsigned epoch_load(const char* box) {
  unsigned e;
  asm volatile("global_load_dword %0, %1, %2 sc1\n\ts_waitcnt vmcnt(0)" : "=v"(e) : "v"(EPOCH_OFF), "s"(box) : "memory");
  return __builtin_amdgcn_readfirstlane(e);
}
__device__ __forceinline__ int tr_off(int row, int col4) {
  const int ch = col4 >> 1;
  return row * ROWB + (((ch & ~7) | ((ch & 7) ^ swz(row & 15))) << 4) + 8 * (col4 & 1);
}
#define PIN3(a, i) "+v"(a[i]), "+v"(a[i + 1]), "+v"(a[i + 2])

#define DMA1(d, b, off, pol) "global_load_lds_dwordx4 %[" #d "], %[" #b "] offset:" #off pol "\n\t"
__device__ __forceinline__ void issue_rows16(const char* a0, unsigned dst, const unsigned (&d)[3]) {
  unsigned keep;
  asm volatile("s_mov_b32 %[keep], m0\n\ts_mov_b32 m0, %[dst]\n\ts_nop 0\n\t"
               DMA1(d0, a, 0, "") DMA1(d1, a, 1024, "") DMA1(d2, a, 2048, "") DMA1(d0, a, 3072, "")
               "s_add_u32 m0, %[dst], 0x1000\n\ts_nop 0\n\t" DMA1(d1, b, 0, "") DMA1(d2, b, 1024, "") "s_mov_b32 m0, %[keep]"
               : [keep] "=&s"(keep) : [dst] "s"(dst), [a] "s"(a0), [b] "s"(a0 + 4096), [d0] "v"(d[0]), [d1] "v"(d[1]), [d2] "v"(d[2]) : "memory", "scc");
}
#define LD_VM "s_getreg_b32 s43, hwreg(HW_REG_IB_STS, 22, 2)\n\ts_getreg_b32 s42, hwreg(HW_REG_IB_STS, 0, 4)\n\ts_lshl4_add_u32 s42, s43, s42\n\t"
#define LD_GATE(n, X) \
  #n "0:\n\t" LD_VM "s_cmp_le_i32 s42, s46\n\ts_cbranch_scc1 " #n "2f\n" \
  #n "1:\n\t" X "s_cmp_gt_u32 s42, %[win]\n\ts_cbranch_scc1 " #n "5f\n\ts_cmp_ge_u32 s45, s44\n\ts_cbranch_scc1 " #n "7f\n" \
  #n "6:\n\ts_memrealtime s[40:41]\n\ts_sleep 1\n\ts_waitcnt lgkmcnt(0)\n\ts_sub_u32 s45, s40, %[t0]\n\ts_lshl_b32 s45, s45, 4\n\ts_cmp_lt_u32 s45, s44\n\ts_cbranch_scc1 " #n "6b\n" \
  #n "7:\n\t" LD_VM
#define LD_NEXT \
  "s_cmp_gt_u32 s42, %[ahi]\n\ts_cselect_b32 s42, %[amg], 0\n\ts_sub_u32 s43, s45, s44\n\ts_cmp_lt_u32 s43, s42\n\ts_cselect_b32 s42, %[p3] + %[adl], s39\n\t" \
  "s_add_u32 s44, s44, s42\n\ts_add_u32 s46, s46, 5\n\t"
#define LD_PUB0(n, a, b, c) \
  #n "2:\n\tv_add_u32 %[pub], 2, %[pub]\n\ts_cmp_ge_u32 s46, 30\n\ts_cbranch_scc1 " #n "4f\n\ts_cmp_ge_u32 s46, 15\n\ts_cbranch_scc1 " #n "3f\n\t" \
  "s_waitcnt vmcnt(" #a ")\n" #n "3:\n\ts_waitcnt vmcnt(" #b ")\n" #n "4:\n\ts_waitcnt vmcnt(" #c ")\n\tds_write_b32 %[af], %[pub] ; NOTRACK\n\t" \
  "s_sub_u32 s46, s46, 15\n\ts_branch " #n "1b\n"
#define LD_PUB(n, a, b, c) LD_PUB0(n, a, b, c) \
  #n "5:\n\ts_sleep 1\n\ts_sub_i32 s47, s47, 1\n\ts_cmp_gt_i32 s47, 0\n\ts_cbranch_scc1 " #n "0b\n\ts_waitcnt vmcnt(%[win])\n\ts_branch " #n "6b\n"
#define LD1(d, b, off) "global_load_lds_dwordx4 %[" #d "], s[" #b "] offset:" #off " nt\n\t"

#define PV_REQ(P, U, M, C, CA) \
  "v_lshl_add_u32 v150, s44, 3, %[apk]\n\tv_lshl_add_u32 v151, s44, 1, %[apu]\n\tv_add_u32 v152, s44, %[apm]\n\t" \
  "ds_read_b32 v" #C ", v" #CA "\n\tds_read_b128 v[" P "], v150\n\tds_read_b64 v[" M "], v152\n\tds_read_b128 v[" U "], v151\n\t"
#define PV_TR(d0, d1, a, o) "ds_read_b64_tr_b16 v[" #d0 ":" #d1 "], v" #a " offset:" #o "\n\t"
#define PV_FETCH(y0, y0b, a0, a0b, b0, b0b, y1, y1b, a1, a1b, b1, b1b, y2, y2b, a2, a2b, b2, b2b) \
  "v_add_u32 v153, s45, %[ox]\n\tv_add_u32 v154, s45, %[oy]\n\t" \
  PV_TR(a0, a0b, 153, 0) PV_TR(b0, b0b, 153, 6144) PV_TR(a1, a1b, 153, 128) PV_TR(b1, b1b, 153, 6272) PV_TR(a2, a2b, 153, 256) PV_TR(b2, b2b, 153, 6400) \
  PV_TR(y0, y0b, 154, 0) PV_TR(y1, y1b, 154, 128) PV_TR(y2, y2b, 154, 256) \
  "s_add_u32 s45, s45, %[slot]\n\ts_cmp_eq_u32 s45, %[ring]\n\ts_cselect_b32 s45, 0, s45\n\t"
#define PV_FETCH_A PV_FETCH(90, 91, 92, 93, 94, 95, 96, 97, 98, 99, 100, 101, 102, 103, 104, 105, 106, 107)
#define PV_FETCH_B PV_FETCH(108, 109, 110, 111, 112, 113, 114, 115, 116, 117, 118, 119, 120, 121, 122, 123, 124, 125)
#define PV_MF(d, a, p) "v_mfma_f32_16x16x32_bf16 v[" d "], v[" a "], v[" p "], v[" d "]\n\t"
#define PV_MOV(d, s) "v_mov_b32 v" #d ", v" #s "\n\t"
#define PV_WAITF(n, CA)   \
  #n ":\n\tds_read_b32 v155, v" #CA "\n\ts_sub_i32 s47, s47, 1\n\ts_cmp_lt_i32 s47, 0\n\ts_cbranch_scc1 " #n "9f\n\ts_waitcnt lgkmcnt(0)\n\t" \
  "v_cmp_lt_u32 vcc, v155, %[need]\n\ts_and_b64 vcc, vcc, 2\n\ts_cbranch_scc0 " #n "9f\n\ts_sleep 2\n\ts_branch " #n "b\n" #n "9:\n\ts_waitcnt lgkmcnt(0)\n\t"
#define PV_STEP(n, P, U, M, m0, m1, C, CA, P2, U2, M2, C2, CA2, X0, X1, X2, Y0, Y1, Y2, MV0, MV1, MV2, HOOK) \
  "s_waitcnt lgkmcnt(10)\n\t" \
  "v_cmp_lt_u32 vcc, v" #C ", %[need]\n\tv_cmp_neq_f32 s[48:49], v" #m0 ", v88\n\tv_cmp_neq_f32 s[50:51], v" #m1 ", v88\n\t" \
  "v_add_u32 v" #CA2 ", 4, v" #CA "\n\ts_or_b64 s[48:49], s[48:49], vcc\n\tv_cmp_eq_u32 s[52:53], v" #CA2 ", %[lim]\n\t" \
  "s_or_b64 s[48:49], s[48:49], s[50:51]\n\ts_cbranch_scc1 " #n "1f\n" \
  #n "0:\n\ts_add_u32 s44, s44, 128\n\ts_cmp_eq_u32 s44, %[pring]\n\ts_cselect_b32 s44, 0, s44\n\t" \
  "v_cndmask_b32 v" #CA2 ", v" #CA2 ", %[base], s[52:53]\n\t" \
  PV_MF("84:87", U, P) PV_MF("72:75", X0, P) PV_MF("76:79", X1, P) MV0 PV_MF("80:83", X2, P) MV1 \
  PV_MF("72:75", Y0, P) MV2 HOOK PV_REQ(P2, U2, M2, C2, CA2) PV_MF("76:79", Y1, P) PV_MF("80:83", Y2, P)
#define PV_REL(CA) \
  "s_mov_b64 exec, 3\n\tds_add_u32 v" #CA ", %[one] offset:64\n\ts_mov_b64 exec, -1\n\tv_add_u32 %[need], 2, %[need]\n\ts_add_u32 s46, s46, 1\n\t"
#define PV_SC1(p) "v_lshlrev_b32 v151, 16, v" #p "\n\tv_and_b32 v152, 0xffff0000, v" #p "\n\tv_mul_f32 v151, v151, v150\n\tv_mul_f32 v152, v152, v150\n\t" \
                  "v_cvt_pk_bf16_f32 v" #p ", v151, v152\n\t"
#define PV_MUL4(a, b, c, d) "v_mul_f32 v" #a ", v" #a ", v150\n\tv_mul_f32 v" #b ", v" #b ", v150\n\tv_mul_f32 v" #c ", v" #c ", v150\n\tv_mul_f32 v" #d ", v" #d ", v150\n\t"
#define PV_SLOW(n, P, U, M, m0, m1, C, CA, p0, p1, p2, p3) \
  #n "1:\n\ts_bitcmp0_b32 vcc_lo, 0\n\ts_cbranch_scc1 " #n "4f\n\ts_cmp_gt_u32 s46, %[tight]\n\ts_cbranch_scc1 " #n "3f\n" \
  #n "2:\n\ts_sleep 2\n\tds_read_b32 v" #C ", v" #CA "\n\ts_sub_i32 s47, s47, 1\n\ts_cmp_lt_i32 s47, 0\n\ts_cbranch_scc1 " #n "3f\n\ts_waitcnt lgkmcnt(0)\n\t" \
  "v_cmp_lt_u32 vcc, v" #C ", %[need]\n\ts_and_b64 vcc, vcc, 1\n\ts_cbranch_scc1 " #n "2b\n" \
  #n "3:\n\t" PV_REQ(P, U, M, C, CA) "s_sub_i32 s47, s47, 1\n\ts_cmp_lt_i32 s47, 0\n\ts_cselect_b32 s54, 0, 1\n\ts_waitcnt lgkmcnt(0)\n\t" \
  "v_cmp_lt_u32 vcc, v" #C ", %[need]\n\tv_cmp_neq_f32 s[48:49], v" #m0 ", v88\n\tv_cmp_neq_f32 s[50:51], v" #m1 ", v88\n\t" \
  "s_and_b32 s54, s54, vcc_lo\n\ts_cbranch_scc1 " #n "3b\n\ts_or_b64 s[48:49], s[48:49], s[50:51]\n\ts_cbranch_scc0 " #n "0b\n\ts_branch " #n "5f\n" \
  #n "4:\n\tv_cmp_neq_f32 s[48:49], v" #m0 ", v88\n\ts_or_b64 s[48:49], s[48:49], s[50:51]\n\ts_cbranch_scc0 " #n "0b\n" \
  #n "5:\n\tv_mov_b32 v150, v88\n\tv_max3_f32 v88, v88, v" #m0 ", v" #m1 "\n\tv_sub_f32 v150, v150, v88\n\tv_exp_f32 v150, v150\n\ts_nop 1\n\t" \
  PV_MUL4(72, 73, 74, 75) PV_MUL4(76, 77, 78, 79) PV_MUL4(80, 81, 82, 83) PV_MUL4(84, 85, 86, 87) \
  "v_sub_f32 v150, v" #m0 ", v88\n\tv_exp_f32 v150, v150\n\ts_nop 1\n\t" PV_SC1(p0) PV_SC1(p1) \
  "v_sub_f32 v150, v" #m1 ", v88\n\tv_exp_f32 v150, v150\n\ts_nop 1\n\t" PV_SC1(p2) PV_SC1(p3) \
  "s_branch " #n "0b\n"
#define PV_W2(a0, a1, b0, b1, c0, c1) \
  "global_load_dwordx4 v[" #a0 ":" #a1 "], v150, %[w2] sc1\n\tglobal_load_dwordx4 v[" #b0 ":" #b1 "], v150, %[w2] offset:1024 sc1\n\t" \
  "global_load_dwordx4 v[" #c0 ":" #c1 "], v150, %[w2] offset:2048 sc1\n\tv_add_u32 v150, 0xc00, v150\n\ts_sub_u32 s54, s54, 1\n\ts_cbranch_scc1 30b\n\t"

#define PV_NRM(a, b, c, d, o)   \
  "v_pk_fma_f32 v[" #a ":" #b "], v[" #a ":" #b "], v[150:151], v[150:151] op_sel:[0,0,1] op_sel_hi:[1,0,1]\n\t" \
  "v_pk_fma_f32 v[" #c ":" #d "], v[" #c ":" #d "], v[150:151], v[150:151] op_sel:[0,0,1] op_sel_hi:[1,0,1]\n\t" \
  "v_cvt_pk_bf16_f32 v" #a ", v" #a ", v" #b "\n\tv_cvt_pk_bf16_f32 v" #b ", v" #c ", v" #d "\n\tds_write_b64 v152, v[" #a ":" #b "] offset:" #o "\n\t"
#define PV_G(n, a, w0, w1, w2, c0, c1, c2) "s_waitcnt lgkmcnt(" #n ")\n\tv_mfma_f32_16x16x32_bf16 v[72:75], v[" a "], v[" w0 "], " c0 "\n\t" \
  "v_mfma_f32_16x16x32_bf16 v[76:79], v[" a "], v[" w1 "], " c1 "\n\tv_mfma_f32_16x16x32_bf16 v[80:83], v[" a "], v[" w2 "], " c2 "\n\t"
#define PV_Z4(a, b, c, d) "v_mov_b32 v" #a ", 0\n\tv_mov_b32 v" #b ", 0\n\tv_mov_b32 v" #c ", 0\n\tv_mov_b32 v" #d ", 0\n\t"

using u32x8 = __attribute__((ext_vector_type(8))) unsigned;
struct SCtx {
  lds_ptr L0;
  unsigned ct, apk, apm, api, amr;
  int o_rb, o_yb, ev;
  u32x8 q[3];
  unsigned tq;
};
__device__ __forceinline__ bf16x8 s_qf(const SCtx& c, int j) {
  const u32x8& v = c.q[j >> 1];
  const int o = 4 * (j & 1);
  return __builtin_bit_cast(bf16x8, u32x4{v[o], v[o + 1], v[o + 2], v[o + 3]});
}
#define S_M(d, a, b) "v_mfma_f32_16x16x32_bf16 v[" d "], v[" a "], v[" b "], v[" d "]\n\t"
#define S_M0(d, a, b) "v_mfma_f32_16x16x32_bf16 v[" d "], v[" a "], v[" b "], 0\n\t"
#define S_R(d, a, o) "ds_read_b128 v[" d "], v" #a " offset:" #o "\n\t"
#define S_W(n) "s_waitcnt lgkmcnt(" #n ")\n\t"
#define S_D " row_mask:0xf bank_mask:0xf\n\t"
#define S_3(x, y) S_M("100:103", x, y) S_M("104:107", x, x) S_M("96:99", y, y)
#define S_SQ(g, h) "v_fmac_f32 v" #g ", 2.0, v" #h "\n\t"
#define S_SQD(g, h) "v_fmac_f32_dpp v" #g ", v" #h ", %[two] quad_perm:[0,0,2,2]" S_D
#define S_U(u, h, y) "v_add_f32_dpp v" #u ", v" #h ", v" #y " quad_perm:[1,1,3,3]" S_D
#define S_C(d, a, b, m) "v_cndmask_b32 v" #d ", v" #a ", v" #b ", %[" #m "]\n\t"
#define S_B(d, n) "v_mov_b32_dpp v" #d ", v157 row_newbcast:" #n S_D
#define S_BASE(d, u, t) "v_pk_fma_f32 v[" d "], v[" u "], v[116:117], v[" t "] op_sel:[0,1,0] op_sel_hi:[1,1,0]\n\t"
#define S_LG(d, r) "v_pk_fma_f32 v[" d "], v[" d "], v[" r "], %[toff] op_sel_hi:[1,1,0]\n\t"
#define S_SUB(p, d) "v_pk_add_f32 v[" p "], v[" d "], v[150:151] op_sel_hi:[1,0] neg_lo:[0,1] neg_hi:[0,1]\n\t"
#define S_E(p) "v_exp_f32 v" #p ", v" #p "\n\t"
#define S_MUL(p, r) "v_pk_mul_f32 v[" p "], v[" p "], v[" r "]\n\t"
#define S_POS   \
  "s_mul_i32 s50, s42, 26\n\ts_lshr_b32 s50, s50, 8\n\ts_mul_i32 s50, s50, -10\n\ts_add_u32 s50, s50, s42\n\ts_mul_i32 s45, s50, %[slot]\n\ts_mov_b32 s43, s45\n\t"
#define S_POLL(n, a, o, need, back, far) \
  #n ":\n\tds_read_b32 v152, v" #a " offset:" #o "\n\ts_sub_i32 s47, s47, 1\n\ts_cmp_lt_i32 s47, 0\n\ts_waitcnt lgkmcnt(0)\n\ts_cbranch_scc1 " back "\n\t" \
  "v_readfirstlane_b32 s40, v152\n\ts_cmp_ge_u32 s40, " need "\n\ts_cbranch_scc1 " back "\n\ts_sleep 2\n\t" \
  "s_add_u32 s40, s40, " far "\n\ts_cmp_ge_u32 s40, " need "\n\ts_cbranch_scc1 " #n "b\n\ts_sleep 12\n\ts_branch " #n "b\n"
__device__ __forceinline__ void s_items(SCtx& c, int k0, int late) {
  unsigned long long tq;
  asm volatile("s_mov_b32 s47, %[cap]\n\ts_mov_b32 s49, 0\n\ts_mov_b32 s42, %[k0]\n\ts_cmp_lg_u32 %[late], 0\n\ts_cbranch_scc1 7f\n"
               "1:\n\ts_cmp_ge_u32 s42, %[end]\n\ts_cbranch_scc1 9f\n\t"
               S_POS
               "s_mul_i32 s48, s42, 43\n\ts_lshr_b32 s48, s48, 8\n\ts_mul_i32 s46, s48, -6\n\ts_add_u32 s46, s46, s42\n\ts_lshl2_add_u32 s48, s48, %[ns]\n\t"
               "v_add_u32 v144, s45, %[orb]\n\tv_mad_u32_u24 v145, s43, %[ev], %[oyb]\n\ts_lshl2_add_u32 s40, s46, %[pfl]\n\ts_lshl2_add_u32 s41, s50, %[full]\n\t"
               "v_xor_b32 v146, 64, v144\n\tv_xor_b32 v147, 64, v145\n\tv_mov_b32 v148, s40\n\tv_mov_b32 v149, s41\n\t"
               "s_lshl1_add_u32 s44, s42, 2\n\ts_lshl_b32 s46, s46, 7\n\ts_mov_b64 vcc, %[dg]\n\ts_cmp_lt_u32 s49, s44\n\ts_cbranch_scc1 5f\n"
               "2:\n\tds_read_b32 v150, %[amr]\n\tds_read_b32 v151, v148 offset:64\n\t"
               S_R("24:27", 145, 0) S_R("48:51", 144, 0) S_R("28:31", 147, 0) S_R("52:55", 146, 0) S_R("32:35", 145, 128) S_R("56:59", 144, 128)
               S_R("36:39", 147, 128) S_R("60:63", 146, 128) S_R("40:43", 145, 256) S_R("64:67", 144, 256) S_R("44:47", 147, 256) S_R("68:71", 146, 256)
               S_W(10) S_M0("100:103", "48:51", "24:27") S_M0("104:107", "48:51", "48:51") S_R("72:75", 144, 6144) S_R("76:79", 146, 6144) S_M0("96:99", "24:27", "24:27")
               S_W(10) S_M("100:103", "52:55", "28:31") S_M("104:107", "52:55", "52:55") S_R("80:83", 144, 6272) S_R("84:87", 146, 6272) S_M("96:99", "28:31", "28:31")
               S_W(10) S_M("100:103", "56:59", "32:35") S_M("104:107", "56:59", "56:59") S_R("88:91", 144, 6400) S_R("92:95", 146, 6400) S_M("96:99", "32:35", "32:35")
               "s_mov_b64 exec, 1\n\tds_add_u32 v149, %[four] offset:64\n\ts_mov_b64 exec, -1\n\tv_readfirstlane_b32 s51, v151\n\ts_setprio 1\n\ts_nop 0\n\t"
               S_W(11) S_3("60:63", "36:39") S_W(9) S_3("64:67", "40:43") S_W(7) S_3("68:71", "44:47")
               S_W(6) S_M0("108:111", "72:75", "24:27") S_M0("112:115", "72:75", "72:75")
               S_W(5) S_M("108:111", "76:79", "28:31") S_M("112:115", "76:79", "76:79")
               S_W(4) S_M("108:111", "80:83", "32:35")
               "v_mov_b32_dpp v152, v96 quad_perm:[1,1,3,3]" S_D "v_mov_b32_dpp v153, v98 quad_perm:[1,1,3,3]" S_D
               S_M("112:115", "80:83", "80:83") S_C(154, 96, 98, r2) S_SQ(104, 100)
               S_W(3) S_M("108:111", "84:87", "36:39") S_SQD(105, 101) S_SQ(106, 102)
               S_M("112:115", "84:87", "84:87") S_SQD(107, 103) "v_add_f32 v154, %[eps], v154\n\t"
               S_W(2) S_M("108:111", "88:91", "40:43") S_U(128, 100, 152) S_U(129, 101, 152)
               S_M("112:115", "88:91", "88:91") S_U(130, 102, 153) S_U(131, 103, 153)
               S_W(1) S_M("108:111", "92:95", "44:47") S_C(104, 104, 105, r1) S_C(106, 106, 107, r1)
               S_M("112:115", "92:95", "92:95") S_C(155, 128, 129, r1) S_C(156, 130, 131, r1)
               "s_cmp_lt_u32 s42, %[early]\n\ts_cbranch_scc1 7f\n"
               "3:\n\t" S_M0("116:119", "24:27", "0:3") S_C(104, 104, 106, r2) S_C(155, 155, 156, r2)
               S_M("116:119", "28:31", "4:7") "v_add_f32_dpp v104, v154, v104 quad_perm:[0,0,2,2]" S_D S_U(132, 108, 152)
               S_M("116:119", "32:35", "8:11") S_U(133, 109, 152) S_U(134, 110, 153)
               S_M("116:119", "36:39", "12:15") S_U(135, 111, 153) S_SQ(112, 108)
               S_M("116:119", "40:43", "16:19") S_SQD(113, 109) S_SQ(114, 110)
               S_M("116:119", "44:47", "20:23") S_SQD(115, 111) S_C(156, 132, 133, r1) S_C(157, 134, 135, r1)
               S_C(112, 112, 113, r1) S_C(114, 114, 115, r1) S_C(156, 156, 157, r2) S_C(112, 112, 114, r2)
               "v_add_f32_dpp v112, v154, v112 quad_perm:[0,0,2,2]" S_D
               "v_mul_f32 v117, %[nic], v117\n\tv_cndmask_b32_dpp v155, v156, v155, vcc row_ror:4" S_D
               S_BASE("120:121", "128:129", "116:117") S_BASE("122:123", "130:131", "118:119")
               "v_cndmask_b32_dpp v104, v112, v104, vcc row_ror:4" S_D "v_mul_f32 v156, %[nic], v155\n\t"
               S_M("120:123", "48:51", "0:3") S_BASE("124:125", "132:133", "116:117") S_BASE("126:127", "134:135", "118:119")
               S_M("120:123", "52:55", "4:7") "v_fmac_f32 v104, v156, v155\n\tv_max_f32 v104, %[eps], v104\n\t"
               S_M("120:123", "56:59", "8:11") "v_rsq_f32 v157, v104\n\t"
               S_M("120:123", "60:63", "12:15") "v_mul_f32 v104, v104, v157\n\tv_mov_b32_dpp v157, v157 row_ror:12 row_mask:0x2 bank_mask:0xf\n\t"
               S_M("120:123", "64:67", "16:19") "v_cvt_pk_bf16_f32 v105, v155, v104\n\tv_mov_b32_dpp v157, v157 row_ror:8 row_mask:0x4 bank_mask:0xf\n\t"
               S_M("120:123", "68:71", "20:23") "v_lshlrev_b32 v106, 16, v105\n\tv_mov_b32_dpp v157, v157 row_ror:4 row_mask:0x8 bank_mask:0xf\n\t"
               S_M("124:127", "72:75", "0:3") "v_and_b32 v107, 0xffff0000, v105\n\t" S_B(136, 0)
               S_M("124:127", "76:79", "4:7") S_B(137, 1) S_B(138, 2)
               S_M("124:127", "80:83", "8:11") S_B(139, 3) S_LG("120:121", "136:137")
               S_M("124:127", "84:87", "12:15") S_LG("122:123", "138:139") S_SUB("128:129", "120:121")
               S_M("124:127", "88:91", "16:19") S_SUB("130:131", "122:123") S_E(128)
               S_M("124:127", "92:95", "20:23") S_E(129) S_E(130) S_E(131)
               S_B(140, 4) S_B(141, 5) S_B(142, 6) S_B(143, 7) "v_sub_f32 v106, v155, v106\n\t"
               S_LG("124:125", "140:141") S_LG("126:127", "142:143") "v_sub_f32 v107, v104, v107\n\tv_max3_f32 v152, v120, v121, v122\n\t"
               S_SUB("132:133", "124:125") S_SUB("134:135", "126:127") "v_max3_f32 v153, v123, v124, v125\n\t"
               S_E(132) S_E(133) S_E(134) S_E(135)
               "v_max3_f32 v152, v152, v126, v127\n\tv_add_f32 v154, %[thr], v150\n\tv_max_f32 v152, v152, v153\n\tv_cvt_pk_bf16_f32 v106, v106, v107\n\t"
               "v_lshl_add_u32 v144, s46, 3, %[apk]\n\tv_cmp_gt_f32 vcc, v152, v154\n\tv_add_u32 v145, s46, %[apm]\n\tv_lshl_add_u32 v146, s46, 1, %[api]\n\t"
               "s_cbranch_vccnz 8f\n"
               "4:\n\t" S_MUL("128:129", "136:137") S_MUL("130:131", "138:139") S_MUL("132:133", "140:141") S_MUL("134:135", "142:143")
               "v_cvt_pk_bf16_f32 v128, v128, v129\n\tv_cvt_pk_bf16_f32 v129, v130, v131\n\tv_cvt_pk_bf16_f32 v130, v132, v133\n\tv_cvt_pk_bf16_f32 v131, v134, v135\n\t"
               "s_cmp_lt_u32 s51, s48\n\ts_cbranch_scc1 6f\n"
               "10:\n\tds_read_b32 v151, v149\n\tds_write_b128 v144, v[128:131]\n\tds_write2_b32 v145, v150, v150 offset0:0 offset1:1\n\ts_mov_b64 exec, %[dg2]\n\t"
               "ds_write_b16 v146, v105\n\tds_write_b16 v146, v106 offset:16\n\tds_write_b16_d16_hi v146, v105 offset:32\n\tds_write_b16_d16_hi v146, v106 offset:48\n\t"
               "s_mov_b64 exec, 1\n\tds_add_u32 v148, %[inc]\n\ts_mov_b64 exec, -1\n\ts_setprio 0\n\ts_add_u32 s42, s42, %[ns]\n\t"
               S_W(7) "v_readfirstlane_b32 s49, v151\n\ts_branch 1b\n"
               S_POLL(5, 149, 0, "s44", "2b", "2")
               S_POLL(6, 148, 64, "s48", "10b", "64")
               "7:\n\tv_mov_b32 v144, %[qi]\n\tv_mov_b32 v145, v152\n\tv_mov_b32 v146, 1\n"
               S_POLL(70, 144, 0, "%[nq]", "71f", "64")
               "71:\n\tv_mov_b32 v152, v145\n\tds_read_b128 v[0:3], %[apk]\n\tds_read_b128 v[4:7], %[apk] offset:1024\n\tds_read_b128 v[8:11], %[apk] offset:2048\n\t"
               "ds_read_b128 v[12:15], %[apk] offset:3072\n\tds_read_b128 v[16:19], %[apk] offset:4096\n\tds_read_b128 v[20:23], %[apk] offset:5120\n\t"
               "v_subrev_u32 v144, %[mc], %[amr]\n\tds_read_b32 v150, %[amr]\n\ts_mov_b64 exec, 63\n\tds_add_u32 v144, v146\n\ts_mov_b64 exec, -1\n\t"
               "s_cmp_lg_u32 %[late], 0\n\ts_waitcnt lgkmcnt(0)\n\ts_nop 1\n\ts_cbranch_scc1 1b\n\ts_branch 3b\n"
               "8:\n\tv_mov_b32 v153, v152\n\ts_nop 1\n\tv_permlane16_swap_b32 v152, v153\n\tv_max_f32 v152, v152, v153\n\tv_mov_b32 v153, v152\n\ts_nop 1\n\t"
               "v_permlane32_swap_b32 v152, v153\n\tv_max_f32 v152, v152, v153\n\tv_mul_f32 v152, 0x3d000000, v152\n\tv_rndne_f32 v152, v152\n\t"
               "v_mul_f32 v152, 0x42000000, v152\n\tv_max_f32 v150, v150, v152\n\tds_max_f32 %[amr], v150\n\t"
               S_SUB("128:129", "120:121") S_SUB("130:131", "122:123") S_SUB("132:133", "124:125") S_SUB("134:135", "126:127")
               S_E(128) S_E(129) S_E(130) S_E(131) S_E(132) S_E(133) S_E(134) S_E(135) "s_branch 4b\n"
               "9:"
               : "=&{v[0:7]}"(c.q[0]), "=&{v[8:15]}"(c.q[1]), "=&{v[16:23]}"(c.q[2]), "=&{s[52:53]}"(tq)
               : [orb] "v"(c.o_rb), [oyb] "v"(c.o_yb), [ev] "v"(c.ev), [apk] "v"(c.apk), [apm] "v"(c.apm), [api] "v"(c.api), [amr] "v"(c.amr),
                 [two] "v"(2.f), [four] "v"(4u), [inc] "v"(2u * NPK), [k0] "s"(k0), [late] "s"(__builtin_amdgcn_readfirstlane(late)), [toff] "s"(f32x2{TOFF, TOFF}),
                 [r1] "s"(0xAAAAAAAAAAAAAAAAull), [r2] "s"(0xCCCCCCCCCCCCCCCCull), [dg] "s"(0xF0000F0000F0000Full), [dg2] "s"(0xF00FFF000FF000FFull),
                 [cap] "i"(SPIN_CAP), [end] "i"(TPW - NH1), [ns] "i"(NS), [pfl] "i"(CT_OFF + C_PFL), [full] "i"(CT_OFF + C_FULL), [early] "i"(NEARLY),
                 [qi] "i"(CT_OFF + C_QI), [nq] "i"(NS - NEARLY), [mc] "i"(C_MREF - C_CONS), [slot] "i"(SLOT),
                 [thr] "i"(__builtin_bit_cast(unsigned, THR)), [eps] "i"(__builtin_bit_cast(unsigned, EPS * CC)), [nic] "i"(__builtin_bit_cast(unsigned, -1.f / CC))
               : "memory", "scc", "vcc", "s40", "s41", "s42", "s43", "s44", "s45", "s46", "s47", "s48", "s49", "s50", "s51",
                 "v24", "v25", "v26", "v27", "v28", "v29", "v30", "v31", "v32", "v33", "v34", "v35", "v36", "v37", "v38", "v39", "v40", "v41", "v42", "v43", "v44", "v45", "v46", "v47", "v48", "v49", "v50", "v51", "v52", "v53", "v54", "v55", "v56", "v57", "v58", "v59", "v60", "v61", "v62", "v63", "v64", "v65", "v66", "v67", "v68", "v69", "v70", "v71", "v72", "v73", "v74", "v75", "v76", "v77", "v78", "v79", "v80", "v81", "v82", "v83", "v84", "v85", "v86", "v87", "v88", "v89", "v90", "v91", "v92", "v93", "v94", "v95", "v96", "v97", "v98", "v99", "v100", "v101", "v102", "v103", "v104", "v105", "v106", "v107", "v108", "v109", "v110", "v111", "v112", "v113", "v114", "v115", "v116", "v117", "v118", "v119", "v120", "v121", "v122", "v123", "v124", "v125", "v126", "v127", "v128", "v129", "v130", "v131", "v132", "v133", "v134", "v135", "v136", "v137", "v138", "v139", "v140", "v141", "v142", "v143", "v144", "v145", "v146", "v147", "v148", "v149", "v150", "v151", "v152", "v153", "v154", "v155", "v156", "v157");
  c.tq = (unsigned)tq;
}

__device__ __forceinline__ void s_half(SCtx& c, int k, int h) {
  const int s = k % NSLOT, e = k % NPK;
  const int sb_ = s * SLOT;
  int yba = c.ev * sb_ + c.o_yb;
  float hc = 1.f - 0.5f * c.ev;
  unsigned xa = (unsigned)(size_t)c.L0 + sb_ + h * 6144 + c.o_rb, xb = xa ^ 64, ai = c.api + e * 256 + h * 8;
  unsigned ap = c.apk + e * 1024 + h * 8, am = c.apm + e * 128 + h * 4, af = c.ct + C_PFL + e * 4, inc = NPK;
  float two = 2.f;
  f32x4 G = {EPS * CC, EPS * CC, EPS * CC, EPS * CC};
  poll_ge_sleep(c.ct + C_CONS + e * 4, (unsigned)(NE * (k / NPK) + NS));
  asm volatile("" : "+v"(yba), "+v"(hc), "+v"(xa), "+v"(xb), "+v"(ai), "+v"(ap), "+v"(am), "+v"(af), "+v"(inc), "+v"(two), "+{v[104:107]}"(G));
  poll_ge_sleep(c.ct + C_FULL, (unsigned)(2 * k + 1));
  __builtin_amdgcn_s_setprio(1);
  bf16x8 yb[6];
#pragma unroll
  for (int j = 0; j < 6; ++j) yb[j] = *(LDS_AS(bf16x8)*)(c.L0 + ((j & 1) ? (yba ^ 64) : yba) + (j >> 1) * 128);
  f32x4 Hx = {0.f, 0.f, 0.f, 0.f}, S = Hx;
#pragma unroll
  for (int j = 0; j < 6; ++j) { Hx = mfma(yb[j], yb[j], Hx); S = mfma(yb[j], s_qf(c, j), S); }
  const float nsq = S[1] * (-1.f / CC);
  Hx = f32x4{Hx[0] * hc, Hx[0] * hc, Hx[2] * hc, Hx[2] * hc};
  S = f32x4{S[0], S[0], S[2], S[2]};
  float mref, mt;
  f32x4 rs;
  unsigned long long raise;
#define SH_M(d, a, b) "v_mfma_f32_16x16x32_bf16 v[" d "], v[" a "], " b ", v[" d "]\n\t"
#define SH_X(n, a, y) "s_waitcnt lgkmcnt(" #n ")\n\t" SH_M("100:103", a, "%[" #y "]") SH_M("104:107", a, "v[" a "]")
#define SH_D " row_mask:0xf bank_mask:0xf\n\t"
  __builtin_amdgcn_s_setprio(0);
  if (k < TPW - 1) poll_ge_sleep(c.ct + C_FULL, (unsigned)(2 * k + 2));
  asm volatile("s_mov_b32 s42, %[cap]\n"
               "1:\n\tds_read_b32 v136, %[afl]\n\tds_read_b128 v[112:115], %[xa]\n\tds_read_b128 v[116:119], %[xb]\n\tds_read_b128 v[120:123], %[xa] offset:128\n\t"
               "ds_read_b128 v[124:127], %[xb] offset:128\n\tds_read_b128 v[128:131], %[xa] offset:256\n\tds_read_b128 v[132:135], %[xb] offset:256\n\t"
               "ds_read_b32 %[mr], %[amr]\n\ts_sub_u32 s42, s42, 1\n\ts_cselect_b32 s41, 0, %[need]\n\ts_waitcnt lgkmcnt(7)\n\t"
               "v_readfirstlane_b32 s40, v136\n\ts_cmp_lt_u32 s40, s41\n\ts_cbranch_scc1 1b\n\ts_setprio 3\n\t"
               SH_X(6, "112:115", y0) SH_X(5, "116:119", y1) SH_X(4, "120:123", y2) SH_X(3, "124:127", y3) SH_X(2, "128:131", y4) SH_X(1, "132:135", y5)
               SH_M("108:111", "112:115", "%[q0]") SH_M("108:111", "116:119", "%[q1]")
               "s_waitcnt lgkmcnt(0)\n\tv_add_f32 v137, %[thr], %[mr]\n\tds_write_b32 %[am], %[mr]\n\ts_nop 1\n\t"
               "v_mov_b32_dpp v140, v100 quad_perm:[1,1,3,3]" SH_D "v_mov_b32_dpp v141, v101 quad_perm:[1,1,3,3]" SH_D
               "v_mov_b32_dpp v142, v102 quad_perm:[1,1,3,3]" SH_D "v_mov_b32_dpp v143, v103 quad_perm:[1,1,3,3]" SH_D
               SH_M("108:111", "120:123", "%[q2]")
               "v_fmac_f32 v104, 2.0, v100\n\tv_fmac_f32_dpp v105, v101, %[two] quad_perm:[0,0,2,2]" SH_D
               "v_fmac_f32 v106, 2.0, v102\n\tv_fmac_f32_dpp v107, v103, %[two] quad_perm:[0,0,2,2]" SH_D
               "v_cndmask_b32 v138, v140, v141, %[r1]\n\tv_cndmask_b32 v139, v142, v143, %[r1]\n\t"
               SH_M("108:111", "124:127", "%[q3]")
               "v_cndmask_b32 v104, v104, v105, %[r1]\n\tv_cndmask_b32 v106, v106, v107, %[r1]\n\tv_cndmask_b32 v138, v138, v139, %[r2]\n\t"
               "v_cndmask_b32 v104, v104, v106, %[r2]\n\tv_mul_f32 v139, %[nic], v138\n\t"
               SH_M("108:111", "128:131", "%[q4]")
               "v_fmac_f32 v104, v139, v138\n\tv_max_f32 v104, %[eps], v104\n\tv_rsq_f32 v105, v104\n\t"
               SH_M("108:111", "132:135", "%[q5]")
               "v_mul_f32 v104, v104, v105\n\t"
               "v_mov_b32_dpp v105, v105 row_ror:12 row_mask:0x2 bank_mask:0xf\n\tv_cvt_pk_bf16_f32 v106, v138, v104\n\ts_nop 0\n\t"
               "v_mov_b32_dpp v105, v105 row_ror:8 row_mask:0x4 bank_mask:0xf\n\tv_lshlrev_b32 v107, 16, v106\n\tv_and_b32 v139, 0xffff0000, v106\n\t"
               "v_mov_b32_dpp v105, v105 row_ror:4 row_mask:0x8 bank_mask:0xf\n\t"
               "v_fmac_f32 v108, v140, %[nsq]\n\tv_fmac_f32 v109, v141, %[nsq]\n\tv_fmac_f32 v110, v142, %[nsq]\n\tv_fmac_f32 v111, v143, %[nsq]\n\t"
               "v_mov_b32_dpp v100, v105 row_newbcast:0" SH_D "v_mov_b32_dpp v101, v105 row_newbcast:1" SH_D
               "v_mov_b32_dpp v102, v105 row_newbcast:2" SH_D "v_mov_b32_dpp v103, v105 row_newbcast:3" SH_D
               "v_fmaak_f32 v108, v108, v100, %[toff]\n\tv_fmaak_f32 v109, v109, v101, %[toff]\n\tv_fmaak_f32 v110, v110, v102, %[toff]\n\tv_fmaak_f32 v111, v111, v103, %[toff]\n\t"
               "v_sub_f32 v140, v108, %[mr]\n\tv_sub_f32 v141, v109, %[mr]\n\tv_sub_f32 v142, v110, %[mr]\n\tv_sub_f32 v143, v111, %[mr]\n\t"
               "v_exp_f32 v140, v140\n\tv_exp_f32 v141, v141\n\tv_exp_f32 v142, v142\n\tv_exp_f32 v143, v143\n\t"
               "v_max3_f32 %[mt], v108, v109, v110\n\tv_sub_f32 v138, v138, v107\n\tv_sub_f32 v104, v104, v139\n\tv_max_f32 %[mt], %[mt], v111\n\t"
               "v_pk_mul_f32 v[140:141], v[140:141], v[100:101]\n\tv_pk_mul_f32 v[142:143], v[142:143], v[102:103]\n\t"
               "v_cmp_gt_f32 %[ra], %[mt], v137\n\tv_cvt_pk_bf16_f32 v107, v138, v104\n\t"
               "v_cvt_pk_bf16_f32 v140, v140, v141\n\tv_cvt_pk_bf16_f32 v141, v142, v143\n\t"
               "ds_write_b64 %[ap], v[140:141]\n\ts_mov_b64 exec, %[dg]\n\t"
               "ds_write_b16 %[ai], v106\n\tds_write_b16 %[ai], v107 offset:16\n\tds_write_b16_d16_hi %[ai], v106 offset:32\n\tds_write_b16_d16_hi %[ai], v107 offset:48\n\t"
               "s_cmp_eq_u64 %[ra], 0\n\ts_cselect_b64 exec, 1, 0\n\tds_add_u32 %[af], %[inc]\n\ts_mov_b64 exec, -1\n\ts_setprio 0"
               : "+{v[100:103]}"(Hx), "+{v[104:107]}"(G), "+{v[108:111]}"(S), [mr] "=&v"(mref), [mt] "=&v"(mt), [ra] "=&s"(raise)
               : [y0] "v"(yb[0]), [y1] "v"(yb[1]), [y2] "v"(yb[2]), [y3] "v"(yb[3]), [y4] "v"(yb[4]), [y5] "v"(yb[5]),
                 [q0] "v"(s_qf(c, 0)), [q1] "v"(s_qf(c, 1)), [q2] "v"(s_qf(c, 2)), [q3] "v"(s_qf(c, 3)), [q4] "v"(s_qf(c, 4)), [q5] "v"(s_qf(c, 5)),
                 [afl] "v"(c.ct + C_FULL), [xa] "v"(xa), [xb] "v"(xb), [amr] "v"(c.amr), [am] "v"(am), [ap] "v"(ap), [ai] "v"(ai),
                 [af] "v"(af), [inc] "v"(inc), [nsq] "v"(nsq), [two] "v"(two),
                 [need] "s"(__builtin_amdgcn_readfirstlane(2 * k + 2)), [r1] "s"(0xAAAAAAAAAAAAAAAAull), [r2] "s"(0xCCCCCCCCCCCCCCCCull),
                 [dg] "s"(0xF0000F0000F0000Full), [cap] "i"(SPIN_CAP), [thr] "i"(__builtin_bit_cast(unsigned, THR)), [toff] "i"(__builtin_bit_cast(unsigned, TOFF)),
                 [eps] "i"(__builtin_bit_cast(unsigned, EPS * CC)), [nic] "i"(__builtin_bit_cast(unsigned, -1.f / CC))
               : "memory", "scc", "s40", "s41", "s42", "v112", "v113", "v114", "v115", "v116", "v117", "v118", "v119", "v120", "v121", "v122", "v123", "v124", "v125",
                 "v126", "v127", "v128", "v129", "v130", "v131", "v132", "v133", "v134", "v135", "v136", "v137", "v138", "v139", "v140", "v141", "v142", "v143");
  if (__builtin_expect(raise != 0ull, 0)) {
    rs = Hx;
    mref = fmaxf(mref, 32.f * __builtin_rintf(xrow16_max(mt) * (1.f / 32.f)));
    float pp[4];
#pragma unroll
    for (int i = 0; i < 4; ++i) pp[i] = __builtin_amdgcn_exp2f(S[i] - mref) * rs[i];
    asm volatile("ds_max_f32 %6, %3\n\tds_write_b32 %2, %3\n\tds_write_b64 %0, %1\n\ts_mov_b64 exec, 1\n\tds_add_u32 %4, %5\n\ts_mov_b64 exec, -1"
                 : : "v"(ap), "v"(u32x2{cvtpk(pp[0], pp[1]), cvtpk(pp[2], pp[3])}), "v"(am), "v"(mref), "v"(af), "v"((unsigned)NPK), "v"(c.amr) : "memory");
  }
}

using u32x16 = __attribute__((ext_vector_type(16))) unsigned;
typedef __attribute__((address_space(1))) unsigned short gus;
extern "C" __global__ __launch_bounds__(768, 3) void dino_main(const void*, const void*, const void*, const void*, const void*,
                                                               const void*, const void*, void*, void*) {
  u32x16 ka; u32x2 kb; unsigned long long tent64;
  __shared__ __attribute__((aligned(16))) char lds[LDS_BYTES];
  const int wave = __builtin_amdgcn_readfirstlane((int)threadIdx.x >> 6);
  const bool helper = blockIdx.x < 8;
  lds_ptr L0 = (lds_ptr)lds;
  const unsigned lbase = (unsigned)(size_t)L0;
  const unsigned ct = lbase + CT_OFF;
#define KARG(v, i) ((char*)(((unsigned long long)v[2 * (i) + 1] << 32) | v[2 * (i)]))
#define q KARG(ka, 0)
#define f0 KARG(ka, 1)
#define f1 KARG(ka, 2)
#define wq KARG(ka, 3)
#define wk KARG(ka, 4)
#define wv KARG(ka, 5)
#define wo KARG(ka, 6)
#define box KARG(ka, 7)
#define tent ((unsigned)tent64)
#define LANE_VARS int lane; asm volatile("v_mbcnt_lo_u32_b32 %0, -1, 0\n\tv_mbcnt_hi_u32_b32 %0, -1, %0" : "=v"(lane)); \
  const int r = lane & 15, g = lane >> 4; (void)r; (void)g;
#define DOFF_VARS unsigned doff[3];   \
  _Pragma("unroll") for (int i = 0; i < 3; ++i) doff[i] = (unsigned)(lane ^ ((swz_tbl(i) >> ((lane >> 3) * 4)) & 7)) << 4;
#define BARRIER() do { asm volatile("s_waitcnt lgkmcnt(0)" ::: "memory"); __builtin_amdgcn_s_barrier(); asm volatile("" ::: "memory"); } while (0)

  {
    unsigned doff[3];
    static_assert(swz_tbl(0) == swz_tbl(3) && swz_tbl(1) == swz_tbl(4) && swz_tbl(2) == swz_tbl(5), "");
    unsigned l16, db;
#define PD(d, b, off, pol) "global_load_lds_dwordx4 %[" #d "], s[" #b "] offset:" #off pol "\n\t"
    asm volatile("s_load_dwordx16 s[64:79], %[kp], 0x0\n\ts_load_dwordx2 s[80:81], %[kp], 0x40\n\ts_memrealtime s[82:83]\n\t"
                 "v_mbcnt_lo_u32_b32 %[l16], -1, 0\n\tv_mbcnt_hi_u32_b32 %[l16], -1, %[l16]\n\t"
                 "s_lshr_b32 s84, %[b], 3\n\ts_and_b32 s85, %[b], 7\n\t"
                 "v_lshrrev_b32 %[db], 1, %[l16]\n\tv_and_b32 %[db], -4, %[db]\n\t"
                 "s_mov_b32 s86, %[t0]\n\ts_mov_b32 s87, %[t1]\n\ts_mov_b32 s88, %[t2]\n\t"
                 "v_bfe_u32 %[d0], s86, %[db], 3\n\tv_bfe_u32 %[d1], s87, %[db], 3\n\tv_bfe_u32 %[d2], s88, %[db], 3\n\t"
                 "v_xor_b32 %[d0], %[d0], %[l16]\n\tv_xor_b32 %[d1], %[d1], %[l16]\n\tv_xor_b32 %[d2], %[d2], %[l16]\n\t"
                 "v_lshlrev_b32 %[d0], 4, %[d0]\n\tv_lshlrev_b32 %[d1], 4, %[d1]\n\tv_lshlrev_b32 %[d2], 4, %[d2]\n\t"
                 "v_lshlrev_b32 %[l16], 4, %[l16]\n\t"
                 "s_cmp_eq_u32 s84, 0\n\ts_cbranch_scc1 4f\n\t"
                 "s_cmp_lg_u32 %[w], 0\n\ts_cbranch_scc1 7f\n\t"
                 "s_setprio 3\n\tv_lshlrev_b32 %[db], 3, %[l16]\n\ts_mov_b32 m0, %[junk]\n\ts_lshl_b32 s89, s84, 13\n\t"
                 "s_cmp_gt_u32 s84, 3\n\ts_cbranch_scc1 2f\n\t"
                 "s_getpc_b64 s[90:91]\n1:\n\ts_sub_u32 s86, s89, 8192\n\ts_min_u32 s86, s86, dino_end-1b-8192\n\t"
                 "s_add_u32 s90, s90, s86\n\ts_addc_u32 s91, s91, 0\n\tglobal_load_lds_dword %[db], s[90:91]\n"
                 "2:\n\ts_mul_i32 s86, s85, 98304\n\ts_mul_i32 s87, s84, 3072\n\ts_sub_u32 s87, s87, 3072\n\t"
                 "s_add_u32 s88, s86, s87\n\ts_lshl1_add_u32 s86, s86, s87\n\ts_lshl_b32 s86, s86, 1\n\t"
                 "s_mul_i32 s87, s85, 12288\n\ts_lshr_b32 s90, s89, 1\n\ts_add_u32 s87, s87, s90\n\ts_sub_u32 s87, s87, 13 * 4096\n\t"
                 "s_sub_u32 s89, s89, 4 * 8192\n\ts_cmp_gt_u32 s85, 5\n\ts_cselect_b32 s89, -1, s89\n\ts_cmp_ge_u32 s89, 11 * 8192\n\t"
                 "s_waitcnt lgkmcnt(0)\n\ts_cbranch_scc1 3f\n\t"
                 "s_cmp_lt_u32 s89, 9 * 8192\n\ts_cselect_b64 s[90:91], s[70:71], s[72:73]\n\ts_cselect_b32 s89, s89, s87\n\t"
                 "s_add_u32 s90, s90, s89\n\ts_addc_u32 s91, s91, 0\n\tglobal_load_lds_dword %[db], s[90:91]\n"
                 "3:\n\ts_mov_b32 m0, 0x3000\n\t"
                 "s_add_u32 s90, s68, s88\n\ts_addc_u32 s91, s69, 0\n\t"
                 "s_add_u32 s92, s66, s86\n\ts_addc_u32 s93, s67, 0\n\t"
                 PD(d0, 90:91, 0, " nt") PD(d1, 90:91, 1024, " nt") PD(d2, 90:91, 2048, " nt")
                 "s_mov_b32 m0, 0\n\ts_nop 0\n\t"
                 PD(d0, 92:93, 0, " nt") PD(d1, 92:93, 1024, " nt")
                 "s_sub_u32 s89, s84, 15\n\ts_cmp_gt_u32 s85, 5\n\ts_cselect_b32 s89, -1, s89\n\ts_cmp_ge_u32 s89, 11\n\ts_cbranch_scc1 9f\n\t"
                 "s_mov_b32 m0, %[junk]\n\ts_sub_u32 s87, s87, 11 * 4096\n\ts_lshl_b32 s89, s89, 13\n\ts_cmp_lt_u32 s89, 9 * 8192\n\t"
                 "s_cselect_b64 s[90:91], s[76:77], s[74:75]\n\ts_cselect_b32 s89, s89, s87\n\t"
                 "s_add_u32 s90, s90, s89\n\ts_addc_u32 s91, s91, 0\n\tglobal_load_lds_dword %[db], s[90:91]\n\t"
                 "s_branch 9f\n"
                 "7:\n\ts_cmp_eq_u32 %[w], 1\n\ts_cbranch_scc1 8f\n\ts_barrier\n\ts_branch 8f\n"
                 "4:\n\t"
                 "s_cmp_gt_u32 %[b], 5\n\ts_cbranch_scc1 8f\n\ts_cmp_gt_u32 %[w], 9\n\ts_cbranch_scc1 8f\n\t"
                 "s_mov_b32 m0, %[img]\n\ts_mul_i32 s86, %[w], 9216\n\ts_and_b32 s88, %[w], 1\n\ts_lshl1_add_u32 s89, %[b], s88\n\t"
                 "s_mul_i32 s88, s88, 6144\n\ts_mul_i32 s89, s89, 6144\n\ts_add_u32 s88, s88, %[hr]\n\t"
                 "s_cmp_lg_u32 %[w], 0\n\ts_waitcnt lgkmcnt(0)\n\ts_cbranch_scc1 5f\n\t"
                 PD(d0, 64:65, 0, "") PD(d1, 64:65, 1024, "") PD(d2, 64:65, 2048, "") PD(d0, 64:65, 3072, "")
                 "s_mov_b32 m0, %[img] + 4096\n\ts_add_u32 s90, s64, 0x1000\n\ts_addc_u32 s91, s65, 0\n\t"
                 PD(d1, 90:91, 0, "") PD(d2, 90:91, 1024, "")
                 "\n5:\n\ts_cmp_gt_u32 %[w], 7\n\ts_cselect_b64 s[92:93], s[72:73], s[70:71]\n\ts_cselect_b32 m0, s88, s86\n\ts_cselect_b32 s86, s89, s86\n\t"
                 "s_cselect_b32 s87, 2, 3\n\ts_add_u32 s92, s92, s86\n\ts_addc_u32 s93, s93, 0\n"
                 "6:\n\ts_sub_u32 s87, s87, 1\n\t"
                 PD(d0, 92:93, 0, "") PD(d1, 92:93, 1024, "") PD(d2, 92:93, 2048, "")
                 "s_add_u32 m0, m0, 3072\n\ts_add_u32 s92, s92, 3072\n\ts_addc_u32 s93, s93, 0\n\ts_cmp_lg_u32 s87, 0\n\ts_cbranch_scc1 6b"
                 "\n8:\n\ts_waitcnt lgkmcnt(0)\n9:"
                 : "={s[64:79]}"(ka), "={s[80:81]}"(kb), "={s[82:83]}"(tent64),
                   [d0] "=&v"(doff[0]), [d1] "=&v"(doff[1]), [d2] "=&v"(doff[2]), [l16] "=&v"(l16), [db] "=&v"(db)
                 : [kp] "s"(__builtin_amdgcn_kernarg_segment_ptr()), [b] "s"(blockIdx.x), [w] "s"(wave),
                   [t0] "i"(swz_tbl(0)), [t1] "i"(swz_tbl(1)), [t2] "i"(swz_tbl(2)), [junk] "i"(JUNK), [img] "i"(IMG_A), [hr] "i"(H_R)
                 : "memory", "scc", "vcc", "s84", "s85", "s86", "s87", "s88", "s89", "s90", "s91", "s92", "s93", "s94", "s95", "s96", "s97", "s98", "s99");
  }
  const unsigned t0 = (unsigned)__builtin_amdgcn_s_memrealtime() - T0BACK;

  if (__builtin_expect(blockIdx.x < 6, 0)) {
    LANE_VARS
    DOFF_VARS
    const int j = blockIdx.x;
    const int qq = (lane >> 2) & 3, p4 = lane & 3, e4 = wave & 3;
    int ra = H_R + r * ROWB + ((g ^ swz(r)) << 4);
    bf16x8 a[6], b[6], w[18];
    auto rows = [&](int t, bf16x8* d) {
#pragma unroll
      for (int kk = 0; kk < 6; ++kk) d[kk] = *(LDS_AS(bf16x8)*)(L0 + t * 6144 + ((kk & 1) ? (ra ^ 64) : ra) + (kk >> 1) * 128);
    };
    int o[3];
#pragma unroll
    for (int t = 0; t < 3; ++t) o[t] = tr_off(4 * g + qq, 4 * (3 * e4 + t) + p4);
    unsigned qo = lbase + H_Q + 3 * e4 * 512 + ((g >> 1) * 16 + r) * 16 + (g & 1) * 8, qa = lbase + IMG_A + tr_off(r, 8 * wave + g);
    if (wave == 11) {
      unsigned d;
      asm volatile("s_load_dword %0, %1, 0x40\n\ts_barrier\n\ts_barrier\n\ts_barrier\n\ts_waitcnt lgkmcnt(0)" : "=&s"(d) : "s"(box + EPOCH_OFF) : "memory");
    } else
    asm volatile("s_waitcnt vmcnt(9)\n\ts_barrier" : PIN3(o, 0), "+v"(qo), "+v"(qa) :: "memory");
    if (wave < 6 || wave == 10) {
      rows((IMG_A - H_R) / 6144, a);
      const u32x2 m0 = *(LDS_AS(u32x2)*)(size_t)qa, m1 = *(LDS_AS(u32x2)*)(size_t)(qa ^ 32);
      static_assert((IMG_A - H_R) % 6144 == 0, "");
      const bf16x8 ones = __builtin_bit_cast(bf16x8, u32x4{0x3f803f80u, 0x3f803f80u, 0x3f803f80u, 0x3f803f80u});
      f32x4 sm = {0.f, 0.f, 0.f, 0.f};
#pragma unroll
      for (int i = 0; i < 6; ++i) sm = mfma(ones, a[i], sm);
      const float mean = sm[0] * (1.f / CC);
      if (__builtin_expect(wave < 6, 1)) {
        img_st(L0, H_C, wave, lane, __builtin_bit_cast(bf16x8, u32x4{cvtpk(bflo(m0[0]) - mean, bfhi(m0[0]) - mean), cvtpk(bflo(m0[1]) - mean, bfhi(m0[1]) - mean),
                                                                   cvtpk(bflo(m1[0]) - mean, bfhi(m1[0]) - mean), cvtpk(bflo(m1[1]) - mean, bfhi(m1[1]) - mean)}));
      } else {
        asm volatile("s_barrier" : PIN3(a, 0), PIN3(a, 3) :: "memory");
        f32x2 v2 = {0.f, 0.f}, v3 = v2;
#pragma unroll
        for (int i = 0; i < 12; ++i) {
          const u32x4 d = __builtin_bit_cast(u32x4, a[i >> 1]);
          const unsigned d0 = d[2 * (i & 1)], d1 = d[2 * (i & 1) + 1];
          const f32x2 x0 = f32x2{bflo(d0), bfhi(d0)} - mean, x1 = f32x2{bflo(d1), bfhi(d1)} - mean;
          v2 += x0 * x0; v3 += x1 * x1;
        }
        v2 += v3;
        *(LDS_AS(float)*)(L0 + CT_OFF + r * 4) = LOG2E * __builtin_amdgcn_rsqf(xrow16_sum(v2[0] + v2[1]) * (1.f / CC) + EPS);
      }
    }
    if (wave < 4) {
      wait_vm<0>();
      BARRIER();
#pragma unroll
      for (int kk = 0; kk < 6; ++kk) {
        b[kk] = img_ld(L0, H_C, kk, lane);
        w[kk] = mk8(tr_read(L0 + kk * 12288 + o[0]), tr_read(L0 + kk * 12288 + 6144 + o[0]));
        __builtin_amdgcn_sched_barrier(0);
      }
#pragma unroll
      for (int i = 6; i < 18; ++i) {
        w[i] = mk8(tr_read(L0 + (i % 6) * 12288 + o[i / 6]), tr_read(L0 + (i % 6) * 12288 + 6144 + o[i / 6]));
        __builtin_amdgcn_sched_barrier(0);
      }
#pragma unroll
      for (int t = 0; t < 3; ++t) {
        f32x4 D = {0.f, 0.f, 0.f, 0.f};
#pragma unroll
        for (int kk = 0; kk < 6; ++kk) D = mfma(w[6 * t + kk], b[kk], D);
        *(LDS_AS(u32x2)*)(size_t)(qo + t * 512) = u32x2{cvtpk(D[0], D[1]), cvtpk(D[2], D[3])};
      }
      BARRIER();
    } else if (wave == 8 || wave == 9) {
      unsigned so = lane * 16 + (2 * j + (wave & 1)) * 1024;
      unsigned epoch;
      asm volatile("global_load_dword %0, %1, %2 sc1\n\ts_barrier\n\ts_waitcnt vmcnt(0)" : "=v"(epoch) : "v"(EPOCH_OFF), "s"(box) : "memory");
      rows(wave & 1, a);
      unsigned ta = line_tag(epoch, 8), tb = line_tag(epoch, 9);
      asm volatile("" : PIN3(a, 0), PIN3(a, 3), "+v"(ta), "+v"(tb), "+v"(so));
      BARRIER();
#pragma unroll
      for (int kk = 0; kk < 6; ++kk) b[kk] = img_ld(L0, H_Q, kk, lane);
      const float sc = *(LDS_AS(float)*)(L0 + CT_OFF + r * 4);
      __builtin_amdgcn_sched_barrier(0);
      f32x4 D = {0.f, 0.f, 0.f, 0.f};
#pragma unroll
      for (int kk = 0; kk < 6; ++kk) D = mfma(a[kk], b[kk], D);
      D *= sc;
      const unsigned p0 = cvtpk(D[0], D[1]), p1 = cvtpk(D[2], D[3]);
      asm volatile("global_store_dwordx4 %0, %1, %2 sc1\n\ts_nop 1" : : "v"(so), "v"(u32x4{p0, p1, p0 ^ p1 ^ ta, tb}), "s"(box + QK_OFF) : "memory");
    } else if (wave != 11) {
      if (wave != 10) asm volatile("s_waitcnt vmcnt(0) lgkmcnt(0)\n\ts_barrier" ::: "memory");
      BARRIER();
    }
    BARRIER();
    issue_rows16(wo + wave * 6144, lbase + wave * 6144, doff);
    if (wave < 2) issue_rows16(wv + (2 * j + wave) * 6144, lbase + H_R + wave * 6144, doff);
    const unsigned epoch = epoch_load(box);
    BARRIER();
    {
      const int o0 = tr_off(8 * g + qq, 4 * wave + p4), o1 = tr_off(8 * g + 4 + qq, 4 * wave + p4);
#pragma unroll
      for (int kk = 0; kk < 6; ++kk) b[kk] = mk8(tr_read(L0 + kk * 12288 + o0), tr_read(L0 + kk * 12288 + o1));
      f32x4 D[2];
#pragma unroll
      for (int h = 0; h < 2; ++h) {
        rows(h, a);
        D[h] = f32x4{0.f, 0.f, 0.f, 0.f};
#pragma unroll
        for (int kk = 0; kk < 6; ++kk) D[h] = mfma(a[kk], b[kk], D[h]);
      }
      const int e = wave / 3;
      asm volatile("global_store_dwordx4 %0, %1, %2 sc1\n\ts_waitcnt vmcnt(0)"
                   : : "v"(lane * 16 + (e * 15 + 3 * j + wave) * 1024), "v"(pack8(D[0] * SQRT_C, D[1] * SQRT_C)), "s"(box + W2_OFF) : "memory");
    }
    BARRIER();
    if (wave == 11)
      asm volatile("s_mov_b64 exec, 1\n\tglobal_store_dwordx2 %0, %1, %2 sc1\n\ts_mov_b64 exec, -1\n\ts_nop 1"
                   : : "v"(j * 128), "v"(u32x2{line_tag(epoch, 10), line_tag(epoch, 11)}), "s"(box + W2F_OFF) : "memory");
  }

  int rslot = -1;
  if (__builtin_expect(wave == 0, 1)) {
    LANE_VARS
    DOFF_VARS
    if (helper) BARRIER();
    const unsigned afull = ct + C_FULL + r * 4;
    unsigned ts = 0;
    {
      const int ti = helper ? 1023 : (lane * 31 + (int)(blockIdx.x >> 3) - 1) & 1023;
      const int sp = ti >> 5, yy = sp * 8 + ((blockIdx.x + sp) & 7), xx = (ti + 5 * sp) & 31;
      unsigned tmp, pub = helper ? 2 * (TPW - 1) : 0, pre = 0, afr = ct + C_FREE + (helper ? ((TPW - 1) % NSLOT) * 4 : 0), e0, e1, e2, g0, g1, g2;
      asm volatile("s_mov_b32 s44, %[due0]\n\ts_mov_b32 s46, %[y0]\n\ts_mov_b32 s47, %[cap]\n\ts_mov_b32 s48, %[i0]\n\ts_mov_b32 s49, %[s0]\n\t"
                   "s_mov_b32 s51, 0\n\ts_mov_b32 s45, 0\n\ts_movk_i32 s39, %[p3s]\n"
                   "1:\n\tv_readlane_b32 s53, %[ob], s48\n\tv_readlane_b32 s52, %[oa], s48\n\ts_add_u32 m0, s49, 0x3000\n\tv_cmp_gt_u32 vcc, s51, %[pre]\n\t"
                   "v_add_u32 %[g0], s53, %[d0]\n\tv_add_u32 %[g1], s53, %[d1]\n\tv_add_u32 %[g2], s53, %[d2]\n\t"
                   "v_add_u32 %[e0], s52, %[d0]\n\tv_add_u32 %[e1], s52, %[d1]\n\tv_add_u32 %[e2], s52, %[d2]\n\t"
                   "s_cbranch_vccnz 5f\n\t"
                   "s_cmp_eq_u32 s48, 0\n\ts_cbranch_scc1 2f\n\ts_cmp_eq_u32 s48, 2\n\ts_cbranch_scc1 8f\n"
                   LD_GATE(1, "")
                   LD1(g0, 68:69, 0) LD1(g1, 68:69, 1024) LD1(g2, 68:69, 2048) "s_mov_b32 m0, s49\n\ts_nop 0\n\t" LD1(e0, 66:67, 0) LD1(e1, 66:67, 1024)
                   LD_NEXT
                   "\n2:\n\tv_add_u32 %[afr], 4, %[afr]\n\ts_cmp_eq_u32 s49, %[lasts]\n\ts_cbranch_scc1 7f\n"
                   "3:\n\tv_mov_b32 %[pre], 0\n\ts_add_u32 m0, s49, 0x800\n\tds_read_b32 %[pre], %[afr] ; NOTRACK\n"
                   LD_GATE(2, "")
                   LD1(e2, 56:57, 0) LD1(e0, 56:57, 1024) LD1(e1, 56:57, 2048) LD1(e2, 56:57, 3072) "s_add_u32 m0, s49, 0x1800\n\ts_add_u32 s48, s48, 1\n\t" LD1(e0, 58:59, 0)
                   LD_NEXT
                   LD_GATE(3, "s_cmp_gt_u32 s48, %[tpw] - 2\n\ts_cbranch_scc1 38f\n39:\n\t")
                   LD1(e1, 58:59, 1024) LD1(e2, 58:59, 2048) LD1(e0, 58:59, 3072) "s_add_u32 m0, s49, 0x2800\n\ts_add_u32 s49, s49, %[slot]\n\t" LD1(e1, 60:61, 0) LD1(e2, 60:61, 1024)
                   LD_NEXT
                   "s_cmp_eq_u32 s49, %[ring]\n\ts_cselect_b32 s49, 0, s49\n\ts_cmp_gt_i32 s46, 30\n\ts_cbranch_scc1 6f\n"
                   "4:\n\ts_cmp_eq_u32 s48, %[pk]\n\ts_cmovk_i32 s39, %[p3]\n\ts_cmp_lg_u32 s48, %[tpw]\n\ts_cbranch_scc1 1b\n\ts_branch 9f\n"
                   LD_PUB(1, 0, 15, 30) LD_PUB(2, 5, 20, 35) LD_PUB(3, 10, 25, 40)
                   "38:\n\ts_cmp_eq_u32 s46, 10\n\ts_cselect_b32 s43, 23, 0\n\ts_cmp_lt_u32 s42, s43\n\ts_cbranch_scc0 39b\n\tv_add_u32 %[tmp], 1, %[pub]\n\ts_waitcnt vmcnt(22)\n\tds_write_b32 %[af], %[tmp] ; NOTRACK\n\ts_branch 39b\n"
                   "5:\n\ts_movk_i32 s40, 0x100\n"
                   "40:\n\tds_read_b32 %[pre], %[afr]\n\t" LD_VM
                   "s_sub_u32 s40, s40, 1\n\ts_cselect_b32 s42, 0, s42\n\ts_cselect_b32 s40, 0, s40\n\t"
                   "s_cmp_le_i32 s42, s46\n\ts_cbranch_scc1 42f\n"
                   "41:\n\ts_sub_i32 s47, s47, 1\n\ts_cmp_lt_i32 s47, 0\n\ts_waitcnt lgkmcnt(0)\n\ts_cbranch_scc1 10b\n\t"
                   "v_cmp_gt_u32 vcc, s51, %[pre]\n\ts_cbranch_vccnz 40b\n\ts_branch 10b\n"
                   LD_PUB0(4, 0, 15, 30)
                   "6:\n\ts_waitcnt vmcnt(45)\n\tv_add_u32 %[pub], 2, %[pub]\n\tds_write_b32 %[af], %[pub]\n\ts_sub_u32 s46, s46, 15\n\ts_branch 4b\n"
                   "7:\n\ts_waitcnt lgkmcnt(0)\n\ts_add_u32 s51, s51, 8\n\tv_subrev_u32 %[afr], %[frb], %[afr]\n\ts_branch 3b\n"
                   "8:\n\ts_barrier\n\ts_add_u32 s46, s46, 64\n\ts_branch 10b\n"
                   "9:\n\ts_cmp_lt_i32 s46, 30\n\ts_cbranch_scc1 91f\n\ts_waitcnt vmcnt(30)\n\tv_add_u32 %[pub], 2, %[pub]\n\tds_write_b32 %[af], %[pub]\n"
                   "91:\n\ts_cmp_lt_i32 s46, 15\n\ts_cbranch_scc1 92f\n\tv_add_u32 %[tmp], 1, %[pub]\n\ts_waitcnt vmcnt(27)\n\tds_write_b32 %[af], %[tmp]\n"
                   "92:\n\ts_waitcnt lgkmcnt(0)"
                   : [pub] "+v"(pub), [pre] "+v"(pre), [afr] "+v"(afr), [ts] "+v"(ts), [tmp] "=&v"(tmp),
                     [e0] "=&v"(e0), [e1] "=&v"(e1), [e2] "=&v"(e2), [g0] "=&v"(g0), [g1] "=&v"(g1), [g2] "=&v"(g2)
                   : "{s[64:79]}"(ka), "{s[56:57]}"(f0 + 0x800), "{s[58:59]}"(f0 + 0x30000), "{s[60:61]}"(f0 + 0x31000), [t0] "s"(t0), [af] "v"(afull),
                     [d0] "v"(doff[0]), [d1] "v"(doff[1]), [d2] "v"(doff[2]), [oa] "v"(yy * 393216 + xx * 6144), [ob] "v"(yy * 98304 + xx * 3072),
                     [due0] "s"(helper ? HDUE * (PACE16 / 3) : PACES / 3), [y0] "s"(helper ? -15 : -74), [i0] "s"(helper ? TPW - 1 : 0),
                     [s0] "s"(helper ? ((TPW - 1) % NSLOT) * SLOT : 0),
                     [cap] "i"(SPIN_CAP), [win] "i"(PWIN), [ahi] "i"(AHI), [adl] "i"(ADL), [amg] "i"(AMG), [p3] "i"(PACE16 / 3), [p3s] "i"(PACES / 3), [pk] "i"(PK1), [slot] "i"(SLOT),
                     [ring] "i"(NSLOT * SLOT), [frb] "i"(NSLOT * 4), [lasts] "i"((NSLOT - 1) * SLOT), [tpw] "i"(TPW)
                   : "memory", "scc", "vcc", "s39", "s40", "s41", "s42", "s43", "s44", "s45", "s46", "s47", "s48", "s49", "s51", "s52", "s53", "s62", "s63");
    }
    lds_st32(ct + C_ISS, 1u);
    wait_vm<15>();
    lds_st32(afull, (unsigned)(2 * TPW - 2));
    wait_vm<12>();
    lds_st32(afull, (unsigned)(2 * TPW - 1));
    wait_vm<0>();
    lds_st32(afull, ~0u);
    {
      unsigned d;
      asm volatile("global_load_dword %0, %1, %2 sc1\n\ts_setprio 0\n\ts_waitcnt vmcnt(0)" : "=v"(d) : "v"(blockIdx.x * 128), "s"(box) : "memory");
    }
    rslot = 7;
  } else {
    if (wave == 1) {
      LANE_VARS
    {
      const unsigned o = lane * 8;
      unsigned z = (o >= C_MREF && o < C_MREF + 64) ? 0xff800000u : 0u;
      if (helper && o >= C_CONS && o < C_MREF) z = NE * ((TPW - 1) / NPK);
      u32x2 zz = {z, z};
      if (o >= C_PFL && o < C_CONS) zz = u32x2{(o - C_PFL) / 2 + 2, (o - C_PFL) / 2 + 4} + (helper ? 2u * NPK * ((TPW - 1) / NPK) : 0u);
      if (o == C_EP) zz[1] = QKW0 + QKW1 * min(t0 + T0BACK - tent, 45u);
      *(LDS_AS(u32x2)*)(L0 + CT_OFF + o) = zz;
      *(LDS_AS(u32x2)*)(L0 + ONES_OFF + (lane < 48 ? o : 0)) = u32x2{0x3f803f80u, 0x3f803f80u};
    }
    }
    if (helper || wave == 1) BARRIER();
    if (wave <= NE) {
      LANE_VARS
      const int ej = wave - 1;
      const int qq = (lane >> 2) & 3, p4 = lane & 3;
      const unsigned apk = lbase + IMG_A + lane * 16;
      const unsigned epoch = epoch_load(box);
      __builtin_amdgcn_s_setprio(2);
      unsigned long long tpv, tp7, tx1;
      {
        static_assert((TPW & 1) == 1 && C_CONS == C_PFL + 64 && C_FREE == C_FULL + 64, "");
        const int k0 = helper ? TPW - 1 : 0;
        unsigned need = 2 * k0 + 2;
        asm volatile("s_mov_b32 s46, 0\n\ts_mov_b64 exec, 0\n\ts_nop 3\n\ts_branch 99f\n"
                     "2:\n\ts_mov_b64 exec, -1\n\ts_nop 7\n\t" PV_Z4(72, 73, 74, 75) PV_Z4(76, 77, 78, 79) PV_Z4(80, 81, 82, 83) PV_Z4(84, 85, 86, 87)
                     "v_mov_b32 v88, 0xff800000\n\t"
                     "s_mov_b32 s46, %[k0]\n\ts_mov_b32 s45, %[s0]\n\ts_mov_b32 s44, %[e0]\n\ts_mov_b32 s47, %[cap]\n\t"
                     "s_cmp_lg_u32 s46, 0\n\ts_cbranch_scc1 32f\n"
                     "3:\n\tv_mov_b32 v148, %[ca]\n\t" PV_WAITF(4, 148) PV_FETCH_A
                     "s_mov_b64 exec, 2\n\tds_add_u32 v148, %[one] offset:64\n\ts_mov_b64 exec, -2\n\t"
                     "v_add_u32 %[need], 2, %[need]\n\tv_add_u32 v148, 4, v148\n\ts_mov_b64 exec, -1\n\t"
                     PV_WAITF(5, 148)
                     "v_mov_b32 v149, v148\n\ts_mov_b64 exec, -2\n\tv_add_u32 %[need], 2, %[need]\n\tv_add_u32 v148, 4, v148\n\t"
                     "s_mov_b64 exec, 1\n\tv_add_u32 %[need], %[npk2], %[need]\n\ts_mov_b64 exec, -1\n\t"
                     PV_REQ("126:129", "130:133", "134:135", 136, 148) PV_FETCH_B
                     "s_mov_b64 exec, 2\n\tds_add_u32 v149, %[one] offset:64\n\ts_mov_b64 exec, -1\n"
                     "6:\n\t"
                     PV_STEP(1, "126:129", "130:133", "134:135", 134, 135, 136, 148, "138:141", "142:145", "146:147", 137, 149,
                             "92:95", "98:101", "104:107", "90:93", "96:99", "102:105",
                             PV_MOV(92, 90) PV_MOV(93, 91), PV_MOV(98, 96) PV_MOV(99, 97), PV_MOV(104, 102) PV_MOV(105, 103),
                             "s_cmp_eq_u32 s46, %[last]\n\ts_cbranch_scc1 99f\n\t")
                     "s_cbranch_vccnz 7f\n"
                     "16:\n\t" PV_FETCH_A PV_REL(148)
                     "s_lshl1_add_u32 s54, s46, -2\n\ts_lshr_b64 s[54:55], %[sch], s54\n\ts_and_b32 s54, s54, 15\n\ts_cbranch_scc1 31f\n"
                     "30:\n\t"
                     PV_STEP(2, "138:141", "142:145", "146:147", 146, 147, 137, 149, "126:129", "130:133", "134:135", 136, 148,
                             "110:113", "116:119", "122:125", "108:111", "114:117", "120:123",
                             PV_MOV(110, 108) PV_MOV(111, 109), PV_MOV(116, 114) PV_MOV(117, 115), PV_MOV(122, 120) PV_MOV(123, 121),
                             "s_cmp_eq_u32 s46, %[last] - 1\n\ts_cbranch_scc1 98f\n\t")
                     "s_cbranch_vccnz 8f\n"
                     "26:\n\t" PV_FETCH_B PV_REL(149)
                     "s_branch 6b\n"
                     PV_WAITF(7, 148) "s_branch 16b\n"
                     PV_WAITF(8, 149) "s_branch 26b\n"
                     "98:\n\t" PV_MF("76:79", "114:117", "138:141") PV_MF("80:83", "120:123", "138:141")
                     "v_add_u32 %[need], 2, %[need]\n\ts_add_u32 s46, s46, 1\n\ts_branch 13f\n"
                     PV_SLOW(1, "126:129", "130:133", "134:135", 134, 135, 136, 148, 126, 127, 128, 129)
                     PV_SLOW(2, "138:141", "142:145", "146:147", 146, 147, 137, 149, 138, 139, 140, 141)
                     "31:\n\ts_cmp_lg_u32 s54, 8\n\ts_cbranch_scc1 34f\n"
                     "32:\n\tv_mbcnt_lo_u32_b32 v152, 31, 0\n\tv_lshlrev_b32 v152, 7, v152\n\ts_mov_b32 s55, %[gcap]\n"
                     "33:\n\tglobal_load_dwordx2 v[150:151], v152, %[fl] sc1\n\ts_sub_u32 s55, s55, 1\n\ts_cselect_b32 s56, 0, -1\n\ts_waitcnt vmcnt(0) lgkmcnt(0)\n\t"
                     "v_cmp_ne_u64 s[48:49], %[tag], v[150:151]\n\ts_sleep 2\n\ts_and_b32 s48, s48, s56\n\ts_cbranch_scc1 33b\n\t"
                     "s_mov_b32 s57, 0\n\tv_mov_b32 v150, %[apk]\n\ts_cmp_lg_u32 s46, %[last]\n\ts_cbranch_scc1 30b\n\ts_mov_b32 s54, 7\n\ts_branch 38f\n"
                     "34:\n\ts_cmp_lg_u32 s46, %[last] - 3\n\ts_cbranch_scc1 36f\n\tv_mov_b32 v151, %[iss]\n"
                     "35:\n\tds_read_b32 v150, v151\n\ts_sub_i32 s47, s47, 1\n\ts_cmp_lt_i32 s47, 0\n\ts_cselect_b32 s55, 0, 1\n\ts_waitcnt lgkmcnt(0)\n\t"
                     "v_readfirstlane_b32 s56, v150\n\ts_cmp_lt_u32 s56, s55\n\ts_cbranch_scc1 35b\n"
                     "36:\n\ts_mul_i32 s55, s57, 3072\n\tv_add_u32 v150, s55, %[apk]\n\ts_getpc_b64 s[48:49]\n"
                     "37:\n\ts_mul_i32 s55, s57, 40\n\ts_add_u32 s55, s55, 38f-37b\n\ts_add_u32 s48, s48, s55\n\ts_addc_u32 s49, s49, 0\n\t"
                     "s_add_u32 s57, s57, s54\n\ts_sub_u32 s54, s54, 1\n\ts_setpc_b64 s[48:49]\n"
                     "38:\n\t" PV_W2(0, 3, 4, 7, 8, 11) PV_W2(12, 15, 16, 19, 20, 23) PV_W2(24, 27, 28, 31, 32, 35) PV_W2(36, 39, 40, 43, 44, 47)
                     PV_W2(48, 51, 52, 55, 56, 59) PV_W2(60, 63, 64, 67, 68, 71)
                     "s_waitcnt lgkmcnt(0)\n\ts_branch 3b\n"
                     "99:\n\t" PV_MF("76:79", "96:99", "126:129") PV_MF("80:83", "102:105", "126:129")
                     "v_add_f32 v153, v86, v87\n\tv_add_f32 v151, v84, v85\n\tv_rcp_f32 v150, v153\n\tv_mul_f32 v151, %[nic], v151\n\t"
                     "v_add_u32 v152, %[aw], %[apk]\n\tv_mul_f32 v151, v151, v150\n\t"
                     PV_NRM(72, 73, 74, 75, 0) PV_NRM(76, 77, 78, 79, 2048) PV_NRM(80, 81, 82, 83, 4096)
                     "v_mov_b32 v154, %[x1]\n\ts_and_b64 exec, exec, 1\n\tds_add_u32 v154, %[one]\n\ts_cselect_b64 exec, -1, 0\n\ts_cselect_b32 s55, %[x1n], 0\n\t"
                     "v_log_f32 v153, v153\n\tv_subrev_f32 v88, %[toff], v88\n\tv_lshrrev_b32 v151, 1, %[apm]\n\tv_add_f32 v88, v88, v153\n\tv_add_u32 v151, %[lwo], v151\n\t"
                     "v_mbcnt_lo_u32_b32 v155, -1, 0\n\tv_mbcnt_hi_u32_b32 v155, -1, v155\n\tds_write_b32 v151, v88\n\t"
                     "v_and_b32 v151, 1, v155\n\tv_and_b32 v155, 48, v155\n\tv_lshl_add_u32 v155, v151, 2, v155\n\tv_add_u32 v155, %[lw], v155\n\t"
                     "ds_read2_b32 v[114:115], v155 offset1:2\n\tv_add_u32 v152, %[ar], %[apk]\n"
                     "97:\n\tds_read_b32 v150, v154\n\tds_read_b128 v[90:93], v152\n\tds_read_b128 v[94:97], v152 offset:1024\n\tds_read_b128 v[98:101], v152 offset:2048\n\t"
                     "ds_read_b128 v[102:105], v152 offset:3072\n\tds_read_b128 v[106:109], v152 offset:4096\n\tds_read_b128 v[110:113], v152 offset:5120\n\t"
                     "s_sub_i32 s47, s47, 1\n\ts_cmp_lt_i32 s47, 0\n\ts_cselect_b32 s55, 0, s55\n\ts_waitcnt vmcnt(0) lgkmcnt(6)\n\t"
                     "v_readfirstlane_b32 s56, v150\n\ts_cmp_lt_u32 s56, s55\n\ts_cbranch_scc1 97b\n\ts_cmp_eq_u32 s46, 0\n\t"
                     PV_G(5, "90:93", "0:3", "4:7", "8:11", "0", "0", "0")
                     PV_G(4, "94:97", "12:15", "16:19", "20:23", "v[72:75]", "v[76:79]", "v[80:83]")
                     PV_G(3, "98:101", "24:27", "28:31", "32:35", "v[72:75]", "v[76:79]", "v[80:83]")
                     PV_G(2, "102:105", "36:39", "40:43", "44:47", "v[72:75]", "v[76:79]", "v[80:83]")
                     PV_G(1, "106:109", "48:51", "52:55", "56:59", "v[72:75]", "v[76:79]", "v[80:83]")
                     PV_G(0, "110:113", "60:63", "64:67", "68:71", "v[72:75]", "v[76:79]", "v[80:83]")
                     "v_xor_b32 v114, v114, %[ltag]\n\tv_xor_b32 v115, v115, %[ltag]\n\ts_nop 3\n\t"
                     "v_cvt_pk_bf16_f32 v116, v72, v73\n\tv_cvt_pk_bf16_f32 v120, v74, v75\n\tv_cvt_pk_bf16_f32 v117, v76, v77\n\tv_cvt_pk_bf16_f32 v121, v78, v79\n\t"
                     "v_cvt_pk_bf16_f32 v118, v80, v81\n\tv_bitop3_b32 v119, v116, v117, v114 bitop3:0x96\n\tv_cvt_pk_bf16_f32 v122, v82, v83\n\t"
                     "v_bitop3_b32 v123, v120, v121, v115 bitop3:0x96\n\tv_xor_b32 v119, v119, v118\n\tv_xor_b32 v123, v123, v122\n\t"
                     "s_cbranch_scc1 2b\n\t"
                     "global_store_dwordx4 %[so], v[116:119], %[bx] sc1\n\tglobal_store_dwordx4 %[so], v[120:123], %[bx2] sc1\n\t"
                     "s_waitcnt lgkmcnt(0)\n\ts_nop 0"
                     : [need] "+v"(need), "={s[58:59]}"(tpv), "={s[60:61]}"(tp7), "={s[62:63]}"(tx1)
                     : [so] "v"(((ej * 16 + (lane >> 3)) * RSTR + blockIdx.x) * 128 + (lane & 7) * 16), [ltag] "v"(line_tag(epoch, lane & 7)),
                       [bx] "s"(box), [bx2] "s"(box + 8 * RSTR * 128), [aw] "s"(A_IMG - IMG_A + (ej >> 1) * 1024 + (ej & 1) * 8), [ar] "i"(A_IMG - IMG_A),
                       [nic] "i"(__builtin_bit_cast(unsigned, -1.f / CC)), [toff] "i"(__builtin_bit_cast(unsigned, TOFF)),
                       [lwo] "i"(CT_OFF + C_LW - PKM / 2), [lw] "i"(CT_OFF + C_LW), [x1] "i"(CT_OFF + C_X1), [x1n] "i"(NE),
                       [apk] "v"(apk), [apm] "v"(lbase + PKM + r * 8), [apu] "v"(lbase + PKU + g * 64 + (r & 3) * 16),
                       [ox] "v"(lbase + tr_off(4 * g + qq, 4 * ej + p4)), [oy] "v"(lbase + 32 * ROWB + tr_off(2 * g + (qq >> 1), 4 * ej + p4)),
                       [ca] "v"(ct + (lane == 0 ? C_PFL + (k0 % NPK) * 4 : C_FULL + (k0 % NSLOT) * 4)),
                       [lim] "v"(ct + (lane == 0 ? C_PFL + NPK * 4 : C_FULL + NSLOT * 4)), [base] "v"(ct + (lane == 0 ? C_PFL : C_FULL)), [one] "v"(1u),
                       [k0] "s"(k0), [s0] "s"(helper ? ((TPW - 1) % NSLOT) * SLOT : 0), [e0] "s"(helper ? ((TPW - 1) % NPK) * 128 : 0),
                       [w2] "s"(box + W2_OFF + ej * 18432 - (lbase + IMG_A)), [fl] "s"(box + W2F_OFF),
                       [tag] "s"(u32x2{line_tag(epoch, 10), line_tag(epoch, 11)}), [gcap] "i"(GSPIN_CAP), [iss] "i"(CT_OFF + C_ISS), [cap] "i"(SPIN_CAP), [slot] "i"(SLOT), [ring] "i"(NSLOT * SLOT), [pring] "i"(NPK * 128),
                       [npk2] "i"(2 * NPK), [last] "i"(TPW - 1), [tight] "i"(TPW - 5),  [sch] "s"(W2SCH)
                     : "memory", "scc", "vcc", "s44", "s45", "s46", "s47", "s48", "s49", "s50", "s51", "s52", "s53", "s54", "s55", "s56", "s57",
                       "v0", "v1", "v2", "v3", "v4", "v5", "v6", "v7", "v8", "v9", "v10", "v11", "v12", "v13", "v14", "v15", "v16", "v17", "v18", "v19", "v20", "v21", "v22", "v23", "v24", "v25", "v26", "v27", "v28", "v29", "v30", "v31", "v32", "v33", "v34", "v35", "v36", "v37", "v38", "v39", "v40", "v41", "v42", "v43", "v44", "v45", "v46", "v47", "v48", "v49", "v50", "v51", "v52", "v53", "v54", "v55", "v56", "v57", "v58", "v59", "v60", "v61", "v62", "v63", "v64", "v65", "v66", "v67", "v68", "v69", "v70", "v71", "v72", "v73", "v74", "v75", "v76", "v77", "v78", "v79", "v80", "v81", "v82", "v83", "v84", "v85", "v86", "v87", "v88",
                       "v90", "v91", "v92", "v93", "v94", "v95", "v96", "v97", "v98", "v99", "v100", "v101", "v102", "v103", "v104", "v105", "v106", "v107",
                       "v108", "v109", "v110", "v111", "v112", "v113", "v114", "v115", "v116", "v117", "v118", "v119", "v120", "v121", "v122", "v123",
                       "v124", "v125", "v126", "v127", "v128", "v129", "v130", "v131", "v132", "v133", "v134", "v135", "v136", "v137", "v138", "v139",
                       "v140", "v141", "v142", "v143", "v144", "v145", "v146", "v147", "v148", "v149", "v150", "v151", "v152", "v153", "v154", "v155");
      }
    } else {
      LANE_VARS
      const int c = wave - 1 - NE;
      SCtx sc;
      sc.L0 = L0; sc.ct = ct; sc.apk = lbase + IMG_A + lane * 16; sc.apm = lbase + PKM + r * 8;
      sc.api = lbase + PKU + g * 64 + (r & 3) * 2 + ((r >> 2) == g ? 0 : 8); sc.amr = ct + C_MREF + r * 4;
      sc.o_rb = r * ROWB + ((g ^ swz(r)) << 4);
      sc.ev = (r & 1) ^ 1;
      sc.o_yb = (r & 1) ? ONES_OFF + (g << 4) : (32 + (r >> 1)) * ROWB + ((g ^ swz(r >> 1)) << 4);
      if (c >= NEARLY) {
        static_assert(NS - NEARLY == 3, "");
        const int u = c - NEARLY;
        const unsigned epoch = epoch_load(box);
        const unsigned ta = line_tag(epoch, 8), tb = line_tag(epoch, 9);
        const unsigned qw = lds_ld32(ct + C_EP + 4);
        while ((unsigned)__builtin_amdgcn_s_memrealtime() - tent < qw) __builtin_amdgcn_s_sleep(4);
        u32x4 v[4];
#pragma unroll 1
        for (int sp = 0; sp < GSPIN_CAP; ++sp) {
          asm volatile("global_load_dwordx4 %0, %4, %5 sc1\n\tglobal_load_dwordx4 %1, %4, %5 offset:1024 sc1\n\tglobal_load_dwordx4 %2, %4, %5 offset:2048 sc1\n\t"
                       "global_load_dwordx4 %3, %4, %5 offset:3072 sc1\n\ts_waitcnt vmcnt(0)"
                       : "=&v"(v[0]), "=&v"(v[1]), "=&v"(v[2]), "=&v"(v[3]) : "v"(lane * 16), "s"(box + QK_OFF + u * 4096) : "memory");
          unsigned bad = 0;
#pragma unroll
          for (int t = 0; t < 4; ++t) bad |= (v[t][0] ^ v[t][1] ^ v[t][2] ^ ta) | (v[t][3] ^ tb);
          if (!__any(bad != 0u)) break;
        }
#pragma unroll
        for (int t = 0; t < 4; ++t)
          *(LDS_AS(u32x2)*)(L0 + IMG_A + (4 * u + t) * 512 + ((g >> 1) * 16 + r) * 16 + (g & 1) * 8) = u32x2{v[t][0], v[t][1]};
        asm volatile("" ::: "memory");
        lds_add(ct + C_QI, 1u);
        lds_st32(ct + C_EP, epoch);
      }
      static_assert(NE == 4 && NPK == 6 && NSLOT == 10, "");
      s_items(sc, helper ? TPW : c, helper || c >= NEARLY);
      static_assert(NH1 == 3 && NS == 7 && TPW == 33, "");
      const int hi = c == 0 ? 5 : c - 2;
      if (hi >= (helper ? 4 : 0)) s_half(sc, TPW - NH1 + (hi >> 1), hi & 1);
      rslot = c;
    }
  }

  if (rslot >= 0 && ((blockIdx.x >> 3) & 3) == 1) {
    LANE_VARS
    __builtin_amdgcn_s_setprio(0);
    const int rid = ((blockIdx.x >> 5) << 3) | (blockIdx.x & 7);
    const int qa = ((rid >> 1) & 3) * 4 + ((rid >> 3) & 1) * 2;
    const char* src = box + (size_t)(rid * RSTR + rslot * 32) * 128;
    const int e = lane & 7;
    const int oidx = (qa + (e & 1)) * CC + 48 * (rid >> 4) + 16 * (e < 6 ? e >> 1 : 0) + 8 * (rid & 1) + (lane >> 3);
    const unsigned short qv = ((const gus*)q)[oidx];
    u32x4 am;
#pragma unroll
    for (int d = 0; d < 4; ++d) am[d] = (e >> 1) == d ? ((e & 1) ? 0xffff0000u : 0x0000ffffu) : 0u;
    const unsigned epoch = *(LDS_AS(unsigned)*)(L0 + CT_OFF + C_EP);
    const unsigned arf = lbase + RED_OFF + 4096 + (lane & 1) * 4;
    poll_ge_sleep(ct + C_FREE + ((TPW - NPK) % NSLOT) * 4, 8u * ((TPW - NPK) / NSLOT + 1));
    lds_st32(arf + rslot * 8, 0u);
    unsigned long long tr;
#define R_LD(d, m, o) "s_mov_b64 exec, s[" m "]\n\tglobal_load_dwordx4 v[" d "], %[vo], %[src] offset:" #o " sc1\n\t"
#define R_W(l, a0, a1, a2, a3) "v_cvt_pk_bf16_f32 v" #a3 ", v" #a3 ", v" #a3 "\n\tv_and_b32 v" #a0 ", v60, v" #a3 "\n\tv_and_b32 v" #a1 ", v61, v" #a3 "\n\t" \
                               "v_and_b32 v" #a2 ", v62, v" #a3 "\n\tv_and_b32 v" #a3 ", v63, v" #a3 "\n\t"
#define R_G(n, w, m, l, d0, d1, d2, d3, a0, a1, a2, a3, c) \
  "s_mov_b64 exec, s[" m "]\n\ts_cbranch_execz " #n "f\n\ts_waitcnt vmcnt(" #w ")\n\tv_bitop3_b32 v" #l ", v" #d0 ", v" #d1 ", v" #d2 " bitop3:0x96\n\t" \
  "v_bitop3_b32 v" #l ", v" #l ", v" #d3 ", %[tag] bitop3:0x96\n\tv_mov_b32 v" #d3 ", s49\n\ts_mov_b64 exec, -1\n\tv_exp_f32 v" #a3 ", v" #l "\n\t" \
  "v_xor_b32_dpp v59, v" #l ", v" #l " quad_perm:[2,3,0,1] row_mask:0xf bank_mask:0x5\n\tv_xor_b32_dpp v59, v" #l ", v" #l " row_shr:4 row_mask:0xf bank_mask:0xa\n\t" \
  R_W(l, a0, a1, a2, a3) "v_cmp_ne_u32 s[" m "], 0, v59\n\ts_nop 0\n" \
  "1" #n ":\n\tv_mfma_f32_16x16x32_bf16 v[36:39], v[" #a0 ":" #a3 "], v[" #d0 ":" #d3 "], " c "\n\t"
#define R_X(n) #n ":\n\ts_mov_b64 exec, -1\n\ts_nop 2\n\ts_branch 1" #n "b\n"
#define R_F(l, d0, d3, a0, a1, a2, a3, c) "v_sub_f32 v" #a3 ", v" #l ", v56\n\tv_exp_f32 v" #a3 ", v" #a3 "\n\ts_nop 0\n\t" R_W(l, a0, a1, a2, a3) \
  "s_nop 1\n\tv_mfma_f32_16x16x32_bf16 v[36:39], v[" #a0 ":" #a3 "], v[" #d0 ":" #d3 "], " c "\n\t"
#define R_RD(d, a, b) "ds_read2st64_b32 v[" d "], %[ard] offset0:" #a " offset1:" #b "\n\t"
#define R_A(d, a) "v_pk_add_f32 v[" d "], v[" d "], v[" a "]\n\t"
#define R_S(x, f) "v_sub_f32 v" #f ", v" #f ", v56\n\tv_exp_f32 v" #f ", v" #f "\n\ts_nop 0\n\tv_mul_f32 v[" #x "], v[" #x "], v" #f "\n\tv_mul_f32 v[" #x " + 1], v[" #x " + 1], v" #f "\n\t"
    asm volatile("s_mov_b64 s[52:53], 0\n\ts_mov_b64 s[40:41], -1\n\ts_mov_b64 s[42:43], -1\n\ts_mov_b64 s[44:45], -1\n\ts_mov_b64 s[46:47], -1\n\ts_mov_b32 s48, %[cap]\n\ts_mov_b32 s49, 0x3f803f80\n\t"
                 "v_mov_b32 v57, 1\n\tv_mov_b32 v58, %[x1]\n"
                 "20:\n\ts_sleep 1\n\tds_read_b32 v56, v58\n\ts_sub_u32 s48, s48, 1\n\ts_cselect_b32 s50, 0, %[x1n]\n\ts_waitcnt lgkmcnt(0)\n\t"
                 "v_readfirstlane_b32 s51, v56\n\ts_cmp_lt_u32 s51, s50\n\ts_cbranch_scc1 20b\n\tv_mov_b32 v58, %[red]\n\ts_mov_b32 s48, %[gcap]\n\ts_sleep %[rs]\n"
                 "21:\n\t" R_LD("0:3", "40:41", 0) R_LD("4:7", "42:43", 1024) R_LD("8:11", "44:45", 2048) R_LD("12:15", "46:47", 3072)
                 "s_cmp_eq_u32 s48, %[gcap]\n\ts_cbranch_scc1 24f\n\ts_waitcnt vmcnt(0)\n24:\n\t"
                 R_G(1, 3, "40:41", 32, 0, 1, 2, 3, 16, 17, 18, 19, "0") R_G(2, 2, "42:43", 33, 4, 5, 6, 7, 20, 21, 22, 23, "v[36:39]")
                 R_G(3, 1, "44:45", 34, 8, 9, 10, 11, 24, 25, 26, 27, "v[36:39]") R_G(4, 0, "46:47", 35, 12, 13, 14, 15, 28, 29, 30, 31, "v[36:39]")
                 "v_max3_f32 v56, v32, v33, v34\n\ts_sub_u32 s48, s48, 1\n\ts_cbranch_scc1 22f\n\ts_or_b64 s[50:51], s[40:41], s[42:43]\n\tv_max_f32 v56, v56, v35\n\t"
                 "s_or_b64 s[50:51], s[50:51], s[44:45]\n\ts_or_b64 s[50:51], s[50:51], s[46:47]\n\tv_cmp_nlt_f32 vcc, |v56|, %[lim]\n\ts_cbranch_scc1 21b\n\t"
                 "s_cbranch_vccnz 25f\n"
                 "22:\n\ts_mov_b64 exec, %[wm]\n\tds_write_b128 %[awr], v[36:39]\n\ts_mov_b64 exec, 1\n\tds_add_rtn_u32 v59, v58, v57\n\ts_mov_b64 exec, -1\n\t"
                 R_RD("40:41", 0, 1) R_RD("42:43", 2, 3) R_RD("44:45", 4, 5) R_RD("46:47", 6, 7) R_RD("48:49", 8, 9) R_RD("50:51", 10, 11) R_RD("52:53", 12, 13) R_RD("54:55", 14, 15)
                 "s_waitcnt lgkmcnt(8)\n\tv_add_u32 v59, v59, v57\n\ts_nop 0\n\tv_readfirstlane_b32 s50, v59\n\ts_cmp_lg_u32 s50, 8\n\ts_cbranch_scc1 26f\n"
                 "23:\n\ts_waitcnt lgkmcnt(6)\n\t" R_A("40:41", "42:43") "s_waitcnt lgkmcnt(4)\n\t" R_A("44:45", "46:47") "s_waitcnt lgkmcnt(2)\n\t" R_A("48:49", "50:51")
                 R_A("40:41", "44:45") "s_waitcnt lgkmcnt(0)\n\t" R_A("52:53", "54:55") R_A("48:49", "52:53") R_A("40:41", "48:49")
                 "v_add_f32 v40, v40, v41\n\ts_nop 1\n\tv_mov_b32_dpp v41, v40 quad_perm:[2,3,2,3] row_mask:0xf bank_mask:0xf\n\ts_nop 1\n\t"
                 "v_mov_b32_dpp v41, v41 row_shl:4 row_mask:0xf bank_mask:0x5\n\tv_rcp_f32 v41, v41\n\ts_mov_b64 exec, %[om]\n\tv_mul_f32 v40, v40, v41\n\t"
                 "v_cvt_pk_bf16_f32 v40, v40, v40\n\tv_lshlrev_b32 v40, 16, v40\n\tv_add_f32 v40, v40, %[qf]\n\tv_cvt_pk_bf16_f32 v40, v40, v40\n\t"
                 "global_store_short %[oo], v40, %[out]\n\t"
                 "s_cmp_lg_u32 %[rid], 0\n\ts_cbranch_scc1 29f\n\ts_mov_b64 exec, 1\n\tv_mov_b32 v41, %[eo]\n\tglobal_store_dword v41, %[ep], %[bx] sc1\n\ts_branch 29f\n"
                 R_X(1) R_X(2) R_X(3) R_X(4)
                 "25:\n\tv_max_f32_dpp v56, v56, v56 row_ror:8 row_mask:0xf bank_mask:0xf\n\tv_mov_b32 v59, v56\n\ts_nop 1\n\tv_permlane16_swap_b32 v56, v59\n\tv_max_f32 v56, v56, v59\n\t"
                 "v_mov_b32 v59, v56\n\ts_nop 1\n\tv_permlane32_swap_b32 v56, v59\n\tv_max_f32 v56, v56, v59\n\tv_mov_b32 v57, 0x101\n\tv_add_u32 v59, %[rs8], %[arf]\n\tds_write_b32 v59, v56\n\t"
                 R_F(32, 0, 3, 16, 17, 18, 19, "0") R_F(33, 4, 7, 20, 21, 22, 23, "v[36:39]") R_F(34, 8, 11, 24, 25, 26, 27, "v[36:39]") R_F(35, 12, 15, 28, 29, 30, 31, "v[36:39]")
                 "s_nop 7\n\ts_branch 22b\n"
                 "26:\n\ts_waitcnt vmcnt(0)\n\ts_and_b32 s50, s50, 0xff\n\ts_cmp_lg_u32 s50, 8\n\ts_cbranch_scc1 29f\n\t"
                 "ds_read2_b32 v[0:1], %[arf] offset1:2\n\tds_read2_b32 v[2:3], %[arf] offset0:4 offset1:6\n\tds_read2_b32 v[4:5], %[arf] offset0:8 offset1:10\n\t"
                 "ds_read2_b32 v[6:7], %[arf] offset0:12 offset1:14\n\ts_waitcnt lgkmcnt(0)\n\tv_max3_f32 v56, v0, v1, v2\n\tv_max3_f32 v56, v56, v3, v4\n\t"
                 "v_max3_f32 v56, v56, v5, v6\n\tv_max_f32 v56, v56, v7\n\t"
                 R_S(40, 0) R_S(42, 1) R_S(44, 2) R_S(46, 3) R_S(48, 4) R_S(50, 5) R_S(52, 6) R_S(54, 7) "s_branch 23b\n"
                 "29:\n\ts_mov_b64 exec, -1\n\ts_waitcnt lgkmcnt(0)"
                 : "=&{s[52:53]}"(tr)
                 : "{v[60:63]}"(am), [vo] "v"(lane * 16), [src] "s"(src), [tag] "v"(line_tag(epoch, e)),
                   [awr] "v"(lbase + RED_OFF + (rslot * 2 + (r >> 3)) * 256 + (r & 7) * 32 + (g & 1) * 16), [ard] "v"(lbase + RED_OFF + lane * 4), [arf] "v"(arf),
                   [qf] "v"((unsigned)qv << 16), [oo] "v"(oidx * 2), [out] "s"(KARG(kb, 0)), [ep] "v"(epoch + 1u), [bx] "s"(box), [rid] "s"(rid), [rs8] "s"(rslot * 8),
                   [wm] "s"(0xff00ff0000ff00ffull), [om] "s"(0x3f3f3f3f3f3f3f3full), [lim] "s"(64.f),
                   [cap] "i"(SPIN_CAP), [gcap] "i"(GSPIN_CAP), [x1] "i"(CT_OFF + C_X1), [x1n] "i"(NE), [red] "i"(CT_OFF + C_RED), [rs] "i"(RSLEEP), [eo] "i"(EPOCH_OFF)
                 : "memory", "scc", "vcc", "s40", "s41", "s42", "s43", "s44", "s45", "s46", "s47", "s48", "s49", "s50", "s51", "v0", "v1", "v2", "v3", "v4", "v5", "v6", "v7", "v8", "v9", "v10", "v11", "v12", "v13", "v14", "v15", "v16", "v17", "v18", "v19", "v20", "v21", "v22", "v23", "v24", "v25", "v26", "v27", "v28", "v29", "v30", "v31", "v32", "v33", "v34", "v35", "v36", "v37", "v38", "v39", "v40", "v41", "v42", "v43", "v44", "v45", "v46", "v47", "v48", "v49", "v50", "v51", "v52", "v53", "v54", "v55", "v56", "v57", "v58", "v59");
  }
}

#undef q
#undef f0
#undef f1
#undef wq
#undef wk
#undef wv
#undef wo
#undef box
extern "C" __global__ void dino_end() {}

extern "C" void kernel_launch(void* const* d_in, const int* in_sizes, int n_in,
                              void* d_out, int out_size, void* d_ws, size_t ws_size,
                              hipStream_t stream) {
  dino_main<<<NWG, 768, 0, stream>>>((const unsigned short*)d_in[0], (const char*)d_in[1], (const char*)d_in[2], (const char*)d_in[3],
                                     (const char*)d_in[4], (const char*)d_in[5], (const char*)d_in[6], (char*)d_ws + ((ws_size - (4u << 20)) & ~(size_t)0x1fffff),
                                     (unsigned short*)d_out);
}
```

```cpp
#include <hip/hip_runtime.h>
#include <hip/hip_bf16.h>
#include <stdint.h>


using bf16x8  = __attribute__((ext_vector_type(8))) short;
using f32x4   = __attribute__((ext_vector_type(4))) float;
using f32x2   = __attribute__((ext_vector_type(2))) float;
using u32x4   = __attribute__((ext_vector_type(4))) unsigned;
using u32x2   = __attribute__((ext_vector_type(2))) unsigned;
using v4i16_t = __attribute__((ext_vector_type(4))) short;
typedef __attribute__((address_space(3))) char* lds_ptr;
#define LDS_AS(T) __attribute__((address_space(3))) T

constexpr int CC = 192;
constexpr int NWG = 256, NS = 7, NE = 4;
constexpr int TPW = 33;
constexpr int ROWB = 384;
constexpr int SLOT = 40 * ROWB;
constexpr int NSLOT = 10;
constexpr int RSTR = 322;
constexpr int EPOCH_OFF = 64 * RSTR * 128, QK_OFF = EPOCH_OFF + 4096, W2F_OFF = QK_OFF + 12288, W2_OFF = W2F_OFF + 1024;
static_assert(W2_OFF + 72 * 1024 <= (4 << 20), "");
constexpr int GSPIN_CAP = 1 << 16;
#define PACE16 777
#define PACES 810
#define PK1 14
#define PWIN 35
#define AHI 22
#define ADL 60
#define AMG 200
#define T0BACK 7
#define HDUE 93
#define QKW0 200
#define QKW1 3
#define RSLEEP 25
#define W2SCH 0x0311180000000000ull
constexpr int NEARLY = 4;
constexpr int NH1 = 3;
constexpr int NPK = 6;
constexpr float EPS = 1e-5f;
constexpr float SQRT_C = 13.856406460551018f;
constexpr float LOG2E = 1.4426950408889634f;
constexpr float THR = 20.0f;
constexpr float TOFF = 192.0f;
constexpr int SPIN_CAP = 1 << 22;

constexpr int IMG_A = NSLOT * SLOT;
constexpr int CT_OFF = IMG_A + 6144;
constexpr int PKM = CT_OFF + 512, PKU = PKM + NPK * 128;
constexpr int ONES_OFF = PKU + NPK * 256;
constexpr int JUNK = ONES_OFF + ROWB;
constexpr int LDS_BYTES = JUNK + 256;
constexpr int H_R = 192 * ROWB, H_Q = H_R + 32 * ROWB, H_C = H_Q + 6144;
static_assert(LDS_BYTES <= 163840 && (PKU & 15) == 0 && (ONES_OFF & 127) == 0, "lds");
constexpr int C_FULL = 0, C_FREE = 64, C_EP = 128, C_RED = 144, C_ISS = 152, C_X1 = 156, C_QI = 160, C_PFL = 192, C_CONS = 256,
              C_MREF = 320, C_LW = 384;

typedef __bf16 bf16x2_t __attribute__((ext_vector_type(2)));
__device__ __forceinline__ unsigned cvtpk(float lo, float hi) {
  return __builtin_bit_cast(unsigned, __builtin_convertvector(f32x2{lo, hi}, bf16x2_t));
}
__device__ __forceinline__ float bflo(unsigned p) { return __uint_as_float(p << 16); }
__device__ __forceinline__ float bfhi(unsigned p) { return __uint_as_float(p & 0xffff0000u); }
__device__ __forceinline__ bf16x8 pack8(const f32x4& a, const f32x4& b) {
  return __builtin_bit_cast(bf16x8, u32x4{cvtpk(a[0], a[1]), cvtpk(a[2], a[3]), cvtpk(b[0], b[1]), cvtpk(b[2], b[3])});
}
__device__ __forceinline__ float xrow16_max(float x) {
  auto s = __builtin_amdgcn_permlane16_swap(__float_as_uint(x), __float_as_uint(x), false, false);
  x = fmaxf(__uint_as_float(s[0]), __uint_as_float(s[1]));
  auto t = __builtin_amdgcn_permlane32_swap(__float_as_uint(x), __float_as_uint(x), false, false);
  return fmaxf(__uint_as_float(t[0]), __uint_as_float(t[1]));
}
__device__ __forceinline__ float xrow16_sum(float x) {
  auto s = __builtin_amdgcn_permlane16_swap(__float_as_uint(x), __float_as_uint(x), false, false);
  x = __uint_as_float(s[0]) + __uint_as_float(s[1]);
  auto t = __builtin_amdgcn_permlane32_swap(__float_as_uint(x), __float_as_uint(x), false, false);
  return __uint_as_float(t[0]) + __uint_as_float(t[1]);
}
__device__ __forceinline__ v4i16_t tr_read(lds_ptr p) {
  return __builtin_amdgcn_ds_read_tr16_b64_v4i16((LDS_AS(v4i16_t)*)p);
}
__device__ __forceinline__ f32x4 mfma(bf16x8 a, bf16x8 b, f32x4 c) {
  return __builtin_amdgcn_mfma_f32_16x16x32_bf16(a, b, c, 0, 0, 0);
}
__device__ __forceinline__ int swz(int p) { return (p & 6) | ((p >> 2) & 1); }
constexpr unsigned swz_tbl(int i) {
  unsigned t = 0;
  for (int j = 0; j < 8; ++j) { const int p = (8 * i + j) / 3; t |= (unsigned)((p & 6) | ((p >> 2) & 1)) << (4 * j); }
  return t;
}
__device__ __forceinline__ bf16x8 mk8(v4i16_t a, v4i16_t b) { return bf16x8{a[0], a[1], a[2], a[3], b[0], b[1], b[2], b[3]}; }

__device__ __forceinline__ void lds_st32(unsigned addr, unsigned v) {
  asm volatile("ds_write_b32 %0, %1" : : "v"(addr), "v"(v) : "memory");
}
__device__ __forceinline__ unsigned lds_ld32(unsigned addr) {
  unsigned v;
  asm volatile("ds_read_b32 %0, %1\n\ts_waitcnt lgkmcnt(0)" : "=v"(v) : "v"(addr) : "memory");
  return __builtin_amdgcn_readfirstlane(v);
}
__device__ __forceinline__ void lds_add(unsigned addr, unsigned n) {
  asm volatile("s_mov_b64 exec, 1\n\tds_add_u32 %0, %1\n\ts_mov_b64 exec, -1\n\ts_nop 3" : : "v"(addr), "v"(n) : "memory");
}
__device__ __forceinline__ void poll_ge_sleep(unsigned addr, unsigned need) {
  need = __builtin_amdgcn_readfirstlane(need);
  unsigned v, s, n2, cnt = SPIN_CAP;
  asm volatile("s_branch 2f\n1:\n\ts_sleep 2\n2:\n\tds_read_b32 %0, %4\n\ts_sub_u32 %3, %3, 1\n\ts_cselect_b32 %2, 0, %5\n\ts_waitcnt lgkmcnt(0)\n\t"
               "v_readfirstlane_b32 %1, %0\n\ts_cmp_lt_u32 %1, %2\n\ts_cbranch_scc1 1b"
               : "=&v"(v), "=&s"(s), "=&s"(n2), "+s"(cnt) : "v"(addr), "s"(need) : "memory", "scc");
}
__device__ __forceinline__ bf16x8 img_ld(lds_ptr L0, int off, int kk, int lane) { return *(LDS_AS(bf16x8)*)(L0 + off + kk * 1024 + lane * 16); }
__device__ __forceinline__ void img_st(lds_ptr L0, int off, int kk, int lane, bf16x8 v) { *(LDS_AS(bf16x8)*)(L0 + off + kk * 1024 + lane * 16) = v; }

template <int N> __device__ __forceinline__ void wait_vm() { asm volatile("s_waitcnt vmcnt(%0)" :: "i"(N) : "memory"); }
constexpr int A_IMG = ((TPW + 1 - NPK) % NSLOT) * SLOT, RED_OFF = ((TPW - NPK) % NSLOT) * SLOT;
__device__ __forceinline__ unsigned line_tag(unsigned epoch, unsigned i) {
  unsigned x = (epoch + 1u + i * 0x9E3779B9u) * 0x85EBCA6Bu;
  x ^= x >> 15; x *= 0xC2B2AE35u;
  return x ^ (x >> 13);
}
__device__ __forceinline__ unsigned epoch_load(const char* box) {
  unsigned e;
  asm volatile("global_load_dword %0, %1, %2 sc1\n\ts_waitcnt vmcnt(0)" : "=v"(e) : "v"(EPOCH_OFF), "s"(box) : "memory");
  return __builtin_amdgcn_readfirstlane(e);
}
__device__ __forceinline__ int tr_off(int row, int col4) {
  const int ch = col4 >> 1;
  return row * ROWB + (((ch & ~7) | ((ch & 7) ^ swz(row & 15))) << 4) + 8 * (col4 & 1);
}
#define PIN3(a, i) "+v"(a[i]), "+v"(a[i + 1]), "+v"(a[i + 2])

#define DMA1(d, b, off, pol) "global_load_lds_dwordx4 %[" #d "], %[" #b "] offset:" #off pol "\n\t"
__device__ __forceinline__ void issue_rows16(const char* a0, unsigned dst, const unsigned (&d)[3]) {
  unsigned keep;
  asm volatile("s_mov_b32 %[keep], m0\n\ts_mov_b32 m0, %[dst]\n\ts_nop 0\n\t"
               DMA1(d0, a, 0, "") DMA1(d1, a, 1024, "") DMA1(d2, a, 2048, "") DMA1(d0, a, 3072, "")
               "s_add_u32 m0, %[dst], 0x1000\n\ts_nop 0\n\t" DMA1(d1, b, 0, "") DMA1(d2, b, 1024, "") "s_mov_b32 m0, %[keep]"
               : [keep] "=&s"(keep) : [dst] "s"(dst), [a] "s"(a0), [b] "s"(a0 + 4096), [d0] "v"(d[0]), [d1] "v"(d[1]), [d2] "v"(d[2]) : "memory", "scc");
}
#define LD_VM "s_getreg_b32 s43, hwreg(HW_REG_IB_STS, 22, 2)\n\ts_getreg_b32 s42, hwreg(HW_REG_IB_STS, 0, 4)\n\ts_lshl4_add_u32 s42, s43, s42\n\t"
#define LD_GATE(n, X) \
  #n "0:\n\t" LD_VM "s_cmp_le_i32 s42, s46\n\ts_cbranch_scc1 " #n "2f\n" \
  #n "1:\n\t" X "s_cmp_gt_u32 s42, %[win]\n\ts_cbranch_scc1 " #n "5f\n\ts_cmp_ge_u32 s45, s44\n\ts_cbranch_scc1 " #n "7f\n" \
  #n "6:\n\ts_memrealtime s[40:41]\n\ts_sleep 1\n\ts_waitcnt lgkmcnt(0)\n\ts_sub_u32 s45, s40, %[t0]\n\ts_lshl_b32 s45, s45, 4\n\ts_cmp_lt_u32 s45, s44\n\ts_cbranch_scc1 " #n "6b\n" \
  #n "7:\n\t" LD_VM
#define LD_NEXT \
  "s_cmp_gt_u32 s42, %[ahi]\n\ts_cselect_b32 s42, %[amg], 0\n\ts_sub_u32 s43, s45, s44\n\ts_cmp_lt_u32 s43, s42\n\ts_cselect_b32 s42, %[p3] + %[adl], s39\n\t" \
  "s_add_u32 s44, s44, s42\n\ts_add_u32 s46, s46, 5\n\t"
#define LD_PUB0(n, a, b, c) \
  #n "2:\n\tv_add_u32 %[pub], 2, %[pub]\n\ts_cmp_ge_u32 s46, 30\n\ts_cbranch_scc1 " #n "4f\n\ts_cmp_ge_u32 s46, 15\n\ts_cbranch_scc1 " #n "3f\n\t" \
  "s_waitcnt vmcnt(" #a ")\n" #n "3:\n\ts_waitcnt vmcnt(" #b ")\n" #n "4:\n\ts_waitcnt vmcnt(" #c ")\n\tds_write_b32 %[af], %[pub]\n\t" \
  "s_sub_u32 s46, s46, 15\n\ts_branch " #n "1b\n"
#define LD_PUB(n, a, b, c) LD_PUB0(n, a, b, c) \
  #n "5:\n\ts_sleep 1\n\ts_sub_i32 s47, s47, 1\n\ts_cmp_gt_i32 s47, 0\n\ts_cbranch_scc1 " #n "0b\n\ts_waitcnt vmcnt(%[win])\n\ts_branch " #n "6b\n"
#define LD1(d, b, off) "global_load_lds_dwordx4 %[" #d "], s[" #b "] offset:" #off " nt\n\t"

#define PV_REQ(P, U, M, C, CA) \
  "v_lshl_add_u32 v150, s44, 3, %[apk]\n\tv_lshl_add_u32 v151, s44, 1, %[apu]\n\tv_add_u32 v152, s44, %[apm]\n\t" \
  "ds_read_b32 v" #C ", v" #CA "\n\tds_read_b128 v[" P "], v150\n\tds_read_b64 v[" M "], v152\n\tds_read_b128 v[" U "], v151\n\t"
#define PV_TR(d0, d1, a, o) "ds_read_b64_tr_b16 v[" #d0 ":" #d1 "], v" #a " offset:" #o "\n\t"
#define PV_FETCH(y0, y0b, a0, a0b, b0, b0b, y1, y1b, a1, a1b, b1, b1b, y2, y2b, a2, a2b, b2, b2b) \
  "v_add_u32 v153, s45, %[ox]\n\tv_add_u32 v154, s45, %[oy]\n\t" \
  PV_TR(a0, a0b, 153, 0) PV_TR(b0, b0b, 153, 6144) PV_TR(a1, a1b, 153, 128) PV_TR(b1, b1b, 153, 6272) PV_TR(a2, a2b, 153, 256) PV_TR(b2, b2b, 153, 6400) \
  PV_TR(y0, y0b, 154, 0) PV_TR(y1, y1b, 154, 128) PV_TR(y2, y2b, 154, 256) \
  "s_add_u32 s45, s45, %[slot]\n\ts_cmp_eq_u32 s45, %[ring]\n\ts_cselect_b32 s45, 0, s45\n\t"
#define PV_FETCH_A PV_FETCH(90, 91, 92, 93, 94, 95, 96, 97, 98, 99, 100, 101, 102, 103, 104, 105, 106, 107)
#define PV_FETCH_B PV_FETCH(108, 109, 110, 111, 112, 113, 114, 115, 116, 117, 118, 119, 120, 121, 122, 123, 124, 125)
#define PV_MF(d, a, p) "v_mfma_f32_16x16x32_bf16 v[" d "], v[" a "], v[" p "], v[" d "]\n\t"
#define PV_MOV(d, s) "v_mov_b32 v" #d ", v" #s "\n\t"
#define PV_WAITF(n, CA)   \
  #n ":\n\tds_read_b32 v155, v" #CA "\n\ts_sub_i32 s47, s47, 1\n\ts_cmp_lt_i32 s47, 0\n\ts_cbranch_scc1 " #n "9f\n\ts_waitcnt lgkmcnt(0)\n\t" \
  "v_cmp_lt_u32 vcc, v155, %[need]\n\ts_and_b64 vcc, vcc, 2\n\ts_cbranch_scc0 " #n "9f\n\ts_sleep 2\n\ts_branch " #n "b\n" #n "9:\n\ts_waitcnt lgkmcnt(0)\n\t"
#define PV_STEP(n, P, U, M, m0, m1, C, CA, P2, U2, M2, C2, CA2, X0, X1, X2, Y0, Y1, Y2, MV0, MV1, MV2, HOOK) \
  "s_waitcnt lgkmcnt(10)\n\t" \
  "v_cmp_lt_u32 vcc, v" #C ", %[need]\n\tv_cmp_neq_f32 s[48:49], v" #m0 ", v88\n\tv_cmp_neq_f32 s[50:51], v" #m1 ", v88\n\t" \
  "v_add_u32 v" #CA2 ", 4, v" #CA "\n\ts_or_b64 s[48:49], s[48:49], vcc\n\tv_cmp_eq_u32 s[52:53], v" #CA2 ", %[lim]\n\t" \
  "s_or_b64 s[48:49], s[48:49], s[50:51]\n\ts_cbranch_scc1 " #n "1f\n" \
  #n "0:\n\ts_add_u32 s44, s44, 128\n\ts_cmp_eq_u32 s44, %[pring]\n\ts_cselect_b32 s44, 0, s44\n\t" \
  "v_cndmask_b32 v" #CA2 ", v" #CA2 ", %[base], s[52:53]\n\t" \
  PV_MF("84:87", U, P) PV_MF("72:75", X0, P) PV_MF("76:79", X1, P) MV0 PV_MF("80:83", X2, P) MV1 \
  PV_MF("72:75", Y0, P) MV2 HOOK PV_REQ(P2, U2, M2, C2, CA2) PV_MF("76:79", Y1, P) PV_MF("80:83", Y2, P)
#define PV_REL(CA) \
  "s_mov_b64 exec, 3\n\tds_add_u32 v" #CA ", %[one] offset:64\n\ts_mov_b64 exec, -1\n\tv_add_u32 %[need], 2, %[need]\n\ts_add_u32 s46, s46, 1\n\t"
#define PV_SC1(p) "v_lshlrev_b32 v151, 16, v" #p "\n\tv_and_b32 v152, 0xffff0000, v" #p "\n\tv_mul_f32 v151, v151, v150\n\tv_mul_f32 v152, v152, v150\n\t" \
                  "v_cvt_pk_bf16_f32 v" #p ", v151, v152\n\t"
#define PV_MUL4(a, b, c, d) "v_mul_f32 v" #a ", v" #a ", v150\n\tv_mul_f32 v" #b ", v" #b ", v150\n\tv_mul_f32 v" #c ", v" #c ", v150\n\tv_mul_f32 v" #d ", v" #d ", v150\n\t"
#define PV_RESC(m0, m1, p0, p1, p2, p3) \
  "v_mov_b32 v150, v88\n\tv_max3_f32 v88, v88, v" #m0 ", v" #m1 "\n\tv_sub_f32 v150, v150, v88\n\tv_exp_f32 v150, v150\n\ts_nop 1\n\t" \
  PV_MUL4(72, 73, 74, 75) PV_MUL4(76, 77, 78, 79) PV_MUL4(80, 81, 82, 83) PV_MUL4(84, 85, 86, 87) \
  "v_sub_f32 v150, v" #m0 ", v88\n\tv_exp_f32 v150, v150\n\ts_nop 1\n\t" PV_SC1(p0) PV_SC1(p1) \
  "v_sub_f32 v150, v" #m1 ", v88\n\tv_exp_f32 v150, v150\n\ts_nop 1\n\t" PV_SC1(p2) PV_SC1(p3)
#define PV_SLOW(n, P, U, M, m0, m1, C, CA, p0, p1, p2, p3) \
  #n "1:\n\ts_bitcmp0_b32 vcc_lo, 0\n\ts_cbranch_scc1 " #n "4f\n\ts_cmp_gt_u32 s46, %[tight]\n\ts_cbranch_scc1 " #n "3f\n" \
  #n "2:\n\ts_sleep 2\n\tds_read_b32 v" #C ", v" #CA "\n\ts_sub_i32 s47, s47, 1\n\ts_cmp_lt_i32 s47, 0\n\ts_cbranch_scc1 " #n "3f\n\ts_waitcnt lgkmcnt(0)\n\t" \
  "v_cmp_lt_u32 vcc, v" #C ", %[need]\n\ts_and_b64 vcc, vcc, 1\n\ts_cbranch_scc1 " #n "2b\n" \
  #n "3:\n\t" PV_REQ(P, U, M, C, CA) "s_sub_i32 s47, s47, 1\n\ts_cmp_lt_i32 s47, 0\n\ts_cselect_b32 s54, 0, 1\n\ts_waitcnt lgkmcnt(0)\n\t" \
  "v_cmp_lt_u32 vcc, v" #C ", %[need]\n\tv_cmp_neq_f32 s[48:49], v" #m0 ", v88\n\tv_cmp_neq_f32 s[50:51], v" #m1 ", v88\n\t" \
  "s_and_b32 s54, s54, vcc_lo\n\ts_cbranch_scc1 " #n "3b\n\ts_or_b64 s[48:49], s[48:49], s[50:51]\n\ts_cbranch_scc0 " #n "0b\n\ts_branch " #n "5f\n" \
  #n "4:\n\tv_cmp_neq_f32 s[48:49], v" #m0 ", v88\n\ts_or_b64 s[48:49], s[48:49], s[50:51]\n\ts_cbranch_scc0 " #n "0b\n" \
  #n "5:\n\t" PV_RESC(m0, m1, p0, p1, p2, p3) \
  "s_branch " #n "0b\n"
#define PV_W2(a0, a1, b0, b1, c0, c1) \
  "global_load_dwordx4 v[" #a0 ":" #a1 "], v150, %[w2] sc1\n\tglobal_load_dwordx4 v[" #b0 ":" #b1 "], v150, %[w2] offset:1024 sc1\n\t" \
  "global_load_dwordx4 v[" #c0 ":" #c1 "], v150, %[w2] offset:2048 sc1\n\tv_add_u32 v150, 0xc00, v150\n\ts_sub_u32 s54, s54, 1\n\ts_cbranch_scc1 30b\n\t"

#define PV_PK(a, b) "v_pk_fma_f32 v[" #a ":" #b "], v[" #a ":" #b "], v[150:151], v[150:151] op_sel:[0,0,1] op_sel_hi:[1,0,1]\n\t"
#define PV_NRM(a, b, c, d, o)   \
  "v_pk_fma_f32 v[" #a ":" #b "], v[" #a ":" #b "], v[150:151], v[150:151] op_sel:[0,0,1] op_sel_hi:[1,0,1]\n\t" \
  "v_pk_fma_f32 v[" #c ":" #d "], v[" #c ":" #d "], v[150:151], v[150:151] op_sel:[0,0,1] op_sel_hi:[1,0,1]\n\t" \
  "v_cvt_pk_bf16_f32 v" #a ", v" #a ", v" #b "\n\tv_cvt_pk_bf16_f32 v" #b ", v" #c ", v" #d "\n\tds_write_b64 v120, v[" #a ":" #b "] offset:" #o "\n\t"
#define PV_G(n, a, w0, w1, w2, c0, c1, c2) "s_waitcnt lgkmcnt(" #n ")\n\tv_mfma_f32_16x16x32_bf16 v[72:75], v[" a "], v[" w0 "], " c0 "\n\t" \
  "v_mfma_f32_16x16x32_bf16 v[76:79], v[" a "], v[" w1 "], " c1 "\n\tv_mfma_f32_16x16x32_bf16 v[80:83], v[" a "], v[" w2 "], " c2 "\n\t"
#define PV_Z4(a, b, c, d) "v_mov_b32 v" #a ", 0\n\tv_mov_b32 v" #b ", 0\n\tv_mov_b32 v" #c ", 0\n\tv_mov_b32 v" #d ", 0\n\t"

using u32x8 = __attribute__((ext_vector_type(8))) unsigned;
struct SCtx {
  lds_ptr L0;
  unsigned ct, apk, apm, api, amr;
  int o_rb, o_yb, ev;
  u32x8 q[3];
};
__device__ __forceinline__ bf16x8 s_qf(const SCtx& c, int j) {
  const u32x8& v = c.q[j >> 1];
  const int o = 4 * (j & 1);
  return __builtin_bit_cast(bf16x8, u32x4{v[o], v[o + 1], v[o + 2], v[o + 3]});
}
#define S_M(d, a, b) "v_mfma_f32_16x16x32_bf16 v[" d "], v[" a "], v[" b "], v[" d "]\n\t"
#define S_M0(d, a, b) "v_mfma_f32_16x16x32_bf16 v[" d "], v[" a "], v[" b "], 0\n\t"
#define S_R(d, a, o) "ds_read_b128 v[" d "], v" #a " offset:" #o "\n\t"
#define S_W(n) "s_waitcnt lgkmcnt(" #n ")\n\t"
#define S_D " row_mask:0xf bank_mask:0xf\n\t"
#define S_3(x, y) S_M("100:103", x, y) S_M("104:107", x, x) S_M("96:99", y, y)
#define S_SQ(g, h) "v_fmac_f32 v" #g ", 2.0, v" #h "\n\t"
#define S_SQD(g, h) "v_fmac_f32_dpp v" #g ", v" #h ", %[two] quad_perm:[0,0,2,2]" S_D
#define S_U(u, h, y) "v_add_f32_dpp v" #u ", v" #h ", v" #y " quad_perm:[1,1,3,3]" S_D
#define S_C(d, a, b, m) "v_cndmask_b32 v" #d ", v" #a ", v" #b ", %[" #m "]\n\t"
#define S_B(d, n) "v_mov_b32_dpp v" #d ", v157 row_newbcast:" #n S_D
#define S_BASE(d, u, t) "v_pk_fma_f32 v[" d "], v[" u "], v[116:117], v[" t "] op_sel:[0,1,0] op_sel_hi:[1,1,0]\n\t"
#define S_LG(d, r) "v_pk_fma_f32 v[" d "], v[" d "], v[" r "], %[toff] op_sel_hi:[1,1,0]\n\t"
#define S_SUB(p, d) "v_pk_add_f32 v[" p "], v[" d "], v[150:151] op_sel_hi:[1,0] neg_lo:[0,1] neg_hi:[0,1]\n\t"
#define S_E(p) "v_exp_f32 v" #p ", v" #p "\n\t"
#define S_MUL(p, r) "v_pk_mul_f32 v[" p "], v[" p "], v[" r "]\n\t"
#define S_POS   \
  "s_mul_i32 s50, s42, 26\n\ts_lshr_b32 s50, s50, 8\n\ts_mul_i32 s50, s50, -10\n\ts_add_u32 s50, s50, s42\n\ts_mul_i32 s45, s50, %[slot]\n\ts_mov_b32 s43, s45\n\t"
#define S_POLL(n, a, o, need, back, far) \
  #n ":\n\tds_read_b32 v152, v" #a " offset:" #o "\n\ts_sub_i32 s47, s47, 1\n\ts_cmp_lt_i32 s47, 0\n\ts_waitcnt lgkmcnt(0)\n\ts_cbranch_scc1 " back "\n\t" \
  "v_readfirstlane_b32 s40, v152\n\ts_cmp_ge_u32 s40, " need "\n\ts_cbranch_scc1 " back "\n\ts_sleep 2\n\t" \
  "s_add_u32 s40, s40, " far "\n\ts_cmp_ge_u32 s40, " need "\n\ts_cbranch_scc1 " #n "b\n\ts_sleep 12\n\ts_branch " #n "b\n"
__device__ __forceinline__ void s_items(SCtx& c, int k0, int late) {
  asm volatile("s_mov_b32 s47, %[cap]\n\ts_mov_b32 s49, 0\n\ts_mov_b32 s42, %[k0]\n\ts_cmp_lg_u32 %[late], 0\n\ts_cbranch_scc1 7f\n"
               "1:\n\ts_cmp_ge_u32 s42, %[end]\n\ts_cbranch_scc1 9f\n\t"
               S_POS
               "s_mul_i32 s48, s42, 43\n\ts_lshr_b32 s48, s48, 8\n\ts_mul_i32 s46, s48, -6\n\ts_add_u32 s46, s46, s42\n\ts_lshl2_add_u32 s48, s48, %[ns]\n\t"
               "v_add_u32 v144, s45, %[orb]\n\tv_mad_u32_u24 v145, s43, %[ev], %[oyb]\n\ts_lshl2_add_u32 s40, s46, %[pfl]\n\ts_lshl2_add_u32 s41, s50, %[full]\n\t"
               "v_xor_b32 v146, 64, v144\n\tv_xor_b32 v147, 64, v145\n\tv_mov_b32 v148, s40\n\tv_mov_b32 v149, s41\n\t"
               "s_lshl1_add_u32 s44, s42, 2\n\ts_lshl_b32 s46, s46, 7\n\ts_mov_b64 vcc, %[dg]\n\ts_cmp_lt_u32 s49, s44\n\ts_cbranch_scc1 5f\n"
               "2:\n\tds_read_b32 v150, %[amr]\n\tds_read_b32 v151, v148 offset:64\n\t"
               S_R("24:27", 145, 0) S_R("48:51", 144, 0) S_R("28:31", 147, 0) S_R("52:55", 146, 0) S_R("32:35", 145, 128) S_R("56:59", 144, 128)
               S_R("36:39", 147, 128) S_R("60:63", 146, 128) S_R("40:43", 145, 256) S_R("64:67", 144, 256) S_R("44:47", 147, 256) S_R("68:71", 146, 256)
               S_W(10) S_M0("100:103", "48:51", "24:27") S_M0("104:107", "48:51", "48:51") S_R("72:75", 144, 6144) S_R("76:79", 146, 6144) S_M0("96:99", "24:27", "24:27")
               S_W(10) S_M("100:103", "52:55", "28:31") S_M("104:107", "52:55", "52:55") S_R("80:83", 144, 6272) S_R("84:87", 146, 6272) S_M("96:99", "28:31", "28:31")
               S_W(10) S_M("100:103", "56:59", "32:35") S_M("104:107", "56:59", "56:59") S_R("88:91", 144, 6400) S_R("92:95", 146, 6400) S_M("96:99", "32:35", "32:35")
               "s_mov_b64 exec, 1\n\tds_add_u32 v149, %[four] offset:64\n\ts_mov_b64 exec, -1\n\tv_readfirstlane_b32 s51, v151\n\ts_setprio 1\n\ts_nop 0\n\t"
               S_W(11) S_3("60:63", "36:39") S_W(9) S_3("64:67", "40:43") S_W(7) S_3("68:71", "44:47")
               S_W(6) S_M0("108:111", "72:75", "24:27") S_M0("112:115", "72:75", "72:75")
               S_W(5) S_M("108:111", "76:79", "28:31") S_M("112:115", "76:79", "76:79")
               S_W(4) S_M("108:111", "80:83", "32:35")
               "v_mov_b32_dpp v152, v96 quad_perm:[1,1,3,3]" S_D "v_mov_b32_dpp v153, v98 quad_perm:[1,1,3,3]" S_D
               S_M("112:115", "80:83", "80:83") S_C(154, 96, 98, r2) S_SQ(104, 100)
               S_W(3) S_M("108:111", "84:87", "36:39") S_SQD(105, 101) S_SQ(106, 102)
               S_M("112:115", "84:87", "84:87") S_SQD(107, 103) "v_add_f32 v154, %[eps], v154\n\t"
               S_W(2) S_M("108:111", "88:91", "40:43") S_U(128, 100, 152) S_U(129, 101, 152)
               S_M("112:115", "88:91", "88:91") S_U(130, 102, 153) S_U(131, 103, 153)
               S_W(1) S_M("108:111", "92:95", "44:47") S_C(104, 104, 105, r1) S_C(106, 106, 107, r1)
               S_M("112:115", "92:95", "92:95") S_C(155, 128, 129, r1) S_C(156, 130, 131, r1)
               "s_cmp_lt_u32 s42, %[early]\n\ts_cbranch_scc1 7f\n"
               "3:\n\t" S_M0("116:119", "24:27", "0:3") S_C(104, 104, 106, r2) S_C(155, 155, 156, r2)
               S_M("116:119", "28:31", "4:7") "v_add_f32_dpp v104, v154, v104 quad_perm:[0,0,2,2]" S_D S_U(132, 108, 152)
               S_M("116:119", "32:35", "8:11") S_U(133, 109, 152) S_U(134, 110, 153)
               S_M("116:119", "36:39", "12:15") S_U(135, 111, 153) S_SQ(112, 108)
               S_M("116:119", "40:43", "16:19") S_SQD(113, 109) S_SQ(114, 110)
               S_M("116:119", "44:47", "20:23") S_SQD(115, 111) S_C(156, 132, 133, r1) S_C(157, 134, 135, r1)
               S_C(112, 112, 113, r1) S_C(114, 114, 115, r1) S_C(156, 156, 157, r2) S_C(112, 112, 114, r2)
               "v_add_f32_dpp v112, v154, v112 quad_perm:[0,0,2,2]" S_D
               "v_mul_f32 v117, %[nic], v117\n\tv_cndmask_b32_dpp v155, v156, v155, vcc row_ror:4" S_D
               S_BASE("120:121", "128:129", "116:117") S_BASE("122:123", "130:131", "118:119")
               "v_cndmask_b32_dpp v104, v112, v104, vcc row_ror:4" S_D "v_mul_f32 v156, %[nic], v155\n\t"
               S_M("120:123", "48:51", "0:3") S_BASE("124:125", "132:133", "116:117") S_BASE("126:127", "134:135", "118:119")
               S_M("120:123", "52:55", "4:7") "v_fmac_f32 v104, v156, v155\n\tv_max_f32 v104, %[eps], v104\n\t"
               S_M("120:123", "56:59", "8:11") "v_rsq_f32 v157, v104\n\t"
               S_M("120:123", "60:63", "12:15") "v_mul_f32 v104, v104, v157\n\tv_mov_b32_dpp v157, v157 row_ror:12 row_mask:0x2 bank_mask:0xf\n\t"
               S_M("120:123", "64:67", "16:19") "v_cvt_pk_bf16_f32 v105, v155, v104\n\tv_mov_b32_dpp v157, v157 row_ror:8 row_mask:0x4 bank_mask:0xf\n\t"
               S_M("120:123", "68:71", "20:23") "v_lshlrev_b32 v106, 16, v105\n\tv_mov_b32_dpp v157, v157 row_ror:4 row_mask:0x8 bank_mask:0xf\n\t"
               S_M("124:127", "72:75", "0:3") "v_and_b32 v107, 0xffff0000, v105\n\t" S_B(136, 0)
               S_M("124:127", "76:79", "4:7") S_B(137, 1) S_B(138, 2)
               S_M("124:127", "80:83", "8:11") S_B(139, 3) S_LG("120:121", "136:137")
               S_M("124:127", "84:87", "12:15") S_LG("122:123", "138:139") S_SUB("128:129", "120:121")
               S_M("124:127", "88:91", "16:19") S_SUB("130:131", "122:123") S_E(128)
               S_M("124:127", "92:95", "20:23") S_E(129) S_E(130) S_E(131)
               S_B(140, 4) S_B(141, 5) S_B(142, 6) S_B(143, 7) "v_sub_f32 v106, v155, v106\n\t"
               S_LG("124:125", "140:141") S_LG("126:127", "142:143") "v_sub_f32 v107, v104, v107\n\tv_max3_f32 v152, v120, v121, v122\n\t"
               S_SUB("132:133", "124:125") S_SUB("134:135", "126:127") "v_max3_f32 v153, v123, v124, v125\n\t"
               S_E(132) S_E(133) S_E(134) S_E(135)
               "v_max3_f32 v152, v152, v126, v127\n\tv_add_f32 v154, %[thr], v150\n\tv_max_f32 v152, v152, v153\n\tv_cvt_pk_bf16_f32 v106, v106, v107\n\t"
               "v_lshl_add_u32 v144, s46, 3, %[apk]\n\tv_cmp_gt_f32 vcc, v152, v154\n\tv_add_u32 v145, s46, %[apm]\n\tv_lshl_add_u32 v146, s46, 1, %[api]\n\t"
               "s_cbranch_vccnz 8f\n"
               "4:\n\t" S_MUL("128:129", "136:137") S_MUL("130:131", "138:139") S_MUL("132:133", "140:141") S_MUL("134:135", "142:143")
               "v_cvt_pk_bf16_f32 v128, v128, v129\n\tv_cvt_pk_bf16_f32 v129, v130, v131\n\tv_cvt_pk_bf16_f32 v130, v132, v133\n\tv_cvt_pk_bf16_f32 v131, v134, v135\n\t"
               "s_cmp_lt_u32 s51, s48\n\ts_cbranch_scc1 6f\n"
               "10:\n\tds_read_b32 v151, v149\n\tds_write_b128 v144, v[128:131]\n\tds_write2_b32 v145, v150, v150 offset0:0 offset1:1\n\ts_mov_b64 exec, %[dg2]\n\t"
               "ds_write_b16 v146, v105\n\tds_write_b16 v146, v106 offset:16\n\tds_write_b16_d16_hi v146, v105 offset:32\n\tds_write_b16_d16_hi v146, v106 offset:48\n\t"
               "s_mov_b64 exec, 1\n\tds_add_u32 v148, %[inc]\n\ts_mov_b64 exec, -1\n\ts_setprio 0\n\ts_add_u32 s42, s42, %[ns]\n\t"
               S_W(7) "v_readfirstlane_b32 s49, v151\n\ts_branch 1b\n"
               S_POLL(5, 149, 0, "s44", "2b", "2")
               S_POLL(6, 148, 64, "s48", "10b", "64")
               "7:\n\tv_mov_b32 v144, %[qi]\n\tv_mov_b32 v145, v152\n\tv_mov_b32 v146, 1\n"
               S_POLL(70, 144, 0, "%[nq]", "71f", "64")
               "71:\n\tv_mov_b32 v152, v145\n\tds_read_b128 v[0:3], %[apk]\n\tds_read_b128 v[4:7], %[apk] offset:1024\n\tds_read_b128 v[8:11], %[apk] offset:2048\n\t"
               "ds_read_b128 v[12:15], %[apk] offset:3072\n\tds_read_b128 v[16:19], %[apk] offset:4096\n\tds_read_b128 v[20:23], %[apk] offset:5120\n\t"
               "v_subrev_u32 v144, %[mc], %[amr]\n\tds_read_b32 v150, %[amr]\n\ts_mov_b64 exec, 63\n\tds_add_u32 v144, v146\n\ts_mov_b64 exec, -1\n\t"
               "s_cmp_lg_u32 %[late], 0\n\ts_waitcnt lgkmcnt(0)\n\ts_nop 1\n\ts_cbranch_scc1 1b\n\ts_branch 3b\n"
               "8:\n\tv_mov_b32 v153, v152\n\ts_nop 1\n\tv_permlane16_swap_b32 v152, v153\n\tv_max_f32 v152, v152, v153\n\tv_mov_b32 v153, v152\n\ts_nop 1\n\t"
               "v_permlane32_swap_b32 v152, v153\n\tv_max_f32 v152, v152, v153\n\tv_mul_f32 v152, 0x3d000000, v152\n\tv_rndne_f32 v152, v152\n\t"
               "v_mul_f32 v152, 0x42000000, v152\n\tv_max_f32 v150, v150, v152\n\tds_max_f32 %[amr], v150\n\t"
               S_SUB("128:129", "120:121") S_SUB("130:131", "122:123") S_SUB("132:133", "124:125") S_SUB("134:135", "126:127")
               S_E(128) S_E(129) S_E(130) S_E(131) S_E(132) S_E(133) S_E(134) S_E(135) "s_branch 4b\n"
               "9:"
               : "=&{v[0:7]}"(c.q[0]), "=&{v[8:15]}"(c.q[1]), "=&{v[16:23]}"(c.q[2])
               : [orb] "v"(c.o_rb), [oyb] "v"(c.o_yb), [ev] "v"(c.ev), [apk] "v"(c.apk), [apm] "v"(c.apm), [api] "v"(c.api), [amr] "v"(c.amr),
                 [two] "v"(2.f), [four] "v"(4u), [inc] "v"(2u * NPK), [k0] "s"(k0), [late] "s"(__builtin_amdgcn_readfirstlane(late)), [toff] "s"(f32x2{TOFF, TOFF}),
                 [r1] "s"(0xAAAAAAAAAAAAAAAAull), [r2] "s"(0xCCCCCCCCCCCCCCCCull), [dg] "s"(0xF0000F0000F0000Full), [dg2] "s"(0xF00FFF000FF000FFull),
                 [cap] "i"(SPIN_CAP), [end] "i"(TPW - NH1), [ns] "i"(NS), [pfl] "i"(CT_OFF + C_PFL), [full] "i"(CT_OFF + C_FULL), [early] "i"(NEARLY),
                 [qi] "i"(CT_OFF + C_QI), [nq] "i"(NS - NEARLY), [mc] "i"(C_MREF - C_CONS), [slot] "i"(SLOT),
                 [thr] "i"(__builtin_bit_cast(unsigned, THR)), [eps] "i"(__builtin_bit_cast(unsigned, EPS * CC)), [nic] "i"(__builtin_bit_cast(unsigned, -1.f / CC))
               : "memory", "scc", "vcc", "s40", "s41", "s42", "s43", "s44", "s45", "s46", "s47", "s48", "s49", "s50", "s51",
                 "v24", "v25", "v26", "v27", "v28", "v29", "v30", "v31", "v32", "v33", "v34", "v35", "v36", "v37", "v38", "v39", "v40", "v41", "v42", "v43", "v44", "v45", "v46", "v47", "v48", "v49", "v50", "v51", "v52", "v53", "v54", "v55", "v56", "v57", "v58", "v59", "v60", "v61", "v62", "v63", "v64", "v65", "v66", "v67", "v68", "v69", "v70", "v71", "v72", "v73", "v74", "v75", "v76", "v77", "v78", "v79", "v80", "v81", "v82", "v83", "v84", "v85", "v86", "v87", "v88", "v89", "v90", "v91", "v92", "v93", "v94", "v95", "v96", "v97", "v98", "v99", "v100", "v101", "v102", "v103", "v104", "v105", "v106", "v107", "v108", "v109", "v110", "v111", "v112", "v113", "v114", "v115", "v116", "v117", "v118", "v119", "v120", "v121", "v122", "v123", "v124", "v125", "v126", "v127", "v128", "v129", "v130", "v131", "v132", "v133", "v134", "v135", "v136", "v137", "v138", "v139", "v140", "v141", "v142", "v143", "v144", "v145", "v146", "v147", "v148", "v149", "v150", "v151", "v152", "v153", "v154", "v155", "v156", "v157");
}

__device__ __forceinline__ void s_half(SCtx& c, int k, int h) {
  const int s = k % NSLOT, e = k % NPK;
  const int sb_ = s * SLOT;
  int yba = c.ev * sb_ + c.o_yb;
  float hc = 1.f - 0.5f * c.ev;
  unsigned xa = (unsigned)(size_t)c.L0 + sb_ + h * 6144 + c.o_rb, xb = xa ^ 64, ai = c.api + e * 256 + h * 8;
  unsigned ap = c.apk + e * 1024 + h * 8, am = c.apm + e * 128 + h * 4, af = c.ct + C_PFL + e * 4, inc = NPK;
  float two = 2.f;
  f32x4 G = {EPS * CC, EPS * CC, EPS * CC, EPS * CC};
  poll_ge_sleep(c.ct + C_CONS + e * 4, (unsigned)(NE * (k / NPK) + NS));
  asm volatile("" : "+v"(yba), "+v"(hc), "+v"(xa), "+v"(xb), "+v"(ai), "+v"(ap), "+v"(am), "+v"(af), "+v"(inc), "+v"(two), "+{v[104:107]}"(G));
  poll_ge_sleep(c.ct + C_FULL, (unsigned)(2 * k + 1));
  __builtin_amdgcn_s_setprio(1);
  bf16x8 yb[6];
#pragma unroll
  for (int j = 0; j < 6; ++j) yb[j] = *(LDS_AS(bf16x8)*)(c.L0 + ((j & 1) ? (yba ^ 64) : yba) + (j >> 1) * 128);
  f32x4 Hx = {0.f, 0.f, 0.f, 0.f}, S = Hx;
#pragma unroll
  for (int j = 0; j < 6; ++j) { Hx = mfma(yb[j], yb[j], Hx); S = mfma(yb[j], s_qf(c, j), S); }
  const float nsq = S[1] * (-1.f / CC);
  Hx = f32x4{Hx[0] * hc, Hx[0] * hc, Hx[2] * hc, Hx[2] * hc};
  S = f32x4{S[0], S[0], S[2], S[2]};
  float mref, mt;
  f32x4 rs;
  unsigned long long raise;
#define SH_M(d, a, b) "v_mfma_f32_16x16x32_bf16 v[" d "], v[" a "], " b ", v[" d "]\n\t"
#define SH_X(n, a, y) "s_waitcnt lgkmcnt(" #n ")\n\t" SH_M("100:103", a, "%[" #y "]") SH_M("104:107", a, "v[" a "]")
#define SH_D " row_mask:0xf bank_mask:0xf\n\t"
  __builtin_amdgcn_s_setprio(0);
  if (k < TPW - 2) poll_ge_sleep(c.ct + C_FULL, (unsigned)(2 * k + 2));
  asm volatile("s_mov_b32 s42, %[cap]\n"
               "1:\n\tds_read_b32 v136, %[afl]\n\tds_read_b128 v[112:115], %[xa]\n\tds_read_b128 v[116:119], %[xb]\n\tds_read_b128 v[120:123], %[xa] offset:128\n\t"
               "ds_read_b128 v[124:127], %[xb] offset:128\n\tds_read_b128 v[128:131], %[xa] offset:256\n\tds_read_b128 v[132:135], %[xb] offset:256\n\t"
               "ds_read_b32 %[mr], %[amr]\n\ts_sub_u32 s42, s42, 1\n\ts_cselect_b32 s41, 0, %[need]\n\ts_waitcnt lgkmcnt(7)\n\t"
               "v_readfirstlane_b32 s40, v136\n\ts_cmp_lt_u32 s40, s41\n\ts_cbranch_scc1 1b\n\ts_setprio 3\n\t"
               SH_X(6, "112:115", y0) SH_X(5, "116:119", y1) SH_X(4, "120:123", y2) SH_X(3, "124:127", y3) SH_X(2, "128:131", y4) SH_X(1, "132:135", y5)
               SH_M("108:111", "112:115", "%[q0]") SH_M("108:111", "116:119", "%[q1]")
               "s_waitcnt lgkmcnt(0)\n\tv_add_f32 v137, %[thr], %[mr]\n\tds_write_b32 %[am], %[mr]\n\ts_nop 1\n\t"
               "v_mov_b32_dpp v140, v100 quad_perm:[1,1,3,3]" SH_D "v_mov_b32_dpp v141, v101 quad_perm:[1,1,3,3]" SH_D
               "v_mov_b32_dpp v142, v102 quad_perm:[1,1,3,3]" SH_D "v_mov_b32_dpp v143, v103 quad_perm:[1,1,3,3]" SH_D
               SH_M("108:111", "120:123", "%[q2]")
               "v_fmac_f32 v104, 2.0, v100\n\tv_fmac_f32_dpp v105, v101, %[two] quad_perm:[0,0,2,2]" SH_D
               "v_fmac_f32 v106, 2.0, v102\n\tv_fmac_f32_dpp v107, v103, %[two] quad_perm:[0,0,2,2]" SH_D
               "v_cndmask_b32 v138, v140, v141, %[r1]\n\tv_cndmask_b32 v139, v142, v143, %[r1]\n\t"
               SH_M("108:111", "124:127", "%[q3]")
               "v_cndmask_b32 v104, v104, v105, %[r1]\n\tv_cndmask_b32 v106, v106, v107, %[r1]\n\tv_cndmask_b32 v138, v138, v139, %[r2]\n\t"
               "v_cndmask_b32 v104, v104, v106, %[r2]\n\tv_mul_f32 v139, %[nic], v138\n\t"
               SH_M("108:111", "128:131", "%[q4]")
               "v_fmac_f32 v104, v139, v138\n\tv_max_f32 v104, %[eps], v104\n\tv_rsq_f32 v105, v104\n\t"
               SH_M("108:111", "132:135", "%[q5]")
               "v_mul_f32 v104, v104, v105\n\t"
               "v_mov_b32_dpp v105, v105 row_ror:12 row_mask:0x2 bank_mask:0xf\n\tv_cvt_pk_bf16_f32 v106, v138, v104\n\ts_nop 0\n\t"
               "v_mov_b32_dpp v105, v105 row_ror:8 row_mask:0x4 bank_mask:0xf\n\tv_lshlrev_b32 v107, 16, v106\n\tv_and_b32 v139, 0xffff0000, v106\n\t"
               "v_mov_b32_dpp v105, v105 row_ror:4 row_mask:0x8 bank_mask:0xf\n\t"
               "v_pk_fma_f32 v[108:109], v[140:141], %[nsq], v[108:109]\n\tv_pk_fma_f32 v[110:111], v[142:143], %[nsq], v[110:111]\n\t"
               "v_mov_b32_dpp v100, v105 row_newbcast:0" SH_D "v_mov_b32_dpp v101, v105 row_newbcast:1" SH_D
               "v_mov_b32_dpp v102, v105 row_newbcast:2" SH_D "v_mov_b32_dpp v103, v105 row_newbcast:3" SH_D
               S_LG("108:109", "100:101") S_LG("110:111", "102:103") S_SUB("140:141", "108:109") S_SUB("142:143", "110:111")
               "v_exp_f32 v140, v140\n\tv_exp_f32 v141, v141\n\tv_exp_f32 v142, v142\n\tv_exp_f32 v143, v143\n\t"
               "v_max3_f32 %[mt], v108, v109, v110\n\tv_sub_f32 v138, v138, v107\n\tv_sub_f32 v104, v104, v139\n\tv_max_f32 %[mt], %[mt], v111\n\t"
               "v_pk_mul_f32 v[140:141], v[140:141], v[100:101]\n\tv_pk_mul_f32 v[142:143], v[142:143], v[102:103]\n\t"
               "v_cmp_gt_f32 %[ra], %[mt], v137\n\tv_cvt_pk_bf16_f32 v107, v138, v104\n\t"
               "v_cvt_pk_bf16_f32 v140, v140, v141\n\tv_cvt_pk_bf16_f32 v141, v142, v143\n\t"
               "ds_write_b64 %[ap], v[140:141]\n\ts_mov_b64 exec, %[dg]\n\t"
               "ds_write_b16 %[ai], v106\n\tds_write_b16 %[ai], v107 offset:16\n\tds_write_b16_d16_hi %[ai], v106 offset:32\n\tds_write_b16_d16_hi %[ai], v107 offset:48\n\t"
               "s_cmp_eq_u64 %[ra], 0\n\ts_cselect_b64 exec, 1, 0\n\tds_add_u32 %[af], %[inc]\n\ts_mov_b64 exec, -1\n\ts_setprio 0"
               : "+{v[100:103]}"(Hx), "+{v[104:107]}"(G), "+{v[108:111]}"(S), [mr] "=&{v150}"(mref), [mt] "=&v"(mt), [ra] "=&s"(raise)
               : [y0] "v"(yb[0]), [y1] "v"(yb[1]), [y2] "v"(yb[2]), [y3] "v"(yb[3]), [y4] "v"(yb[4]), [y5] "v"(yb[5]),
                 [q0] "v"(s_qf(c, 0)), [q1] "v"(s_qf(c, 1)), [q2] "v"(s_qf(c, 2)), [q3] "v"(s_qf(c, 3)), [q4] "v"(s_qf(c, 4)), [q5] "v"(s_qf(c, 5)),
                 [afl] "v"(c.ct + C_FULL), [xa] "v"(xa), [xb] "v"(xb), [amr] "v"(c.amr), [am] "v"(am), [ap] "v"(ap), [ai] "v"(ai),
                 [af] "v"(af), [inc] "v"(inc), [nsq] "v"(f32x2{nsq, nsq}), [two] "v"(two),
                 [need] "s"(__builtin_amdgcn_readfirstlane(2 * k + 2)), [r1] "s"(0xAAAAAAAAAAAAAAAAull), [r2] "s"(0xCCCCCCCCCCCCCCCCull),
                 [dg] "s"(0xF0000F0000F0000Full), [cap] "i"(SPIN_CAP), [thr] "i"(__builtin_bit_cast(unsigned, THR)), [toff] "s"(f32x2{TOFF, TOFF}),
                 [eps] "i"(__builtin_bit_cast(unsigned, EPS * CC)), [nic] "i"(__builtin_bit_cast(unsigned, -1.f / CC))
               : "memory", "scc", "s40", "s41", "s42", "v112", "v113", "v114", "v115", "v116", "v117", "v118", "v119", "v120", "v121", "v122", "v123", "v124", "v125",
                 "v126", "v127", "v128", "v129", "v130", "v131", "v132", "v133", "v134", "v135", "v136", "v137", "v138", "v139", "v140", "v141", "v142", "v143");
  if (__builtin_expect(raise != 0ull, 0)) {
    rs = Hx;
    mref = fmaxf(mref, 32.f * __builtin_rintf(xrow16_max(mt) * (1.f / 32.f)));
    float pp[4];
#pragma unroll
    for (int i = 0; i < 4; ++i) pp[i] = __builtin_amdgcn_exp2f(S[i] - mref) * rs[i];
    asm volatile("ds_max_f32 %6, %3\n\tds_write_b32 %2, %3\n\tds_write_b64 %0, %1\n\ts_mov_b64 exec, 1\n\tds_add_u32 %4, %5\n\ts_mov_b64 exec, -1"
                 : : "v"(ap), "v"(u32x2{cvtpk(pp[0], pp[1]), cvtpk(pp[2], pp[3])}), "v"(am), "v"(mref), "v"(af), "v"((unsigned)NPK), "v"(c.amr) : "memory");
  }
}

using u32x16 = __attribute__((ext_vector_type(16))) unsigned;
typedef __attribute__((address_space(1))) unsigned short gus;
extern "C" __global__ __launch_bounds__(768, 3) void dino_main(const void*, const void*, const void*, const void*, const void*,
                                                               const void*, const void*, void*, void*) {
  u32x16 ka; u32x2 kb; unsigned long long tent64;
  __shared__ __attribute__((aligned(16))) char lds[LDS_BYTES];
  const int wave = __builtin_amdgcn_readfirstlane((int)threadIdx.x >> 6);
  const bool helper = blockIdx.x < 8;
  lds_ptr L0 = (lds_ptr)lds;
  const unsigned lbase = (unsigned)(size_t)L0;
  const unsigned ct = lbase + CT_OFF;
#define KARG(v, i) ((char*)(((unsigned long long)v[2 * (i) + 1] << 32) | v[2 * (i)]))
#define q KARG(ka, 0)
#define f0 KARG(ka, 1)
#define f1 KARG(ka, 2)
#define wq KARG(ka, 3)
#define wk KARG(ka, 4)
#define wv KARG(ka, 5)
#define wo KARG(ka, 6)
#define box KARG(ka, 7)
#define tent ((unsigned)tent64)
#define LANE_VARS int lane; asm volatile("v_mbcnt_lo_u32_b32 %0, -1, 0\n\tv_mbcnt_hi_u32_b32 %0, -1, %0" : "=v"(lane)); \
  const int r = lane & 15, g = lane >> 4; (void)r; (void)g;
#define DOFF_VARS unsigned doff[3];   \
  _Pragma("unroll") for (int i = 0; i < 3; ++i) doff[i] = (unsigned)(lane ^ ((swz_tbl(i) >> ((lane >> 3) * 4)) & 7)) << 4;
#define BARRIER() do { asm volatile("s_waitcnt lgkmcnt(0)" ::: "memory"); __builtin_amdgcn_s_barrier(); asm volatile("" ::: "memory"); } while (0)

  {
    unsigned doff[3];
    static_assert(swz_tbl(0) == swz_tbl(3) && swz_tbl(1) == swz_tbl(4) && swz_tbl(2) == swz_tbl(5), "");
    unsigned l16, db;
#define PD(d, b, off, pol) "global_load_lds_dwordx4 %[" #d "], s[" #b "] offset:" #off pol "\n\t"
    asm volatile("s_load_dwordx16 s[64:79], %[kp], 0x0\n\ts_load_dwordx2 s[80:81], %[kp], 0x40\n\ts_memrealtime s[82:83]\n\t"
                 "v_mbcnt_lo_u32_b32 %[l16], -1, 0\n\tv_mbcnt_hi_u32_b32 %[l16], -1, %[l16]\n\t"
                 "s_lshr_b32 s84, %[b], 3\n\ts_and_b32 s85, %[b], 7\n\t"
                 "v_lshrrev_b32 %[db], 1, %[l16]\n\tv_and_b32 %[db], -4, %[db]\n\t"
                 "s_mov_b32 s86, %[t0]\n\ts_mov_b32 s87, %[t1]\n\ts_mov_b32 s88, %[t2]\n\t"
                 "v_bfe_u32 %[d0], s86, %[db], 3\n\tv_bfe_u32 %[d1], s87, %[db], 3\n\tv_bfe_u32 %[d2], s88, %[db], 3\n\t"
                 "v_xor_b32 %[d0], %[d0], %[l16]\n\tv_xor_b32 %[d1], %[d1], %[l16]\n\tv_xor_b32 %[d2], %[d2], %[l16]\n\t"
                 "v_lshlrev_b32 %[d0], 4, %[d0]\n\tv_lshlrev_b32 %[d1], 4, %[d1]\n\tv_lshlrev_b32 %[d2], 4, %[d2]\n\t"
                 "v_lshlrev_b32 %[l16], 4, %[l16]\n\t"
                 "s_cmp_eq_u32 s84, 0\n\ts_cbranch_scc1 4f\n\t"
                 "s_cmp_lg_u32 %[w], 0\n\ts_cbranch_scc1 7f\n\t"
                 "s_setprio 3\n\tv_lshlrev_b32 %[db], 3, %[l16]\n\ts_mov_b32 m0, %[junk]\n\ts_lshl_b32 s89, s84, 13\n\t"
                 "s_cmp_gt_u32 s84, 3\n\ts_cbranch_scc1 2f\n\t"
                 "s_getpc_b64 s[90:91]\n1:\n\ts_sub_u32 s86, s89, 8192\n\ts_min_u32 s86, s86, dino_end-1b-8192\n\t"
                 "s_add_u32 s90, s90, s86\n\ts_addc_u32 s91, s91, 0\n\tglobal_load_lds_dword %[db], s[90:91]\n"
                 "2:\n\ts_mul_i32 s86, s85, 98304\n\ts_mul_i32 s87, s84, 3072\n\ts_sub_u32 s87, s87, 3072\n\t"
                 "s_add_u32 s88, s86, s87\n\ts_lshl1_add_u32 s86, s86, s87\n\ts_lshl_b32 s86, s86, 1\n\t"
                 "s_mul_i32 s87, s85, 12288\n\ts_lshr_b32 s90, s89, 1\n\ts_add_u32 s87, s87, s90\n\ts_sub_u32 s87, s87, 13 * 4096\n\t"
                 "s_sub_u32 s89, s89, 4 * 8192\n\ts_cmp_gt_u32 s85, 5\n\ts_cselect_b32 s89, -1, s89\n\ts_cmp_ge_u32 s89, 11 * 8192\n\t"
                 "s_waitcnt lgkmcnt(0)\n\ts_cbranch_scc1 3f\n\t"
                 "s_cmp_lt_u32 s89, 9 * 8192\n\ts_cselect_b64 s[90:91], s[70:71], s[72:73]\n\ts_cselect_b32 s89, s89, s87\n\t"
                 "s_add_u32 s90, s90, s89\n\ts_addc_u32 s91, s91, 0\n\tglobal_load_lds_dword %[db], s[90:91]\n"
                 "3:\n\ts_mov_b32 m0, 0x3000\n\t"
                 "s_add_u32 s90, s68, s88\n\ts_addc_u32 s91, s69, 0\n\t"
                 "s_add_u32 s92, s66, s86\n\ts_addc_u32 s93, s67, 0\n\t"
                 PD(d0, 90:91, 0, " nt") PD(d1, 90:91, 1024, " nt") PD(d2, 90:91, 2048, " nt")
                 "s_mov_b32 m0, 0\n\ts_nop 0\n\t"
                 PD(d0, 92:93, 0, " nt") PD(d1, 92:93, 1024, " nt")
                 "s_sub_u32 s89, s84, 15\n\ts_cmp_gt_u32 s85, 5\n\ts_cselect_b32 s89, -1, s89\n\ts_cmp_ge_u32 s89, 11\n\ts_cbranch_scc1 9f\n\t"
                 "s_mov_b32 m0, %[junk]\n\ts_sub_u32 s87, s87, 11 * 4096\n\ts_lshl_b32 s89, s89, 13\n\ts_cmp_lt_u32 s89, 9 * 8192\n\t"
                 "s_cselect_b64 s[90:91], s[76:77], s[74:75]\n\ts_cselect_b32 s89, s89, s87\n\t"
                 "s_add_u32 s90, s90, s89\n\ts_addc_u32 s91, s91, 0\n\tglobal_load_lds_dword %[db], s[90:91]\n\t"
                 "s_branch 9f\n"
                 "7:\n\ts_cmp_eq_u32 %[w], 1\n\ts_cbranch_scc1 8f\n\ts_barrier\n\ts_branch 8f\n"
                 "4:\n\t"
                 "s_cmp_gt_u32 %[b], 5\n\ts_cbranch_scc1 8f\n\ts_cmp_gt_u32 %[w], 9\n\ts_cbranch_scc1 8f\n\t"
                 "s_mov_b32 m0, %[img]\n\ts_mul_i32 s86, %[w], 9216\n\ts_and_b32 s88, %[w], 1\n\ts_lshl1_add_u32 s89, %[b], s88\n\t"
                 "s_mul_i32 s88, s88, 6144\n\ts_mul_i32 s89, s89, 6144\n\ts_add_u32 s88, s88, %[hr]\n\t"
                 "s_cmp_lg_u32 %[w], 0\n\ts_waitcnt lgkmcnt(0)\n\ts_cbranch_scc1 5f\n\t"
                 PD(d0, 64:65, 0, "") PD(d1, 64:65, 1024, "") PD(d2, 64:65, 2048, "") PD(d0, 64:65, 3072, "")
                 "s_mov_b32 m0, %[img] + 4096\n\ts_add_u32 s90, s64, 0x1000\n\ts_addc_u32 s91, s65, 0\n\t"
                 PD(d1, 90:91, 0, "") PD(d2, 90:91, 1024, "")
                 "\n5:\n\ts_cmp_gt_u32 %[w], 7\n\ts_cselect_b64 s[92:93], s[72:73], s[70:71]\n\ts_cselect_b32 m0, s88, s86\n\ts_cselect_b32 s86, s89, s86\n\t"
                 "s_cselect_b32 s87, 2, 3\n\ts_add_u32 s92, s92, s86\n\ts_addc_u32 s93, s93, 0\n"
                 "6:\n\ts_sub_u32 s87, s87, 1\n\t"
                 PD(d0, 92:93, 0, "") PD(d1, 92:93, 1024, "") PD(d2, 92:93, 2048, "")
                 "s_add_u32 m0, m0, 3072\n\ts_add_u32 s92, s92, 3072\n\ts_addc_u32 s93, s93, 0\n\ts_cmp_lg_u32 s87, 0\n\ts_cbranch_scc1 6b"
                 "\n8:\n\ts_waitcnt lgkmcnt(0)\n9:"
                 : "={s[64:79]}"(ka), "={s[80:81]}"(kb), "={s[82:83]}"(tent64),
                   [d0] "=&v"(doff[0]), [d1] "=&v"(doff[1]), [d2] "=&v"(doff[2]), [l16] "=&v"(l16), [db] "=&v"(db)
                 : [kp] "s"(__builtin_amdgcn_kernarg_segment_ptr()), [b] "s"(blockIdx.x), [w] "s"(wave),
                   [t0] "i"(swz_tbl(0)), [t1] "i"(swz_tbl(1)), [t2] "i"(swz_tbl(2)), [junk] "i"(JUNK), [img] "i"(IMG_A), [hr] "i"(H_R)
                 : "memory", "scc", "vcc", "s84", "s85", "s86", "s87", "s88", "s89", "s90", "s91", "s92", "s93", "s94", "s95", "s96", "s97", "s98", "s99");
  }
  const unsigned t0 = (unsigned)__builtin_amdgcn_s_memrealtime() - T0BACK;

  if (__builtin_expect(blockIdx.x < 6, 0)) {
    LANE_VARS
    DOFF_VARS
    const int j = blockIdx.x;
    const int qq = (lane >> 2) & 3, p4 = lane & 3, e4 = wave & 3;
    int ra = H_R + r * ROWB + ((g ^ swz(r)) << 4);
    bf16x8 a[6], b[6], w[18];
    auto rows = [&](int t, bf16x8* d) {
#pragma unroll
      for (int kk = 0; kk < 6; ++kk) d[kk] = *(LDS_AS(bf16x8)*)(L0 + t * 6144 + ((kk & 1) ? (ra ^ 64) : ra) + (kk >> 1) * 128);
    };
    int o[3];
#pragma unroll
    for (int t = 0; t < 3; ++t) o[t] = tr_off(4 * g + qq, 4 * (3 * e4 + t) + p4);
    unsigned qo = lbase + H_Q + 3 * e4 * 512 + ((g >> 1) * 16 + r) * 16 + (g & 1) * 8, qa = lbase + IMG_A + tr_off(r, 8 * wave + g);
    if (wave == 11) {
      unsigned d;
      asm volatile("s_load_dword %0, %1, 0x40\n\ts_barrier\n\ts_barrier\n\ts_barrier\n\ts_waitcnt lgkmcnt(0)" : "=&s"(d) : "s"(box + EPOCH_OFF) : "memory");
    } else
    asm volatile("s_waitcnt vmcnt(9)\n\ts_barrier" : PIN3(o, 0), "+v"(qo), "+v"(qa) :: "memory");
    if (wave < 6 || wave == 10) {
      rows((IMG_A - H_R) / 6144, a);
      const u32x2 m0 = *(LDS_AS(u32x2)*)(size_t)qa, m1 = *(LDS_AS(u32x2)*)(size_t)(qa ^ 32);
      static_assert((IMG_A - H_R) % 6144 == 0, "");
      const bf16x8 ones = __builtin_bit_cast(bf16x8, u32x4{0x3f803f80u, 0x3f803f80u, 0x3f803f80u, 0x3f803f80u});
      f32x4 sm = {0.f, 0.f, 0.f, 0.f};
#pragma unroll
      for (int i = 0; i < 6; ++i) sm = mfma(ones, a[i], sm);
      const float mean = sm[0] * (1.f / CC);
      if (__builtin_expect(wave < 6, 1)) {
        img_st(L0, H_C, wave, lane, __builtin_bit_cast(bf16x8, u32x4{cvtpk(bflo(m0[0]) - mean, bfhi(m0[0]) - mean), cvtpk(bflo(m0[1]) - mean, bfhi(m0[1]) - mean),
                                                                   cvtpk(bflo(m1[0]) - mean, bfhi(m1[0]) - mean), cvtpk(bflo(m1[1]) - mean, bfhi(m1[1]) - mean)}));
      } else {
        asm volatile("s_barrier" : PIN3(a, 0), PIN3(a, 3) :: "memory");
        f32x2 v2 = {0.f, 0.f}, v3 = v2;
#pragma unroll
        for (int i = 0; i < 12; ++i) {
          const u32x4 d = __builtin_bit_cast(u32x4, a[i >> 1]);
          const unsigned d0 = d[2 * (i & 1)], d1 = d[2 * (i & 1) + 1];
          const f32x2 x0 = f32x2{bflo(d0), bfhi(d0)} - mean, x1 = f32x2{bflo(d1), bfhi(d1)} - mean;
          v2 += x0 * x0; v3 += x1 * x1;
        }
        v2 += v3;
        *(LDS_AS(float)*)(L0 + CT_OFF + r * 4) = LOG2E * __builtin_amdgcn_rsqf(xrow16_sum(v2[0] + v2[1]) * (1.f / CC) + EPS);
      }
    }
    if (wave < 4) {
      wait_vm<0>();
      BARRIER();
#pragma unroll
      for (int kk = 0; kk < 6; ++kk) {
        b[kk] = img_ld(L0, H_C, kk, lane);
        w[kk] = mk8(tr_read(L0 + kk * 12288 + o[0]), tr_read(L0 + kk * 12288 + 6144 + o[0]));
        __builtin_amdgcn_sched_barrier(0);
      }
#pragma unroll
      for (int i = 6; i < 18; ++i) {
        w[i] = mk8(tr_read(L0 + (i % 6) * 12288 + o[i / 6]), tr_read(L0 + (i % 6) * 12288 + 6144 + o[i / 6]));
        __builtin_amdgcn_sched_barrier(0);
      }
#pragma unroll
      for (int t = 0; t < 3; ++t) {
        f32x4 D = {0.f, 0.f, 0.f, 0.f};
#pragma unroll
        for (int kk = 0; kk < 6; ++kk) D = mfma(w[6 * t + kk], b[kk], D);
        *(LDS_AS(u32x2)*)(size_t)(qo + t * 512) = u32x2{cvtpk(D[0], D[1]), cvtpk(D[2], D[3])};
      }
      BARRIER();
    } else if (wave == 8 || wave == 9) {
      unsigned so = lane * 16 + (2 * j + (wave & 1)) * 1024;
      unsigned epoch;
      asm volatile("global_load_dword %0, %1, %2 sc1\n\ts_barrier\n\ts_waitcnt vmcnt(0)" : "=v"(epoch) : "v"(EPOCH_OFF), "s"(box) : "memory");
      rows(wave & 1, a);
      unsigned ta = line_tag(epoch, 8), tb = line_tag(epoch, 9);
      asm volatile("" : PIN3(a, 0), PIN3(a, 3), "+v"(ta), "+v"(tb), "+v"(so));
      BARRIER();
#pragma unroll
      for (int kk = 0; kk < 6; ++kk) b[kk] = img_ld(L0, H_Q, kk, lane);
      const float sc = *(LDS_AS(float)*)(L0 + CT_OFF + r * 4);
      __builtin_amdgcn_sched_barrier(0);
      f32x4 D = {0.f, 0.f, 0.f, 0.f};
#pragma unroll
      for (int kk = 0; kk < 6; ++kk) D = mfma(a[kk], b[kk], D);
      D *= sc;
      const unsigned p0 = cvtpk(D[0], D[1]), p1 = cvtpk(D[2], D[3]);
      asm volatile("global_store_dwordx4 %0, %1, %2 sc1\n\ts_nop 1" : : "v"(so), "v"(u32x4{p0, p1, p0 ^ p1 ^ ta, tb}), "s"(box + QK_OFF) : "memory");
    } else if (wave != 11) {
      if (wave != 10) asm volatile("s_waitcnt vmcnt(0) lgkmcnt(0)\n\ts_barrier" ::: "memory");
      BARRIER();
    }
    BARRIER();
    issue_rows16(wo + wave * 6144, lbase + wave * 6144, doff);
    if (wave < 2) issue_rows16(wv + (2 * j + wave) * 6144, lbase + H_R + wave * 6144, doff);
    const unsigned epoch = epoch_load(box);
    BARRIER();
    {
      const int o0 = tr_off(8 * g + qq, 4 * wave + p4), o1 = tr_off(8 * g + 4 + qq, 4 * wave + p4);
#pragma unroll
      for (int kk = 0; kk < 6; ++kk) b[kk] = mk8(tr_read(L0 + kk * 12288 + o0), tr_read(L0 + kk * 12288 + o1));
      f32x4 D[2];
#pragma unroll
      for (int h = 0; h < 2; ++h) {
        rows(h, a);
        D[h] = f32x4{0.f, 0.f, 0.f, 0.f};
#pragma unroll
        for (int kk = 0; kk < 6; ++kk) D[h] = mfma(a[kk], b[kk], D[h]);
      }
      const int e = wave / 3;
      asm volatile("global_store_dwordx4 %0, %1, %2 sc1\n\ts_waitcnt vmcnt(0)"
                   : : "v"(lane * 16 + (e * 15 + 3 * j + wave) * 1024), "v"(pack8(D[0] * SQRT_C, D[1] * SQRT_C)), "s"(box + W2_OFF) : "memory");
    }
    BARRIER();
    if (wave == 11)
      asm volatile("s_mov_b64 exec, 1\n\tglobal_store_dwordx2 %0, %1, %2 sc1\n\ts_mov_b64 exec, -1\n\ts_nop 1"
                   : : "v"(j * 128), "v"(u32x2{line_tag(epoch, 10), line_tag(epoch, 11)}), "s"(box + W2F_OFF) : "memory");
  }

  int rslot = -1;
  if (__builtin_expect(wave == 0, 1)) {
    LANE_VARS
    DOFF_VARS
    if (helper) BARRIER();
    const unsigned afull = ct + C_FULL + r * 4;
    {
      const int ti = helper ? 1023 : (lane * 31 + (int)(blockIdx.x >> 3) - 1) & 1023;
      const int sp = ti >> 5, yy = sp * 8 + ((blockIdx.x + sp) & 7), xx = (ti + 5 * sp) & 31;
      unsigned tmp, pub = helper ? 2 * (TPW - 1) : 0, pre = 0, afr = ct + C_FREE + (helper ? ((TPW - 1) % NSLOT) * 4 : 0), e0, e1, e2, g0, g1, g2;
      asm volatile("s_mov_b32 s44, %[due0]\n\ts_mov_b32 s46, %[y0]\n\ts_mov_b32 s47, %[cap]\n\ts_mov_b32 s48, %[i0]\n\ts_mov_b32 s49, %[s0]\n\t"
                   "s_mov_b32 s51, 0\n\ts_mov_b32 s45, 0\n\ts_movk_i32 s39, %[p3s]\n"
                   "1:\n\tv_readlane_b32 s53, %[ob], s48\n\tv_readlane_b32 s52, %[oa], s48\n\ts_add_u32 m0, s49, 0x3000\n\tv_cmp_gt_u32 vcc, s51, %[pre]\n\t"
                   "v_add_u32 %[g0], s53, %[d0]\n\tv_add_u32 %[g1], s53, %[d1]\n\tv_add_u32 %[g2], s53, %[d2]\n\t"
                   "v_add_u32 %[e0], s52, %[d0]\n\tv_add_u32 %[e1], s52, %[d1]\n\tv_add_u32 %[e2], s52, %[d2]\n\t"
                   "s_cbranch_vccnz 5f\n\t"
                   "s_cmp_eq_u32 s48, 0\n\ts_cbranch_scc1 2f\n\ts_cmp_eq_u32 s48, 2\n\ts_cbranch_scc1 8f\n"
                   LD_GATE(1, "")
                   LD1(g0, 68:69, 0) LD1(g1, 68:69, 1024) LD1(g2, 68:69, 2048) "s_mov_b32 m0, s49\n\ts_nop 0\n\t" LD1(e0, 66:67, 0) LD1(e1, 66:67, 1024)
                   LD_NEXT
                   "\n2:\n\tv_add_u32 %[afr], 4, %[afr]\n\ts_cmp_eq_u32 s49, %[lasts]\n\ts_cbranch_scc1 7f\n"
                   "3:\n\tv_mov_b32 %[pre], 0\n\ts_add_u32 m0, s49, 0x800\n\tds_read_b32 %[pre], %[afr]\n"
                   LD_GATE(2, "")
                   LD1(e2, 56:57, 0) LD1(e0, 56:57, 1024) LD1(e1, 56:57, 2048) LD1(e2, 56:57, 3072) "s_add_u32 m0, s49, 0x1800\n\ts_add_u32 s48, s48, 1\n\t" LD1(e0, 58:59, 0)
                   LD_NEXT
                   LD_GATE(3, "s_cmp_gt_u32 s48, %[tpw] - 2\n\ts_cbranch_scc1 38f\n39:\n\t")
                   LD1(e1, 58:59, 1024) LD1(e2, 58:59, 2048) LD1(e0, 58:59, 3072) "s_add_u32 m0, s49, 0x2800\n\ts_add_u32 s49, s49, %[slot]\n\t" LD1(e1, 60:61, 0) LD1(e2, 60:61, 1024)
                   LD_NEXT
                   "s_cmp_eq_u32 s49, %[ring]\n\ts_cselect_b32 s49, 0, s49\n\ts_cmp_gt_i32 s46, 30\n\ts_cbranch_scc1 6f\n"
                   "4:\n\ts_cmp_eq_u32 s48, %[pk]\n\ts_cmovk_i32 s39, %[p3]\n\ts_cmp_lg_u32 s48, %[tpw]\n\ts_cbranch_scc1 1b\n\ts_branch 9f\n"
                   LD_PUB(1, 0, 15, 30) LD_PUB(2, 5, 20, 35) LD_PUB(3, 10, 25, 40)
                   "38:\n\ts_cmp_eq_u32 s46, 10\n\ts_cselect_b32 s43, 23, 0\n\ts_cmp_lt_u32 s42, s43\n\ts_cbranch_scc0 39b\n\tv_add_u32 %[tmp], 1, %[pub]\n\ts_waitcnt vmcnt(22)\n\tds_write_b32 %[af], %[tmp]\n\ts_branch 39b\n"
                   "5:\n\ts_movk_i32 s40, 0x100\n"
                   "40:\n\tds_read_b32 %[pre], %[afr]\n\t" LD_VM
                   "s_sub_u32 s40, s40, 1\n\ts_cselect_b32 s42, 0, s42\n\ts_cselect_b32 s40, 0, s40\n\t"
                   "s_cmp_le_i32 s42, s46\n\ts_cbranch_scc1 42f\n"
                   "41:\n\ts_sub_i32 s47, s47, 1\n\ts_cmp_lt_i32 s47, 0\n\ts_waitcnt lgkmcnt(0)\n\ts_cbranch_scc1 10b\n\t"
                   "v_cmp_gt_u32 vcc, s51, %[pre]\n\ts_cbranch_vccnz 40b\n\ts_branch 10b\n"
                   LD_PUB0(4, 0, 15, 30)
                   "6:\n\ts_waitcnt vmcnt(45)\n\tv_add_u32 %[pub], 2, %[pub]\n\tds_write_b32 %[af], %[pub]\n\ts_sub_u32 s46, s46, 15\n\ts_branch 4b\n"
                   "7:\n\ts_waitcnt lgkmcnt(0)\n\ts_add_u32 s51, s51, 8\n\tv_subrev_u32 %[afr], %[frb], %[afr]\n\ts_branch 3b\n"
                   "8:\n\ts_barrier\n\ts_add_u32 s46, s46, 64\n\ts_branch 10b\n"
                   "9:\n\ts_cmp_lt_i32 s46, 30\n\ts_cbranch_scc1 91f\n\ts_waitcnt vmcnt(30)\n\tv_add_u32 %[pub], 2, %[pub]\n\tds_write_b32 %[af], %[pub]\n"
                   "91:\n\ts_cmp_lt_i32 s46, 15\n\ts_cbranch_scc1 92f\n\tv_add_u32 %[tmp], 1, %[pub]\n\ts_waitcnt vmcnt(27)\n\tds_write_b32 %[af], %[tmp]\n"
                   "92:\n\ts_waitcnt lgkmcnt(0)"
                   : [pub] "+v"(pub), [pre] "+v"(pre), [afr] "+v"(afr), [tmp] "=&v"(tmp),
                     [e0] "=&v"(e0), [e1] "=&v"(e1), [e2] "=&v"(e2), [g0] "=&v"(g0), [g1] "=&v"(g1), [g2] "=&v"(g2)
                   : "{s[64:79]}"(ka), "{s[56:57]}"(f0 + 0x800), "{s[58:59]}"(f0 + 0x30000), "{s[60:61]}"(f0 + 0x31000), [t0] "s"(t0), [af] "v"(afull),
                     [d0] "v"(doff[0]), [d1] "v"(doff[1]), [d2] "v"(doff[2]), [oa] "v"(yy * 393216 + xx * 6144), [ob] "v"(yy * 98304 + xx * 3072),
                     [due0] "s"(helper ? HDUE * (PACE16 / 3) : PACES / 3), [y0] "s"(helper ? -15 : -74), [i0] "s"(helper ? TPW - 1 : 0),
                     [s0] "s"(helper ? ((TPW - 1) % NSLOT) * SLOT : 0),
                     [cap] "i"(SPIN_CAP), [win] "i"(PWIN), [ahi] "i"(AHI), [adl] "i"(ADL), [amg] "i"(AMG), [p3] "i"(PACE16 / 3), [p3s] "i"(PACES / 3), [pk] "i"(PK1), [slot] "i"(SLOT),
                     [ring] "i"(NSLOT * SLOT), [frb] "i"(NSLOT * 4), [lasts] "i"((NSLOT - 1) * SLOT), [tpw] "i"(TPW)
                   : "memory", "scc", "vcc", "s39", "s40", "s41", "s42", "s43", "s44", "s45", "s46", "s47", "s48", "s49", "s51", "s52", "s53");
    }
    lds_st32(ct + C_ISS, 1u);
    asm volatile("s_waitcnt vmcnt(15)\n\tds_write_b32 %0, %1\n\ts_waitcnt vmcnt(12)\n\tds_write_b32 %0, %2\n\ts_waitcnt vmcnt(0)\n\tds_write_b32 %0, %3"
                 : : "v"(afull), "v"(2 * TPW - 2), "v"(2 * TPW - 1), "v"(~0u) : "memory");
    {
      unsigned d;
      asm volatile("global_load_dword %0, %1, %2 sc1\n\ts_setprio 0\n\ts_waitcnt vmcnt(0)" : "=v"(d) : "v"(blockIdx.x * 128), "s"(box) : "memory");
    }
    rslot = 7;
  } else {
    if (wave == 1) {
      LANE_VARS
    {
      const unsigned o = lane * 8;
      unsigned z = (o >= C_MREF && o < C_MREF + 64) ? 0xff800000u : 0u;
      if (helper && o >= C_CONS && o < C_MREF) z = NE * ((TPW - 1) / NPK);
      u32x2 zz = {z, z};
      if (o >= C_PFL && o < C_CONS) zz = u32x2{(o - C_PFL) / 2 + 2, (o - C_PFL) / 2 + 4} + (helper ? 2u * NPK * ((TPW - 1) / NPK) : 0u);
      if (o == C_EP) zz[1] = QKW0 + QKW1 * min(t0 + T0BACK - tent, 45u);
      *(LDS_AS(u32x2)*)(L0 + CT_OFF + o) = zz;
      *(LDS_AS(u32x2)*)(L0 + ONES_OFF + (lane < 48 ? o : 0)) = u32x2{0x3f803f80u, 0x3f803f80u};
    }
    }
    if (helper || wave == 1) BARRIER();
    if (wave <= NE) {
      LANE_VARS
      const int ej = wave - 1;
      const int qq = (lane >> 2) & 3, p4 = lane & 3;
      const unsigned apk = lbase + IMG_A + lane * 16;
      const unsigned epoch = epoch_load(box);
      __builtin_amdgcn_s_setprio(2);
      {
        static_assert((TPW & 1) == 1 && C_CONS == C_PFL + 64 && C_FREE == C_FULL + 64, "");
        const int k0 = helper ? TPW - 1 : 0;
        unsigned need = 2 * k0 + 2;
        asm volatile("s_mov_b32 s46, 0\n\ts_mov_b64 exec, 0\n\ts_nop 3\n\ts_branch 99f\n"
                     "2:\n\ts_mov_b64 exec, -1\n\ts_nop 7\n\t" PV_Z4(72, 73, 74, 75) PV_Z4(76, 77, 78, 79) PV_Z4(80, 81, 82, 83) PV_Z4(84, 85, 86, 87)
                     "v_mov_b32 v88, 0xff800000\n\t"
                     "s_mov_b32 s46, %[k0]\n\ts_mov_b32 s45, %[s0]\n\ts_mov_b32 s44, %[e0]\n\ts_mov_b32 s47, %[cap]\n\t"
                     "s_cmp_lg_u32 s46, 0\n\ts_cbranch_scc1 32f\n"
                     "3:\n\tv_mov_b32 v148, %[ca]\n\t" PV_WAITF(4, 148) PV_FETCH_A
                     "s_cmp_lg_u32 s46, 0\n\ts_cbranch_scc1 99f\n\t"
                     "s_mov_b64 exec, 2\n\tds_add_u32 v148, %[one] offset:64\n\ts_mov_b64 exec, -2\n\t"
                     "v_add_u32 %[need], 2, %[need]\n\tv_add_u32 v148, 4, v148\n\ts_mov_b64 exec, -1\n\t"
                     PV_WAITF(5, 148)
                     "v_mov_b32 v149, v148\n\ts_mov_b64 exec, -2\n\tv_add_u32 %[need], 2, %[need]\n\tv_add_u32 v148, 4, v148\n\t"
                     "s_mov_b64 exec, 1\n\tv_add_u32 %[need], %[npk2], %[need]\n\ts_mov_b64 exec, -1\n\t"
                     PV_REQ("126:129", "130:133", "134:135", 136, 148) PV_FETCH_B
                     "s_mov_b64 exec, 2\n\tds_add_u32 v149, %[one] offset:64\n\ts_mov_b64 exec, -1\n"
                     "6:\n\t"
                     PV_STEP(1, "126:129", "130:133", "134:135", 134, 135, 136, 148, "138:141", "142:145", "146:147", 137, 149,
                             "92:95", "98:101", "104:107", "90:93", "96:99", "102:105",
                             PV_MOV(92, 90) PV_MOV(93, 91), PV_MOV(98, 96) PV_MOV(99, 97), PV_MOV(104, 102) PV_MOV(105, 103), "")
                     "s_cbranch_vccnz 7f\n"
                     "16:\n\t" PV_FETCH_A PV_REL(148)
                     "s_lshl1_add_u32 s54, s46, -2\n\ts_lshr_b64 s[54:55], %[sch], s54\n\ts_and_b32 s54, s54, 15\n\ts_cbranch_scc1 31f\n"
                     "30:\n\t"
                     PV_STEP(2, "138:141", "142:145", "146:147", 146, 147, 137, 149, "126:129", "130:133", "134:135", 136, 148,
                             "110:113", "116:119", "122:125", "108:111", "114:117", "120:123",
                             PV_MOV(110, 108) PV_MOV(111, 109), PV_MOV(116, 114) PV_MOV(117, 115), PV_MOV(122, 120) PV_MOV(123, 121),
                             "s_cmp_eq_u32 s46, %[last] - 1\n\ts_cbranch_scc1 98f\n\t")
                     "s_cbranch_vccnz 8f\n"
                     "26:\n\t" PV_FETCH_B PV_REL(149)
                     "s_branch 6b\n"
                     PV_WAITF(7, 148) "s_branch 16b\n"
                     PV_WAITF(8, 149) "s_branch 26b\n"
                     PV_SLOW(1, "126:129", "130:133", "134:135", 134, 135, 136, 148, 126, 127, 128, 129)
                     PV_SLOW(2, "138:141", "142:145", "146:147", 146, 147, 137, 149, 138, 139, 140, 141)
                     "31:\n\ts_cmp_lg_u32 s54, 8\n\ts_cbranch_scc1 34f\n"
                     "32:\n\tv_mbcnt_lo_u32_b32 v152, 31, 0\n\tv_lshlrev_b32 v152, 7, v152\n\ts_mov_b32 s55, %[gcap]\n"
                     "33:\n\tglobal_load_dwordx2 v[150:151], v152, %[fl] sc1\n\ts_sub_u32 s55, s55, 1\n\ts_cselect_b32 s56, 0, -1\n\ts_waitcnt vmcnt(0) lgkmcnt(0)\n\t"
                     "v_cmp_ne_u64 s[48:49], %[tag], v[150:151]\n\ts_sleep 2\n\ts_and_b32 s48, s48, s56\n\ts_cbranch_scc1 33b\n\t"
                     "s_mov_b32 s57, 0\n\tv_mov_b32 v150, %[apk]\n\ts_cmp_lg_u32 s46, %[last]\n\ts_cbranch_scc1 30b\n\ts_mov_b32 s54, 7\n\ts_branch 38f\n"
                     "34:\n\ts_cmp_lg_u32 s46, %[last] - 3\n\ts_cbranch_scc1 36f\n\tv_mov_b32 v151, %[iss]\n"
                     "35:\n\tds_read_b32 v150, v151\n\ts_sub_i32 s47, s47, 1\n\ts_cmp_lt_i32 s47, 0\n\ts_cselect_b32 s55, 0, 1\n\ts_waitcnt lgkmcnt(0)\n\t"
                     "v_readfirstlane_b32 s56, v150\n\ts_cmp_lt_u32 s56, s55\n\ts_cbranch_scc1 35b\n"
                     "36:\n\ts_mul_i32 s55, s57, 3072\n\tv_add_u32 v150, s55, %[apk]\n\ts_getpc_b64 s[48:49]\n"
                     "37:\n\ts_mul_i32 s55, s57, 40\n\ts_add_u32 s55, s55, 38f-37b\n\ts_add_u32 s48, s48, s55\n\ts_addc_u32 s49, s49, 0\n\t"
                     "s_add_u32 s57, s57, s54\n\ts_sub_u32 s54, s54, 1\n\ts_setpc_b64 s[48:49]\n"
                     "38:\n\t" PV_W2(0, 3, 4, 7, 8, 11) PV_W2(12, 15, 16, 19, 20, 23) PV_W2(24, 27, 28, 31, 32, 35) PV_W2(36, 39, 40, 43, 44, 47)
                     PV_W2(48, 51, 52, 55, 56, 59) PV_W2(60, 63, 64, 67, 68, 71)
                     "s_waitcnt lgkmcnt(0)\n\ts_branch 3b\n"
                     "96:\n\t" PV_RESC(134, 135, 126, 127, 128, 129) "s_nop 0\n\ts_branch 95f\n"
                     "98:\n\t" PV_MF("76:79", "114:117", "138:141") PV_MF("80:83", "120:123", "138:141")
                     "99:\n\ts_cmp_eq_u32 s46, 0\n\ts_cselect_b32 s54, 0, %[nl]\n\ts_cselect_b32 s55, 0, %[x1n]\n\ts_waitcnt lgkmcnt(0)\n\t"
                     "v_mov_b64 v[108:109], v[90:91]\n\tv_mov_b64 v[110:111], v[90:91]\n\tv_mov_b64 v[112:113], v[96:97]\n\tv_mov_b64 v[114:115], v[96:97]\n\t"
                     "v_mov_b64 v[116:117], v[102:103]\n\tv_mov_b64 v[118:119], v[102:103]\n\tv_add_u32 v120, %[aw], %[apk]\n\tv_add_u32 v121, %[ar], %[apk]\n\t"
                     "v_mov_b32 v122, %[x1]\n\tv_lshrrev_b32 v123, 1, %[apm]\n\tv_add_u32 v123, %[lwo], v123\n\t"
                     "v_mbcnt_lo_u32_b32 v124, -1, 0\n\tv_mbcnt_hi_u32_b32 v124, -1, v124\n\tv_and_b32 v125, 1, v124\n\tv_and_b32 v124, 48, v124\n\t"
                     "v_lshl_add_u32 v124, v125, 2, v124\n\tv_add_u32 v124, %[lw], v124\n"
                     "94:\n\tds_read_b32 v136, v122 offset:%[pfd]\n\tds_read_b64 v[134:135], %[apm] offset:%[el]\n\tds_read_b128 v[126:129], %[apk] offset:8 * %[el]\n\t"
                     "ds_read_b128 v[130:133], %[apu] offset:2 * %[el]\n\ts_sub_i32 s47, s47, 1\n\ts_cmp_lt_i32 s47, 0\n\ts_cselect_b32 s54, 0, s54\n\ts_waitcnt lgkmcnt(3)\n\t"
                     "v_readfirstlane_b32 s56, v136\n\ts_cmp_lt_u32 s56, s54\n\ts_cbranch_scc1 94b\n\ts_waitcnt lgkmcnt(2)\n\t"
                     "v_cmp_neq_f32 vcc, v134, v88\n\tv_cmp_neq_f32 s[50:51], v135, v88\n\ts_or_b64 vcc, vcc, s[50:51]\n\ts_waitcnt lgkmcnt(0)\n\ts_cbranch_scc1 96b\n"
                     "95:\n\t" PV_MF("84:87", "130:133", "126:129") "s_nop 1\n\t" PV_MF("72:75", "92:95", "126:129") "s_nop 1\n\t" PV_MF("72:75", "108:111", "126:129")
                     "s_nop 1\n\tv_add_f32 v153, v86, v87\n\tv_add_f32 v151, v84, v85\n\t" PV_MF("76:79", "98:101", "126:129")
                     "v_rcp_f32 v150, v153\n\tv_mul_f32 v151, %[nic], v151\n\t" PV_MF("76:79", "112:115", "126:129")
                     "v_mul_f32 v151, v151, v150\n\t" PV_PK(72, 73) PV_MF("80:83", "104:107", "126:129") PV_PK(74, 75)
                     "v_cvt_pk_bf16_f32 v72, v72, v73\n\t" PV_MF("80:83", "116:119", "126:129") "v_cvt_pk_bf16_f32 v73, v74, v75\n\tds_write_b64 v120, v[72:73]\n\t"
                     PV_NRM(76, 77, 78, 79, 2048) "v_log_f32 v153, v153\n\t" PV_NRM(80, 81, 82, 83, 4096)
                     "s_and_b64 exec, exec, 1\n\tds_add_u32 v122, %[one]\n\ts_cselect_b64 exec, -1, 0\n\t"
                     "v_subrev_f32 v88, %[toff], v88\n\tv_add_f32 v88, v88, v153\n\tds_write_b32 v123, v88\n\tds_read2_b32 v[114:115], v124 offset1:2\n"
                     "97:\n\tds_read_b32 v150, v122\n\tds_read_b128 v[90:93], v121\n\tds_read_b128 v[94:97], v121 offset:1024\n\tds_read_b128 v[98:101], v121 offset:2048\n\t"
                     "ds_read_b128 v[102:105], v121 offset:3072\n\tds_read_b128 v[106:109], v121 offset:4096\n\tds_read_b128 v[110:113], v121 offset:5120\n\t"
                     "s_sub_i32 s47, s47, 1\n\ts_cmp_lt_i32 s47, 0\n\ts_cselect_b32 s55, 0, s55\n\ts_waitcnt vmcnt(0) lgkmcnt(6)\n\t"
                     "v_readfirstlane_b32 s56, v150\n\ts_cmp_lt_u32 s56, s55\n\ts_cbranch_scc1 97b\n\ts_cmp_eq_u32 s46, 0\n\t"
                     PV_G(5, "90:93", "0:3", "4:7", "8:11", "0", "0", "0")
                     PV_G(4, "94:97", "12:15", "16:19", "20:23", "v[72:75]", "v[76:79]", "v[80:83]")
                     PV_G(3, "98:101", "24:27", "28:31", "32:35", "v[72:75]", "v[76:79]", "v[80:83]")
                     PV_G(2, "102:105", "36:39", "40:43", "44:47", "v[72:75]", "v[76:79]", "v[80:83]")
                     PV_G(1, "106:109", "48:51", "52:55", "56:59", "v[72:75]", "v[76:79]", "v[80:83]")
                     PV_G(0, "110:113", "60:63", "64:67", "68:71", "v[72:75]", "v[76:79]", "v[80:83]")
                     "v_xor_b32 v114, v114, %[ltag]\n\tv_xor_b32 v115, v115, %[ltag]\n\ts_nop 3\n\t"
                     "v_cvt_pk_bf16_f32 v116, v72, v73\n\tv_cvt_pk_bf16_f32 v120, v74, v75\n\tv_cvt_pk_bf16_f32 v117, v76, v77\n\tv_cvt_pk_bf16_f32 v121, v78, v79\n\t"
                     "v_cvt_pk_bf16_f32 v118, v80, v81\n\tv_bitop3_b32 v119, v116, v117, v114 bitop3:0x96\n\tv_cvt_pk_bf16_f32 v122, v82, v83\n\t"
                     "v_bitop3_b32 v123, v120, v121, v115 bitop3:0x96\n\tv_xor_b32 v119, v119, v118\n\tv_xor_b32 v123, v123, v122\n\t"
                     "s_cbranch_scc1 2b\n\t"
                     "global_store_dwordx4 %[so], v[116:119], %[bx] sc1\n\tglobal_store_dwordx4 %[so], v[120:123], %[bx2] sc1\n\t"
                     "s_waitcnt lgkmcnt(0)\n\ts_nop 0"
                     : [need] "+v"(need)
                     : [so] "v"(((ej * 16 + (lane >> 3)) * RSTR + blockIdx.x) * 128 + (lane & 7) * 16), [ltag] "v"(line_tag(epoch, lane & 7)),
                       [bx] "s"(box), [bx2] "s"(box + 8 * RSTR * 128), [aw] "s"(A_IMG - IMG_A + (ej >> 1) * 1024 + (ej & 1) * 8), [ar] "i"(A_IMG - IMG_A),
                       [nic] "i"(__builtin_bit_cast(unsigned, -1.f / CC)), [toff] "i"(__builtin_bit_cast(unsigned, TOFF)),
                       [lwo] "i"(CT_OFF + C_LW - PKM / 2), [lw] "i"(CT_OFF + C_LW), [x1] "i"(CT_OFF + C_X1), [x1n] "i"(NE),
                       [nl] "i"(2 * TPW + 2 * NPK), [el] "i"(((TPW - 1) % NPK) * 128), [pfd] "i"(C_PFL + ((TPW - 1) % NPK) * 4 - C_X1),
                       [apk] "v"(apk), [apm] "v"(lbase + PKM + r * 8), [apu] "v"(lbase + PKU + g * 64 + (r & 3) * 16),
                       [ox] "v"(lbase + tr_off(4 * g + qq, 4 * ej + p4)), [oy] "v"(lbase + 32 * ROWB + tr_off(2 * g + (qq >> 1), 4 * ej + p4)),
                       [ca] "v"(ct + (lane == 0 ? C_PFL + (k0 % NPK) * 4 : C_FULL + (k0 % NSLOT) * 4)),
                       [lim] "v"(ct + (lane == 0 ? C_PFL + NPK * 4 : C_FULL + NSLOT * 4)), [base] "v"(ct + (lane == 0 ? C_PFL : C_FULL)), [one] "v"(1u),
                       [k0] "s"(k0), [s0] "s"(helper ? ((TPW - 1) % NSLOT) * SLOT : 0), [e0] "s"(helper ? ((TPW - 1) % NPK) * 128 : 0),
                       [w2] "s"(box + W2_OFF + ej * 18432 - (lbase + IMG_A)), [fl] "s"(box + W2F_OFF),
                       [tag] "s"(u32x2{line_tag(epoch, 10), line_tag(epoch, 11)}), [gcap] "i"(GSPIN_CAP), [iss] "i"(CT_OFF + C_ISS), [cap] "i"(SPIN_CAP), [slot] "i"(SLOT), [ring] "i"(NSLOT * SLOT), [pring] "i"(NPK * 128),
                       [npk2] "i"(2 * NPK), [last] "i"(TPW - 1), [tight] "i"(TPW - 5),  [sch] "s"(W2SCH)
                     : "memory", "scc", "vcc", "s44", "s45", "s46", "s47", "s48", "s49", "s50", "s51", "s52", "s53", "s54", "s55", "s56", "s57",
                       "v0", "v1", "v2", "v3", "v4", "v5", "v6", "v7", "v8", "v9", "v10", "v11", "v12", "v13", "v14", "v15", "v16", "v17", "v18", "v19", "v20", "v21", "v22", "v23", "v24", "v25", "v26", "v27", "v28", "v29", "v30", "v31", "v32", "v33", "v34", "v35", "v36", "v37", "v38", "v39", "v40", "v41", "v42", "v43", "v44", "v45", "v46", "v47", "v48", "v49", "v50", "v51", "v52", "v53", "v54", "v55", "v56", "v57", "v58", "v59", "v60", "v61", "v62", "v63", "v64", "v65", "v66", "v67", "v68", "v69", "v70", "v71", "v72", "v73", "v74", "v75", "v76", "v77", "v78", "v79", "v80", "v81", "v82", "v83", "v84", "v85", "v86", "v87", "v88",
                       "v90", "v91", "v92", "v93", "v94", "v95", "v96", "v97", "v98", "v99", "v100", "v101", "v102", "v103", "v104", "v105", "v106", "v107",
                       "v108", "v109", "v110", "v111", "v112", "v113", "v114", "v115", "v116", "v117", "v118", "v119", "v120", "v121", "v122", "v123",
                       "v124", "v125", "v126", "v127", "v128", "v129", "v130", "v131", "v132", "v133", "v134", "v135", "v136", "v137", "v138", "v139",
                       "v140", "v141", "v142", "v143", "v144", "v145", "v146", "v147", "v148", "v149", "v150", "v151", "v152", "v153", "v154", "v155");
      }
    } else {
      LANE_VARS
      const int c = wave - 1 - NE;
      SCtx sc;
      sc.L0 = L0; sc.ct = ct; sc.apk = lbase + IMG_A + lane * 16; sc.apm = lbase + PKM + r * 8;
      sc.api = lbase + PKU + g * 64 + (r & 3) * 2 + ((r >> 2) == g ? 0 : 8); sc.amr = ct + C_MREF + r * 4;
      sc.o_rb = r * ROWB + ((g ^ swz(r)) << 4);
      sc.ev = (r & 1) ^ 1;
      sc.o_yb = (r & 1) ? ONES_OFF + (g << 4) : (32 + (r >> 1)) * ROWB + ((g ^ swz(r >> 1)) << 4);
      if (c >= NEARLY) {
        static_assert(NS - NEARLY == 3, "");
        const int u = c - NEARLY;
        const unsigned epoch = epoch_load(box);
        const unsigned ta = line_tag(epoch, 8), tb = line_tag(epoch, 9);
        const unsigned qw = lds_ld32(ct + C_EP + 4);
        while ((unsigned)__builtin_amdgcn_s_memrealtime() - tent < qw) __builtin_amdgcn_s_sleep(4);
        u32x4 v[4];
#pragma unroll 1
        for (int sp = 0; sp < GSPIN_CAP; ++sp) {
          asm volatile("global_load_dwordx4 %0, %4, %5 sc1\n\tglobal_load_dwordx4 %1, %4, %5 offset:1024 sc1\n\tglobal_load_dwordx4 %2, %4, %5 offset:2048 sc1\n\t"
                       "global_load_dwordx4 %3, %4, %5 offset:3072 sc1\n\ts_waitcnt vmcnt(0)"
                       : "=&v"(v[0]), "=&v"(v[1]), "=&v"(v[2]), "=&v"(v[3]) : "v"(lane * 16), "s"(box + QK_OFF + u * 4096) : "memory");
          unsigned bad = 0;
#pragma unroll
          for (int t = 0; t < 4; ++t) bad |= (v[t][0] ^ v[t][1] ^ v[t][2] ^ ta) | (v[t][3] ^ tb);
          if (!__any(bad != 0u)) break;
        }
#pragma unroll
        for (int t = 0; t < 4; ++t)
          *(LDS_AS(u32x2)*)(L0 + IMG_A + (4 * u + t) * 512 + ((g >> 1) * 16 + r) * 16 + (g & 1) * 8) = u32x2{v[t][0], v[t][1]};
        asm volatile("" ::: "memory");
        lds_add(ct + C_QI, 1u);
        lds_st32(ct + C_EP, epoch);
      }
      static_assert(NE == 4 && NPK == 6 && NSLOT == 10, "");
      s_items(sc, helper ? TPW : c, helper || c >= NEARLY);
      static_assert(NH1 == 3 && NS == 7 && TPW == 33, "");
      const int hi = c == 0 ? 5 : c - 2;
      if (hi >= (helper ? 4 : 0)) s_half(sc, TPW - NH1 + (hi >> 1), hi & 1);
      rslot = c;
    }
  }

  if (rslot >= 0 && ((blockIdx.x >> 3) & 3) == 1) {
    LANE_VARS
    __builtin_amdgcn_s_setprio(0);
    const int rid = ((blockIdx.x >> 5) << 3) | (blockIdx.x & 7);
    const int qa = ((rid >> 1) & 3) * 4 + ((rid >> 3) & 1) * 2;
    const char* src = box + (size_t)(rid * RSTR + rslot * 32) * 128;
    const int e = lane & 7;
    const int oidx = (qa + (e & 1)) * CC + 48 * (rid >> 4) + 16 * (e < 6 ? e >> 1 : 0) + 8 * (rid & 1) + (lane >> 3);
    const unsigned short qv = ((const gus*)q)[oidx];
    u32x4 am;
#pragma unroll
    for (int d = 0; d < 4; ++d) am[d] = (e >> 1) == d ? ((e & 1) ? 0xffff0000u : 0x0000ffffu) : 0u;
    const unsigned epoch = *(LDS_AS(unsigned)*)(L0 + CT_OFF + C_EP);
    const unsigned arf = lbase + RED_OFF + 4096 + (lane & 1) * 4;
    poll_ge_sleep(ct + C_FREE + ((TPW - NPK) % NSLOT) * 4, 8u * ((TPW - NPK) / NSLOT + 1));
    lds_st32(arf + rslot * 8, 0u);
#define R_LD(d, m, o) "s_mov_b64 exec, s[" m "]\n\tglobal_load_dwordx4 v[" d "], %[vo], %[src] offset:" #o " sc1\n\t"
#define R_W(l, a0, a1, a2, a3) "v_cvt_pk_bf16_f32 v" #a3 ", v" #a3 ", v" #a3 "\n\tv_and_b32 v" #a0 ", v60, v" #a3 "\n\tv_and_b32 v" #a1 ", v61, v" #a3 "\n\t" \
                               "v_and_b32 v" #a2 ", v62, v" #a3 "\n\tv_and_b32 v" #a3 ", v63, v" #a3 "\n\t"
#define R_G(n, w, m, l, d0, d1, d2, d3, a0, a1, a2, a3, c) \
  "s_mov_b64 exec, s[" m "]\n\ts_cbranch_execz " #n "f\n\ts_waitcnt vmcnt(" #w ")\n\tv_bitop3_b32 v" #l ", v" #d0 ", v" #d1 ", v" #d2 " bitop3:0x96\n\t" \
  "v_bitop3_b32 v" #l ", v" #l ", v" #d3 ", %[tag] bitop3:0x96\n\tv_mov_b32 v" #d3 ", s49\n\ts_mov_b64 exec, -1\n\tv_exp_f32 v" #a3 ", v" #l "\n\t" \
  "v_xor_b32_dpp v59, v" #l ", v" #l " quad_perm:[2,3,0,1] row_mask:0xf bank_mask:0x5\n\tv_xor_b32_dpp v59, v" #l ", v" #l " row_shr:4 row_mask:0xf bank_mask:0xa\n\t" \
  R_W(l, a0, a1, a2, a3) "v_cmp_ne_u32 s[" m "], 0, v59\n\ts_nop 0\n" \
  "1" #n ":\n\tv_mfma_f32_16x16x32_bf16 v[36:39], v[" #a0 ":" #a3 "], v[" #d0 ":" #d3 "], " c "\n\t"
#define R_X(n) #n ":\n\ts_mov_b64 exec, -1\n\ts_nop 2\n\ts_branch 1" #n "b\n"
#define R_F(l, d0, d3, a0, a1, a2, a3, c) "v_sub_f32 v" #a3 ", v" #l ", v56\n\tv_exp_f32 v" #a3 ", v" #a3 "\n\ts_nop 0\n\t" R_W(l, a0, a1, a2, a3) \
  "s_nop 1\n\tv_mfma_f32_16x16x32_bf16 v[36:39], v[" #a0 ":" #a3 "], v[" #d0 ":" #d3 "], " c "\n\t"
#define R_RD(d, a, b) "ds_read2st64_b32 v[" d "], %[ard] offset0:" #a " offset1:" #b "\n\t"
#define R_A(d, a) "v_pk_add_f32 v[" d "], v[" d "], v[" a "]\n\t"
#define R_S(x, f) "v_sub_f32 v" #f ", v" #f ", v56\n\tv_exp_f32 v" #f ", v" #f "\n\ts_nop 0\n\tv_mul_f32 v[" #x "], v[" #x "], v" #f "\n\tv_mul_f32 v[" #x " + 1], v[" #x " + 1], v" #f "\n\t"
    asm volatile("s_mov_b64 s[40:41], -1\n\ts_mov_b64 s[42:43], -1\n\ts_mov_b64 s[44:45], -1\n\ts_mov_b64 s[46:47], -1\n\ts_mov_b32 s48, %[cap]\n\ts_mov_b32 s49, 0x3f803f80\n\t"
                 "v_mov_b32 v57, 1\n\tv_mov_b32 v58, %[x1]\n"
                 "20:\n\ts_sleep 1\n\tds_read_b32 v56, v58\n\ts_sub_u32 s48, s48, 1\n\ts_cselect_b32 s50, 0, %[x1n]\n\ts_waitcnt lgkmcnt(0)\n\t"
                 "v_readfirstlane_b32 s51, v56\n\ts_cmp_lt_u32 s51, s50\n\ts_cbranch_scc1 20b\n\tv_mov_b32 v58, %[red]\n\ts_mov_b32 s48, %[gcap]\n\ts_sleep %[rs]\n"
                 "21:\n\t" R_LD("0:3", "40:41", 0) R_LD("4:7", "42:43", 1024) R_LD("8:11", "44:45", 2048) R_LD("12:15", "46:47", 3072)
                 "s_cmp_eq_u32 s48, %[gcap]\n\ts_cbranch_scc1 24f\n\ts_waitcnt vmcnt(0)\n24:\n\t"
                 R_G(1, 3, "40:41", 32, 0, 1, 2, 3, 16, 17, 18, 19, "0") R_G(2, 2, "42:43", 33, 4, 5, 6, 7, 20, 21, 22, 23, "v[36:39]")
                 R_G(3, 1, "44:45", 34, 8, 9, 10, 11, 24, 25, 26, 27, "v[36:39]") R_G(4, 0, "46:47", 35, 12, 13, 14, 15, 28, 29, 30, 31, "v[36:39]")
                 "v_max3_f32 v56, v32, v33, v34\n\ts_sub_u32 s48, s48, 1\n\ts_cbranch_scc1 22f\n\ts_or_b64 s[50:51], s[40:41], s[42:43]\n\tv_max_f32 v56, v56, v35\n\t"
                 "s_or_b64 s[50:51], s[50:51], s[44:45]\n\ts_or_b64 s[50:51], s[50:51], s[46:47]\n\tv_cmp_nlt_f32 vcc, |v56|, %[lim]\n\ts_cbranch_scc1 21b\n\t"
                 "s_cbranch_vccnz 25f\n"
                 "22:\n\ts_mov_b64 exec, %[wm]\n\tds_write_b128 %[awr], v[36:39]\n\ts_mov_b64 exec, 1\n\tds_add_rtn_u32 v59, v58, v57\n\ts_mov_b64 exec, -1\n\t"
                 R_RD("40:41", 0, 1) R_RD("42:43", 2, 3) R_RD("44:45", 4, 5) R_RD("46:47", 6, 7) R_RD("48:49", 8, 9) R_RD("50:51", 10, 11) R_RD("52:53", 12, 13) R_RD("54:55", 14, 15)
                 "s_waitcnt lgkmcnt(8)\n\tv_add_u32 v59, v59, v57\n\ts_nop 0\n\tv_readfirstlane_b32 s50, v59\n\ts_cmp_lg_u32 s50, 8\n\ts_cbranch_scc1 26f\n"
                 "23:\n\ts_waitcnt lgkmcnt(6)\n\t" R_A("40:41", "42:43") "s_waitcnt lgkmcnt(4)\n\t" R_A("44:45", "46:47") "s_waitcnt lgkmcnt(2)\n\t" R_A("48:49", "50:51")
                 R_A("40:41", "44:45") "s_waitcnt lgkmcnt(0)\n\t" R_A("52:53", "54:55") R_A("48:49", "52:53") R_A("40:41", "48:49")
                 "v_add_f32 v40, v40, v41\n\ts_nop 1\n\tv_mov_b32_dpp v41, v40 quad_perm:[2,3,2,3] row_mask:0xf bank_mask:0xf\n\ts_nop 1\n\t"
                 "v_mov_b32_dpp v41, v41 row_shl:4 row_mask:0xf bank_mask:0x5\n\tv_rcp_f32 v41, v41\n\ts_mov_b64 exec, %[om]\n\tv_mul_f32 v40, v40, v41\n\t"
                 "v_cvt_pk_bf16_f32 v40, v40, v40\n\tv_lshlrev_b32 v40, 16, v40\n\tv_add_f32 v40, v40, %[qf]\n\tv_cvt_pk_bf16_f32 v40, v40, v40\n\t"
                 "global_store_short %[oo], v40, %[out]\n\t"
                 "s_cmp_lg_u32 %[rid], 0\n\ts_cbranch_scc1 29f\n\ts_mov_b64 exec, 1\n\tv_mov_b32 v41, %[eo]\n\tglobal_store_dword v41, %[ep], %[bx] sc1\n\ts_branch 29f\n"
                 R_X(1) R_X(2) R_X(3) R_X(4)
                 "25:\n\tv_max_f32_dpp v56, v56, v56 row_ror:8 row_mask:0xf bank_mask:0xf\n\tv_mov_b32 v59, v56\n\ts_nop 1\n\tv_permlane16_swap_b32 v56, v59\n\tv_max_f32 v56, v56, v59\n\t"
                 "v_mov_b32 v59, v56\n\ts_nop 1\n\tv_permlane32_swap_b32 v56, v59\n\tv_max_f32 v56, v56, v59\n\tv_mov_b32 v57, 0x101\n\tv_add_u32 v59, %[rs8], %[arf]\n\tds_write_b32 v59, v56\n\t"
                 R_F(32, 0, 3, 16, 17, 18, 19, "0") R_F(33, 4, 7, 20, 21, 22, 23, "v[36:39]") R_F(34, 8, 11, 24, 25, 26, 27, "v[36:39]") R_F(35, 12, 15, 28, 29, 30, 31, "v[36:39]")
                 "s_nop 7\n\ts_branch 22b\n"
                 "26:\n\ts_waitcnt vmcnt(0)\n\ts_and_b32 s50, s50, 0xff\n\ts_cmp_lg_u32 s50, 8\n\ts_cbranch_scc1 29f\n\t"
                 "ds_read2_b32 v[0:1], %[arf] offset1:2\n\tds_read2_b32 v[2:3], %[arf] offset0:4 offset1:6\n\tds_read2_b32 v[4:5], %[arf] offset0:8 offset1:10\n\t"
                 "ds_read2_b32 v[6:7], %[arf] offset0:12 offset1:14\n\ts_waitcnt lgkmcnt(0)\n\tv_max3_f32 v56, v0, v1, v2\n\tv_max3_f32 v56, v56, v3, v4\n\t"
                 "v_max3_f32 v56, v56, v5, v6\n\tv_max_f32 v56, v56, v7\n\t"
                 R_S(40, 0) R_S(42, 1) R_S(44, 2) R_S(46, 3) R_S(48, 4) R_S(50, 5) R_S(52, 6) R_S(54, 7) "s_branch 23b\n"
                 "29:\n\ts_mov_b64 exec, -1\n\ts_waitcnt lgkmcnt(0)"
                 :
                 : "{v[60:63]}"(am), [vo] "v"(lane * 16), [src] "s"(src), [tag] "v"(line_tag(epoch, e)),
                   [awr] "v"(lbase + RED_OFF + (rslot * 2 + (r >> 3)) * 256 + (r & 7) * 32 + (g & 1) * 16), [ard] "v"(lbase + RED_OFF + lane * 4), [arf] "v"(arf),
                   [qf] "v"((unsigned)qv << 16), [oo] "v"(oidx * 2), [out] "s"(KARG(kb, 0)), [ep] "v"(epoch + 1u), [bx] "s"(box), [rid] "s"(rid), [rs8] "s"(rslot * 8),
                   [wm] "s"(0xff00ff0000ff00ffull), [om] "s"(0x3f3f3f3f3f3f3f3full), [lim] "s"(64.f),
                   [cap] "i"(SPIN_CAP), [gcap] "i"(GSPIN_CAP), [x1] "i"(CT_OFF + C_X1), [x1n] "i"(NE), [red] "i"(CT_OFF + C_RED), [rs] "i"(RSLEEP), [eo] "i"(EPOCH_OFF)
                 : "memory", "scc", "vcc", "s40", "s41", "s42", "s43", "s44", "s45", "s46", "s47", "s48", "s49", "s50", "s51", "v0", "v1", "v2", "v3", "v4", "v5", "v6", "v7", "v8", "v9", "v10", "v11", "v12", "v13", "v14", "v15", "v16", "v17", "v18", "v19", "v20", "v21", "v22", "v23", "v24", "v25", "v26", "v27", "v28", "v29", "v30", "v31", "v32", "v33", "v34", "v35", "v36", "v37", "v38", "v39", "v40", "v41", "v42", "v43", "v44", "v45", "v46", "v47", "v48", "v49", "v50", "v51", "v52", "v53", "v54", "v55", "v56", "v57", "v58", "v59");
  }
}

#undef q
#undef f0
#undef f1
#undef wq
#undef wk
#undef wv
#undef wo
#undef box
extern "C" __global__ void dino_end() {}

extern "C" void kernel_launch(void* const* d_in, const int* in_sizes, int n_in,
                              void* d_out, int out_size, void* d_ws, size_t ws_size,
                              hipStream_t stream) {
  dino_main<<<NWG, 768, 0, stream>>>((const unsigned short*)d_in[0], (const char*)d_in[1], (const char*)d_in[2], (const char*)d_in[3],
                                     (const char*)d_in[4], (const char*)d_in[5], (const char*)d_in[6], (char*)d_ws + ((ws_size - (4u << 20)) & ~(size_t)0x1fffff),
                                     (unsigned short*)d_out);
}
```

```cpp
#include <hip/hip_runtime.h>
#include <hip/hip_bf16.h>
#include <stdint.h>

#define PROFOFF (380ll << 20)
#define TNOW(v) do {} while (0)
#define TPUT(id, v) do {} while (0)

using bf16x8  = __attribute__((ext_vector_type(8))) short;
using f32x4   = __attribute__((ext_vector_type(4))) float;
using f32x2   = __attribute__((ext_vector_type(2))) float;
using u32x4   = __attribute__((ext_vector_type(4))) unsigned;
using u32x2   = __attribute__((ext_vector_type(2))) unsigned;
using v4i16_t = __attribute__((ext_vector_type(4))) short;
typedef __attribute__((address_space(3))) char* lds_ptr;
#define LDS_AS(T) __attribute__((address_space(3))) T

constexpr int CC = 192;
constexpr int NWG = 256, NS = 7, NE = 4;
constexpr int TPW = 33;
constexpr int ROWB = 384;
constexpr int SLOT = 40 * ROWB;
constexpr int NSLOT = 10;
constexpr int RSTR = 322;
constexpr int EPOCH_OFF = 64 * RSTR * 128, QK_OFF = EPOCH_OFF + 4096, W2F_OFF = QK_OFF + 12288, W2_OFF = W2F_OFF + 1024;
static_assert(W2_OFF + 72 * 1024 <= (4 << 20), "");
constexpr int GSPIN_CAP = 1 << 16;
#define PACE16 777
#define PACES 810
#define PK1 14
#define PWIN 35
#define AHI 22
#define ADL 60
#define AMG 200
#define SYNCK 13
#define SYNCO 22
#define SYNCM 100
#define T0BACK 7
#define HDUE 93
#define QKW0 200
#define QKW1 3
#define RSLEEP 25
#define W2SCH 0x0311180000000000ull
constexpr int NEARLY = 4;
constexpr int NH1 = 3;
constexpr int NPK = 6;
constexpr float EPS = 1e-5f;
constexpr float SQRT_C = 13.856406460551018f;
constexpr float LOG2E = 1.4426950408889634f;
constexpr float THR = 20.0f;
constexpr float TOFF = 192.0f;
constexpr int SPIN_CAP = 1 << 22;

constexpr int IMG_A = NSLOT * SLOT;
constexpr int CT_OFF = IMG_A + 6144;
constexpr int PKM = CT_OFF + 512, PKU = PKM + NPK * 128;
constexpr int ONES_OFF = PKU + NPK * 256;
constexpr int JUNK = ONES_OFF + ROWB;
constexpr int LDS_BYTES = JUNK + 256;
constexpr int H_R = 192 * ROWB, H_Q = H_R + 32 * ROWB, H_C = H_Q + 6144;
static_assert(LDS_BYTES <= 163840 && (PKU & 15) == 0 && (ONES_OFF & 127) == 0, "lds");
constexpr int C_FULL = 0, C_FREE = 64, C_EP = 128, C_RED = 144, C_ISS = 152, C_X1 = 156, C_QI = 160, C_PFL = 192, C_CONS = 256,
              C_MREF = 320, C_LW = 384;

typedef __bf16 bf16x2_t __attribute__((ext_vector_type(2)));
__device__ __forceinline__ unsigned cvtpk(float lo, float hi) {
  return __builtin_bit_cast(unsigned, __builtin_convertvector(f32x2{lo, hi}, bf16x2_t));
}
__device__ __forceinline__ float bflo(unsigned p) { return __uint_as_float(p << 16); }
__device__ __forceinline__ float bfhi(unsigned p) { return __uint_as_float(p & 0xffff0000u); }
__device__ __forceinline__ bf16x8 pack8(const f32x4& a, const f32x4& b) {
  return __builtin_bit_cast(bf16x8, u32x4{cvtpk(a[0], a[1]), cvtpk(a[2], a[3]), cvtpk(b[0], b[1]), cvtpk(b[2], b[3])});
}
__device__ __forceinline__ float xrow16_max(float x) {
  auto s = __builtin_amdgcn_permlane16_swap(__float_as_uint(x), __float_as_uint(x), false, false);
  x = fmaxf(__uint_as_float(s[0]), __uint_as_float(s[1]));
  auto t = __builtin_amdgcn_permlane32_swap(__float_as_uint(x), __float_as_uint(x), false, false);
  return fmaxf(__uint_as_float(t[0]), __uint_as_float(t[1]));
}
__device__ __forceinline__ float xrow16_sum(float x) {
  auto s = __builtin_amdgcn_permlane16_swap(__float_as_uint(x), __float_as_uint(x), false, false);
  x = __uint_as_float(s[0]) + __uint_as_float(s[1]);
  auto t = __builtin_amdgcn_permlane32_swap(__float_as_uint(x), __float_as_uint(x), false, false);
  return __uint_as_float(t[0]) + __uint_as_float(t[1]);
}
__device__ __forceinline__ v4i16_t tr_read(lds_ptr p) {
  return __builtin_amdgcn_ds_read_tr16_b64_v4i16((LDS_AS(v4i16_t)*)p);
}
__device__ __forceinline__ f32x4 mfma(bf16x8 a, bf16x8 b, f32x4 c) {
  return __builtin_amdgcn_mfma_f32_16x16x32_bf16(a, b, c, 0, 0, 0);
}
__device__ __forceinline__ int swz(int p) { return (p & 6) | ((p >> 2) & 1); }
constexpr unsigned swz_tbl(int i) {
  unsigned t = 0;
  for (int j = 0; j < 8; ++j) { const int p = (8 * i + j) / 3; t |= (unsigned)((p & 6) | ((p >> 2) & 1)) << (4 * j); }
  return t;
}
__device__ __forceinline__ bf16x8 mk8(v4i16_t a, v4i16_t b) { return bf16x8{a[0], a[1], a[2], a[3], b[0], b[1], b[2], b[3]}; }

__device__ __forceinline__ void lds_st32(unsigned addr, unsigned v) {
  asm volatile("ds_write_b32 %0, %1" : : "v"(addr), "v"(v) : "memory");
}
__device__ __forceinline__ unsigned lds_ld32(unsigned addr) {
  unsigned v;
  asm volatile("ds_read_b32 %0, %1\n\ts_waitcnt lgkmcnt(0)" : "=v"(v) : "v"(addr) : "memory");
  return __builtin_amdgcn_readfirstlane(v);
}
__device__ __forceinline__ void lds_add(unsigned addr, unsigned n) {
  asm volatile("s_mov_b64 exec, 1\n\tds_add_u32 %0, %1\n\ts_mov_b64 exec, -1\n\ts_nop 3" : : "v"(addr), "v"(n) : "memory");
}
__device__ __forceinline__ void poll_ge_sleep(unsigned addr, unsigned need) {
  need = __builtin_amdgcn_readfirstlane(need);
  unsigned v, s, n2, cnt = SPIN_CAP;
  asm volatile("s_branch 2f\n1:\n\ts_sleep 2\n2:\n\tds_read_b32 %0, %4\n\ts_sub_u32 %3, %3, 1\n\ts_cselect_b32 %2, 0, %5\n\ts_waitcnt lgkmcnt(0)\n\t"
               "v_readfirstlane_b32 %1, %0\n\ts_cmp_lt_u32 %1, %2\n\ts_cbranch_scc1 1b"
               : "=&v"(v), "=&s"(s), "=&s"(n2), "+s"(cnt) : "v"(addr), "s"(need) : "memory", "scc");
}
__device__ __forceinline__ bf16x8 img_ld(lds_ptr L0, int off, int kk, int lane) { return *(LDS_AS(bf16x8)*)(L0 + off + kk * 1024 + lane * 16); }
__device__ __forceinline__ void img_st(lds_ptr L0, int off, int kk, int lane, bf16x8 v) { *(LDS_AS(bf16x8)*)(L0 + off + kk * 1024 + lane * 16) = v; }

template <int N> __device__ __forceinline__ void wait_vm() { asm volatile("s_waitcnt vmcnt(%0)" :: "i"(N) : "memory"); }
constexpr int A_IMG = ((TPW + 1 - NPK) % NSLOT) * SLOT, RED_OFF = ((TPW - NPK) % NSLOT) * SLOT;
__device__ __forceinline__ unsigned line_tag(unsigned epoch, unsigned i) {
  unsigned x = (epoch + 1u + i * 0x9E3779B9u) * 0x85EBCA6Bu;
  x ^= x >> 15; x *= 0xC2B2AE35u;
  return x ^ (x >> 13);
}
__device__ __forceinline__ unsigned epoch_load(const char* box) {
  unsigned e;
  asm volatile("global_load_dword %0, %1, %2 sc1\n\ts_waitcnt vmcnt(0)" : "=v"(e) : "v"(EPOCH_OFF), "s"(box) : "memory");
  return __builtin_amdgcn_readfirstlane(e);
}
__device__ __forceinline__ int tr_off(int row, int col4) {
  const int ch = col4 >> 1;
  return row * ROWB + (((ch & ~7) | ((ch & 7) ^ swz(row & 15))) << 4) + 8 * (col4 & 1);
}
#define PIN3(a, i) "+v"(a[i]), "+v"(a[i + 1]), "+v"(a[i + 2])

#define DMA1(d, b, off, pol) "global_load_lds_dwordx4 %[" #d "], %[" #b "] offset:" #off pol "\n\t"
__device__ __forceinline__ void issue_rows16(const char* a0, unsigned dst, const unsigned (&d)[3]) {
  unsigned keep;
  asm volatile("s_mov_b32 %[keep], m0\n\ts_mov_b32 m0, %[dst]\n\ts_nop 0\n\t"
               DMA1(d0, a, 0, "") DMA1(d1, a, 1024, "") DMA1(d2, a, 2048, "") DMA1(d0, a, 3072, "")
               "s_add_u32 m0, %[dst], 0x1000\n\ts_nop 0\n\t" DMA1(d1, b, 0, "") DMA1(d2, b, 1024, "") "s_mov_b32 m0, %[keep]"
               : [keep] "=&s"(keep) : [dst] "s"(dst), [a] "s"(a0), [b] "s"(a0 + 4096), [d0] "v"(d[0]), [d1] "v"(d[1]), [d2] "v"(d[2]) : "memory", "scc");
}
#define LD_VM "s_getreg_b32 s43, hwreg(HW_REG_IB_STS, 22, 2)\n\ts_getreg_b32 s42, hwreg(HW_REG_IB_STS, 0, 4)\n\ts_lshl4_add_u32 s42, s43, s42\n\t"
#define LD_GATE(n, X) \
  #n "0:\n\t" LD_VM "s_cmp_le_i32 s42, s46\n\ts_cbranch_scc1 " #n "2f\n" \
  #n "1:\n\t" X "s_cmp_gt_u32 s42, %[win]\n\ts_cbranch_scc1 " #n "5f\n\ts_cmp_ge_u32 s45, s44\n\ts_cbranch_scc1 " #n "7f\n" \
  #n "6:\n\ts_memrealtime s[40:41]\n\ts_sleep 1\n\ts_waitcnt lgkmcnt(0)\n\ts_sub_u32 s45, s40, %[t0]\n\ts_lshl_b32 s45, s45, 4\n\ts_cmp_lt_u32 s45, s44\n\ts_cbranch_scc1 " #n "6b\n" \
  #n "7:\n\t" LD_VM
#define LD_NEXT \
  "s_cmp_gt_u32 s42, %[ahi]\n\ts_cselect_b32 s42, %[amg], 0\n\ts_sub_u32 s43, s45, s44\n\ts_cmp_lt_u32 s43, s42\n\ts_cselect_b32 s42, %[p3] + %[adl], s39\n\t" \
  "s_add_u32 s44, s44, s42\n\ts_add_u32 s46, s46, 5\n\t"
#define LD_PUB0(n, a, b, c) \
  #n "2:\n\tv_add_u32 %[pub], 2, %[pub]\n\ts_cmp_ge_u32 s46, 30\n\ts_cbranch_scc1 " #n "4f\n\ts_cmp_ge_u32 s46, 15\n\ts_cbranch_scc1 " #n "3f\n\t" \
  "s_waitcnt vmcnt(" #a ")\n" #n "3:\n\ts_waitcnt vmcnt(" #b ")\n" #n "4:\n\ts_waitcnt vmcnt(" #c ")\n\tds_write_b32 %[af], %[pub] ; NOTRACK\n\t" \
  "s_sub_u32 s46, s46, 15\n\ts_branch " #n "1b\n"
#define LD_PUB(n, a, b, c) LD_PUB0(n, a, b, c) \
  #n "5:\n\ts_sleep 1\n\ts_sub_i32 s47, s47, 1\n\ts_cmp_gt_i32 s47, 0\n\ts_cbranch_scc1 " #n "0b\n\ts_waitcnt vmcnt(%[win])\n\ts_branch " #n "6b\n"
#define LD1(d, b, off) "global_load_lds_dwordx4 %[" #d "], s[" #b "] offset:" #off " nt\n\t"

#define PV_REQ(P, U, M, C, CA) \
  "v_lshl_add_u32 v150, s44, 3, %[apk]\n\tv_lshl_add_u32 v151, s44, 1, %[apu]\n\tv_add_u32 v152, s44, %[apm]\n\t" \
  "ds_read_b32 v" #C ", v" #CA "\n\tds_read_b128 v[" P "], v150\n\tds_read_b64 v[" M "], v152\n\tds_read_b128 v[" U "], v151\n\t"
#define PV_TR(d0, d1, a, o) "ds_read_b64_tr_b16 v[" #d0 ":" #d1 "], v" #a " offset:" #o "\n\t"
#define PV_FETCH(y0, y0b, a0, a0b, b0, b0b, y1, y1b, a1, a1b, b1, b1b, y2, y2b, a2, a2b, b2, b2b) \
  "v_add_u32 v153, s45, %[ox]\n\tv_add_u32 v154, s45, %[oy]\n\t" \
  PV_TR(a0, a0b, 153, 0) PV_TR(b0, b0b, 153, 6144) PV_TR(a1, a1b, 153, 128) PV_TR(b1, b1b, 153, 6272) PV_TR(a2, a2b, 153, 256) PV_TR(b2, b2b, 153, 6400) \
  PV_TR(y0, y0b, 154, 0) PV_TR(y1, y1b, 154, 128) PV_TR(y2, y2b, 154, 256) \
  "s_add_u32 s45, s45, %[slot]\n\ts_cmp_eq_u32 s45, %[ring]\n\ts_cselect_b32 s45, 0, s45\n\t"
#define PV_FETCH_A PV_FETCH(90, 91, 92, 93, 94, 95, 96, 97, 98, 99, 100, 101, 102, 103, 104, 105, 106, 107)
#define PV_FETCH_B PV_FETCH(108, 109, 110, 111, 112, 113, 114, 115, 116, 117, 118, 119, 120, 121, 122, 123, 124, 125)
#define PV_MF(d, a, p) "v_mfma_f32_16x16x32_bf16 v[" d "], v[" a "], v[" p "], v[" d "]\n\t"
#define PV_MOV(d, s) "v_mov_b32 v" #d ", v" #s "\n\t"
#define PV_WAITF(n, CA)   \
  #n ":\n\tds_read_b32 v155, v" #CA "\n\ts_sub_i32 s47, s47, 1\n\ts_cmp_lt_i32 s47, 0\n\ts_cbranch_scc1 " #n "9f\n\ts_waitcnt lgkmcnt(0)\n\t" \
  "v_cmp_lt_u32 vcc, v155, %[need]\n\ts_and_b64 vcc, vcc, 2\n\ts_cbranch_scc0 " #n "9f\n\ts_sleep 2\n\ts_branch " #n "b\n" #n "9:\n\ts_waitcnt lgkmcnt(0)\n\t"
#define PV_STEP(n, P, U, M, m0, m1, C, CA, P2, U2, M2, C2, CA2, X0, X1, X2, Y0, Y1, Y2, MV0, MV1, MV2, HOOK) \
  "s_waitcnt lgkmcnt(10)\n\t" \
  "v_cmp_lt_u32 vcc, v" #C ", %[need]\n\tv_cmp_neq_f32 s[48:49], v" #m0 ", v88\n\tv_cmp_neq_f32 s[50:51], v" #m1 ", v88\n\t" \
  "v_add_u32 v" #CA2 ", 4, v" #CA "\n\ts_or_b64 s[48:49], s[48:49], vcc\n\tv_cmp_eq_u32 s[52:53], v" #CA2 ", %[lim]\n\t" \
  "s_or_b64 s[48:49], s[48:49], s[50:51]\n\ts_cbranch_scc1 " #n "1f\n" \
  #n "0:\n\ts_add_u32 s44, s44, 128\n\ts_cmp_eq_u32 s44, %[pring]\n\ts_cselect_b32 s44, 0, s44\n\t" \
  "v_cndmask_b32 v" #CA2 ", v" #CA2 ", %[base], s[52:53]\n\t" \
  PV_MF("84:87", U, P) PV_MF("72:75", X0, P) PV_MF("76:79", X1, P) MV0 PV_MF("80:83", X2, P) MV1 \
  PV_MF("72:75", Y0, P) MV2 HOOK PV_REQ(P2, U2, M2, C2, CA2) PV_MF("76:79", Y1, P) PV_MF("80:83", Y2, P)
#define PV_REL(CA) \
  "s_mov_b64 exec, 3\n\tds_add_u32 v" #CA ", %[one] offset:64\n\ts_mov_b64 exec, -1\n\tv_add_u32 %[need], 2, %[need]\n\ts_add_u32 s46, s46, 1\n\t"
#define PV_SC1(p) "v_lshlrev_b32 v151, 16, v" #p "\n\tv_and_b32 v152, 0xffff0000, v" #p "\n\tv_mul_f32 v151, v151, v150\n\tv_mul_f32 v152, v152, v150\n\t" \
                  "v_cvt_pk_bf16_f32 v" #p ", v151, v152\n\t"
#define PV_MUL4(a, b, c, d) "v_mul_f32 v" #a ", v" #a ", v150\n\tv_mul_f32 v" #b ", v" #b ", v150\n\tv_mul_f32 v" #c ", v" #c ", v150\n\tv_mul_f32 v" #d ", v" #d ", v150\n\t"
#define PV_RESC(m0, m1, p0, p1, p2, p3) \
  "v_mov_b32 v150, v88\n\tv_max3_f32 v88, v88, v" #m0 ", v" #m1 "\n\tv_sub_f32 v150, v150, v88\n\tv_exp_f32 v150, v150\n\ts_nop 1\n\t" \
  PV_MUL4(72, 73, 74, 75) PV_MUL4(76, 77, 78, 79) PV_MUL4(80, 81, 82, 83) PV_MUL4(84, 85, 86, 87) \
  "v_sub_f32 v150, v" #m0 ", v88\n\tv_exp_f32 v150, v150\n\ts_nop 1\n\t" PV_SC1(p0) PV_SC1(p1) \
  "v_sub_f32 v150, v" #m1 ", v88\n\tv_exp_f32 v150, v150\n\ts_nop 1\n\t" PV_SC1(p2) PV_SC1(p3)
#define PV_SLOW(n, P, U, M, m0, m1, C, CA, p0, p1, p2, p3) \
  #n "1:\n\ts_bitcmp0_b32 vcc_lo, 0\n\ts_cbranch_scc1 " #n "4f\n\ts_cmp_gt_u32 s46, %[tight]\n\ts_cbranch_scc1 " #n "3f\n" \
  #n "2:\n\ts_sleep 2\n\tds_read_b32 v" #C ", v" #CA "\n\ts_sub_i32 s47, s47, 1\n\ts_cmp_lt_i32 s47, 0\n\ts_cbranch_scc1 " #n "3f\n\ts_waitcnt lgkmcnt(0)\n\t" \
  "v_cmp_lt_u32 vcc, v" #C ", %[need]\n\ts_and_b64 vcc, vcc, 1\n\ts_cbranch_scc1 " #n "2b\n" \
  #n "3:\n\t" PV_REQ(P, U, M, C, CA) "s_sub_i32 s47, s47, 1\n\ts_cmp_lt_i32 s47, 0\n\ts_cselect_b32 s54, 0, 1\n\ts_waitcnt lgkmcnt(0)\n\t" \
  "v_cmp_lt_u32 vcc, v" #C ", %[need]\n\tv_cmp_neq_f32 s[48:49], v" #m0 ", v88\n\tv_cmp_neq_f32 s[50:51], v" #m1 ", v88\n\t" \
  "s_and_b32 s54, s54, vcc_lo\n\ts_cbranch_scc1 " #n "3b\n\ts_or_b64 s[48:49], s[48:49], s[50:51]\n\ts_cbranch_scc0 " #n "0b\n\ts_branch " #n "5f\n" \
  #n "4:\n\tv_cmp_neq_f32 s[48:49], v" #m0 ", v88\n\ts_or_b64 s[48:49], s[48:49], s[50:51]\n\ts_cbranch_scc0 " #n "0b\n" \
  #n "5:\n\t" PV_RESC(m0, m1, p0, p1, p2, p3) \
  "s_branch " #n "0b\n"
#define PV_W2(a0, a1, b0, b1, c0, c1) \
  "global_load_dwordx4 v[" #a0 ":" #a1 "], v150, %[w2] sc1\n\tglobal_load_dwordx4 v[" #b0 ":" #b1 "], v150, %[w2] offset:1024 sc1\n\t" \
  "global_load_dwordx4 v[" #c0 ":" #c1 "], v150, %[w2] offset:2048 sc1\n\tv_add_u32 v150, 0xc00, v150\n\ts_sub_u32 s54, s54, 1\n\ts_cbranch_scc1 30b\n\t"

#define PV_PK(a, b) "v_pk_fma_f32 v[" #a ":" #b "], v[" #a ":" #b "], v[150:151], v[150:151] op_sel:[0,0,1] op_sel_hi:[1,0,1]\n\t"
#define PV_NRM(a, b, c, d, o)   \
  "v_pk_fma_f32 v[" #a ":" #b "], v[" #a ":" #b "], v[150:151], v[150:151] op_sel:[0,0,1] op_sel_hi:[1,0,1]\n\t" \
  "v_pk_fma_f32 v[" #c ":" #d "], v[" #c ":" #d "], v[150:151], v[150:151] op_sel:[0,0,1] op_sel_hi:[1,0,1]\n\t" \
  "v_cvt_pk_bf16_f32 v" #a ", v" #a ", v" #b "\n\tv_cvt_pk_bf16_f32 v" #b ", v" #c ", v" #d "\n\tds_write_b64 v120, v[" #a ":" #b "] offset:" #o "\n\t"
#define PV_G(n, a, w0, w1, w2, c0, c1, c2) "s_waitcnt lgkmcnt(" #n ")\n\tv_mfma_f32_16x16x32_bf16 v[72:75], v[" a "], v[" w0 "], " c0 "\n\t" \
  "v_mfma_f32_16x16x32_bf16 v[76:79], v[" a "], v[" w1 "], " c1 "\n\tv_mfma_f32_16x16x32_bf16 v[80:83], v[" a "], v[" w2 "], " c2 "\n\t"
#define PV_Z4(a, b, c, d) "v_mov_b32 v" #a ", 0\n\tv_mov_b32 v" #b ", 0\n\tv_mov_b32 v" #c ", 0\n\tv_mov_b32 v" #d ", 0\n\t"
#define PV_T(n)

using u32x8 = __attribute__((ext_vector_type(8))) unsigned;
struct SCtx {
  lds_ptr L0;
  unsigned ct, apk, apm, api, amr;
  int o_rb, o_yb, ev;
  u32x8 q[3];
  unsigned tq;
};
__device__ __forceinline__ bf16x8 s_qf(const SCtx& c, int j) {
  const u32x8& v = c.q[j >> 1];
  const int o = 4 * (j & 1);
  return __builtin_bit_cast(bf16x8, u32x4{v[o], v[o + 1], v[o + 2], v[o + 3]});
}
#define S_M(d, a, b) "v_mfma_f32_16x16x32_bf16 v[" d "], v[" a "], v[" b "], v[" d "]\n\t"
#define S_M0(d, a, b) "v_mfma_f32_16x16x32_bf16 v[" d "], v[" a "], v[" b "], 0\n\t"
#define S_R(d, a, o) "ds_read_b128 v[" d "], v" #a " offset:" #o "\n\t"
#define S_W(n) "s_waitcnt lgkmcnt(" #n ")\n\t"
#define S_D " row_mask:0xf bank_mask:0xf\n\t"
#define S_3(x, y) S_M("100:103", x, y) S_M("104:107", x, x) S_M("96:99", y, y)
#define S_SQ(g, h) "v_fmac_f32 v" #g ", 2.0, v" #h "\n\t"
#define S_SQD(g, h) "v_fmac_f32_dpp v" #g ", v" #h ", %[two] quad_perm:[0,0,2,2]" S_D
#define S_U(u, h, y) "v_add_f32_dpp v" #u ", v" #h ", v" #y " quad_perm:[1,1,3,3]" S_D
#define S_C(d, a, b, m) "v_cndmask_b32 v" #d ", v" #a ", v" #b ", %[" #m "]\n\t"
#define S_B(d, n) "v_mov_b32_dpp v" #d ", v157 row_newbcast:" #n S_D
#define S_BASE(d, u, t) "v_pk_fma_f32 v[" d "], v[" u "], v[116:117], v[" t "] op_sel:[0,1,0] op_sel_hi:[1,1,0]\n\t"
#define S_LG(d, r) "v_pk_fma_f32 v[" d "], v[" d "], v[" r "], %[toff] op_sel_hi:[1,1,0]\n\t"
#define S_SUB(p, d) "v_pk_add_f32 v[" p "], v[" d "], v[150:151] op_sel_hi:[1,0] neg_lo:[0,1] neg_hi:[0,1]\n\t"
#define S_E(p) "v_exp_f32 v" #p ", v" #p "\n\t"
#define S_MUL(p, r) "v_pk_mul_f32 v[" p "], v[" p "], v[" r "]\n\t"
#define S_POS   \
  "s_mul_i32 s50, s42, 26\n\ts_lshr_b32 s50, s50, 8\n\ts_mul_i32 s50, s50, -10\n\ts_add_u32 s50, s50, s42\n\ts_mul_i32 s45, s50, %[slot]\n\ts_mov_b32 s43, s45\n\t"
#define S_POLL(n, a, o, need, back, far) \
  #n ":\n\tds_read_b32 v152, v" #a " offset:" #o "\n\ts_sub_i32 s47, s47, 1\n\ts_cmp_lt_i32 s47, 0\n\ts_waitcnt lgkmcnt(0)\n\ts_cbranch_scc1 " back "\n\t" \
  "v_readfirstlane_b32 s40, v152\n\ts_cmp_ge_u32 s40, " need "\n\ts_cbranch_scc1 " back "\n\ts_sleep 2\n\t" \
  "s_add_u32 s40, s40, " far "\n\ts_cmp_ge_u32 s40, " need "\n\ts_cbranch_scc1 " #n "b\n\ts_sleep 12\n\ts_branch " #n "b\n"
__device__ __forceinline__ void s_items(SCtx& c, int k0, int late) {
  unsigned long long tq;
  asm volatile("s_mov_b32 s47, %[cap]\n\ts_mov_b32 s49, 0\n\ts_mov_b32 s42, %[k0]\n\ts_cmp_lg_u32 %[late], 0\n\ts_cbranch_scc1 7f\n"
               "1:\n\ts_cmp_ge_u32 s42, %[end]\n\ts_cbranch_scc1 9f\n\t"
               S_POS
               "s_mul_i32 s48, s42, 43\n\ts_lshr_b32 s48, s48, 8\n\ts_mul_i32 s46, s48, -6\n\ts_add_u32 s46, s46, s42\n\ts_lshl2_add_u32 s48, s48, %[ns]\n\t"
               "v_add_u32 v144, s45, %[orb]\n\tv_mad_u32_u24 v145, s43, %[ev], %[oyb]\n\ts_lshl2_add_u32 s40, s46, %[pfl]\n\ts_lshl2_add_u32 s41, s50, %[full]\n\t"
               "v_xor_b32 v146, 64, v144\n\tv_xor_b32 v147, 64, v145\n\tv_mov_b32 v148, s40\n\tv_mov_b32 v149, s41\n\t"
               "s_lshl1_add_u32 s44, s42, 2\n\ts_lshl_b32 s46, s46, 7\n\ts_mov_b64 vcc, %[dg]\n\ts_cmp_lt_u32 s49, s44\n\ts_cbranch_scc1 5f\n"
               "2:\n\tds_read_b32 v150, %[amr]\n\tds_read_b32 v151, v148 offset:64\n\t"
               S_R("24:27", 145, 0) S_R("48:51", 144, 0) S_R("28:31", 147, 0) S_R("52:55", 146, 0) S_R("32:35", 145, 128) S_R("56:59", 144, 128)
               S_R("36:39", 147, 128) S_R("60:63", 146, 128) S_R("40:43", 145, 256) S_R("64:67", 144, 256) S_R("44:47", 147, 256) S_R("68:71", 146, 256)
               S_W(10) S_M0("100:103", "48:51", "24:27") S_M0("104:107", "48:51", "48:51") S_R("72:75", 144, 6144) S_R("76:79", 146, 6144) S_M0("96:99", "24:27", "24:27")
               S_W(10) S_M("100:103", "52:55", "28:31") S_M("104:107", "52:55", "52:55") S_R("80:83", 144, 6272) S_R("84:87", 146, 6272) S_M("96:99", "28:31", "28:31")
               S_W(10) S_M("100:103", "56:59", "32:35") S_M("104:107", "56:59", "56:59") S_R("88:91", 144, 6400) S_R("92:95", 146, 6400) S_M("96:99", "32:35", "32:35")
               "s_mov_b64 exec, 1\n\tds_add_u32 v149, %[four] offset:64\n\ts_mov_b64 exec, -1\n\tv_readfirstlane_b32 s51, v151\n\ts_setprio 1\n\ts_nop 0\n\t"
               S_W(11) S_3("60:63", "36:39") S_W(9) S_3("64:67", "40:43") S_W(7) S_3("68:71", "44:47")
               S_W(6) S_M0("108:111", "72:75", "24:27") S_M0("112:115", "72:75", "72:75")
               S_W(5) S_M("108:111", "76:79", "28:31") S_M("112:115", "76:79", "76:79")
               S_W(4) S_M("108:111", "80:83", "32:35")
               "v_mov_b32_dpp v152, v96 quad_perm:[1,1,3,3]" S_D "v_mov_b32_dpp v153, v98 quad_perm:[1,1,3,3]" S_D
               S_M("112:115", "80:83", "80:83") S_C(154, 96, 98, r2) S_SQ(104, 100)
               S_W(3) S_M("108:111", "84:87", "36:39") S_SQD(105, 101) S_SQ(106, 102)
               S_M("112:115", "84:87", "84:87") S_SQD(107, 103) "v_add_f32 v154, %[eps], v154\n\t"
               S_W(2) S_M("108:111", "88:91", "40:43") S_U(128, 100, 152) S_U(129, 101, 152)
               S_M("112:115", "88:91", "88:91") S_U(130, 102, 153) S_U(131, 103, 153)
               S_W(1) S_M("108:111", "92:95", "44:47") S_C(104, 104, 105, r1) S_C(106, 106, 107, r1)
               S_M("112:115", "92:95", "92:95") S_C(155, 128, 129, r1) S_C(156, 130, 131, r1)
               "s_cmp_lt_u32 s42, %[early]\n\ts_cbranch_scc1 7f\n"
               "3:\n\t" S_M0("116:119", "24:27", "0:3") S_C(104, 104, 106, r2) S_C(155, 155, 156, r2)
               S_M("116:119", "28:31", "4:7") "v_add_f32_dpp v104, v154, v104 quad_perm:[0,0,2,2]" S_D S_U(132, 108, 152)
               S_M("116:119", "32:35", "8:11") S_U(133, 109, 152) S_U(134, 110, 153)
               S_M("116:119", "36:39", "12:15") S_U(135, 111, 153) S_SQ(112, 108)
               S_M("116:119", "40:43", "16:19") S_SQD(113, 109) S_SQ(114, 110)
               S_M("116:119", "44:47", "20:23") S_SQD(115, 111) S_C(156, 132, 133, r1) S_C(157, 134, 135, r1)
               S_C(112, 112, 113, r1) S_C(114, 114, 115, r1) S_C(156, 156, 157, r2) S_C(112, 112, 114, r2)
               "v_add_f32_dpp v112, v154, v112 quad_perm:[0,0,2,2]" S_D
               "v_mul_f32 v117, %[nic], v117\n\tv_cndmask_b32_dpp v155, v156, v155, vcc row_ror:4" S_D
               S_BASE("120:121", "128:129", "116:117") S_BASE("122:123", "130:131", "118:119")
               "v_cndmask_b32_dpp v104, v112, v104, vcc row_ror:4" S_D "v_mul_f32 v156, %[nic], v155\n\t"
               S_M("120:123", "48:51", "0:3") S_BASE("124:125", "132:133", "116:117") S_BASE("126:127", "134:135", "118:119")
               S_M("120:123", "52:55", "4:7") "v_fmac_f32 v104, v156, v155\n\tv_max_f32 v104, %[eps], v104\n\t"
               S_M("120:123", "56:59", "8:11") "v_rsq_f32 v157, v104\n\t"
               S_M("120:123", "60:63", "12:15") "v_mul_f32 v104, v104, v157\n\tv_mov_b32_dpp v157, v157 row_ror:12 row_mask:0x2 bank_mask:0xf\n\t"
               S_M("120:123", "64:67", "16:19") "v_cvt_pk_bf16_f32 v105, v155, v104\n\tv_mov_b32_dpp v157, v157 row_ror:8 row_mask:0x4 bank_mask:0xf\n\t"
               S_M("120:123", "68:71", "20:23") "v_lshlrev_b32 v106, 16, v105\n\tv_mov_b32_dpp v157, v157 row_ror:4 row_mask:0x8 bank_mask:0xf\n\t"
               S_M("124:127", "72:75", "0:3") "v_and_b32 v107, 0xffff0000, v105\n\t" S_B(136, 0)
               S_M("124:127", "76:79", "4:7") S_B(137, 1) S_B(138, 2)
               S_M("124:127", "80:83", "8:11") S_B(139, 3) S_LG("120:121", "136:137")
               S_M("124:127", "84:87", "12:15") S_LG("122:123", "138:139") S_SUB("128:129", "120:121")
               S_M("124:127", "88:91", "16:19") S_SUB("130:131", "122:123") S_E(128)
               S_M("124:127", "92:95", "20:23") S_E(129) S_E(130) S_E(131)
               S_B(140, 4) S_B(141, 5) S_B(142, 6) S_B(143, 7) "v_sub_f32 v106, v155, v106\n\t"
               S_LG("124:125", "140:141") S_LG("126:127", "142:143") "v_sub_f32 v107, v104, v107\n\tv_max3_f32 v152, v120, v121, v122\n\t"
               S_SUB("132:133", "124:125") S_SUB("134:135", "126:127") "v_max3_f32 v153, v123, v124, v125\n\t"
               S_E(132) S_E(133) S_E(134) S_E(135)
               "v_max3_f32 v152, v152, v126, v127\n\tv_add_f32 v154, %[thr], v150\n\tv_max_f32 v152, v152, v153\n\tv_cvt_pk_bf16_f32 v106, v106, v107\n\t"
               "v_lshl_add_u32 v144, s46, 3, %[apk]\n\tv_cmp_gt_f32 vcc, v152, v154\n\tv_add_u32 v145, s46, %[apm]\n\tv_lshl_add_u32 v146, s46, 1, %[api]\n\t"
               "s_cbranch_vccnz 8f\n"
               "4:\n\t" S_MUL("128:129", "136:137") S_MUL("130:131", "138:139") S_MUL("132:133", "140:141") S_MUL("134:135", "142:143")
               "v_cvt_pk_bf16_f32 v128, v128, v129\n\tv_cvt_pk_bf16_f32 v129, v130, v131\n\tv_cvt_pk_bf16_f32 v130, v132, v133\n\tv_cvt_pk_bf16_f32 v131, v134, v135\n\t"
               "s_cmp_lt_u32 s51, s48\n\ts_cbranch_scc1 6f\n"
               "10:\n\tds_read_b32 v151, v149\n\tds_write_b128 v144, v[128:131]\n\tds_write2_b32 v145, v150, v150 offset0:0 offset1:1\n\ts_mov_b64 exec, %[dg2]\n\t"
               "ds_write_b16 v146, v105\n\tds_write_b16 v146, v106 offset:16\n\tds_write_b16_d16_hi v146, v105 offset:32\n\tds_write_b16_d16_hi v146, v106 offset:48\n\t"
               "s_mov_b64 exec, 1\n\tds_add_u32 v148, %[inc]\n\ts_mov_b64 exec, -1\n\ts_setprio 0\n\ts_add_u32 s42, s42, %[ns]\n\t"
               S_W(7) "v_readfirstlane_b32 s49, v151\n\ts_branch 1b\n"
               S_POLL(5, 149, 0, "s44", "2b", "2")
               S_POLL(6, 148, 64, "s48", "10b", "64")
               "7:\n\tv_mov_b32 v144, %[qi]\n\tv_mov_b32 v145, v152\n\tv_mov_b32 v146, 1\n"
               S_POLL(70, 144, 0, "%[nq]", "71f", "64")
               "71:\n\tv_mov_b32 v152, v145\n\tds_read_b128 v[0:3], %[apk]\n\tds_read_b128 v[4:7], %[apk] offset:1024\n\tds_read_b128 v[8:11], %[apk] offset:2048\n\t"
               "ds_read_b128 v[12:15], %[apk] offset:3072\n\tds_read_b128 v[16:19], %[apk] offset:4096\n\tds_read_b128 v[20:23], %[apk] offset:5120\n\t"
               "v_subrev_u32 v144, %[mc], %[amr]\n\tds_read_b32 v150, %[amr]\n\ts_mov_b64 exec, 63\n\tds_add_u32 v144, v146\n\ts_mov_b64 exec, -1\n\t"
               "s_cmp_lg_u32 %[late], 0\n\ts_waitcnt lgkmcnt(0)\n\ts_nop 1\n\ts_cbranch_scc1 1b\n\ts_branch 3b\n"
               "8:\n\tv_mov_b32 v153, v152\n\ts_nop 1\n\tv_permlane16_swap_b32 v152, v153\n\tv_max_f32 v152, v152, v153\n\tv_mov_b32 v153, v152\n\ts_nop 1\n\t"
               "v_permlane32_swap_b32 v152, v153\n\tv_max_f32 v152, v152, v153\n\tv_mul_f32 v152, 0x3d000000, v152\n\tv_rndne_f32 v152, v152\n\t"
               "v_mul_f32 v152, 0x42000000, v152\n\tv_max_f32 v150, v150, v152\n\tds_max_f32 %[amr], v150\n\t"
               S_SUB("128:129", "120:121") S_SUB("130:131", "122:123") S_SUB("132:133", "124:125") S_SUB("134:135", "126:127")
               S_E(128) S_E(129) S_E(130) S_E(131) S_E(132) S_E(133) S_E(134) S_E(135) "s_branch 4b\n"
               "9:"
               : "=&{v[0:7]}"(c.q[0]), "=&{v[8:15]}"(c.q[1]), "=&{v[16:23]}"(c.q[2]), "=&{s[52:53]}"(tq)
               : [orb] "v"(c.o_rb), [oyb] "v"(c.o_yb), [ev] "v"(c.ev), [apk] "v"(c.apk), [apm] "v"(c.apm), [api] "v"(c.api), [amr] "v"(c.amr),
                 [two] "v"(2.f), [four] "v"(4u), [inc] "v"(2u * NPK), [k0] "s"(k0), [late] "s"(__builtin_amdgcn_readfirstlane(late)), [toff] "s"(f32x2{TOFF, TOFF}),
                 [r1] "s"(0xAAAAAAAAAAAAAAAAull), [r2] "s"(0xCCCCCCCCCCCCCCCCull), [dg] "s"(0xF0000F0000F0000Full), [dg2] "s"(0xF00FFF000FF000FFull),
                 [cap] "i"(SPIN_CAP), [end] "i"(TPW - NH1), [ns] "i"(NS), [pfl] "i"(CT_OFF + C_PFL), [full] "i"(CT_OFF + C_FULL), [early] "i"(NEARLY),
                 [qi] "i"(CT_OFF + C_QI), [nq] "i"(NS - NEARLY), [mc] "i"(C_MREF - C_CONS), [slot] "i"(SLOT),
                 [thr] "i"(__builtin_bit_cast(unsigned, THR)), [eps] "i"(__builtin_bit_cast(unsigned, EPS * CC)), [nic] "i"(__builtin_bit_cast(unsigned, -1.f / CC))
               : "memory", "scc", "vcc", "s40", "s41", "s42", "s43", "s44", "s45", "s46", "s47", "s48", "s49", "s50", "s51",
                 "v24", "v25", "v26", "v27", "v28", "v29", "v30", "v31", "v32", "v33", "v34", "v35", "v36", "v37", "v38", "v39", "v40", "v41", "v42", "v43", "v44", "v45", "v46", "v47", "v48", "v49", "v50", "v51", "v52", "v53", "v54", "v55", "v56", "v57", "v58", "v59", "v60", "v61", "v62", "v63", "v64", "v65", "v66", "v67", "v68", "v69", "v70", "v71", "v72", "v73", "v74", "v75", "v76", "v77", "v78", "v79", "v80", "v81", "v82", "v83", "v84", "v85", "v86", "v87", "v88", "v89", "v90", "v91", "v92", "v93", "v94", "v95", "v96", "v97", "v98", "v99", "v100", "v101", "v102", "v103", "v104", "v105", "v106", "v107", "v108", "v109", "v110", "v111", "v112", "v113", "v114", "v115", "v116", "v117", "v118", "v119", "v120", "v121", "v122", "v123", "v124", "v125", "v126", "v127", "v128", "v129", "v130", "v131", "v132", "v133", "v134", "v135", "v136", "v137", "v138", "v139", "v140", "v141", "v142", "v143", "v144", "v145", "v146", "v147", "v148", "v149", "v150", "v151", "v152", "v153", "v154", "v155", "v156", "v157");
  c.tq = (unsigned)tq;
}

__device__ __forceinline__ void s_half(SCtx& c, int k, int h) {
  const int s = k % NSLOT, e = k % NPK;
  const int sb_ = s * SLOT;
  int yba = c.ev * sb_ + c.o_yb;
  float hc = 1.f - 0.5f * c.ev;
  unsigned xa = (unsigned)(size_t)c.L0 + sb_ + h * 6144 + c.o_rb, xb = xa ^ 64, ai = c.api + e * 256 + h * 8;
  unsigned ap = c.apk + e * 1024 + h * 8, am = c.apm + e * 128 + h * 4, af = c.ct + C_PFL + e * 4, inc = NPK;
  float two = 2.f;
  f32x4 G = {EPS * CC, EPS * CC, EPS * CC, EPS * CC};
  poll_ge_sleep(c.ct + C_CONS + e * 4, (unsigned)(NE * (k / NPK) + NS));
  asm volatile("" : "+v"(yba), "+v"(hc), "+v"(xa), "+v"(xb), "+v"(ai), "+v"(ap), "+v"(am), "+v"(af), "+v"(inc), "+v"(two), "+{v[104:107]}"(G));
  poll_ge_sleep(c.ct + C_FULL, (unsigned)(2 * k + 1));
  __builtin_amdgcn_s_setprio(1);
  bf16x8 yb[6];
#pragma unroll
  for (int j = 0; j < 6; ++j) yb[j] = *(LDS_AS(bf16x8)*)(c.L0 + ((j & 1) ? (yba ^ 64) : yba) + (j >> 1) * 128);
  f32x4 Hx = {0.f, 0.f, 0.f, 0.f}, S = Hx;
#pragma unroll
  for (int j = 0; j < 6; ++j) { Hx = mfma(yb[j], yb[j], Hx); S = mfma(yb[j], s_qf(c, j), S); }
  const float nsq = S[1] * (-1.f / CC);
  Hx = f32x4{Hx[0] * hc, Hx[0] * hc, Hx[2] * hc, Hx[2] * hc};
  S = f32x4{S[0], S[0], S[2], S[2]};
  float mref, mt;
  f32x4 rs;
  unsigned long long raise;
#define SH_M(d, a, b) "v_mfma_f32_16x16x32_bf16 v[" d "], v[" a "], " b ", v[" d "]\n\t"
#define SH_X(n, a, y) "s_waitcnt lgkmcnt(" #n ")\n\t" SH_M("100:103", a, "%[" #y "]") SH_M("104:107", a, "v[" a "]")
#define SH_D " row_mask:0xf bank_mask:0xf\n\t"
  __builtin_amdgcn_s_setprio(0);
  if (k < TPW - 2) poll_ge_sleep(c.ct + C_FULL, (unsigned)(2 * k + 2));
  asm volatile("s_mov_b32 s42, %[cap]\n"
               "1:\n\tds_read_b32 v136, %[afl]\n\tds_read_b128 v[112:115], %[xa]\n\tds_read_b128 v[116:119], %[xb]\n\tds_read_b128 v[120:123], %[xa] offset:128\n\t"
               "ds_read_b128 v[124:127], %[xb] offset:128\n\tds_read_b128 v[128:131], %[xa] offset:256\n\tds_read_b128 v[132:135], %[xb] offset:256\n\t"
               "ds_read_b32 %[mr], %[amr]\n\ts_sub_u32 s42, s42, 1\n\ts_cselect_b32 s41, 0, %[need]\n\ts_waitcnt lgkmcnt(7)\n\t"
               "v_readfirstlane_b32 s40, v136\n\ts_cmp_lt_u32 s40, s41\n\ts_cbranch_scc1 1b\n\ts_setprio 3\n\t"
               SH_X(6, "112:115", y0) SH_X(5, "116:119", y1) SH_X(4, "120:123", y2) SH_X(3, "124:127", y3) SH_X(2, "128:131", y4) SH_X(1, "132:135", y5)
               SH_M("108:111", "112:115", "%[q0]") SH_M("108:111", "116:119", "%[q1]")
               "s_waitcnt lgkmcnt(0)\n\tv_add_f32 v137, %[thr], %[mr]\n\tds_write_b32 %[am], %[mr]\n\ts_nop 1\n\t"
               "v_mov_b32_dpp v140, v100 quad_perm:[1,1,3,3]" SH_D "v_mov_b32_dpp v141, v101 quad_perm:[1,1,3,3]" SH_D
               "v_mov_b32_dpp v142, v102 quad_perm:[1,1,3,3]" SH_D "v_mov_b32_dpp v143, v103 quad_perm:[1,1,3,3]" SH_D
               SH_M("108:111", "120:123", "%[q2]")
               "v_fmac_f32 v104, 2.0, v100\n\tv_fmac_f32_dpp v105, v101, %[two] quad_perm:[0,0,2,2]" SH_D
               "v_fmac_f32 v106, 2.0, v102\n\tv_fmac_f32_dpp v107, v103, %[two] quad_perm:[0,0,2,2]" SH_D
               "v_cndmask_b32 v138, v140, v141, %[r1]\n\tv_cndmask_b32 v139, v142, v143, %[r1]\n\t"
               SH_M("108:111", "124:127", "%[q3]")
               "v_cndmask_b32 v104, v104, v105, %[r1]\n\tv_cndmask_b32 v106, v106, v107, %[r1]\n\tv_cndmask_b32 v138, v138, v139, %[r2]\n\t"
               "v_cndmask_b32 v104, v104, v106, %[r2]\n\tv_mul_f32 v139, %[nic], v138\n\t"
               SH_M("108:111", "128:131", "%[q4]")
               "v_fmac_f32 v104, v139, v138\n\tv_max_f32 v104, %[eps], v104\n\tv_rsq_f32 v105, v104\n\t"
               SH_M("108:111", "132:135", "%[q5]")
               "v_mul_f32 v104, v104, v105\n\t"
               "v_mov_b32_dpp v105, v105 row_ror:12 row_mask:0x2 bank_mask:0xf\n\tv_cvt_pk_bf16_f32 v106, v138, v104\n\ts_nop 0\n\t"
               "v_mov_b32_dpp v105, v105 row_ror:8 row_mask:0x4 bank_mask:0xf\n\tv_lshlrev_b32 v107, 16, v106\n\tv_and_b32 v139, 0xffff0000, v106\n\t"
               "v_mov_b32_dpp v105, v105 row_ror:4 row_mask:0x8 bank_mask:0xf\n\t"
               "v_pk_fma_f32 v[108:109], v[140:141], %[nsq], v[108:109]\n\tv_pk_fma_f32 v[110:111], v[142:143], %[nsq], v[110:111]\n\t"
               "v_mov_b32_dpp v100, v105 row_newbcast:0" SH_D "v_mov_b32_dpp v101, v105 row_newbcast:1" SH_D
               "v_mov_b32_dpp v102, v105 row_newbcast:2" SH_D "v_mov_b32_dpp v103, v105 row_newbcast:3" SH_D
               S_LG("108:109", "100:101") S_LG("110:111", "102:103") S_SUB("140:141", "108:109") S_SUB("142:143", "110:111")
               "v_exp_f32 v140, v140\n\tv_exp_f32 v141, v141\n\tv_exp_f32 v142, v142\n\tv_exp_f32 v143, v143\n\t"
               "v_max3_f32 %[mt], v108, v109, v110\n\tv_sub_f32 v138, v138, v107\n\tv_sub_f32 v104, v104, v139\n\tv_max_f32 %[mt], %[mt], v111\n\t"
               "v_pk_mul_f32 v[140:141], v[140:141], v[100:101]\n\tv_pk_mul_f32 v[142:143], v[142:143], v[102:103]\n\t"
               "v_cmp_gt_f32 %[ra], %[mt], v137\n\tv_cvt_pk_bf16_f32 v107, v138, v104\n\t"
               "v_cvt_pk_bf16_f32 v140, v140, v141\n\tv_cvt_pk_bf16_f32 v141, v142, v143\n\t"
               "ds_write_b64 %[ap], v[140:141]\n\ts_mov_b64 exec, %[dg]\n\t"
               "ds_write_b16 %[ai], v106\n\tds_write_b16 %[ai], v107 offset:16\n\tds_write_b16_d16_hi %[ai], v106 offset:32\n\tds_write_b16_d16_hi %[ai], v107 offset:48\n\t"
               "s_cmp_eq_u64 %[ra], 0\n\ts_cselect_b64 exec, 1, 0\n\tds_add_u32 %[af], %[inc]\n\ts_mov_b64 exec, -1\n\ts_setprio 0"
               : "+{v[100:103]}"(Hx), "+{v[104:107]}"(G), "+{v[108:111]}"(S), [mr] "=&{v150}"(mref), [mt] "=&v"(mt), [ra] "=&s"(raise)
               : [y0] "v"(yb[0]), [y1] "v"(yb[1]), [y2] "v"(yb[2]), [y3] "v"(yb[3]), [y4] "v"(yb[4]), [y5] "v"(yb[5]),
                 [q0] "v"(s_qf(c, 0)), [q1] "v"(s_qf(c, 1)), [q2] "v"(s_qf(c, 2)), [q3] "v"(s_qf(c, 3)), [q4] "v"(s_qf(c, 4)), [q5] "v"(s_qf(c, 5)),
                 [afl] "v"(c.ct + C_FULL), [xa] "v"(xa), [xb] "v"(xb), [amr] "v"(c.amr), [am] "v"(am), [ap] "v"(ap), [ai] "v"(ai),
                 [af] "v"(af), [inc] "v"(inc), [nsq] "v"(f32x2{nsq, nsq}), [two] "v"(two),
                 [need] "s"(__builtin_amdgcn_readfirstlane(2 * k + 2)), [r1] "s"(0xAAAAAAAAAAAAAAAAull), [r2] "s"(0xCCCCCCCCCCCCCCCCull),
                 [dg] "s"(0xF0000F0000F0000Full), [cap] "i"(SPIN_CAP), [thr] "i"(__builtin_bit_cast(unsigned, THR)), [toff] "s"(f32x2{TOFF, TOFF}),
                 [eps] "i"(__builtin_bit_cast(unsigned, EPS * CC)), [nic] "i"(__builtin_bit_cast(unsigned, -1.f / CC))
               : "memory", "scc", "s40", "s41", "s42", "v112", "v113", "v114", "v115", "v116", "v117", "v118", "v119", "v120", "v121", "v122", "v123", "v124", "v125",
                 "v126", "v127", "v128", "v129", "v130", "v131", "v132", "v133", "v134", "v135", "v136", "v137", "v138", "v139", "v140", "v141", "v142", "v143");
  if (__builtin_expect(raise != 0ull, 0)) {
    rs = Hx;
    mref = fmaxf(mref, 32.f * __builtin_rintf(xrow16_max(mt) * (1.f / 32.f)));
    float pp[4];
#pragma unroll
    for (int i = 0; i < 4; ++i) pp[i] = __builtin_amdgcn_exp2f(S[i] - mref) * rs[i];
    asm volatile("ds_max_f32 %6, %3\n\tds_write_b32 %2, %3\n\tds_write_b64 %0, %1\n\ts_mov_b64 exec, 1\n\tds_add_u32 %4, %5\n\ts_mov_b64 exec, -1"
                 : : "v"(ap), "v"(u32x2{cvtpk(pp[0], pp[1]), cvtpk(pp[2], pp[3])}), "v"(am), "v"(mref), "v"(af), "v"((unsigned)NPK), "v"(c.amr) : "memory");
  }
}

using u32x16 = __attribute__((ext_vector_type(16))) unsigned;
typedef __attribute__((address_space(1))) unsigned short gus;
extern "C" __global__ __launch_bounds__(768, 3) void dino_main(const void*, const void*, const void*, const void*, const void*,
                                                               const void*, const void*, void*, void*) {
  u32x16 ka; u32x2 kb; unsigned long long tent64;
  __shared__ __attribute__((aligned(16))) char lds[LDS_BYTES];
  const int wave = __builtin_amdgcn_readfirstlane((int)threadIdx.x >> 6);
  const bool helper = blockIdx.x < 8;
  lds_ptr L0 = (lds_ptr)lds;
  const unsigned lbase = (unsigned)(size_t)L0;
  const unsigned ct = lbase + CT_OFF;
#define KARG(v, i) ((char*)(((unsigned long long)v[2 * (i) + 1] << 32) | v[2 * (i)]))
#define q KARG(ka, 0)
#define f0 KARG(ka, 1)
#define f1 KARG(ka, 2)
#define wq KARG(ka, 3)
#define wk KARG(ka, 4)
#define wv KARG(ka, 5)
#define wo KARG(ka, 6)
#define box KARG(ka, 7)
#define tent ((unsigned)tent64)
#define LANE_VARS int lane; asm volatile("v_mbcnt_lo_u32_b32 %0, -1, 0\n\tv_mbcnt_hi_u32_b32 %0, -1, %0" : "=v"(lane)); \
  const int r = lane & 15, g = lane >> 4; (void)r; (void)g;
#define DOFF_VARS unsigned doff[3];   \
  _Pragma("unroll") for (int i = 0; i < 3; ++i) doff[i] = (unsigned)(lane ^ ((swz_tbl(i) >> ((lane >> 3) * 4)) & 7)) << 4;
#define BARRIER() do { asm volatile("s_waitcnt lgkmcnt(0)" ::: "memory"); __builtin_amdgcn_s_barrier(); asm volatile("" ::: "memory"); } while (0)

  {
    unsigned doff[3];
    static_assert(swz_tbl(0) == swz_tbl(3) && swz_tbl(1) == swz_tbl(4) && swz_tbl(2) == swz_tbl(5), "");
    unsigned l16, db;
#define PD(d, b, off, pol) "global_load_lds_dwordx4 %[" #d "], s[" #b "] offset:" #off pol "\n\t"
    asm volatile("s_load_dwordx16 s[64:79], %[kp], 0x0\n\ts_load_dwordx2 s[80:81], %[kp], 0x40\n\ts_memrealtime s[82:83]\n\t"
                 "v_mbcnt_lo_u32_b32 %[l16], -1, 0\n\tv_mbcnt_hi_u32_b32 %[l16], -1, %[l16]\n\t"
                 "s_lshr_b32 s84, %[b], 3\n\ts_and_b32 s85, %[b], 7\n\t"
                 "v_lshrrev_b32 %[db], 1, %[l16]\n\tv_and_b32 %[db], -4, %[db]\n\t"
                 "s_mov_b32 s86, %[t0]\n\ts_mov_b32 s87, %[t1]\n\ts_mov_b32 s88, %[t2]\n\t"
                 "v_bfe_u32 %[d0], s86, %[db], 3\n\tv_bfe_u32 %[d1], s87, %[db], 3\n\tv_bfe_u32 %[d2], s88, %[db], 3\n\t"
                 "v_xor_b32 %[d0], %[d0], %[l16]\n\tv_xor_b32 %[d1], %[d1], %[l16]\n\tv_xor_b32 %[d2], %[d2], %[l16]\n\t"
                 "v_lshlrev_b32 %[d0], 4, %[d0]\n\tv_lshlrev_b32 %[d1], 4, %[d1]\n\tv_lshlrev_b32 %[d2], 4, %[d2]\n\t"
                 "v_lshlrev_b32 %[l16], 4, %[l16]\n\t"
                 "s_cmp_eq_u32 s84, 0\n\ts_cbranch_scc1 4f\n\t"
                 "s_cmp_lg_u32 %[w], 0\n\ts_cbranch_scc1 7f\n\t"
                 "s_setprio 3\n\tv_lshlrev_b32 %[db], 3, %[l16]\n\ts_mov_b32 m0, %[junk]\n\ts_lshl_b32 s89, s84, 13\n\t"
                 "s_cmp_gt_u32 s84, 3\n\ts_cbranch_scc1 2f\n\t"
                 "s_getpc_b64 s[90:91]\n1:\n\ts_sub_u32 s86, s89, 8192\n\ts_min_u32 s86, s86, dino_end-1b-8192\n\t"
                 "s_add_u32 s90, s90, s86\n\ts_addc_u32 s91, s91, 0\n\tglobal_load_lds_dword %[db], s[90:91]\n"
                 "2:\n\ts_mul_i32 s86, s85, 98304\n\ts_mul_i32 s87, s84, 3072\n\ts_sub_u32 s87, s87, 3072\n\t"
                 "s_add_u32 s88, s86, s87\n\ts_lshl1_add_u32 s86, s86, s87\n\ts_lshl_b32 s86, s86, 1\n\t"
                 "s_mul_i32 s87, s85, 12288\n\ts_lshr_b32 s90, s89, 1\n\ts_add_u32 s87, s87, s90\n\ts_sub_u32 s87, s87, 13 * 4096\n\t"
                 "s_sub_u32 s89, s89, 4 * 8192\n\ts_cmp_gt_u32 s85, 5\n\ts_cselect_b32 s89, -1, s89\n\ts_cmp_ge_u32 s89, 11 * 8192\n\t"
                 "s_waitcnt lgkmcnt(0)\n\ts_cbranch_scc1 3f\n\t"
                 "s_cmp_lt_u32 s89, 9 * 8192\n\ts_cselect_b64 s[90:91], s[70:71], s[72:73]\n\ts_cselect_b32 s89, s89, s87\n\t"
                 "s_add_u32 s90, s90, s89\n\ts_addc_u32 s91, s91, 0\n\tglobal_load_lds_dword %[db], s[90:91]\n"
                 "3:\n\ts_mov_b32 m0, 0x3000\n\t"
                 "s_add_u32 s90, s68, s88\n\ts_addc_u32 s91, s69, 0\n\t"
                 "s_add_u32 s92, s66, s86\n\ts_addc_u32 s93, s67, 0\n\t"
                 PD(d0, 90:91, 0, " nt") PD(d1, 90:91, 1024, " nt") PD(d2, 90:91, 2048, " nt")
                 "s_mov_b32 m0, 0\n\ts_nop 0\n\t"
                 PD(d0, 92:93, 0, " nt") PD(d1, 92:93, 1024, " nt")
                 "s_sub_u32 s89, s84, 15\n\ts_cmp_gt_u32 s85, 5\n\ts_cselect_b32 s89, -1, s89\n\ts_cmp_ge_u32 s89, 11\n\ts_cbranch_scc1 9f\n\t"
                 "s_mov_b32 m0, %[junk]\n\ts_sub_u32 s87, s87, 11 * 4096\n\ts_lshl_b32 s89, s89, 13\n\ts_cmp_lt_u32 s89, 9 * 8192\n\t"
                 "s_cselect_b64 s[90:91], s[76:77], s[74:75]\n\ts_cselect_b32 s89, s89, s87\n\t"
                 "s_add_u32 s90, s90, s89\n\ts_addc_u32 s91, s91, 0\n\tglobal_load_lds_dword %[db], s[90:91]\n\t"
                 "s_branch 9f\n"
                 "7:\n\ts_cmp_eq_u32 %[w], 1\n\ts_cbranch_scc1 8f\n\ts_barrier\n\ts_branch 8f\n"
                 "4:\n\t"
                 "s_cmp_gt_u32 %[b], 5\n\ts_cbranch_scc1 8f\n\ts_cmp_gt_u32 %[w], 9\n\ts_cbranch_scc1 8f\n\t"
                 "s_mov_b32 m0, %[img]\n\ts_mul_i32 s86, %[w], 9216\n\ts_and_b32 s88, %[w], 1\n\ts_lshl1_add_u32 s89, %[b], s88\n\t"
                 "s_mul_i32 s88, s88, 6144\n\ts_mul_i32 s89, s89, 6144\n\ts_add_u32 s88, s88, %[hr]\n\t"
                 "s_cmp_lg_u32 %[w], 0\n\ts_waitcnt lgkmcnt(0)\n\ts_cbranch_scc1 5f\n\t"
                 PD(d0, 64:65, 0, "") PD(d1, 64:65, 1024, "") PD(d2, 64:65, 2048, "") PD(d0, 64:65, 3072, "")
                 "s_mov_b32 m0, %[img] + 4096\n\ts_add_u32 s90, s64, 0x1000\n\ts_addc_u32 s91, s65, 0\n\t"
                 PD(d1, 90:91, 0, "") PD(d2, 90:91, 1024, "")
                 "\n5:\n\ts_cmp_gt_u32 %[w], 7\n\ts_cselect_b64 s[92:93], s[72:73], s[70:71]\n\ts_cselect_b32 m0, s88, s86\n\ts_cselect_b32 s86, s89, s86\n\t"
                 "s_cselect_b32 s87, 2, 3\n\ts_add_u32 s92, s92, s86\n\ts_addc_u32 s93, s93, 0\n"
                 "6:\n\ts_sub_u32 s87, s87, 1\n\t"
                 PD(d0, 92:93, 0, "") PD(d1, 92:93, 1024, "") PD(d2, 92:93, 2048, "")
                 "s_add_u32 m0, m0, 3072\n\ts_add_u32 s92, s92, 3072\n\ts_addc_u32 s93, s93, 0\n\ts_cmp_lg_u32 s87, 0\n\ts_cbranch_scc1 6b"
                 "\n8:\n\ts_waitcnt lgkmcnt(0)\n9:"
                 : "={s[64:79]}"(ka), "={s[80:81]}"(kb), "={s[82:83]}"(tent64),
                   [d0] "=&v"(doff[0]), [d1] "=&v"(doff[1]), [d2] "=&v"(doff[2]), [l16] "=&v"(l16), [db] "=&v"(db)
                 : [kp] "s"(__builtin_amdgcn_kernarg_segment_ptr()), [b] "s"(blockIdx.x), [w] "s"(wave),
                   [t0] "i"(swz_tbl(0)), [t1] "i"(swz_tbl(1)), [t2] "i"(swz_tbl(2)), [junk] "i"(JUNK), [img] "i"(IMG_A), [hr] "i"(H_R)
                 : "memory", "scc", "vcc", "s84", "s85", "s86", "s87", "s88", "s89", "s90", "s91", "s92", "s93", "s94", "s95", "s96", "s97", "s98", "s99");
  }
  const unsigned t0 = (unsigned)__builtin_amdgcn_s_memrealtime() - T0BACK;

  if (__builtin_expect(blockIdx.x < 6, 0)) {
    LANE_VARS
    DOFF_VARS
    const int j = blockIdx.x;
    TNOW(ta0);
    TNOW(ta1);
    const int qq = (lane >> 2) & 3, p4 = lane & 3, e4 = wave & 3;
    int ra = H_R + r * ROWB + ((g ^ swz(r)) << 4);
    bf16x8 a[6], b[6], w[18];
    auto rows = [&](int t, bf16x8* d) {
#pragma unroll
      for (int kk = 0; kk < 6; ++kk) d[kk] = *(LDS_AS(bf16x8)*)(L0 + t * 6144 + ((kk & 1) ? (ra ^ 64) : ra) + (kk >> 1) * 128);
    };
#define TSET(v)
    int o[3];
#pragma unroll
    for (int t = 0; t < 3; ++t) o[t] = tr_off(4 * g + qq, 4 * (3 * e4 + t) + p4);
    unsigned qo = lbase + H_Q + 3 * e4 * 512 + ((g >> 1) * 16 + r) * 16 + (g & 1) * 8, qa = lbase + IMG_A + tr_off(r, 8 * wave + g);
    if (wave == 11) {
      unsigned d;
      asm volatile("s_load_dword %0, %1, 0x40\n\ts_barrier\n\ts_barrier\n\ts_barrier\n\ts_waitcnt lgkmcnt(0)" : "=&s"(d) : "s"(box + EPOCH_OFF) : "memory");
    } else
    asm volatile("s_waitcnt vmcnt(9)\n\ts_barrier" : PIN3(o, 0), "+v"(qo), "+v"(qa) :: "memory");
    if (wave < 6 || wave == 10) {
      rows((IMG_A - H_R) / 6144, a);
      const u32x2 m0 = *(LDS_AS(u32x2)*)(size_t)qa, m1 = *(LDS_AS(u32x2)*)(size_t)(qa ^ 32);
      static_assert((IMG_A - H_R) % 6144 == 0, "");
      const bf16x8 ones = __builtin_bit_cast(bf16x8, u32x4{0x3f803f80u, 0x3f803f80u, 0x3f803f80u, 0x3f803f80u});
      f32x4 sm = {0.f, 0.f, 0.f, 0.f};
#pragma unroll
      for (int i = 0; i < 6; ++i) sm = mfma(ones, a[i], sm);
      const float mean = sm[0] * (1.f / CC);
      if (__builtin_expect(wave < 6, 1)) {
        img_st(L0, H_C, wave, lane, __builtin_bit_cast(bf16x8, u32x4{cvtpk(bflo(m0[0]) - mean, bfhi(m0[0]) - mean), cvtpk(bflo(m0[1]) - mean, bfhi(m0[1]) - mean),
                                                                   cvtpk(bflo(m1[0]) - mean, bfhi(m1[0]) - mean), cvtpk(bflo(m1[1]) - mean, bfhi(m1[1]) - mean)}));
      } else {
        asm volatile("s_barrier" : PIN3(a, 0), PIN3(a, 3) :: "memory");
        f32x2 v2 = {0.f, 0.f}, v3 = v2;
#pragma unroll
        for (int i = 0; i < 12; ++i) {
          const u32x4 d = __builtin_bit_cast(u32x4, a[i >> 1]);
          const unsigned d0 = d[2 * (i & 1)], d1 = d[2 * (i & 1) + 1];
          const f32x2 x0 = f32x2{bflo(d0), bfhi(d0)} - mean, x1 = f32x2{bflo(d1), bfhi(d1)} - mean;
          v2 += x0 * x0; v3 += x1 * x1;
        }
        v2 += v3;
        *(LDS_AS(float)*)(L0 + CT_OFF + r * 4) = LOG2E * __builtin_amdgcn_rsqf(xrow16_sum(v2[0] + v2[1]) * (1.f / CC) + EPS);
      }
      TSET(ta2);
    }
    if (wave < 4) {
      wait_vm<0>();
      TSET(ta3);
      BARRIER();
      TSET(th1);
#pragma unroll
      for (int kk = 0; kk < 6; ++kk) {
        b[kk] = img_ld(L0, H_C, kk, lane);
        w[kk] = mk8(tr_read(L0 + kk * 12288 + o[0]), tr_read(L0 + kk * 12288 + 6144 + o[0]));
        __builtin_amdgcn_sched_barrier(0);
      }
#pragma unroll
      for (int i = 6; i < 18; ++i) {
        w[i] = mk8(tr_read(L0 + (i % 6) * 12288 + o[i / 6]), tr_read(L0 + (i % 6) * 12288 + 6144 + o[i / 6]));
        __builtin_amdgcn_sched_barrier(0);
      }
#pragma unroll
      for (int t = 0; t < 3; ++t) {
        f32x4 D = {0.f, 0.f, 0.f, 0.f};
#pragma unroll
        for (int kk = 0; kk < 6; ++kk) D = mfma(w[6 * t + kk], b[kk], D);
        *(LDS_AS(u32x2)*)(size_t)(qo + t * 512) = u32x2{cvtpk(D[0], D[1]), cvtpk(D[2], D[3])};
      }
      TSET(ta4);
      BARRIER();
    } else if (wave == 8 || wave == 9) {
      unsigned so = lane * 16 + (2 * j + (wave & 1)) * 1024;
      unsigned epoch;
      asm volatile("global_load_dword %0, %1, %2 sc1\n\ts_barrier\n\ts_waitcnt vmcnt(0)" : "=v"(epoch) : "v"(EPOCH_OFF), "s"(box) : "memory");
      TSET(ta3);
      rows(wave & 1, a);
      unsigned tb = line_tag(epoch, 9) ^ tent, ta = line_tag(epoch, 8) ^ tb;
      asm volatile("" : PIN3(a, 0), PIN3(a, 3), "+v"(ta), "+v"(tb), "+v"(so));
      TSET(ta4);
      BARRIER();
      TSET(ta5);
#pragma unroll
      for (int kk = 0; kk < 6; ++kk) b[kk] = img_ld(L0, H_Q, kk, lane);
      const float sc = *(LDS_AS(float)*)(L0 + CT_OFF + r * 4);
      __builtin_amdgcn_sched_barrier(0);
      f32x4 D = {0.f, 0.f, 0.f, 0.f};
#pragma unroll
      for (int kk = 0; kk < 6; ++kk) D = mfma(a[kk], b[kk], D);
      D *= sc;
      const unsigned p0 = cvtpk(D[0], D[1]), p1 = cvtpk(D[2], D[3]);
      asm volatile("global_store_dwordx4 %0, %1, %2 sc1\n\ts_nop 1" : : "v"(so), "v"(u32x4{p0, p1, p0 ^ p1 ^ ta, tb}), "s"(box + QK_OFF) : "memory");
      TSET(th2);
    } else if (wave != 11) {
      if (wave != 10) asm volatile("s_waitcnt vmcnt(0) lgkmcnt(0)\n\ts_barrier" ::: "memory");
      BARRIER();
    }
    BARRIER();
    issue_rows16(wo + wave * 6144, lbase + wave * 6144, doff);
    if (wave < 2) issue_rows16(wv + (2 * j + wave) * 6144, lbase + H_R + wave * 6144, doff);
    const unsigned epoch = epoch_load(box);
    BARRIER();
    TSET(ta3);
    {
      const int o0 = tr_off(8 * g + qq, 4 * wave + p4), o1 = tr_off(8 * g + 4 + qq, 4 * wave + p4);
#pragma unroll
      for (int kk = 0; kk < 6; ++kk) b[kk] = mk8(tr_read(L0 + kk * 12288 + o0), tr_read(L0 + kk * 12288 + o1));
      f32x4 D[2];
#pragma unroll
      for (int h = 0; h < 2; ++h) {
        rows(h, a);
        D[h] = f32x4{0.f, 0.f, 0.f, 0.f};
#pragma unroll
        for (int kk = 0; kk < 6; ++kk) D[h] = mfma(a[kk], b[kk], D[h]);
      }
      const int e = wave / 3;
      asm volatile("global_store_dwordx4 %0, %1, %2 sc1\n\ts_waitcnt vmcnt(0)"
                   : : "v"(lane * 16 + (e * 15 + 3 * j + wave) * 1024), "v"(pack8(D[0] * SQRT_C, D[1] * SQRT_C)), "s"(box + W2_OFF) : "memory");
    }
    BARRIER();
    if (wave == 11)
      asm volatile("s_mov_b64 exec, 1\n\tglobal_store_dwordx2 %0, %1, %2 sc1\n\ts_mov_b64 exec, -1\n\ts_nop 1"
                   : : "v"(j * 128), "v"(u32x2{line_tag(epoch, 10), line_tag(epoch, 11)}), "s"(box + W2F_OFF) : "memory");
    TPUT(60 + wave * 5, ta0); TPUT(61 + wave * 5, ta1); TPUT(62 + wave * 5, ta2); TPUT(63 + wave * 5, ta3); TPUT(64 + wave * 5, ta4);
  }

  int rslot = -1;
  if (__builtin_expect(wave == 0, 1)) {
    LANE_VARS
    DOFF_VARS
    if (helper) BARRIER();
    const unsigned afull = ct + C_FULL + r * 4;
    unsigned ts = 0;
    {
      const int ti = helper ? 1023 : (lane * 31 + (int)(blockIdx.x >> 3) - 1) & 1023;
      const int sp = ti >> 5, yy = sp * 8 + ((blockIdx.x + sp) & 7), xx = (ti + 5 * sp) & 31;
      unsigned tmp, pub = helper ? 2 * (TPW - 1) : 0, pre = 0, afr = ct + C_FREE + (helper ? ((TPW - 1) % NSLOT) * 4 : 0), e0, e1, e2, g0, g1, g2;
      asm volatile("s_mov_b32 s44, %[due0]\n\ts_mov_b32 s46, %[y0]\n\ts_mov_b32 s47, %[cap]\n\ts_mov_b32 s48, %[i0]\n\ts_mov_b32 s49, %[s0]\n\t"
                   "s_mov_b32 s51, 0\n\ts_mov_b32 s45, 0\n\ts_movk_i32 s39, %[p3s]\n"
                   "1:\n\tv_readlane_b32 s53, %[ob], s48\n\tv_readlane_b32 s52, %[oa], s48\n\ts_add_u32 m0, s49, 0x3000\n\tv_cmp_gt_u32 vcc, s51, %[pre]\n\t"
                   "v_add_u32 %[g0], s53, %[d0]\n\tv_add_u32 %[g1], s53, %[d1]\n\tv_add_u32 %[g2], s53, %[d2]\n\t"
                   "v_add_u32 %[e0], s52, %[d0]\n\tv_add_u32 %[e1], s52, %[d1]\n\tv_add_u32 %[e2], s52, %[d2]\n\t"
                   "s_cbranch_vccnz 5f\n\t"
                   "s_cmp_eq_u32 s48, 0\n\ts_cbranch_scc1 2f\n\ts_cmp_eq_u32 s48, 2\n\ts_cbranch_scc1 8f\n"
                   LD_GATE(1, "")
                   LD1(g0, 68:69, 0) LD1(g1, 68:69, 1024) LD1(g2, 68:69, 2048) "s_mov_b32 m0, s49\n\ts_nop 0\n\t" LD1(e0, 66:67, 0) LD1(e1, 66:67, 1024)
                   LD_NEXT
                   "\n2:\n\tv_add_u32 %[afr], 4, %[afr]\n\ts_cmp_eq_u32 s49, %[lasts]\n\ts_cbranch_scc1 7f\n"
                   "3:\n\tv_mov_b32 %[pre], 0\n\ts_add_u32 m0, s49, 0x800\n\tds_read_b32 %[pre], %[afr] ; NOTRACK\n"
                   LD_GATE(2, "")
                   LD1(e2, 56:57, 0) LD1(e0, 56:57, 1024) LD1(e1, 56:57, 2048) LD1(e2, 56:57, 3072) "s_add_u32 m0, s49, 0x1800\n\ts_add_u32 s48, s48, 1\n\t" LD1(e0, 58:59, 0)
                   LD_NEXT
                   LD_GATE(3, "s_cmp_gt_u32 s48, %[tpw] - 2\n\ts_cbranch_scc1 38f\n39:\n\t")
                   LD1(e1, 58:59, 1024) LD1(e2, 58:59, 2048) LD1(e0, 58:59, 3072) "s_add_u32 m0, s49, 0x2800\n\ts_add_u32 s49, s49, %[slot]\n\t" LD1(e1, 60:61, 0) LD1(e2, 60:61, 1024)
                   LD_NEXT
                   "s_cmp_eq_u32 s49, %[ring]\n\ts_cselect_b32 s49, 0, s49\n\ts_cmp_gt_i32 s46, 30\n\ts_cbranch_scc1 6f\n"
                   "4:\n\ts_cmp_eq_u32 s48, %[sk]\n\ts_cbranch_scc1 60f\n"
                   "61:\n\ts_cmp_eq_u32 s48, %[pk]\n\ts_cmovk_i32 s39, %[p3]\n\ts_cmp_lg_u32 s48, %[tpw]\n\ts_cbranch_scc1 1b\n\ts_branch 9f\n"
                   LD_PUB(1, 0, 15, 30) LD_PUB(2, 5, 20, 35) LD_PUB(3, 10, 25, 40)
                   "38:\n\ts_cmp_eq_u32 s46, 10\n\ts_cselect_b32 s43, 23, 0\n\ts_cmp_lt_u32 s42, s43\n\ts_cbranch_scc0 39b\n\tv_add_u32 %[tmp], 1, %[pub]\n\ts_waitcnt vmcnt(22)\n\tds_write_b32 %[af], %[tmp] ; NOTRACK\n\ts_branch 39b\n"
                   "60:\n\tv_mov_b32 %[tmp], %[qi]\n\tds_read_b32 %[g0], %[tmp]\n\tds_read_b32 %[g1], %[tmp] offset:4\n\ts_mul_i32 s42, %[t0], 6\n\ts_waitcnt lgkmcnt(0)\n\t"
                   "v_readfirstlane_b32 s52, %[g0]\n\tv_readfirstlane_b32 s53, %[g1]\n\ts_sub_u32 s53, s53, s42\n\ts_mul_i32 s53, s53, 43\n\ts_ashr_i32 s53, s53, 4\n\t"
                   "s_add_u32 s53, s53, %[toff]\n\ts_min_i32 s53, s53, %[smax]\n\ts_max_i32 s53, s53, -3200\n\ts_cmp_eq_u32 s52, 3\n\ts_cselect_b32 s53, s53, 0\n\ts_add_u32 s44, s44, s53\n\ts_branch 61b\n"
                   "5:\n\ts_movk_i32 s40, 0x100\n"
                   "40:\n\tds_read_b32 %[pre], %[afr]\n\t" LD_VM
                   "s_sub_u32 s40, s40, 1\n\ts_cselect_b32 s42, 0, s42\n\ts_cselect_b32 s40, 0, s40\n\t"
                   "s_cmp_le_i32 s42, s46\n\ts_cbranch_scc1 42f\n"
                   "41:\n\ts_sub_i32 s47, s47, 1\n\ts_cmp_lt_i32 s47, 0\n\ts_waitcnt lgkmcnt(0)\n\ts_cbranch_scc1 10b\n\t"
                   "v_cmp_gt_u32 vcc, s51, %[pre]\n\ts_cbranch_vccnz 40b\n\ts_branch 10b\n"
                   LD_PUB0(4, 0, 15, 30)
                   "6:\n\ts_waitcnt vmcnt(45)\n\tv_add_u32 %[pub], 2, %[pub]\n\tds_write_b32 %[af], %[pub]\n\ts_sub_u32 s46, s46, 15\n\ts_branch 4b\n"
                   "7:\n\ts_waitcnt lgkmcnt(0)\n\ts_add_u32 s51, s51, 8\n\tv_subrev_u32 %[afr], %[frb], %[afr]\n\ts_branch 3b\n"
                   "8:\n\ts_barrier\n\ts_add_u32 s46, s46, 64\n\ts_branch 10b\n"
                   "9:\n\ts_cmp_lt_i32 s46, 30\n\ts_cbranch_scc1 91f\n\ts_waitcnt vmcnt(30)\n\tv_add_u32 %[pub], 2, %[pub]\n\tds_write_b32 %[af], %[pub]\n"
                   "91:\n\ts_cmp_lt_i32 s46, 15\n\ts_cbranch_scc1 92f\n\tv_add_u32 %[tmp], 1, %[pub]\n\ts_waitcnt vmcnt(27)\n\tds_write_b32 %[af], %[tmp]\n"
                   "92:\n\ts_waitcnt lgkmcnt(0)"
                   : [pub] "+v"(pub), [pre] "+v"(pre), [afr] "+v"(afr), [ts] "+v"(ts), [tmp] "=&v"(tmp),
                     [e0] "=&v"(e0), [e1] "=&v"(e1), [e2] "=&v"(e2), [g0] "=&v"(g0), [g1] "=&v"(g1), [g2] "=&v"(g2)
                   : "{s[64:79]}"(ka), "{s[56:57]}"(f0 + 0x800), "{s[58:59]}"(f0 + 0x30000), "{s[60:61]}"(f0 + 0x31000), [t0] "s"(t0), [af] "v"(afull),
                     [d0] "v"(doff[0]), [d1] "v"(doff[1]), [d2] "v"(doff[2]), [oa] "v"(yy * 393216 + xx * 6144), [ob] "v"(yy * 98304 + xx * 3072),
                     [due0] "s"(helper ? HDUE * (PACE16 / 3) : PACES / 3), [y0] "s"(helper ? -15 : -74), [i0] "s"(helper ? TPW - 1 : 0),
                     [s0] "s"(helper ? ((TPW - 1) % NSLOT) * SLOT : 0),
                     [cap] "i"(SPIN_CAP), [win] "i"(PWIN), [ahi] "i"(AHI), [adl] "i"(ADL), [amg] "i"(AMG), [p3] "i"(PACE16 / 3), [p3s] "i"(PACES / 3), [pk] "i"(PK1), [slot] "i"(SLOT),
                     [ring] "i"(NSLOT * SLOT), [frb] "i"(NSLOT * 4), [lasts] "i"((NSLOT - 1) * SLOT), [tpw] "i"(TPW), [sk] "i"(SYNCK), [qi] "i"(CT_OFF + C_QI), [toff] "i"(SYNCO * 16), [smax] "i"(SYNCM * 16)
                   : "memory", "scc", "vcc", "s39", "s40", "s41", "s42", "s43", "s44", "s45", "s46", "s47", "s48", "s49", "s51", "s52", "s53", "s62", "s63");
    }
    lds_st32(ct + C_ISS, 1u);
    asm volatile("s_waitcnt vmcnt(15)\n\tds_write_b32 %0, %1\n\ts_waitcnt vmcnt(12)\n\tds_write_b32 %0, %2\n\ts_waitcnt vmcnt(0)\n\tds_write_b32 %0, %3"
                 : : "v"(afull), "v"(2 * TPW - 2), "v"(2 * TPW - 1), "v"(~0u) : "memory");
    TNOW(tl);
    {
      unsigned d;
      asm volatile("global_load_dword %0, %1, %2 sc1\n\ts_setprio 0\n\ts_waitcnt vmcnt(0)" : "=v"(d) : "v"((lane * RSTR + blockIdx.x) * 128), "s"(box) : "memory");
    }
    TNOW(tlx);
    rslot = 7;
  } else {
    if (wave == 1) {
      LANE_VARS
    {
      const unsigned o = lane * 8;
      unsigned z = (o >= C_MREF && o < C_MREF + 64) ? 0xff800000u : 0u;
      if (helper && o >= C_CONS && o < C_MREF) z = NE * ((TPW - 1) / NPK);
      u32x2 zz = {z, z};
      if (o >= C_PFL && o < C_CONS) zz = u32x2{(o - C_PFL) / 2 + 2, (o - C_PFL) / 2 + 4} + (helper ? 2u * NPK * ((TPW - 1) / NPK) : 0u);
      if (o == C_EP) zz[1] = QKW0 + QKW1 * min(t0 + T0BACK - tent, 45u);
      *(LDS_AS(u32x2)*)(L0 + CT_OFF + o) = zz;
      *(LDS_AS(u32x2)*)(L0 + ONES_OFF + (lane < 48 ? o : 0)) = u32x2{0x3f803f80u, 0x3f803f80u};
    }
    }
    if (helper || wave == 1) BARRIER();
    if (wave <= NE) {
      LANE_VARS
      const int ej = wave - 1;
      const int qq = (lane >> 2) & 3, p4 = lane & 3;
      const unsigned apk = lbase + IMG_A + lane * 16;
      const unsigned epoch = epoch_load(box);
      __builtin_amdgcn_s_setprio(2);
      unsigned long long tpv, tp7, tx1;
      {
        static_assert((TPW & 1) == 1 && C_CONS == C_PFL + 64 && C_FREE == C_FULL + 64, "");
        const int k0 = helper ? TPW - 1 : 0;
        unsigned need = 2 * k0 + 2;
        asm volatile("s_mov_b32 s46, 0\n\ts_mov_b64 exec, 0\n\ts_nop 3\n\ts_branch 99f\n"
                     "2:\n\ts_mov_b64 exec, -1\n\ts_nop 7\n\t" PV_Z4(72, 73, 74, 75) PV_Z4(76, 77, 78, 79) PV_Z4(80, 81, 82, 83) PV_Z4(84, 85, 86, 87)
                     "v_mov_b32 v88, 0xff800000\n\t"
                     "s_mov_b32 s46, %[k0]\n\ts_mov_b32 s45, %[s0]\n\ts_mov_b32 s44, %[e0]\n\ts_mov_b32 s47, %[cap]\n\t"
                     "s_cmp_lg_u32 s46, 0\n\ts_cbranch_scc1 32f\n"
                     "3:\n\tv_mov_b32 v148, %[ca]\n\t" PV_WAITF(4, 148) PV_FETCH_A
                     "s_cmp_lg_u32 s46, 0\n\ts_cbranch_scc1 99f\n\t"
                     "s_mov_b64 exec, 2\n\tds_add_u32 v148, %[one] offset:64\n\ts_mov_b64 exec, -2\n\t"
                     "v_add_u32 %[need], 2, %[need]\n\tv_add_u32 v148, 4, v148\n\ts_mov_b64 exec, -1\n\t"
                     PV_WAITF(5, 148)
                     "v_mov_b32 v149, v148\n\ts_mov_b64 exec, -2\n\tv_add_u32 %[need], 2, %[need]\n\tv_add_u32 v148, 4, v148\n\t"
                     "s_mov_b64 exec, 1\n\tv_add_u32 %[need], %[npk2], %[need]\n\ts_mov_b64 exec, -1\n\t"
                     PV_REQ("126:129", "130:133", "134:135", 136, 148) PV_FETCH_B
                     "s_mov_b64 exec, 2\n\tds_add_u32 v149, %[one] offset:64\n\ts_mov_b64 exec, -1\n"
                     "6:\n\t"
                     PV_STEP(1, "126:129", "130:133", "134:135", 134, 135, 136, 148, "138:141", "142:145", "146:147", 137, 149,
                             "92:95", "98:101", "104:107", "90:93", "96:99", "102:105",
                             PV_MOV(92, 90) PV_MOV(93, 91), PV_MOV(98, 96) PV_MOV(99, 97), PV_MOV(104, 102) PV_MOV(105, 103), "")
                     "s_cbranch_vccnz 7f\n"
                     "16:\n\t" PV_FETCH_A PV_REL(148)
                     "s_lshl1_add_u32 s54, s46, -2\n\ts_lshr_b64 s[54:55], %[sch], s54\n\ts_and_b32 s54, s54, 15\n\ts_cbranch_scc1 31f\n"
                     "30:\n\t"
                     PV_STEP(2, "138:141", "142:145", "146:147", 146, 147, 137, 149, "126:129", "130:133", "134:135", 136, 148,
                             "110:113", "116:119", "122:125", "108:111", "114:117", "120:123",
                             PV_MOV(110, 108) PV_MOV(111, 109), PV_MOV(116, 114) PV_MOV(117, 115), PV_MOV(122, 120) PV_MOV(123, 121),
                             "s_cmp_eq_u32 s46, %[last] - 1\n\ts_cbranch_scc1 98f\n\t")
                     "s_cbranch_vccnz 8f\n"
                     "26:\n\t" PV_FETCH_B PV_REL(149)
                     "s_branch 6b\n"
                     PV_WAITF(7, 148) "s_branch 16b\n"
                     PV_WAITF(8, 149) "s_branch 26b\n"
                     PV_SLOW(1, "126:129", "130:133", "134:135", 134, 135, 136, 148, 126, 127, 128, 129)
                     PV_SLOW(2, "138:141", "142:145", "146:147", 146, 147, 137, 149, 138, 139, 140, 141)
                     "31:\n\ts_cmp_lg_u32 s54, 8\n\ts_cbranch_scc1 34f\n"
                     "32:\n\tv_mbcnt_lo_u32_b32 v152, 31, 0\n\tv_lshlrev_b32 v152, 7, v152\n\ts_mov_b32 s55, %[gcap]\n"
                     "33:\n\tglobal_load_dwordx2 v[150:151], v152, %[fl] sc1\n\ts_sub_u32 s55, s55, 1\n\ts_cselect_b32 s56, 0, -1\n\ts_waitcnt vmcnt(0) lgkmcnt(0)\n\t"
                     "v_cmp_ne_u64 s[48:49], %[tag], v[150:151]\n\ts_sleep 2\n\ts_and_b32 s48, s48, s56\n\ts_cbranch_scc1 33b\n\t"
                     "s_mov_b32 s57, 0\n\tv_mov_b32 v150, %[apk]\n\ts_cmp_lg_u32 s46, %[last]\n\ts_cbranch_scc1 30b\n\ts_mov_b32 s54, 7\n\ts_branch 38f\n"
                     "34:\n\ts_cmp_lg_u32 s46, %[last] - 3\n\ts_cbranch_scc1 36f\n\tv_mov_b32 v151, %[iss]\n"
                     "35:\n\tds_read_b32 v150, v151\n\ts_sub_i32 s47, s47, 1\n\ts_cmp_lt_i32 s47, 0\n\ts_cselect_b32 s55, 0, 1\n\ts_waitcnt lgkmcnt(0)\n\t"
                     "v_readfirstlane_b32 s56, v150\n\ts_cmp_lt_u32 s56, s55\n\ts_cbranch_scc1 35b\n"
                     "36:\n\ts_mul_i32 s55, s57, 3072\n\tv_add_u32 v150, s55, %[apk]\n\ts_getpc_b64 s[48:49]\n"
                     "37:\n\ts_mul_i32 s55, s57, 40\n\ts_add_u32 s55, s55, 38f-37b\n\ts_add_u32 s48, s48, s55\n\ts_addc_u32 s49, s49, 0\n\t"
                     "s_add_u32 s57, s57, s54\n\ts_sub_u32 s54, s54, 1\n\ts_setpc_b64 s[48:49]\n"
                     "38:\n\t" PV_W2(0, 3, 4, 7, 8, 11) PV_W2(12, 15, 16, 19, 20, 23) PV_W2(24, 27, 28, 31, 32, 35) PV_W2(36, 39, 40, 43, 44, 47)
                     PV_W2(48, 51, 52, 55, 56, 59) PV_W2(60, 63, 64, 67, 68, 71)
                     "s_waitcnt lgkmcnt(0)\n\ts_branch 3b\n"
                     "96:\n\t" PV_RESC(134, 135, 126, 127, 128, 129) "s_nop 0\n\ts_branch 95f\n"
                     "98:\n\t" PV_MF("76:79", "114:117", "138:141") PV_MF("80:83", "120:123", "138:141")
                     "99:\n\ts_cmp_eq_u32 s46, 0\n\ts_cselect_b32 s54, 0, %[nl]\n\ts_cselect_b32 s55, 0, %[x1n]\n\ts_waitcnt lgkmcnt(0)\n\t"
                     "v_mov_b64 v[108:109], v[90:91]\n\tv_mov_b64 v[110:111], v[90:91]\n\tv_mov_b64 v[112:113], v[96:97]\n\tv_mov_b64 v[114:115], v[96:97]\n\t"
                     "v_mov_b64 v[116:117], v[102:103]\n\tv_mov_b64 v[118:119], v[102:103]\n\tv_add_u32 v120, %[aw], %[apk]\n\tv_add_u32 v121, %[ar], %[apk]\n\t"
                     "v_mov_b32 v122, %[x1]\n\tv_lshrrev_b32 v123, 1, %[apm]\n\tv_add_u32 v123, %[lwo], v123\n\t"
                     "v_mbcnt_lo_u32_b32 v124, -1, 0\n\tv_mbcnt_hi_u32_b32 v124, -1, v124\n\tv_and_b32 v125, 1, v124\n\tv_and_b32 v124, 48, v124\n\t"
                     "v_lshl_add_u32 v124, v125, 2, v124\n\tv_add_u32 v124, %[lw], v124\n"
                     "94:\n\tds_read_b32 v136, v122 offset:%[pfd]\n\tds_read_b64 v[134:135], %[apm] offset:%[el]\n\tds_read_b128 v[126:129], %[apk] offset:8 * %[el]\n\t"
                     "ds_read_b128 v[130:133], %[apu] offset:2 * %[el]\n\ts_sub_i32 s47, s47, 1\n\ts_cmp_lt_i32 s47, 0\n\ts_cselect_b32 s54, 0, s54\n\ts_waitcnt lgkmcnt(3)\n\t"
                     "v_readfirstlane_b32 s56, v136\n\ts_cmp_lt_u32 s56, s54\n\ts_cbranch_scc1 94b\n\ts_waitcnt lgkmcnt(2)\n\t"
                     "v_cmp_neq_f32 vcc, v134, v88\n\tv_cmp_neq_f32 s[50:51], v135, v88\n\ts_or_b64 vcc, vcc, s[50:51]\n\ts_waitcnt lgkmcnt(0)\n\ts_cbranch_scc1 96b\n"
                     "95:\n\t" PV_MF("84:87", "130:133", "126:129") "s_nop 1\n\t" PV_MF("72:75", "92:95", "126:129") "s_nop 1\n\t" PV_MF("72:75", "108:111", "126:129") PV_T(58)
                     "s_nop 1\n\tv_add_f32 v153, v86, v87\n\tv_add_f32 v151, v84, v85\n\t" PV_MF("76:79", "98:101", "126:129")
                     "v_rcp_f32 v150, v153\n\tv_mul_f32 v151, %[nic], v151\n\t" PV_MF("76:79", "112:115", "126:129")
                     "v_mul_f32 v151, v151, v150\n\t" PV_PK(72, 73) PV_MF("80:83", "104:107", "126:129") PV_PK(74, 75)
                     "v_cvt_pk_bf16_f32 v72, v72, v73\n\t" PV_MF("80:83", "116:119", "126:129") "v_cvt_pk_bf16_f32 v73, v74, v75\n\tds_write_b64 v120, v[72:73]\n\t"
                     PV_NRM(76, 77, 78, 79, 2048) "v_log_f32 v153, v153\n\t" PV_NRM(80, 81, 82, 83, 4096)
                     "s_and_b64 exec, exec, 1\n\tds_add_u32 v122, %[one]\n\ts_cselect_b64 exec, -1, 0\n\t" PV_T(60)
                     "v_subrev_f32 v88, %[toff], v88\n\tv_add_f32 v88, v88, v153\n\tds_write_b32 v123, v88\n\tds_read2_b32 v[114:115], v124 offset1:2\n"
                     "97:\n\tds_read_b32 v150, v122\n\tds_read_b128 v[90:93], v121\n\tds_read_b128 v[94:97], v121 offset:1024\n\tds_read_b128 v[98:101], v121 offset:2048\n\t"
                     "ds_read_b128 v[102:105], v121 offset:3072\n\tds_read_b128 v[106:109], v121 offset:4096\n\tds_read_b128 v[110:113], v121 offset:5120\n\t"
                     "s_sub_i32 s47, s47, 1\n\ts_cmp_lt_i32 s47, 0\n\ts_cselect_b32 s55, 0, s55\n\ts_waitcnt vmcnt(0) lgkmcnt(6)\n\t"
                     "v_readfirstlane_b32 s56, v150\n\ts_cmp_lt_u32 s56, s55\n\ts_cbranch_scc1 97b\n\ts_cmp_eq_u32 s46, 0\n\t"
                     PV_G(5, "90:93", "0:3", "4:7", "8:11", "0", "0", "0")
                     PV_G(4, "94:97", "12:15", "16:19", "20:23", "v[72:75]", "v[76:79]", "v[80:83]")
                     PV_G(3, "98:101", "24:27", "28:31", "32:35", "v[72:75]", "v[76:79]", "v[80:83]")
                     PV_G(2, "102:105", "36:39", "40:43", "44:47", "v[72:75]", "v[76:79]", "v[80:83]")
                     PV_G(1, "106:109", "48:51", "52:55", "56:59", "v[72:75]", "v[76:79]", "v[80:83]")
                     PV_G(0, "110:113", "60:63", "64:67", "68:71", "v[72:75]", "v[76:79]", "v[80:83]")
                     "v_xor_b32 v114, v114, %[ltag]\n\tv_xor_b32 v115, v115, %[ltag]\n\ts_nop 3\n\t"
                     "v_cvt_pk_bf16_f32 v116, v72, v73\n\tv_cvt_pk_bf16_f32 v120, v74, v75\n\tv_cvt_pk_bf16_f32 v117, v76, v77\n\tv_cvt_pk_bf16_f32 v121, v78, v79\n\t"
                     "v_cvt_pk_bf16_f32 v118, v80, v81\n\tv_bitop3_b32 v119, v116, v117, v114 bitop3:0x96\n\tv_cvt_pk_bf16_f32 v122, v82, v83\n\t"
                     "v_bitop3_b32 v123, v120, v121, v115 bitop3:0x96\n\tv_xor_b32 v119, v119, v118\n\tv_xor_b32 v123, v123, v122\n\t"
                     "s_cbranch_scc1 2b\n\t"
                     "global_store_dwordx4 %[so], v[116:119], %[bx] sc1\n\tglobal_store_dwordx4 %[so], v[120:123], %[bx2] sc1\n\t"
                     PV_T(62) "s_waitcnt lgkmcnt(0)\n\ts_nop 0"
                     : [need] "+v"(need), "={s[58:59]}"(tpv), "={s[60:61]}"(tp7), "={s[62:63]}"(tx1)
                     : [so] "v"(((ej * 16 + (lane >> 3)) * RSTR + blockIdx.x) * 128 + (lane & 7) * 16), [ltag] "v"(line_tag(epoch, lane & 7)),
                       [bx] "s"(box), [bx2] "s"(box + 8 * RSTR * 128), [aw] "s"(A_IMG - IMG_A + (ej >> 1) * 1024 + (ej & 1) * 8), [ar] "i"(A_IMG - IMG_A),
                       [nic] "i"(__builtin_bit_cast(unsigned, -1.f / CC)), [toff] "i"(__builtin_bit_cast(unsigned, TOFF)),
                       [lwo] "i"(CT_OFF + C_LW - PKM / 2), [lw] "i"(CT_OFF + C_LW), [x1] "i"(CT_OFF + C_X1), [x1n] "i"(NE),
                       [nl] "i"(2 * TPW + 2 * NPK), [el] "i"(((TPW - 1) % NPK) * 128), [pfd] "i"(C_PFL + ((TPW - 1) % NPK) * 4 - C_X1),
                       [apk] "v"(apk), [apm] "v"(lbase + PKM + r * 8), [apu] "v"(lbase + PKU + g * 64 + (r & 3) * 16),
                       [ox] "v"(lbase + tr_off(4 * g + qq, 4 * ej + p4)), [oy] "v"(lbase + 32 * ROWB + tr_off(2 * g + (qq >> 1), 4 * ej + p4)),
                       [ca] "v"(ct + (lane == 0 ? C_PFL + (k0 % NPK) * 4 : C_FULL + (k0 % NSLOT) * 4)),
                       [lim] "v"(ct + (lane == 0 ? C_PFL + NPK * 4 : C_FULL + NSLOT * 4)), [base] "v"(ct + (lane == 0 ? C_PFL : C_FULL)), [one] "v"(1u),
                       [k0] "s"(k0), [s0] "s"(helper ? ((TPW - 1) % NSLOT) * SLOT : 0), [e0] "s"(helper ? ((TPW - 1) % NPK) * 128 : 0),
                       [w2] "s"(box + W2_OFF + ej * 18432 - (lbase + IMG_A)), [fl] "s"(box + W2F_OFF),
                       [tag] "s"(u32x2{line_tag(epoch, 10), line_tag(epoch, 11)}), [gcap] "i"(GSPIN_CAP), [iss] "i"(CT_OFF + C_ISS), [cap] "i"(SPIN_CAP), [slot] "i"(SLOT), [ring] "i"(NSLOT * SLOT), [pring] "i"(NPK * 128),
                       [npk2] "i"(2 * NPK), [last] "i"(TPW - 1), [tight] "i"(TPW - 5),  [sch] "s"(W2SCH)
                     : "memory", "scc", "vcc", "s44", "s45", "s46", "s47", "s48", "s49", "s50", "s51", "s52", "s53", "s54", "s55", "s56", "s57",
                       "v0", "v1", "v2", "v3", "v4", "v5", "v6", "v7", "v8", "v9", "v10", "v11", "v12", "v13", "v14", "v15", "v16", "v17", "v18", "v19", "v20", "v21", "v22", "v23", "v24", "v25", "v26", "v27", "v28", "v29", "v30", "v31", "v32", "v33", "v34", "v35", "v36", "v37", "v38", "v39", "v40", "v41", "v42", "v43", "v44", "v45", "v46", "v47", "v48", "v49", "v50", "v51", "v52", "v53", "v54", "v55", "v56", "v57", "v58", "v59", "v60", "v61", "v62", "v63", "v64", "v65", "v66", "v67", "v68", "v69", "v70", "v71", "v72", "v73", "v74", "v75", "v76", "v77", "v78", "v79", "v80", "v81", "v82", "v83", "v84", "v85", "v86", "v87", "v88",
                       "v90", "v91", "v92", "v93", "v94", "v95", "v96", "v97", "v98", "v99", "v100", "v101", "v102", "v103", "v104", "v105", "v106", "v107",
                       "v108", "v109", "v110", "v111", "v112", "v113", "v114", "v115", "v116", "v117", "v118", "v119", "v120", "v121", "v122", "v123",
                       "v124", "v125", "v126", "v127", "v128", "v129", "v130", "v131", "v132", "v133", "v134", "v135", "v136", "v137", "v138", "v139",
                       "v140", "v141", "v142", "v143", "v144", "v145", "v146", "v147", "v148", "v149", "v150", "v151", "v152", "v153", "v154", "v155");
      }
    } else {
      LANE_VARS
      const int c = wave - 1 - NE;
      SCtx sc;
      sc.L0 = L0; sc.ct = ct; sc.apk = lbase + IMG_A + lane * 16; sc.apm = lbase + PKM + r * 8;
      sc.api = lbase + PKU + g * 64 + (r & 3) * 2 + ((r >> 2) == g ? 0 : 8); sc.amr = ct + C_MREF + r * 4;
      sc.o_rb = r * ROWB + ((g ^ swz(r)) << 4);
      sc.ev = (r & 1) ^ 1;
      sc.o_yb = (r & 1) ? ONES_OFF + (g << 4) : (32 + (r >> 1)) * ROWB + ((g ^ swz(r >> 1)) << 4);
      TNOW(tq0);
      if (c >= NEARLY) {
        static_assert(NS - NEARLY == 3, "");
        const int u = c - NEARLY;
        const unsigned epoch = epoch_load(box);
        const unsigned ta = line_tag(epoch, 8), tb = line_tag(epoch, 9);
        const unsigned qw = lds_ld32(ct + C_EP + 4);
        while ((unsigned)__builtin_amdgcn_s_memrealtime() - tent < qw) __builtin_amdgcn_s_sleep(4);
        u32x4 v[4];
#pragma unroll 1
        for (int sp = 0; sp < GSPIN_CAP; ++sp) {
          asm volatile("global_load_dwordx4 %0, %4, %5 sc1\n\tglobal_load_dwordx4 %1, %4, %5 offset:1024 sc1\n\tglobal_load_dwordx4 %2, %4, %5 offset:2048 sc1\n\t"
                       "global_load_dwordx4 %3, %4, %5 offset:3072 sc1\n\ts_waitcnt vmcnt(0)"
                       : "=&v"(v[0]), "=&v"(v[1]), "=&v"(v[2]), "=&v"(v[3]) : "v"(lane * 16), "s"(box + QK_OFF + u * 4096) : "memory");
          unsigned bad = 0;
#pragma unroll
          for (int t = 0; t < 4; ++t) bad |= (v[t][0] ^ v[t][1] ^ v[t][2] ^ v[t][3] ^ ta) | (v[t][3] ^ __builtin_amdgcn_readfirstlane(v[t][3]));
          if (!__any(bad != 0u)) break;
        }
        TSET(tqa);
#pragma unroll
        for (int t = 0; t < 4; ++t)
          *(LDS_AS(u32x2)*)(L0 + IMG_A + (4 * u + t) * 512 + ((g >> 1) * 16 + r) * 16 + (g & 1) * 8) = u32x2{v[t][0], v[t][1]};
        asm volatile("" ::: "memory");
        lds_add(ct + C_QI + 4, (v[0][3] ^ tb) + (v[2][3] ^ tb));
        lds_add(ct + C_QI, 1u);
        lds_st32(ct + C_EP, epoch);
      }
      static_assert(NE == 4 && NPK == 6 && NSLOT == 10, "");
      s_items(sc, helper ? TPW : c, helper || c >= NEARLY);
      static_assert(NH1 == 3 && NS == 7 && TPW == 33, "");
      const int hi = c == 0 ? 5 : c - 2;
      if (hi >= (helper ? 4 : 0)) s_half(sc, TPW - NH1 + (hi >> 1), hi & 1);
      TNOW(tse);
      rslot = c;
    }
  }

  if (rslot >= 0 && ((blockIdx.x >> 3) & 3) == 1) {
    LANE_VARS
    __builtin_amdgcn_s_setprio(0);
    const int rid = ((blockIdx.x >> 5) << 3) | (blockIdx.x & 7);
    const int qa = ((rid >> 1) & 3) * 4 + ((rid >> 3) & 1) * 2;
    const char* src = box + (size_t)(rid * RSTR + rslot * 32) * 128;
    const int e = lane & 7;
    const int oidx = (qa + (e & 1)) * CC + 48 * (rid >> 4) + 16 * (e < 6 ? e >> 1 : 0) + 8 * (rid & 1) + (lane >> 3);
    const unsigned short qv = ((const gus*)q)[oidx];
    u32x4 am;
#pragma unroll
    for (int d = 0; d < 4; ++d) am[d] = (e >> 1) == d ? ((e & 1) ? 0xffff0000u : 0x0000ffffu) : 0u;
    const unsigned epoch = *(LDS_AS(unsigned)*)(L0 + CT_OFF + C_EP);
    const unsigned arf = lbase + RED_OFF + 4096 + (lane & 1) * 4;
    poll_ge_sleep(ct + C_FREE + ((TPW - NPK) % NSLOT) * 4, 8u * ((TPW - NPK) / NSLOT + 1));
    lds_st32(arf + rslot * 8, 0u);
    unsigned long long tr;
#define R_LD(d, m, o) "s_mov_b64 exec, s[" m "]\n\tglobal_load_dwordx4 v[" d "], %[vo], %[src] offset:" #o " sc1\n\t"
#define R_W(l, a0, a1, a2, a3) "v_cvt_pk_bf16_f32 v" #a3 ", v" #a3 ", v" #a3 "\n\tv_and_b32 v" #a0 ", v60, v" #a3 "\n\tv_and_b32 v" #a1 ", v61, v" #a3 "\n\t" \
                               "v_and_b32 v" #a2 ", v62, v" #a3 "\n\tv_and_b32 v" #a3 ", v63, v" #a3 "\n\t"
#define R_G(n, w, m, l, d0, d1, d2, d3, a0, a1, a2, a3, c) \
  "s_mov_b64 exec, s[" m "]\n\ts_cbranch_execz " #n "f\n\ts_waitcnt vmcnt(" #w ")\n\tv_bitop3_b32 v" #l ", v" #d0 ", v" #d1 ", v" #d2 " bitop3:0x96\n\t" \
  "v_bitop3_b32 v" #l ", v" #l ", v" #d3 ", %[tag] bitop3:0x96\n\tv_mov_b32 v" #d3 ", s49\n\ts_mov_b64 exec, -1\n\tv_exp_f32 v" #a3 ", v" #l "\n\t" \
  "v_xor_b32_dpp v59, v" #l ", v" #l " quad_perm:[2,3,0,1] row_mask:0xf bank_mask:0x5\n\tv_xor_b32_dpp v59, v" #l ", v" #l " row_shr:4 row_mask:0xf bank_mask:0xa\n\t" \
  R_W(l, a0, a1, a2, a3) "v_cmp_ne_u32 s[" m "], 0, v59\n\ts_nop 0\n" \
  "1" #n ":\n\tv_mfma_f32_16x16x32_bf16 v[36:39], v[" #a0 ":" #a3 "], v[" #d0 ":" #d3 "], " c "\n\t"
#define R_X(n) #n ":\n\ts_mov_b64 exec, -1\n\ts_nop 2\n\ts_branch 1" #n "b\n"
#define R_F(l, d0, d3, a0, a1, a2, a3, c) "v_sub_f32 v" #a3 ", v" #l ", v56\n\tv_exp_f32 v" #a3 ", v" #a3 "\n\ts_nop 0\n\t" R_W(l, a0, a1, a2, a3) \
  "s_nop 1\n\tv_mfma_f32_16x16x32_bf16 v[36:39], v[" #a0 ":" #a3 "], v[" #d0 ":" #d3 "], " c "\n\t"
#define R_RD(d, a, b) "ds_read2st64_b32 v[" d "], %[ard] offset0:" #a " offset1:" #b "\n\t"
#define R_A(d, a) "v_pk_add_f32 v[" d "], v[" d "], v[" a "]\n\t"
#define R_S(x, f) "v_sub_f32 v" #f ", v" #f ", v56\n\tv_exp_f32 v" #f ", v" #f "\n\ts_nop 0\n\tv_mul_f32 v[" #x "], v[" #x "], v" #f "\n\tv_mul_f32 v[" #x " + 1], v[" #x " + 1], v" #f "\n\t"
    asm volatile("s_mov_b64 s[52:53], 0\n\ts_mov_b64 s[40:41], -1\n\ts_mov_b64 s[42:43], -1\n\ts_mov_b64 s[44:45], -1\n\ts_mov_b64 s[46:47], -1\n\ts_mov_b32 s48, %[cap]\n\ts_mov_b32 s49, 0x3f803f80\n\t"
                 "v_mov_b32 v57, 1\n\tv_mov_b32 v58, %[x1]\n"
                 "20:\n\ts_sleep 1\n\tds_read_b32 v56, v58\n\ts_sub_u32 s48, s48, 1\n\ts_cselect_b32 s50, 0, %[x1n]\n\ts_waitcnt lgkmcnt(0)\n\t"
                 "v_readfirstlane_b32 s51, v56\n\ts_cmp_lt_u32 s51, s50\n\ts_cbranch_scc1 20b\n\tv_mov_b32 v58, %[red]\n\ts_mov_b32 s48, %[gcap]\n\ts_sleep %[rs]\n"
                 "21:\n\t" R_LD("0:3", "40:41", 0) R_LD("4:7", "42:43", 1024) R_LD("8:11", "44:45", 2048) R_LD("12:15", "46:47", 3072)
                 "s_cmp_eq_u32 s48, %[gcap]\n\ts_cbranch_scc1 24f\n\ts_waitcnt vmcnt(0)\n24:\n\t"
                 R_G(1, 3, "40:41", 32, 0, 1, 2, 3, 16, 17, 18, 19, "0") R_G(2, 2, "42:43", 33, 4, 5, 6, 7, 20, 21, 22, 23, "v[36:39]")
                 R_G(3, 1, "44:45", 34, 8, 9, 10, 11, 24, 25, 26, 27, "v[36:39]") R_G(4, 0, "46:47", 35, 12, 13, 14, 15, 28, 29, 30, 31, "v[36:39]")
                 "v_max3_f32 v56, v32, v33, v34\n\ts_sub_u32 s48, s48, 1\n\ts_cbranch_scc1 22f\n\ts_or_b64 s[50:51], s[40:41], s[42:43]\n\tv_max_f32 v56, v56, v35\n\t"
                 "s_or_b64 s[50:51], s[50:51], s[44:45]\n\ts_or_b64 s[50:51], s[50:51], s[46:47]\n\tv_cmp_nlt_f32 vcc, |v56|, %[lim]\n\ts_cbranch_scc1 21b\n\t"
                 "s_cbranch_vccnz 25f\n"
                 "22:\n\ts_mov_b64 exec, %[wm]\n\tds_write_b128 %[awr], v[36:39]\n\ts_mov_b64 exec, 1\n\tds_add_rtn_u32 v59, v58, v57\n\ts_mov_b64 exec, -1\n\t"
                 R_RD("40:41", 0, 1) R_RD("42:43", 2, 3) R_RD("44:45", 4, 5) R_RD("46:47", 6, 7) R_RD("48:49", 8, 9) R_RD("50:51", 10, 11) R_RD("52:53", 12, 13) R_RD("54:55", 14, 15)
                 "s_waitcnt lgkmcnt(8)\n\tv_add_u32 v59, v59, v57\n\ts_nop 0\n\tv_readfirstlane_b32 s50, v59\n\ts_cmp_lg_u32 s50, 8\n\ts_cbranch_scc1 26f\n"
                 "23:\n\ts_waitcnt lgkmcnt(6)\n\t" R_A("40:41", "42:43") "s_waitcnt lgkmcnt(4)\n\t" R_A("44:45", "46:47") "s_waitcnt lgkmcnt(2)\n\t" R_A("48:49", "50:51")
                 R_A("40:41", "44:45") "s_waitcnt lgkmcnt(0)\n\t" R_A("52:53", "54:55") R_A("48:49", "52:53") R_A("40:41", "48:49")
                 "v_add_f32 v40, v40, v41\n\ts_nop 1\n\tv_mov_b32_dpp v41, v40 quad_perm:[2,3,2,3] row_mask:0xf bank_mask:0xf\n\ts_nop 1\n\t"
                 "v_mov_b32_dpp v41, v41 row_shl:4 row_mask:0xf bank_mask:0x5\n\tv_rcp_f32 v41, v41\n\ts_mov_b64 exec, %[om]\n\tv_mul_f32 v40, v40, v41\n\t"
                 "v_cvt_pk_bf16_f32 v40, v40, v40\n\tv_lshlrev_b32 v40, 16, v40\n\tv_add_f32 v40, v40, %[qf]\n\tv_cvt_pk_bf16_f32 v40, v40, v40\n\t"
                 "global_store_short %[oo], v40, %[out]\n\t"
                 "s_cmp_lg_u32 %[rid], 0\n\ts_cbranch_scc1 29f\n\ts_mov_b64 exec, 1\n\tv_mov_b32 v41, %[eo]\n\tglobal_store_dword v41, %[ep], %[bx] sc1\n\ts_branch 29f\n"
                 R_X(1) R_X(2) R_X(3) R_X(4)
                 "25:\n\tv_max_f32_dpp v56, v56, v56 row_ror:8 row_mask:0xf bank_mask:0xf\n\tv_mov_b32 v59, v56\n\ts_nop 1\n\tv_permlane16_swap_b32 v56, v59\n\tv_max_f32 v56, v56, v59\n\t"
                 "v_mov_b32 v59, v56\n\ts_nop 1\n\tv_permlane32_swap_b32 v56, v59\n\tv_max_f32 v56, v56, v59\n\tv_mov_b32 v57, 0x101\n\tv_add_u32 v59, %[rs8], %[arf]\n\tds_write_b32 v59, v56\n\t"
                 R_F(32, 0, 3, 16, 17, 18, 19, "0") R_F(33, 4, 7, 20, 21, 22, 23, "v[36:39]") R_F(34, 8, 11, 24, 25, 26, 27, "v[36:39]") R_F(35, 12, 15, 28, 29, 30, 31, "v[36:39]")
                 "s_nop 7\n\ts_branch 22b\n"
                 "26:\n\ts_waitcnt vmcnt(0)\n\ts_and_b32 s50, s50, 0xff\n\ts_cmp_lg_u32 s50, 8\n\ts_cbranch_scc1 29f\n\t"
                 "ds_read2_b32 v[0:1], %[arf] offset1:2\n\tds_read2_b32 v[2:3], %[arf] offset0:4 offset1:6\n\tds_read2_b32 v[4:5], %[arf] offset0:8 offset1:10\n\t"
                 "ds_read2_b32 v[6:7], %[arf] offset0:12 offset1:14\n\ts_waitcnt lgkmcnt(0)\n\tv_max3_f32 v56, v0, v1, v2\n\tv_max3_f32 v56, v56, v3, v4\n\t"
                 "v_max3_f32 v56, v56, v5, v6\n\tv_max_f32 v56, v56, v7\n\t"
                 R_S(40, 0) R_S(42, 1) R_S(44, 2) R_S(46, 3) R_S(48, 4) R_S(50, 5) R_S(52, 6) R_S(54, 7) "s_branch 23b\n"
                 "29:\n\ts_mov_b64 exec, -1\n\ts_waitcnt lgkmcnt(0)"
                 : "=&{s[52:53]}"(tr)
                 : "{v[60:63]}"(am), [vo] "v"(lane * 16), [src] "s"(src), [tag] "v"(line_tag(epoch, e)),
                   [awr] "v"(lbase + RED_OFF + (rslot * 2 + (r >> 3)) * 256 + (r & 7) * 32 + (g & 1) * 16), [ard] "v"(lbase + RED_OFF + lane * 4), [arf] "v"(arf),
                   [qf] "v"((unsigned)qv << 16), [oo] "v"(oidx * 2), [out] "s"(KARG(kb, 0)), [ep] "v"(epoch + 1u), [bx] "s"(box), [rid] "s"(rid), [rs8] "s"(rslot * 8),
                   [wm] "s"(0xff00ff0000ff00ffull), [om] "s"(0x3f3f3f3f3f3f3f3full), [lim] "s"(64.f),
                   [cap] "i"(SPIN_CAP), [gcap] "i"(GSPIN_CAP), [x1] "i"(CT_OFF + C_X1), [x1n] "i"(NE), [red] "i"(CT_OFF + C_RED), [rs] "i"(RSLEEP), [eo] "i"(EPOCH_OFF)
                 : "memory", "scc", "vcc", "s40", "s41", "s42", "s43", "s44", "s45", "s46", "s47", "s48", "s49", "s50", "s51", "v0", "v1", "v2", "v3", "v4", "v5", "v6", "v7", "v8", "v9", "v10", "v11", "v12", "v13", "v14", "v15", "v16", "v17", "v18", "v19", "v20", "v21", "v22", "v23", "v24", "v25", "v26", "v27", "v28", "v29", "v30", "v31", "v32", "v33", "v34", "v35", "v36", "v37", "v38", "v39", "v40", "v41", "v42", "v43", "v44", "v45", "v46", "v47", "v48", "v49", "v50", "v51", "v52", "v53", "v54", "v55", "v56", "v57", "v58", "v59");
  }
}

#undef q
#undef f0
#undef f1
#undef wq
#undef wk
#undef wv
#undef wo
#undef box
extern "C" __global__ void dino_end() {}

extern "C" void kernel_launch(void* const* d_in, const int* in_sizes, int n_in,
                              void* d_out, int out_size, void* d_ws, size_t ws_size,
                              hipStream_t stream) {
  dino_main<<<NWG, 768, 0, stream>>>((const unsigned short*)d_in[0], (const char*)d_in[1], (const char*)d_in[2], (const char*)d_in[3],
                                     (const char*)d_in[4], (const char*)d_in[5], (const char*)d_in[6], (char*)d_ws + ((ws_size - (4u << 20)) & ~(size_t)0x1fffff),
                                     (unsigned short*)d_out);
}
```

```cpp
#include <hip/hip_runtime.h>
#include <hip/hip_bf16.h>
#include <stdint.h>

#define PROFOFF (380ll << 20)
#define TNOW(v) do {} while (0)
#define TPUT(id, v) do {} while (0)

using bf16x8  = __attribute__((ext_vector_type(8))) short;
using f32x4   = __attribute__((ext_vector_type(4))) float;
using f32x2   = __attribute__((ext_vector_type(2))) float;
using u32x4   = __attribute__((ext_vector_type(4))) unsigned;
using u32x2   = __attribute__((ext_vector_type(2))) unsigned;
using v4i16_t = __attribute__((ext_vector_type(4))) short;
typedef __attribute__((address_space(3))) char* lds_ptr;
#define LDS_AS(T) __attribute__((address_space(3))) T

constexpr int CC = 192;
constexpr int NWG = 256, NS = 7, NE = 4;
constexpr int TPW = 33;
constexpr int ROWB = 384;
constexpr int SLOT = 40 * ROWB;
constexpr int NSLOT = 10;
constexpr int RSTR = 322;
constexpr int EPOCH_OFF = 64 * RSTR * 128, QK_OFF = EPOCH_OFF + 4096, W2F_OFF = QK_OFF + 12288, W2_OFF = W2F_OFF + 1024;
static_assert(W2_OFF + 72 * 1024 <= (4 << 20), "");
constexpr int GSPIN_CAP = 1 << 16;
#define PACE16 777
#define PACES 810
#define PK1 14
#define PWIN 35
#define AHI 22
#define ADL 60
#define AMG 200
#define T0BACK 7
#define HDUE 93
#define QKW0 200
#define QKW1 3
#define RSLEEP 25
#define W2SCH 0x0311180000000000ull
constexpr int NEARLY = 4;
constexpr int NH1 = 3;
constexpr int NPK = 6;
constexpr float EPS = 1e-5f;
constexpr float SQRT_C = 13.856406460551018f;
constexpr float LOG2E = 1.4426950408889634f;
constexpr float THR = 20.0f;
constexpr float TOFF = 192.0f;
constexpr int SPIN_CAP = 1 << 22;

constexpr int IMG_A = NSLOT * SLOT;
constexpr int CT_OFF = IMG_A + 6144;
constexpr int PKM = CT_OFF + 512, PKU = PKM + NPK * 128;
constexpr int ONES_OFF = PKU + NPK * 256;
constexpr int JUNK = ONES_OFF + ROWB;
constexpr int LDS_BYTES = JUNK + 256;
constexpr int H_R = 192 * ROWB, H_Q = H_R + 32 * ROWB, H_C = H_Q + 6144;
static_assert(LDS_BYTES <= 163840 && (PKU & 15) == 0 && (ONES_OFF & 127) == 0, "lds");
constexpr int C_FULL = 0, C_FREE = 64, C_EP = 128, C_RED = 144, C_ISS = 152, C_X1 = 156, C_QI = 160, C_PFL = 192, C_CONS = 256,
              C_MREF = 320, C_LW = 384;

typedef __bf16 bf16x2_t __attribute__((ext_vector_type(2)));
__device__ __forceinline__ unsigned cvtpk(float lo, float hi) {
  return __builtin_bit_cast(unsigned, __builtin_convertvector(f32x2{lo, hi}, bf16x2_t));
}
__device__ __forceinline__ float bflo(unsigned p) { return __uint_as_float(p << 16); }
__device__ __forceinline__ float bfhi(unsigned p) { return __uint_as_float(p & 0xffff0000u); }
__device__ __forceinline__ bf16x8 pack8(const f32x4& a, const f32x4& b) {
  return __builtin_bit_cast(bf16x8, u32x4{cvtpk(a[0], a[1]), cvtpk(a[2], a[3]), cvtpk(b[0], b[1]), cvtpk(b[2], b[3])});
}
__device__ __forceinline__ float xrow16_max(float x) {
  auto s = __builtin_amdgcn_permlane16_swap(__float_as_uint(x), __float_as_uint(x), false, false);
  x = fmaxf(__uint_as_float(s[0]), __uint_as_float(s[1]));
  auto t = __builtin_amdgcn_permlane32_swap(__float_as_uint(x), __float_as_uint(x), false, false);
  return fmaxf(__uint_as_float(t[0]), __uint_as_float(t[1]));
}
__device__ __forceinline__ float xrow16_sum(float x) {
  auto s = __builtin_amdgcn_permlane16_swap(__float_as_uint(x), __float_as_uint(x), false, false);
  x = __uint_as_float(s[0]) + __uint_as_float(s[1]);
  auto t = __builtin_amdgcn_permlane32_swap(__float_as_uint(x), __float_as_uint(x), false, false);
  return __uint_as_float(t[0]) + __uint_as_float(t[1]);
}
__device__ __forceinline__ v4i16_t tr_read(lds_ptr p) {
  return __builtin_amdgcn_ds_read_tr16_b64_v4i16((LDS_AS(v4i16_t)*)p);
}
__device__ __forceinline__ f32x4 mfma(bf16x8 a, bf16x8 b, f32x4 c) {
  return __builtin_amdgcn_mfma_f32_16x16x32_bf16(a, b, c, 0, 0, 0);
}
__device__ __forceinline__ int swz(int p) { return (p & 6) | ((p >> 2) & 1); }
constexpr unsigned swz_tbl(int i) {
  unsigned t = 0;
  for (int j = 0; j < 8; ++j) { const int p = (8 * i + j) / 3; t |= (unsigned)((p & 6) | ((p >> 2) & 1)) << (4 * j); }
  return t;
}
__device__ __forceinline__ bf16x8 mk8(v4i16_t a, v4i16_t b) { return bf16x8{a[0], a[1], a[2], a[3], b[0], b[1], b[2], b[3]}; }

__device__ __forceinline__ void lds_st32(unsigned addr, unsigned v) {
  asm volatile("ds_write_b32 %0, %1" : : "v"(addr), "v"(v) : "memory");
}
__device__ __forceinline__ unsigned lds_ld32(unsigned addr) {
  unsigned v;
  asm volatile("ds_read_b32 %0, %1\n\ts_waitcnt lgkmcnt(0)" : "=v"(v) : "v"(addr) : "memory");
  return __builtin_amdgcn_readfirstlane(v);
}
__device__ __forceinline__ void lds_add(unsigned addr, unsigned n) {
  asm volatile("s_mov_b64 exec, 1\n\tds_add_u32 %0, %1\n\ts_mov_b64 exec, -1\n\ts_nop 3" : : "v"(addr), "v"(n) : "memory");
}
__device__ __forceinline__ void poll_ge_sleep(unsigned addr, unsigned need) {
  need = __builtin_amdgcn_readfirstlane(need);
  unsigned v, s, n2, cnt = SPIN_CAP;
  asm volatile("s_branch 2f\n1:\n\ts_sleep 2\n2:\n\tds_read_b32 %0, %4\n\ts_sub_u32 %3, %3, 1\n\ts_cselect_b32 %2, 0, %5\n\ts_waitcnt lgkmcnt(0)\n\t"
               "v_readfirstlane_b32 %1, %0\n\ts_cmp_lt_u32 %1, %2\n\ts_cbranch_scc1 1b"
               : "=&v"(v), "=&s"(s), "=&s"(n2), "+s"(cnt) : "v"(addr), "s"(need) : "memory", "scc");
}
__device__ __forceinline__ bf16x8 img_ld(lds_ptr L0, int off, int kk, int lane) { return *(LDS_AS(bf16x8)*)(L0 + off + kk * 1024 + lane * 16); }
__device__ __forceinline__ void img_st(lds_ptr L0, int off, int kk, int lane, bf16x8 v) { *(LDS_AS(bf16x8)*)(L0 + off + kk * 1024 + lane * 16) = v; }

template <int N> __device__ __forceinline__ void wait_vm() { asm volatile("s_waitcnt vmcnt(%0)" :: "i"(N) : "memory"); }
constexpr int A_IMG = ((TPW + 1 - NPK) % NSLOT) * SLOT, RED_OFF = ((TPW - NPK) % NSLOT) * SLOT;
__device__ __forceinline__ unsigned line_tag(unsigned epoch, unsigned i) {
  unsigned x = (epoch + 1u + i * 0x9E3779B9u) * 0x85EBCA6Bu;
  x ^= x >> 15; x *= 0xC2B2AE35u;
  return x ^ (x >> 13);
}
__device__ __forceinline__ unsigned epoch_load(const char* box) {
  unsigned e;
  asm volatile("global_load_dword %0, %1, %2 sc1\n\ts_waitcnt vmcnt(0)" : "=v"(e) : "v"(EPOCH_OFF), "s"(box) : "memory");
  return __builtin_amdgcn_readfirstlane(e);
}
__device__ __forceinline__ int tr_off(int row, int col4) {
  const int ch = col4 >> 1;
  return row * ROWB + (((ch & ~7) | ((ch & 7) ^ swz(row & 15))) << 4) + 8 * (col4 & 1);
}
#define PIN3(a, i) "+v"(a[i]), "+v"(a[i + 1]), "+v"(a[i + 2])

#define DMA1(d, b, off, pol) "global_load_lds_dwordx4 %[" #d "], %[" #b "] offset:" #off pol "\n\t"
__device__ __forceinline__ void issue_rows16(const char* a0, unsigned dst, const unsigned (&d)[3]) {
  unsigned keep;
  asm volatile("s_mov_b32 %[keep], m0\n\ts_mov_b32 m0, %[dst]\n\ts_nop 0\n\t"
               DMA1(d0, a, 0, "") DMA1(d1, a, 1024, "") DMA1(d2, a, 2048, "") DMA1(d0, a, 3072, "")
               "s_add_u32 m0, %[dst], 0x1000\n\ts_nop 0\n\t" DMA1(d1, b, 0, "") DMA1(d2, b, 1024, "") "s_mov_b32 m0, %[keep]"
               : [keep] "=&s"(keep) : [dst] "s"(dst), [a] "s"(a0), [b] "s"(a0 + 4096), [d0] "v"(d[0]), [d1] "v"(d[1]), [d2] "v"(d[2]) : "memory", "scc");
}
#define LD_VM "s_getreg_b32 s43, hwreg(HW_REG_IB_STS, 22, 2)\n\ts_getreg_b32 s42, hwreg(HW_REG_IB_STS, 0, 4)\n\ts_lshl4_add_u32 s42, s43, s42\n\t"
#define LD_GATE(n, X) \
  #n "0:\n\t" LD_VM "s_cmp_le_i32 s42, s46\n\ts_cbranch_scc1 " #n "2f\n" \
  #n "1:\n\t" X "s_cmp_gt_u32 s42, %[win]\n\ts_cbranch_scc1 " #n "5f\n\ts_cmp_ge_u32 s45, s44\n\ts_cbranch_scc1 " #n "7f\n" \
  #n "6:\n\ts_memrealtime s[40:41]\n\ts_sleep 1\n\ts_waitcnt lgkmcnt(0)\n\ts_sub_u32 s45, s40, %[t0]\n\ts_lshl_b32 s45, s45, 4\n\ts_cmp_lt_u32 s45, s44\n\ts_cbranch_scc1 " #n "6b\n" \
  #n "7:\n\t" LD_VM
#define LD_NEXT \
  "s_cmp_gt_u32 s42, %[ahi]\n\ts_cselect_b32 s42, %[amg], 0\n\ts_sub_u32 s43, s45, s44\n\ts_cmp_lt_u32 s43, s42\n\ts_cselect_b32 s42, %[p3] + %[adl], s39\n\t" \
  "s_add_u32 s44, s44, s42\n\ts_add_u32 s46, s46, 5\n\t"
#define LD_PUB0(n, a, b, c) \
  #n "2:\n\tv_add_u32 %[pub], 2, %[pub]\n\ts_cmp_ge_u32 s46, 30\n\ts_cbranch_scc1 " #n "4f\n\ts_cmp_ge_u32 s46, 15\n\ts_cbranch_scc1 " #n "3f\n\t" \
  "s_waitcnt vmcnt(" #a ")\n" #n "3:\n\ts_waitcnt vmcnt(" #b ")\n" #n "4:\n\ts_waitcnt vmcnt(" #c ")\n\tds_write_b32 %[af], %[pub] ; NOTRACK\n\t" \
  "s_sub_u32 s46, s46, 15\n\ts_branch " #n "1b\n"
#define LD_PUB(n, a, b, c) LD_PUB0(n, a, b, c) \
  #n "5:\n\ts_sleep 1\n\ts_sub_i32 s47, s47, 1\n\ts_cmp_gt_i32 s47, 0\n\ts_cbranch_scc1 " #n "0b\n\ts_waitcnt vmcnt(%[win])\n\ts_branch " #n "6b\n"
#define LD1(d, b, off) "global_load_lds_dwordx4 %[" #d "], s[" #b "] offset:" #off " nt\n\t"

#define PV_REQ(P, U, M, C, CA) \
  "v_lshl_add_u32 v150, s44, 3, %[apk]\n\tv_lshl_add_u32 v151, s44, 1, %[apu]\n\tv_add_u32 v152, s44, %[apm]\n\t" \
  "ds_read_b32 v" #C ", v" #CA "\n\tds_read_b128 v[" P "], v150\n\tds_read_b64 v[" M "], v152\n\tds_read_b128 v[" U "], v151\n\t"
#define PV_TR(d0, d1, a, o) "ds_read_b64_tr_b16 v[" #d0 ":" #d1 "], v" #a " offset:" #o "\n\t"
#define PV_FETCH(y0, y0b, a0, a0b, b0, b0b, y1, y1b, a1, a1b, b1, b1b, y2, y2b, a2, a2b, b2, b2b) \
  "v_add_u32 v153, s45, %[ox]\n\tv_add_u32 v154, s45, %[oy]\n\t" \
  PV_TR(a0, a0b, 153, 0) PV_TR(b0, b0b, 153, 6144) PV_TR(a1, a1b, 153, 128) PV_TR(b1, b1b, 153, 6272) PV_TR(a2, a2b, 153, 256) PV_TR(b2, b2b, 153, 6400) \
  PV_TR(y0, y0b, 154, 0) PV_TR(y1, y1b, 154, 128) PV_TR(y2, y2b, 154, 256) \
  "s_add_u32 s45, s45, %[slot]\n\ts_cmp_eq_u32 s45, %[ring]\n\ts_cselect_b32 s45, 0, s45\n\t"
#define PV_FETCH_A PV_FETCH(90, 91, 92, 93, 94, 95, 96, 97, 98, 99, 100, 101, 102, 103, 104, 105, 106, 107)
#define PV_FETCH_B PV_FETCH(108, 109, 110, 111, 112, 113, 114, 115, 116, 117, 118, 119, 120, 121, 122, 123, 124, 125)
#define PV_MF(d, a, p) "v_mfma_f32_16x16x32_bf16 v[" d "], v[" a "], v[" p "], v[" d "]\n\t"
#define PV_MOV(d, s) "v_mov_b32 v" #d ", v" #s "\n\t"
#define PV_WAITF(n, CA)   \
  #n ":\n\tds_read_b32 v155, v" #CA "\n\ts_sub_i32 s47, s47, 1\n\ts_cmp_lt_i32 s47, 0\n\ts_cbranch_scc1 " #n "9f\n\ts_waitcnt lgkmcnt(0)\n\t" \
  "v_cmp_lt_u32 vcc, v155, %[need]\n\ts_and_b64 vcc, vcc, 2\n\ts_cbranch_scc0 " #n "9f\n\ts_sleep 2\n\ts_branch " #n "b\n" #n "9:\n\ts_waitcnt lgkmcnt(0)\n\t"
#define PV_STEP(n, P, U, M, m0, m1, C, CA, P2, U2, M2, C2, CA2, X0, X1, X2, Y0, Y1, Y2, MV0, MV1, MV2, HOOK) \
  "s_waitcnt lgkmcnt(10)\n\t" \
  "v_cmp_lt_u32 vcc, v" #C ", %[need]\n\tv_cmp_neq_f32 s[48:49], v" #m0 ", v88\n\tv_cmp_neq_f32 s[50:51], v" #m1 ", v88\n\t" \
  "v_add_u32 v" #CA2 ", 4, v" #CA "\n\ts_or_b64 s[48:49], s[48:49], vcc\n\tv_cmp_eq_u32 s[52:53], v" #CA2 ", %[lim]\n\t" \
  "s_or_b64 s[48:49], s[48:49], s[50:51]\n\ts_cbranch_scc1 " #n "1f\n" \
  #n "0:\n\ts_add_u32 s44, s44, 128\n\ts_cmp_eq_u32 s44, %[pring]\n\ts_cselect_b32 s44, 0, s44\n\t" \
  "v_cndmask_b32 v" #CA2 ", v" #CA2 ", %[base], s[52:53]\n\t" \
  PV_MF("84:87", U, P) PV_MF("72:75", X0, P) PV_MF("76:79", X1, P) MV0 PV_MF("80:83", X2, P) MV1 \
  PV_MF("72:75", Y0, P) MV2 HOOK PV_REQ(P2, U2, M2, C2, CA2) PV_MF("76:79", Y1, P) PV_MF("80:83", Y2, P)
#define PV_REL(CA) \
  "s_mov_b64 exec, 3\n\tds_add_u32 v" #CA ", %[one] offset:64\n\ts_mov_b64 exec, -1\n\tv_add_u32 %[need], 2, %[need]\n\ts_add_u32 s46, s46, 1\n\t"
#define PV_SC1(p) "v_lshlrev_b32 v151, 16, v" #p "\n\tv_and_b32 v152, 0xffff0000, v" #p "\n\tv_mul_f32 v151, v151, v150\n\tv_mul_f32 v152, v152, v150\n\t" \
                  "v_cvt_pk_bf16_f32 v" #p ", v151, v152\n\t"
#define PV_MUL4(a, b, c, d) "v_mul_f32 v" #a ", v" #a ", v150\n\tv_mul_f32 v" #b ", v" #b ", v150\n\tv_mul_f32 v" #c ", v" #c ", v150\n\tv_mul_f32 v" #d ", v" #d ", v150\n\t"
#define PV_RESC(m0, m1, p0, p1, p2, p3) \
  "v_mov_b32 v150, v88\n\tv_max3_f32 v88, v88, v" #m0 ", v" #m1 "\n\tv_sub_f32 v150, v150, v88\n\tv_exp_f32 v150, v150\n\ts_nop 1\n\t" \
  PV_MUL4(72, 73, 74, 75) PV_MUL4(76, 77, 78, 79) PV_MUL4(80, 81, 82, 83) PV_MUL4(84, 85, 86, 87) \
  "v_sub_f32 v150, v" #m0 ", v88\n\tv_exp_f32 v150, v150\n\ts_nop 1\n\t" PV_SC1(p0) PV_SC1(p1) \
  "v_sub_f32 v150, v" #m1 ", v88\n\tv_exp_f32 v150, v150\n\ts_nop 1\n\t" PV_SC1(p2) PV_SC1(p3)
#define PV_SLOW(n, P, U, M, m0, m1, C, CA, p0, p1, p2, p3) \
  #n "1:\n\ts_bitcmp0_b32 vcc_lo, 0\n\ts_cbranch_scc1 " #n "4f\n\ts_cmp_gt_u32 s46, %[tight]\n\ts_cbranch_scc1 " #n "3f\n" \
  #n "2:\n\ts_sleep 2\n\tds_read_b32 v" #C ", v" #CA "\n\ts_sub_i32 s47, s47, 1\n\ts_cmp_lt_i32 s47, 0\n\ts_cbranch_scc1 " #n "3f\n\ts_waitcnt lgkmcnt(0)\n\t" \
  "v_cmp_lt_u32 vcc, v" #C ", %[need]\n\ts_and_b64 vcc, vcc, 1\n\ts_cbranch_scc1 " #n "2b\n" \
  #n "3:\n\t" PV_REQ(P, U, M, C, CA) "s_sub_i32 s47, s47, 1\n\ts_cmp_lt_i32 s47, 0\n\ts_cselect_b32 s54, 0, 1\n\ts_waitcnt lgkmcnt(0)\n\t" \
  "v_cmp_lt_u32 vcc, v" #C ", %[need]\n\tv_cmp_neq_f32 s[48:49], v" #m0 ", v88\n\tv_cmp_neq_f32 s[50:51], v" #m1 ", v88\n\t" \
  "s_and_b32 s54, s54, vcc_lo\n\ts_cbranch_scc1 " #n "3b\n\ts_or_b64 s[48:49], s[48:49], s[50:51]\n\ts_cbranch_scc0 " #n "0b\n\ts_branch " #n "5f\n" \
  #n "4:\n\tv_cmp_neq_f32 s[48:49], v" #m0 ", v88\n\ts_or_b64 s[48:49], s[48:49], s[50:51]\n\ts_cbranch_scc0 " #n "0b\n" \
  #n "5:\n\t" PV_RESC(m0, m1, p0, p1, p2, p3) \
  "s_branch " #n "0b\n"
#define PV_W2(a0, a1, b0, b1, c0, c1) \
  "global_load_dwordx4 v[" #a0 ":" #a1 "], v150, %[w2] sc1\n\tglobal_load_dwordx4 v[" #b0 ":" #b1 "], v150, %[w2] offset:1024 sc1\n\t" \
  "global_load_dwordx4 v[" #c0 ":" #c1 "], v150, %[w2] offset:2048 sc1\n\tv_add_u32 v150, 0xc00, v150\n\ts_sub_u32 s54, s54, 1\n\ts_cbranch_scc1 30b\n\t"

#define PV_PK(a, b) "v_pk_fma_f32 v[" #a ":" #b "], v[" #a ":" #b "], v[150:151], v[150:151] op_sel:[0,0,1] op_sel_hi:[1,0,1]\n\t"
#define PV_NRM(a, b, c, d, o)   \
  "v_pk_fma_f32 v[" #a ":" #b "], v[" #a ":" #b "], v[150:151], v[150:151] op_sel:[0,0,1] op_sel_hi:[1,0,1]\n\t" \
  "v_pk_fma_f32 v[" #c ":" #d "], v[" #c ":" #d "], v[150:151], v[150:151] op_sel:[0,0,1] op_sel_hi:[1,0,1]\n\t" \
  "v_cvt_pk_bf16_f32 v" #a ", v" #a ", v" #b "\n\tv_cvt_pk_bf16_f32 v" #b ", v" #c ", v" #d "\n\tds_write_b64 v120, v[" #a ":" #b "] offset:" #o "\n\t"
#define PV_G(n, a, w0, w1, w2, c0, c1, c2) "s_waitcnt lgkmcnt(" #n ")\n\tv_mfma_f32_16x16x32_bf16 v[72:75], v[" a "], v[" w0 "], " c0 "\n\t" \
  "v_mfma_f32_16x16x32_bf16 v[76:79], v[" a "], v[" w1 "], " c1 "\n\tv_mfma_f32_16x16x32_bf16 v[80:83], v[" a "], v[" w2 "], " c2 "\n\t"
#define PV_Z4(a, b, c, d) "v_mov_b32 v" #a ", 0\n\tv_mov_b32 v" #b ", 0\n\tv_mov_b32 v" #c ", 0\n\tv_mov_b32 v" #d ", 0\n\t"
#define PV_T(n)

using u32x8 = __attribute__((ext_vector_type(8))) unsigned;
struct SCtx {
  lds_ptr L0;
  unsigned ct, apk, apm, api, amr;
  int o_rb, o_yb, ev;
  u32x8 q[3];
  unsigned tq;
};
__device__ __forceinline__ bf16x8 s_qf(const SCtx& c, int j) {
  const u32x8& v = c.q[j >> 1];
  const int o = 4 * (j & 1);
  return __builtin_bit_cast(bf16x8, u32x4{v[o], v[o + 1], v[o + 2], v[o + 3]});
}
#define S_M(d, a, b) "v_mfma_f32_16x16x32_bf16 v[" d "], v[" a "], v[" b "], v[" d "]\n\t"
#define S_M0(d, a, b) "v_mfma_f32_16x16x32_bf16 v[" d "], v[" a "], v[" b "], 0\n\t"
#define S_R(d, a, o) "ds_read_b128 v[" d "], v" #a " offset:" #o "\n\t"
#define S_W(n) "s_waitcnt lgkmcnt(" #n ")\n\t"
#define S_D " row_mask:0xf bank_mask:0xf\n\t"
#define S_3(x, y) S_M("100:103", x, y) S_M("104:107", x, x) S_M("96:99", y, y)
#define S_SQ(g, h) "v_fmac_f32 v" #g ", 2.0, v" #h "\n\t"
#define S_SQD(g, h) "v_fmac_f32_dpp v" #g ", v" #h ", %[two] quad_perm:[0,0,2,2]" S_D
#define S_U(u, h, y) "v_add_f32_dpp v" #u ", v" #h ", v" #y " quad_perm:[1,1,3,3]" S_D
#define S_C(d, a, b, m) "v_cndmask_b32 v" #d ", v" #a ", v" #b ", %[" #m "]\n\t"
#define S_B(d, n) "v_mov_b32_dpp v" #d ", v157 row_newbcast:" #n S_D
#define S_BASE(d, u, t) "v_pk_fma_f32 v[" d "], v[" u "], v[116:117], v[" t "] op_sel:[0,1,0] op_sel_hi:[1,1,0]\n\t"
#define S_LG(d, r) "v_pk_fma_f32 v[" d "], v[" d "], v[" r "], %[toff] op_sel_hi:[1,1,0]\n\t"
#define S_SUB(p, d) "v_pk_add_f32 v[" p "], v[" d "], v[150:151] op_sel_hi:[1,0] neg_lo:[0,1] neg_hi:[0,1]\n\t"
#define S_E(p) "v_exp_f32 v" #p ", v" #p "\n\t"
#define S_MUL(p, r) "v_pk_mul_f32 v[" p "], v[" p "], v[" r "]\n\t"
#define S_POS   \
  "s_mul_i32 s50, s42, 26\n\ts_lshr_b32 s50, s50, 8\n\ts_mul_i32 s50, s50, -10\n\ts_add_u32 s50, s50, s42\n\ts_mul_i32 s45, s50, %[slot]\n\ts_mov_b32 s43, s45\n\t"
#define S_POLL(n, a, o, need, back, far) \
  #n ":\n\tds_read_b32 v152, v" #a " offset:" #o "\n\ts_sub_i32 s47, s47, 1\n\ts_cmp_lt_i32 s47, 0\n\ts_waitcnt lgkmcnt(0)\n\ts_cbranch_scc1 " back "\n\t" \
  "v_readfirstlane_b32 s40, v152\n\ts_cmp_ge_u32 s40, " need "\n\ts_cbranch_scc1 " back "\n\ts_sleep 2\n\t" \
  "s_add_u32 s40, s40, " far "\n\ts_cmp_ge_u32 s40, " need "\n\ts_cbranch_scc1 " #n "b\n\ts_sleep 12\n\ts_branch " #n "b\n"
__device__ __forceinline__ void s_items(SCtx& c, int k0, int late) {
  unsigned long long tq;
  asm volatile("s_mov_b32 s47, %[cap]\n\ts_mov_b32 s49, 0\n\ts_mov_b32 s42, %[k0]\n\ts_cmp_lg_u32 %[late], 0\n\ts_cbranch_scc1 7f\n"
               "1:\n\ts_cmp_ge_u32 s42, %[end]\n\ts_cbranch_scc1 9f\n\t"
               S_POS
               "s_mul_i32 s48, s42, 43\n\ts_lshr_b32 s48, s48, 8\n\ts_mul_i32 s46, s48, -6\n\ts_add_u32 s46, s46, s42\n\ts_lshl2_add_u32 s48, s48, %[ns]\n\t"
               "v_add_u32 v144, s45, %[orb]\n\tv_mad_u32_u24 v145, s43, %[ev], %[oyb]\n\ts_lshl2_add_u32 s40, s46, %[pfl]\n\ts_lshl2_add_u32 s41, s50, %[full]\n\t"
               "v_xor_b32 v146, 64, v144\n\tv_xor_b32 v147, 64, v145\n\tv_mov_b32 v148, s40\n\tv_mov_b32 v149, s41\n\t"
               "s_lshl1_add_u32 s44, s42, 2\n\ts_lshl_b32 s46, s46, 7\n\ts_mov_b64 vcc, %[dg]\n\ts_cmp_lt_u32 s49, s44\n\ts_cbranch_scc1 5f\n"
               "2:\n\tds_read_b32 v150, %[amr]\n\tds_read_b32 v151, v148 offset:64\n\t"
               S_R("24:27", 145, 0) S_R("48:51", 144, 0) S_R("28:31", 147, 0) S_R("52:55", 146, 0) S_R("32:35", 145, 128) S_R("56:59", 144, 128)
               S_R("36:39", 147, 128) S_R("60:63", 146, 128) S_R("40:43", 145, 256) S_R("64:67", 144, 256) S_R("44:47", 147, 256) S_R("68:71", 146, 256)
               S_W(10) S_M0("100:103", "48:51", "24:27") S_M0("104:107", "48:51", "48:51") S_R("72:75", 144, 6144) S_R("76:79", 146, 6144) S_M0("96:99", "24:27", "24:27")
               S_W(10) S_M("100:103", "52:55", "28:31") S_M("104:107", "52:55", "52:55") S_R("80:83", 144, 6272) S_R("84:87", 146, 6272) S_M("96:99", "28:31", "28:31")
               S_W(10) S_M("100:103", "56:59", "32:35") S_M("104:107", "56:59", "56:59") S_R("88:91", 144, 6400) S_R("92:95", 146, 6400) S_M("96:99", "32:35", "32:35")
               "s_mov_b64 exec, 1\n\tds_add_u32 v149, %[four] offset:64\n\ts_mov_b64 exec, -1\n\tv_readfirstlane_b32 s51, v151\n\ts_setprio 1\n\ts_nop 0\n\t"
               S_W(11) S_3("60:63", "36:39") S_W(9) S_3("64:67", "40:43") S_W(7) S_3("68:71", "44:47")
               S_W(6) S_M0("108:111", "72:75", "24:27") S_M0("112:115", "72:75", "72:75")
               S_W(5) S_M("108:111", "76:79", "28:31") S_M("112:115", "76:79", "76:79")
               S_W(4) S_M("108:111", "80:83", "32:35")
               "v_mov_b32_dpp v152, v96 quad_perm:[1,1,3,3]" S_D "v_mov_b32_dpp v153, v98 quad_perm:[1,1,3,3]" S_D
               S_M("112:115", "80:83", "80:83") S_C(154, 96, 98, r2) S_SQ(104, 100)
               S_W(3) S_M("108:111", "84:87", "36:39") S_SQD(105, 101) S_SQ(106, 102)
               S_M("112:115", "84:87", "84:87") S_SQD(107, 103) "v_add_f32 v154, %[eps], v154\n\t"
               S_W(2) S_M("108:111", "88:91", "40:43") S_U(128, 100, 152) S_U(129, 101, 152)
               S_M("112:115", "88:91", "88:91") S_U(130, 102, 153) S_U(131, 103, 153)
               S_W(1) S_M("108:111", "92:95", "44:47") S_C(104, 104, 105, r1) S_C(106, 106, 107, r1)
               S_M("112:115", "92:95", "92:95") S_C(155, 128, 129, r1) S_C(156, 130, 131, r1)
               "s_cmp_lt_u32 s42, %[early]\n\ts_cbranch_scc1 7f\n"
               "3:\n\t" S_M0("116:119", "24:27", "0:3") S_C(104, 104, 106, r2) S_C(155, 155, 156, r2)
               S_M("116:119", "28:31", "4:7") "v_add_f32_dpp v104, v154, v104 quad_perm:[0,0,2,2]" S_D S_U(132, 108, 152)
               S_M("116:119", "32:35", "8:11") S_U(133, 109, 152) S_U(134, 110, 153)
               S_M("116:119", "36:39", "12:15") S_U(135, 111, 153) S_SQ(112, 108)
               S_M("116:119", "40:43", "16:19") S_SQD(113, 109) S_SQ(114, 110)
               S_M("116:119", "44:47", "20:23") S_SQD(115, 111) S_C(156, 132, 133, r1) S_C(157, 134, 135, r1)
               S_C(112, 112, 113, r1) S_C(114, 114, 115, r1) S_C(156, 156, 157, r2) S_C(112, 112, 114, r2)
               "v_add_f32_dpp v112, v154, v112 quad_perm:[0,0,2,2]" S_D
               "v_mul_f32 v117, %[nic], v117\n\tv_cndmask_b32_dpp v155, v156, v155, vcc row_ror:4" S_D
               S_BASE("120:121", "128:129", "116:117") S_BASE("122:123", "130:131", "118:119")
               "v_cndmask_b32_dpp v104, v112, v104, vcc row_ror:4" S_D "v_mul_f32 v156, %[nic], v155\n\t"
               S_M("120:123", "48:51", "0:3") S_BASE("124:125", "132:133", "116:117") S_BASE("126:127", "134:135", "118:119")
               S_M("120:123", "52:55", "4:7") "v_fmac_f32 v104, v156, v155\n\tv_max_f32 v104, %[eps], v104\n\t"
               S_M("120:123", "56:59", "8:11") "v_rsq_f32 v157, v104\n\t"
               S_M("120:123", "60:63", "12:15") "v_mul_f32 v104, v104, v157\n\tv_mov_b32_dpp v157, v157 row_ror:12 row_mask:0x2 bank_mask:0xf\n\t"
               S_M("120:123", "64:67", "16:19") "v_cvt_pk_bf16_f32 v105, v155, v104\n\tv_mov_b32_dpp v157, v157 row_ror:8 row_mask:0x4 bank_mask:0xf\n\t"
               S_M("120:123", "68:71", "20:23") "v_lshlrev_b32 v106, 16, v105\n\tv_mov_b32_dpp v157, v157 row_ror:4 row_mask:0x8 bank_mask:0xf\n\t"
               S_M("124:127", "72:75", "0:3") "v_and_b32 v107, 0xffff0000, v105\n\t" S_B(136, 0)
               S_M("124:127", "76:79", "4:7") S_B(137, 1) S_B(138, 2)
               S_M("124:127", "80:83", "8:11") S_B(139, 3) S_LG("120:121", "136:137")
               S_M("124:127", "84:87", "12:15") S_LG("122:123", "138:139") S_SUB("128:129", "120:121")
               S_M("124:127", "88:91", "16:19") S_SUB("130:131", "122:123") S_E(128)
               S_M("124:127", "92:95", "20:23") S_E(129) S_E(130) S_E(131)
               S_B(140, 4) S_B(141, 5) S_B(142, 6) S_B(143, 7) "v_sub_f32 v106, v155, v106\n\t"
               S_LG("124:125", "140:141") S_LG("126:127", "142:143") "v_sub_f32 v107, v104, v107\n\tv_max3_f32 v152, v120, v121, v122\n\t"
               S_SUB("132:133", "124:125") S_SUB("134:135", "126:127") "v_max3_f32 v153, v123, v124, v125\n\t"
               S_E(132) S_E(133) S_E(134) S_E(135)
               "v_max3_f32 v152, v152, v126, v127\n\tv_add_f32 v154, %[thr], v150\n\tv_max_f32 v152, v152, v153\n\tv_cvt_pk_bf16_f32 v106, v106, v107\n\t"
               "v_lshl_add_u32 v144, s46, 3, %[apk]\n\tv_cmp_gt_f32 vcc, v152, v154\n\tv_add_u32 v145, s46, %[apm]\n\tv_lshl_add_u32 v146, s46, 1, %[api]\n\t"
               "s_cbranch_vccnz 8f\n"
               "4:\n\t" S_MUL("128:129", "136:137") S_MUL("130:131", "138:139") S_MUL("132:133", "140:141") S_MUL("134:135", "142:143")
               "v_cvt_pk_bf16_f32 v128, v128, v129\n\tv_cvt_pk_bf16_f32 v129, v130, v131\n\tv_cvt_pk_bf16_f32 v130, v132, v133\n\tv_cvt_pk_bf16_f32 v131, v134, v135\n\t"
               "s_cmp_lt_u32 s51, s48\n\ts_cbranch_scc1 6f\n"
               "10:\n\tds_read_b32 v151, v149\n\tds_write_b128 v144, v[128:131]\n\tds_write2_b32 v145, v150, v150 offset0:0 offset1:1\n\ts_mov_b64 exec, %[dg2]\n\t"
               "ds_write_b16 v146, v105\n\tds_write_b16 v146, v106 offset:16\n\tds_write_b16_d16_hi v146, v105 offset:32\n\tds_write_b16_d16_hi v146, v106 offset:48\n\t"
               "s_mov_b64 exec, 1\n\tds_add_u32 v148, %[inc]\n\ts_mov_b64 exec, -1\n\ts_setprio 0\n\ts_add_u32 s42, s42, %[ns]\n\t"
               S_W(7) "v_readfirstlane_b32 s49, v151\n\ts_branch 1b\n"
               S_POLL(5, 149, 0, "s44", "2b", "2")
               S_POLL(6, 148, 64, "s48", "10b", "64")
               "7:\n\tv_mov_b32 v144, %[qi]\n\tv_mov_b32 v145, v152\n\tv_mov_b32 v146, 1\n"
               S_POLL(70, 144, 0, "%[nq]", "71f", "64")
               "71:\n\tv_mov_b32 v152, v145\n\tds_read_b128 v[0:3], %[apk]\n\tds_read_b128 v[4:7], %[apk] offset:1024\n\tds_read_b128 v[8:11], %[apk] offset:2048\n\t"
               "ds_read_b128 v[12:15], %[apk] offset:3072\n\tds_read_b128 v[16:19], %[apk] offset:4096\n\tds_read_b128 v[20:23], %[apk] offset:5120\n\t"
               "v_subrev_u32 v144, %[mc], %[amr]\n\tds_read_b32 v150, %[amr]\n\ts_mov_b64 exec, 63\n\tds_add_u32 v144, v146\n\ts_mov_b64 exec, -1\n\t"
               "s_cmp_lg_u32 %[late], 0\n\ts_waitcnt lgkmcnt(0)\n\ts_nop 1\n\ts_cbranch_scc1 1b\n\ts_branch 3b\n"
               "8:\n\tv_mov_b32 v153, v152\n\ts_nop 1\n\tv_permlane16_swap_b32 v152, v153\n\tv_max_f32 v152, v152, v153\n\tv_mov_b32 v153, v152\n\ts_nop 1\n\t"
               "v_permlane32_swap_b32 v152, v153\n\tv_max_f32 v152, v152, v153\n\tv_mul_f32 v152, 0x3d000000, v152\n\tv_rndne_f32 v152, v152\n\t"
               "v_mul_f32 v152, 0x42000000, v152\n\tv_max_f32 v150, v150, v152\n\tds_max_f32 %[amr], v150\n\t"
               S_SUB("128:129", "120:121") S_SUB("130:131", "122:123") S_SUB("132:133", "124:125") S_SUB("134:135", "126:127")
               S_E(128) S_E(129) S_E(130) S_E(131) S_E(132) S_E(133) S_E(134) S_E(135) "s_branch 4b\n"
               "9:"
               : "=&{v[0:7]}"(c.q[0]), "=&{v[8:15]}"(c.q[1]), "=&{v[16:23]}"(c.q[2]), "=&{s[52:53]}"(tq)
               : [orb] "v"(c.o_rb), [oyb] "v"(c.o_yb), [ev] "v"(c.ev), [apk] "v"(c.apk), [apm] "v"(c.apm), [api] "v"(c.api), [amr] "v"(c.amr),
                 [two] "v"(2.f), [four] "v"(4u), [inc] "v"(2u * NPK), [k0] "s"(k0), [late] "s"(__builtin_amdgcn_readfirstlane(late)), [toff] "s"(f32x2{TOFF, TOFF}),
                 [r1] "s"(0xAAAAAAAAAAAAAAAAull), [r2] "s"(0xCCCCCCCCCCCCCCCCull), [dg] "s"(0xF0000F0000F0000Full), [dg2] "s"(0xF00FFF000FF000FFull),
                 [cap] "i"(SPIN_CAP), [end] "i"(TPW - NH1), [ns] "i"(NS), [pfl] "i"(CT_OFF + C_PFL), [full] "i"(CT_OFF + C_FULL), [early] "i"(NEARLY),
                 [qi] "i"(CT_OFF + C_QI), [nq] "i"(NS - NEARLY), [mc] "i"(C_MREF - C_CONS), [slot] "i"(SLOT),
                 [thr] "i"(__builtin_bit_cast(unsigned, THR)), [eps] "i"(__builtin_bit_cast(unsigned, EPS * CC)), [nic] "i"(__builtin_bit_cast(unsigned, -1.f / CC))
               : "memory", "scc", "vcc", "s40", "s41", "s42", "s43", "s44", "s45", "s46", "s47", "s48", "s49", "s50", "s51",
                 "v24", "v25", "v26", "v27", "v28", "v29", "v30", "v31", "v32", "v33", "v34", "v35", "v36", "v37", "v38", "v39", "v40", "v41", "v42", "v43", "v44", "v45", "v46", "v47", "v48", "v49", "v50", "v51", "v52", "v53", "v54", "v55", "v56", "v57", "v58", "v59", "v60", "v61", "v62", "v63", "v64", "v65", "v66", "v67", "v68", "v69", "v70", "v71", "v72", "v73", "v74", "v75", "v76", "v77", "v78", "v79", "v80", "v81", "v82", "v83", "v84", "v85", "v86", "v87", "v88", "v89", "v90", "v91", "v92", "v93", "v94", "v95", "v96", "v97", "v98", "v99", "v100", "v101", "v102", "v103", "v104", "v105", "v106", "v107", "v108", "v109", "v110", "v111", "v112", "v113", "v114", "v115", "v116", "v117", "v118", "v119", "v120", "v121", "v122", "v123", "v124", "v125", "v126", "v127", "v128", "v129", "v130", "v131", "v132", "v133", "v134", "v135", "v136", "v137", "v138", "v139", "v140", "v141", "v142", "v143", "v144", "v145", "v146", "v147", "v148", "v149", "v150", "v151", "v152", "v153", "v154", "v155", "v156", "v157");
  c.tq = (unsigned)tq;
}

__device__ __forceinline__ void s_half(SCtx& c, int k, int h) {
  const int s = k % NSLOT, e = k % NPK;
  const int sb_ = s * SLOT;
  int yba = c.ev * sb_ + c.o_yb;
  float hc = 1.f - 0.5f * c.ev;
  unsigned xa = (unsigned)(size_t)c.L0 + sb_ + h * 6144 + c.o_rb, xb = xa ^ 64, ai = c.api + e * 256 + h * 8;
  unsigned ap = c.apk + e * 1024 + h * 8, am = c.apm + e * 128 + h * 4, af = c.ct + C_PFL + e * 4, inc = NPK;
  float two = 2.f;
  f32x4 G = {EPS * CC, EPS * CC, EPS * CC, EPS * CC};
  poll_ge_sleep(c.ct + C_CONS + e * 4, (unsigned)(NE * (k / NPK) + NS));
  asm volatile("" : "+v"(yba), "+v"(hc), "+v"(xa), "+v"(xb), "+v"(ai), "+v"(ap), "+v"(am), "+v"(af), "+v"(inc), "+v"(two), "+{v[104:107]}"(G));
  poll_ge_sleep(c.ct + C_FULL, (unsigned)(2 * k + 1));
  __builtin_amdgcn_s_setprio(1);
  bf16x8 yb[6];
#pragma unroll
  for (int j = 0; j < 6; ++j) yb[j] = *(LDS_AS(bf16x8)*)(c.L0 + ((j & 1) ? (yba ^ 64) : yba) + (j >> 1) * 128);
  f32x4 Hx = {0.f, 0.f, 0.f, 0.f}, S = Hx;
#pragma unroll
  for (int j = 0; j < 6; ++j) { Hx = mfma(yb[j], yb[j], Hx); S = mfma(yb[j], s_qf(c, j), S); }
  const float nsq = S[1] * (-1.f / CC);
  Hx = f32x4{Hx[0] * hc, Hx[0] * hc, Hx[2] * hc, Hx[2] * hc};
  S = f32x4{S[0], S[0], S[2], S[2]};
  float mref, mt;
  f32x4 rs;
  unsigned long long raise;
#define SH_M(d, a, b) "v_mfma_f32_16x16x32_bf16 v[" d "], v[" a "], " b ", v[" d "]\n\t"
#define SH_X(n, a, y) "s_waitcnt lgkmcnt(" #n ")\n\t" SH_M("100:103", a, "%[" #y "]") SH_M("104:107", a, "v[" a "]")
#define SH_D " row_mask:0xf bank_mask:0xf\n\t"
  __builtin_amdgcn_s_setprio(0);
  if (k < TPW - 2) poll_ge_sleep(c.ct + C_FULL, (unsigned)(2 * k + 2));
  asm volatile("s_mov_b32 s42, %[cap]\n"
               "1:\n\tds_read_b32 v136, %[afl]\n\tds_read_b128 v[112:115], %[xa]\n\tds_read_b128 v[116:119], %[xb]\n\tds_read_b128 v[120:123], %[xa] offset:128\n\t"
               "ds_read_b128 v[124:127], %[xb] offset:128\n\tds_read_b128 v[128:131], %[xa] offset:256\n\tds_read_b128 v[132:135], %[xb] offset:256\n\t"
               "ds_read_b32 %[mr], %[amr]\n\ts_sub_u32 s42, s42, 1\n\ts_cselect_b32 s41, 0, %[need]\n\ts_waitcnt lgkmcnt(7)\n\t"
               "v_readfirstlane_b32 s40, v136\n\ts_cmp_lt_u32 s40, s41\n\ts_cbranch_scc1 1b\n\ts_setprio 3\n\t"
               SH_X(6, "112:115", y0) SH_X(5, "116:119", y1) SH_X(4, "120:123", y2) SH_X(3, "124:127", y3) SH_X(2, "128:131", y4) SH_X(1, "132:135", y5)
               SH_M("108:111", "112:115", "%[q0]") SH_M("108:111", "116:119", "%[q1]")
               "s_waitcnt lgkmcnt(0)\n\tv_add_f32 v137, %[thr], %[mr]\n\tds_write_b32 %[am], %[mr]\n\ts_nop 1\n\t"
               "v_mov_b32_dpp v140, v100 quad_perm:[1,1,3,3]" SH_D "v_mov_b32_dpp v141, v101 quad_perm:[1,1,3,3]" SH_D
               "v_mov_b32_dpp v142, v102 quad_perm:[1,1,3,3]" SH_D "v_mov_b32_dpp v143, v103 quad_perm:[1,1,3,3]" SH_D
               SH_M("108:111", "120:123", "%[q2]")
               "v_fmac_f32 v104, 2.0, v100\n\tv_fmac_f32_dpp v105, v101, %[two] quad_perm:[0,0,2,2]" SH_D
               "v_fmac_f32 v106, 2.0, v102\n\tv_fmac_f32_dpp v107, v103, %[two] quad_perm:[0,0,2,2]" SH_D
               "v_cndmask_b32 v138, v140, v141, %[r1]\n\tv_cndmask_b32 v139, v142, v143, %[r1]\n\t"
               SH_M("108:111", "124:127", "%[q3]")
               "v_cndmask_b32 v104, v104, v105, %[r1]\n\tv_cndmask_b32 v106, v106, v107, %[r1]\n\tv_cndmask_b32 v138, v138, v139, %[r2]\n\t"
               "v_cndmask_b32 v104, v104, v106, %[r2]\n\tv_mul_f32 v139, %[nic], v138\n\t"
               SH_M("108:111", "128:131", "%[q4]")
               "v_fmac_f32 v104, v139, v138\n\tv_max_f32 v104, %[eps], v104\n\tv_rsq_f32 v105, v104\n\t"
               SH_M("108:111", "132:135", "%[q5]")
               "v_mul_f32 v104, v104, v105\n\t"
               "v_mov_b32_dpp v105, v105 row_ror:12 row_mask:0x2 bank_mask:0xf\n\tv_cvt_pk_bf16_f32 v106, v138, v104\n\ts_nop 0\n\t"
               "v_mov_b32_dpp v105, v105 row_ror:8 row_mask:0x4 bank_mask:0xf\n\tv_lshlrev_b32 v107, 16, v106\n\tv_and_b32 v139, 0xffff0000, v106\n\t"
               "v_mov_b32_dpp v105, v105 row_ror:4 row_mask:0x8 bank_mask:0xf\n\t"
               "v_pk_fma_f32 v[108:109], v[140:141], %[nsq], v[108:109]\n\tv_pk_fma_f32 v[110:111], v[142:143], %[nsq], v[110:111]\n\t"
               "v_mov_b32_dpp v100, v105 row_newbcast:0" SH_D "v_mov_b32_dpp v101, v105 row_newbcast:1" SH_D
               "v_mov_b32_dpp v102, v105 row_newbcast:2" SH_D "v_mov_b32_dpp v103, v105 row_newbcast:3" SH_D
               S_LG("108:109", "100:101") S_LG("110:111", "102:103") S_SUB("140:141", "108:109") S_SUB("142:143", "110:111")
               "v_exp_f32 v140, v140\n\tv_exp_f32 v141, v141\n\tv_exp_f32 v142, v142\n\tv_exp_f32 v143, v143\n\t"
               "v_max3_f32 %[mt], v108, v109, v110\n\tv_sub_f32 v138, v138, v107\n\tv_sub_f32 v104, v104, v139\n\tv_max_f32 %[mt], %[mt], v111\n\t"
               "v_pk_mul_f32 v[140:141], v[140:141], v[100:101]\n\tv_pk_mul_f32 v[142:143], v[142:143], v[102:103]\n\t"
               "v_cmp_gt_f32 %[ra], %[mt], v137\n\tv_cvt_pk_bf16_f32 v107, v138, v104\n\t"
               "v_cvt_pk_bf16_f32 v140, v140, v141\n\tv_cvt_pk_bf16_f32 v141, v142, v143\n\t"
               "ds_write_b64 %[ap], v[140:141]\n\ts_mov_b64 exec, %[dg]\n\t"
               "ds_write_b16 %[ai], v106\n\tds_write_b16 %[ai], v107 offset:16\n\tds_write_b16_d16_hi %[ai], v106 offset:32\n\tds_write_b16_d16_hi %[ai], v107 offset:48\n\t"
               "s_cmp_eq_u64 %[ra], 0\n\ts_cselect_b64 exec, 1, 0\n\tds_add_u32 %[af], %[inc]\n\ts_mov_b64 exec, -1\n\ts_setprio 0"
               : "+{v[100:103]}"(Hx), "+{v[104:107]}"(G), "+{v[108:111]}"(S), [mr] "=&{v150}"(mref), [mt] "=&v"(mt), [ra] "=&s"(raise)
               : [y0] "v"(yb[0]), [y1] "v"(yb[1]), [y2] "v"(yb[2]), [y3] "v"(yb[3]), [y4] "v"(yb[4]), [y5] "v"(yb[5]),
                 [q0] "v"(s_qf(c, 0)), [q1] "v"(s_qf(c, 1)), [q2] "v"(s_qf(c, 2)), [q3] "v"(s_qf(c, 3)), [q4] "v"(s_qf(c, 4)), [q5] "v"(s_qf(c, 5)),
                 [afl] "v"(c.ct + C_FULL), [xa] "v"(xa), [xb] "v"(xb), [amr] "v"(c.amr), [am] "v"(am), [ap] "v"(ap), [ai] "v"(ai),
                 [af] "v"(af), [inc] "v"(inc), [nsq] "v"(f32x2{nsq, nsq}), [two] "v"(two),
                 [need] "s"(__builtin_amdgcn_readfirstlane(2 * k + 2)), [r1] "s"(0xAAAAAAAAAAAAAAAAull), [r2] "s"(0xCCCCCCCCCCCCCCCCull),
                 [dg] "s"(0xF0000F0000F0000Full), [cap] "i"(SPIN_CAP), [thr] "i"(__builtin_bit_cast(unsigned, THR)), [toff] "s"(f32x2{TOFF, TOFF}),
                 [eps] "i"(__builtin_bit_cast(unsigned, EPS * CC)), [nic] "i"(__builtin_bit_cast(unsigned, -1.f / CC))
               : "memory", "scc", "s40", "s41", "s42", "v112", "v113", "v114", "v115", "v116", "v117", "v118", "v119", "v120", "v121", "v122", "v123", "v124", "v125",
                 "v126", "v127", "v128", "v129", "v130", "v131", "v132", "v133", "v134", "v135", "v136", "v137", "v138", "v139", "v140", "v141", "v142", "v143");
  if (__builtin_expect(raise != 0ull, 0)) {
    rs = Hx;
    mref = fmaxf(mref, 32.f * __builtin_rintf(xrow16_max(mt) * (1.f / 32.f)));
    float pp[4];
#pragma unroll
    for (int i = 0; i < 4; ++i) pp[i] = __builtin_amdgcn_exp2f(S[i] - mref) * rs[i];
    asm volatile("ds_max_f32 %6, %3\n\tds_write_b32 %2, %3\n\tds_write_b64 %0, %1\n\ts_mov_b64 exec, 1\n\tds_add_u32 %4, %5\n\ts_mov_b64 exec, -1"
                 : : "v"(ap), "v"(u32x2{cvtpk(pp[0], pp[1]), cvtpk(pp[2], pp[3])}), "v"(am), "v"(mref), "v"(af), "v"((unsigned)NPK), "v"(c.amr) : "memory");
  }
}

using u32x16 = __attribute__((ext_vector_type(16))) unsigned;
typedef __attribute__((address_space(1))) unsigned short gus;
extern "C" __global__ __launch_bounds__(768, 3) void dino_main(const void*, const void*, const void*, const void*, const void*,
                                                               const void*, const void*, void*, void*) {
  u32x16 ka; u32x2 kb; unsigned long long tent64;
  __shared__ __attribute__((aligned(16))) char lds[LDS_BYTES];
  const int wave = __builtin_amdgcn_readfirstlane((int)threadIdx.x >> 6);
  const bool helper = blockIdx.x < 8;
  lds_ptr L0 = (lds_ptr)lds;
  const unsigned lbase = (unsigned)(size_t)L0;
  const unsigned ct = lbase + CT_OFF;
#define KARG(v, i) ((char*)(((unsigned long long)v[2 * (i) + 1] << 32) | v[2 * (i)]))
#define q KARG(ka, 0)
#define f0 KARG(ka, 1)
#define f1 KARG(ka, 2)
#define wq KARG(ka, 3)
#define wk KARG(ka, 4)
#define wv KARG(ka, 5)
#define wo KARG(ka, 6)
#define box KARG(ka, 7)
#define tent ((unsigned)tent64)
#define LANE_VARS int lane; asm volatile("v_mbcnt_lo_u32_b32 %0, -1, 0\n\tv_mbcnt_hi_u32_b32 %0, -1, %0" : "=v"(lane)); \
  const int r = lane & 15, g = lane >> 4; (void)r; (void)g;
#define DOFF_VARS unsigned doff[3];   \
  _Pragma("unroll") for (int i = 0; i < 3; ++i) doff[i] = (unsigned)(lane ^ ((swz_tbl(i) >> ((lane >> 3) * 4)) & 7)) << 4;
#define BARRIER() do { asm volatile("s_waitcnt lgkmcnt(0)" ::: "memory"); __builtin_amdgcn_s_barrier(); asm volatile("" ::: "memory"); } while (0)

  {
    unsigned doff[3];
    static_assert(swz_tbl(0) == swz_tbl(3) && swz_tbl(1) == swz_tbl(4) && swz_tbl(2) == swz_tbl(5), "");
    unsigned l16, db;
#define PD(d, b, off, pol) "global_load_lds_dwordx4 %[" #d "], s[" #b "] offset:" #off pol "\n\t"
    asm volatile("s_load_dwordx16 s[64:79], %[kp], 0x0\n\ts_load_dwordx2 s[80:81], %[kp], 0x40\n\ts_memrealtime s[82:83]\n\t"
                 "v_mbcnt_lo_u32_b32 %[l16], -1, 0\n\tv_mbcnt_hi_u32_b32 %[l16], -1, %[l16]\n\t"
                 "s_lshr_b32 s84, %[b], 3\n\ts_and_b32 s85, %[b], 7\n\t"
                 "v_lshrrev_b32 %[db], 1, %[l16]\n\tv_and_b32 %[db], -4, %[db]\n\t"
                 "s_mov_b32 s86, %[t0]\n\ts_mov_b32 s87, %[t1]\n\ts_mov_b32 s88, %[t2]\n\t"
                 "v_bfe_u32 %[d0], s86, %[db], 3\n\tv_bfe_u32 %[d1], s87, %[db], 3\n\tv_bfe_u32 %[d2], s88, %[db], 3\n\t"
                 "v_xor_b32 %[d0], %[d0], %[l16]\n\tv_xor_b32 %[d1], %[d1], %[l16]\n\tv_xor_b32 %[d2], %[d2], %[l16]\n\t"
                 "v_lshlrev_b32 %[d0], 4, %[d0]\n\tv_lshlrev_b32 %[d1], 4, %[d1]\n\tv_lshlrev_b32 %[d2], 4, %[d2]\n\t"
                 "v_lshlrev_b32 %[l16], 4, %[l16]\n\t"
                 "s_cmp_eq_u32 s84, 0\n\ts_cbranch_scc1 4f\n\t"
                 "s_cmp_lg_u32 %[w], 0\n\ts_cbranch_scc1 7f\n\t"
                 "s_setprio 3\n\tv_lshlrev_b32 %[db], 3, %[l16]\n\ts_mov_b32 m0, %[junk]\n\ts_lshl_b32 s89, s84, 13\n\t"
                 "s_cmp_gt_u32 s84, 3\n\ts_cbranch_scc1 2f\n\t"
                 "s_getpc_b64 s[90:91]\n1:\n\ts_sub_u32 s86, s89, 8192\n\ts_min_u32 s86, s86, dino_end-1b-8192\n\t"
                 "s_add_u32 s90, s90, s86\n\ts_addc_u32 s91, s91, 0\n\tglobal_load_lds_dword %[db], s[90:91]\n"
                 "2:\n\ts_mul_i32 s86, s85, 98304\n\ts_mul_i32 s87, s84, 3072\n\ts_sub_u32 s87, s87, 3072\n\t"
                 "s_add_u32 s88, s86, s87\n\ts_lshl1_add_u32 s86, s86, s87\n\ts_lshl_b32 s86, s86, 1\n\t"
                 "s_mul_i32 s87, s85, 12288\n\ts_lshr_b32 s90, s89, 1\n\ts_add_u32 s87, s87, s90\n\ts_sub_u32 s87, s87, 13 * 4096\n\t"
                 "s_sub_u32 s89, s89, 4 * 8192\n\ts_cmp_gt_u32 s85, 5\n\ts_cselect_b32 s89, -1, s89\n\ts_cmp_ge_u32 s89, 11 * 8192\n\t"
                 "s_waitcnt lgkmcnt(0)\n\ts_cbranch_scc1 3f\n\t"
                 "s_cmp_lt_u32 s89, 9 * 8192\n\ts_cselect_b64 s[90:91], s[70:71], s[72:73]\n\ts_cselect_b32 s89, s89, s87\n\t"
                 "s_add_u32 s90, s90, s89\n\ts_addc_u32 s91, s91, 0\n\tglobal_load_lds_dword %[db], s[90:91]\n"
                 "3:\n\ts_mov_b32 m0, 0x3000\n\t"
                 "s_add_u32 s90, s68, s88\n\ts_addc_u32 s91, s69, 0\n\t"
                 "s_add_u32 s92, s66, s86\n\ts_addc_u32 s93, s67, 0\n\t"
                 PD(d0, 90:91, 0, " nt") PD(d1, 90:91, 1024, " nt") PD(d2, 90:91, 2048, " nt")
                 "s_mov_b32 m0, 0\n\ts_nop 0\n\t"
                 PD(d0, 92:93, 0, " nt") PD(d1, 92:93, 1024, " nt")
                 "s_sub_u32 s89, s84, 15\n\ts_cmp_gt_u32 s85, 5\n\ts_cselect_b32 s89, -1, s89\n\ts_cmp_ge_u32 s89, 11\n\ts_cbranch_scc1 9f\n\t"
                 "s_mov_b32 m0, %[junk]\n\ts_sub_u32 s87, s87, 11 * 4096\n\ts_lshl_b32 s89, s89, 13\n\ts_cmp_lt_u32 s89, 9 * 8192\n\t"
                 "s_cselect_b64 s[90:91], s[76:77], s[74:75]\n\ts_cselect_b32 s89, s89, s87\n\t"
                 "s_add_u32 s90, s90, s89\n\ts_addc_u32 s91, s91, 0\n\tglobal_load_lds_dword %[db], s[90:91]\n\t"
                 "s_branch 9f\n"
                 "7:\n\ts_cmp_eq_u32 %[w], 1\n\ts_cbranch_scc1 8f\n\ts_barrier\n\ts_branch 8f\n"
                 "4:\n\t"
                 "s_cmp_gt_u32 %[b], 5\n\ts_cbranch_scc1 8f\n\ts_cmp_gt_u32 %[w], 9\n\ts_cbranch_scc1 8f\n\t"
                 "s_mov_b32 m0, %[img]\n\ts_mul_i32 s86, %[w], 9216\n\ts_and_b32 s88, %[w], 1\n\ts_lshl1_add_u32 s89, %[b], s88\n\t"
                 "s_mul_i32 s88, s88, 6144\n\ts_mul_i32 s89, s89, 6144\n\ts_add_u32 s88, s88, %[hr]\n\t"
                 "s_cmp_lg_u32 %[w], 0\n\ts_waitcnt lgkmcnt(0)\n\ts_cbranch_scc1 5f\n\t"
                 PD(d0, 64:65, 0, "") PD(d1, 64:65, 1024, "") PD(d2, 64:65, 2048, "") PD(d0, 64:65, 3072, "")
                 "s_mov_b32 m0, %[img] + 4096\n\ts_add_u32 s90, s64, 0x1000\n\ts_addc_u32 s91, s65, 0\n\t"
                 PD(d1, 90:91, 0, "") PD(d2, 90:91, 1024, "")
                 "\n5:\n\ts_cmp_gt_u32 %[w], 7\n\ts_cselect_b64 s[92:93], s[72:73], s[70:71]\n\ts_cselect_b32 m0, s88, s86\n\ts_cselect_b32 s86, s89, s86\n\t"
                 "s_cselect_b32 s87, 2, 3\n\ts_add_u32 s92, s92, s86\n\ts_addc_u32 s93, s93, 0\n"
                 "6:\n\ts_sub_u32 s87, s87, 1\n\t"
                 PD(d0, 92:93, 0, "") PD(d1, 92:93, 1024, "") PD(d2, 92:93, 2048, "")
                 "s_add_u32 m0, m0, 3072\n\ts_add_u32 s92, s92, 3072\n\ts_addc_u32 s93, s93, 0\n\ts_cmp_lg_u32 s87, 0\n\ts_cbranch_scc1 6b"
                 "\n8:\n\ts_waitcnt lgkmcnt(0)\n9:"
                 : "={s[64:79]}"(ka), "={s[80:81]}"(kb), "={s[82:83]}"(tent64),
                   [d0] "=&v"(doff[0]), [d1] "=&v"(doff[1]), [d2] "=&v"(doff[2]), [l16] "=&v"(l16), [db] "=&v"(db)
                 : [kp] "s"(__builtin_amdgcn_kernarg_segment_ptr()), [b] "s"(blockIdx.x), [w] "s"(wave),
                   [t0] "i"(swz_tbl(0)), [t1] "i"(swz_tbl(1)), [t2] "i"(swz_tbl(2)), [junk] "i"(JUNK), [img] "i"(IMG_A), [hr] "i"(H_R)
                 : "memory", "scc", "vcc", "s84", "s85", "s86", "s87", "s88", "s89", "s90", "s91", "s92", "s93", "s94", "s95", "s96", "s97", "s98", "s99");
  }
  const unsigned t0 = (unsigned)__builtin_amdgcn_s_memrealtime() - T0BACK;

  if (__builtin_expect(blockIdx.x < 6, 0)) {
    LANE_VARS
    DOFF_VARS
    const int j = blockIdx.x;
    TNOW(ta0);
    TNOW(ta1);
    const int qq = (lane >> 2) & 3, p4 = lane & 3, e4 = wave & 3;
    int ra = H_R + r * ROWB + ((g ^ swz(r)) << 4);
    bf16x8 a[6], b[6], w[18];
    auto rows = [&](int t, bf16x8* d) {
#pragma unroll
      for (int kk = 0; kk < 6; ++kk) d[kk] = *(LDS_AS(bf16x8)*)(L0 + t * 6144 + ((kk & 1) ? (ra ^ 64) : ra) + (kk >> 1) * 128);
    };
#define TSET(v)
    int o[3];
#pragma unroll
    for (int t = 0; t < 3; ++t) o[t] = tr_off(4 * g + qq, 4 * (3 * e4 + t) + p4);
    unsigned qo = lbase + H_Q + 3 * e4 * 512 + ((g >> 1) * 16 + r) * 16 + (g & 1) * 8, qa = lbase + IMG_A + tr_off(r, 8 * wave + g);
    if (wave == 11) {
      unsigned d;
      asm volatile("s_load_dword %0, %1, 0x40\n\ts_barrier\n\ts_barrier\n\ts_barrier\n\ts_waitcnt lgkmcnt(0)" : "=&s"(d) : "s"(box + EPOCH_OFF) : "memory");
    } else
    asm volatile("s_waitcnt vmcnt(9)\n\ts_barrier" : PIN3(o, 0), "+v"(qo), "+v"(qa) :: "memory");
    if (wave < 6 || wave == 10) {
      rows((IMG_A - H_R) / 6144, a);
      const u32x2 m0 = *(LDS_AS(u32x2)*)(size_t)qa, m1 = *(LDS_AS(u32x2)*)(size_t)(qa ^ 32);
      static_assert((IMG_A - H_R) % 6144 == 0, "");
      const bf16x8 ones = __builtin_bit_cast(bf16x8, u32x4{0x3f803f80u, 0x3f803f80u, 0x3f803f80u, 0x3f803f80u});
      f32x4 sm = {0.f, 0.f, 0.f, 0.f};
#pragma unroll
      for (int i = 0; i < 6; ++i) sm = mfma(ones, a[i], sm);
      const float mean = sm[0] * (1.f / CC);
      if (__builtin_expect(wave < 6, 1)) {
        img_st(L0, H_C, wave, lane, __builtin_bit_cast(bf16x8, u32x4{cvtpk(bflo(m0[0]) - mean, bfhi(m0[0]) - mean), cvtpk(bflo(m0[1]) - mean, bfhi(m0[1]) - mean),
                                                                   cvtpk(bflo(m1[0]) - mean, bfhi(m1[0]) - mean), cvtpk(bflo(m1[1]) - mean, bfhi(m1[1]) - mean)}));
      } else {
        asm volatile("s_barrier" : PIN3(a, 0), PIN3(a, 3) :: "memory");
        f32x2 v2 = {0.f, 0.f}, v3 = v2;
#pragma unroll
        for (int i = 0; i < 12; ++i) {
          const u32x4 d = __builtin_bit_cast(u32x4, a[i >> 1]);
          const unsigned d0 = d[2 * (i & 1)], d1 = d[2 * (i & 1) + 1];
          const f32x2 x0 = f32x2{bflo(d0), bfhi(d0)} - mean, x1 = f32x2{bflo(d1), bfhi(d1)} - mean;
          v2 += x0 * x0; v3 += x1 * x1;
        }
        v2 += v3;
        *(LDS_AS(float)*)(L0 + CT_OFF + r * 4) = LOG2E * __builtin_amdgcn_rsqf(xrow16_sum(v2[0] + v2[1]) * (1.f / CC) + EPS);
      }
      TSET(ta2);
    }
    if (wave < 4) {
      wait_vm<0>();
      TSET(ta3);
      BARRIER();
      TSET(th1);
#pragma unroll
      for (int kk = 0; kk < 6; ++kk) {
        b[kk] = img_ld(L0, H_C, kk, lane);
        w[kk] = mk8(tr_read(L0 + kk * 12288 + o[0]), tr_read(L0 + kk * 12288 + 6144 + o[0]));
        __builtin_amdgcn_sched_barrier(0);
      }
#pragma unroll
      for (int i = 6; i < 18; ++i) {
        w[i] = mk8(tr_read(L0 + (i % 6) * 12288 + o[i / 6]), tr_read(L0 + (i % 6) * 12288 + 6144 + o[i / 6]));
        __builtin_amdgcn_sched_barrier(0);
      }
#pragma unroll
      for (int t = 0; t < 3; ++t) {
        f32x4 D = {0.f, 0.f, 0.f, 0.f};
#pragma unroll
        for (int kk = 0; kk < 6; ++kk) D = mfma(w[6 * t + kk], b[kk], D);
        *(LDS_AS(u32x2)*)(size_t)(qo + t * 512) = u32x2{cvtpk(D[0], D[1]), cvtpk(D[2], D[3])};
      }
      TSET(ta4);
      BARRIER();
    } else if (wave == 8 || wave == 9) {
      unsigned so = lane * 16 + (2 * j + (wave & 1)) * 1024;
      unsigned epoch;
      asm volatile("global_load_dword %0, %1, %2 sc1\n\ts_barrier\n\ts_waitcnt vmcnt(0)" : "=v"(epoch) : "v"(EPOCH_OFF), "s"(box) : "memory");
      TSET(ta3);
      rows(wave & 1, a);
      unsigned ta = line_tag(epoch, 8), tb = line_tag(epoch, 9);
      asm volatile("" : PIN3(a, 0), PIN3(a, 3), "+v"(ta), "+v"(tb), "+v"(so));
      TSET(ta4);
      BARRIER();
      TSET(ta5);
#pragma unroll
      for (int kk = 0; kk < 6; ++kk) b[kk] = img_ld(L0, H_Q, kk, lane);
      const float sc = *(LDS_AS(float)*)(L0 + CT_OFF + r * 4);
      __builtin_amdgcn_sched_barrier(0);
      f32x4 D = {0.f, 0.f, 0.f, 0.f};
#pragma unroll
      for (int kk = 0; kk < 6; ++kk) D = mfma(a[kk], b[kk], D);
      D *= sc;
      const unsigned p0 = cvtpk(D[0], D[1]), p1 = cvtpk(D[2], D[3]);
      asm volatile("global_store_dwordx4 %0, %1, %2 sc1\n\ts_nop 1" : : "v"(so), "v"(u32x4{p0, p1, p0 ^ p1 ^ ta, tb}), "s"(box + QK_OFF) : "memory");
      TSET(th2);
    } else if (wave != 11) {
      if (wave != 10) asm volatile("s_waitcnt vmcnt(0) lgkmcnt(0)\n\ts_barrier" ::: "memory");
      BARRIER();
    }
    BARRIER();
    issue_rows16(wo + wave * 6144, lbase + wave * 6144, doff);
    if (wave < 2) issue_rows16(wv + (2 * j + wave) * 6144, lbase + H_R + wave * 6144, doff);
    const unsigned epoch = epoch_load(box);
    BARRIER();
    TSET(ta3);
    {
      const int o0 = tr_off(8 * g + qq, 4 * wave + p4), o1 = tr_off(8 * g + 4 + qq, 4 * wave + p4);
#pragma unroll
      for (int kk = 0; kk < 6; ++kk) b[kk] = mk8(tr_read(L0 + kk * 12288 + o0), tr_read(L0 + kk * 12288 + o1));
      f32x4 D[2];
#pragma unroll
      for (int h = 0; h < 2; ++h) {
        rows(h, a);
        D[h] = f32x4{0.f, 0.f, 0.f, 0.f};
#pragma unroll
        for (int kk = 0; kk < 6; ++kk) D[h] = mfma(a[kk], b[kk], D[h]);
      }
      const int e = wave / 3;
      asm volatile("global_store_dwordx4 %0, %1, %2 sc1\n\ts_waitcnt vmcnt(0)"
                   : : "v"(lane * 16 + (e * 15 + 3 * j + wave) * 1024), "v"(pack8(D[0] * SQRT_C, D[1] * SQRT_C)), "s"(box + W2_OFF) : "memory");
    }
    BARRIER();
    if (wave == 11)
      asm volatile("s_mov_b64 exec, 1\n\tglobal_store_dwordx2 %0, %1, %2 sc1\n\ts_mov_b64 exec, -1\n\ts_nop 1"
                   : : "v"(j * 128), "v"(u32x2{line_tag(epoch, 10), line_tag(epoch, 11)}), "s"(box + W2F_OFF) : "memory");
    TPUT(60 + wave * 5, ta0); TPUT(61 + wave * 5, ta1); TPUT(62 + wave * 5, ta2); TPUT(63 + wave * 5, ta3); TPUT(64 + wave * 5, ta4);
  }

  unsigned dq = 0;
  int rslot = -1;
  if (__builtin_expect(wave == 0, 1)) {
    LANE_VARS
    DOFF_VARS
    if (helper) BARRIER();
    const unsigned afull = ct + C_FULL + r * 4;
    unsigned ts = 0;
    {
      const int ti = helper ? 1023 : (lane * 31 + (int)(blockIdx.x >> 3) - 1) & 1023;
      const int sp = ti >> 5, yy = sp * 8 + ((blockIdx.x + sp) & 7), xx = (ti + 5 * sp) & 31;
      unsigned tmp, pub = helper ? 2 * (TPW - 1) : 0, pre = 0, afr = ct + C_FREE + (helper ? ((TPW - 1) % NSLOT) * 4 : 0), e0, e1, e2, g0, g1, g2;
      asm volatile("s_mov_b32 s44, %[due0]\n\ts_mov_b32 s46, %[y0]\n\ts_mov_b32 s47, %[cap]\n\ts_mov_b32 s48, %[i0]\n\ts_mov_b32 s49, %[s0]\n\t"
                   "s_mov_b32 s51, 0\n\ts_mov_b32 s45, 0\n\ts_movk_i32 s39, %[p3s]\n"
                   "1:\n\tv_readlane_b32 s53, %[ob], s48\n\tv_readlane_b32 s52, %[oa], s48\n\ts_add_u32 m0, s49, 0x3000\n\tv_cmp_gt_u32 vcc, s51, %[pre]\n\t"
                   "v_add_u32 %[g0], s53, %[d0]\n\tv_add_u32 %[g1], s53, %[d1]\n\tv_add_u32 %[g2], s53, %[d2]\n\t"
                   "v_add_u32 %[e0], s52, %[d0]\n\tv_add_u32 %[e1], s52, %[d1]\n\tv_add_u32 %[e2], s52, %[d2]\n\t"
                   "s_cbranch_vccnz 5f\n\t"
                   "s_cmp_eq_u32 s48, 0\n\ts_cbranch_scc1 2f\n\ts_cmp_eq_u32 s48, 2\n\ts_cbranch_scc1 8f\n"
                   LD_GATE(1, "")
                   LD1(g0, 68:69, 0) LD1(g1, 68:69, 1024) LD1(g2, 68:69, 2048) "s_mov_b32 m0, s49\n\ts_nop 0\n\t" LD1(e0, 66:67, 0) LD1(e1, 66:67, 1024)
                   LD_NEXT
                   "\n2:\n\tv_add_u32 %[afr], 4, %[afr]\n\ts_cmp_eq_u32 s49, %[lasts]\n\ts_cbranch_scc1 7f\n"
                   "3:\n\tv_mov_b32 %[pre], 0\n\ts_add_u32 m0, s49, 0x800\n\tds_read_b32 %[pre], %[afr] ; NOTRACK\n"
                   LD_GATE(2, "")
                   LD1(e2, 56:57, 0) LD1(e0, 56:57, 1024) LD1(e1, 56:57, 2048) LD1(e2, 56:57, 3072) "s_add_u32 m0, s49, 0x1800\n\ts_add_u32 s48, s48, 1\n\t" LD1(e0, 58:59, 0)
                   LD_NEXT
                   LD_GATE(3, "s_cmp_gt_u32 s48, %[tpw] - 2\n\ts_cbranch_scc1 38f\n39:\n\t")
                   LD1(e1, 58:59, 1024) LD1(e2, 58:59, 2048) LD1(e0, 58:59, 3072) "s_add_u32 m0, s49, 0x2800\n\ts_add_u32 s49, s49, %[slot]\n\t" LD1(e1, 60:61, 0) LD1(e2, 60:61, 1024)
                   LD_NEXT
                   "s_cmp_eq_u32 s49, %[ring]\n\ts_cselect_b32 s49, 0, s49\n\ts_cmp_gt_i32 s46, 30\n\ts_cbranch_scc1 6f\n"
                   "4:\n\ts_cmp_eq_u32 s48, %[pk]\n\ts_cmovk_i32 s39, %[p3]\n\ts_cmp_lg_u32 s48, %[tpw]\n\ts_cbranch_scc1 1b\n\ts_branch 9f\n"
                   LD_PUB(1, 0, 15, 30) LD_PUB(2, 5, 20, 35) LD_PUB(3, 10, 25, 40)
                   "38:\n\ts_cmp_eq_u32 s46, 10\n\ts_cselect_b32 s43, 23, 0\n\ts_cmp_lt_u32 s42, s43\n\ts_cbranch_scc0 39b\n\tv_add_u32 %[tmp], 1, %[pub]\n\ts_waitcnt vmcnt(22)\n\tds_write_b32 %[af], %[tmp] ; NOTRACK\n\ts_branch 39b\n"
                   "5:\n\ts_movk_i32 s40, 0x100\n"
                   "40:\n\tds_read_b32 %[pre], %[afr]\n\t" LD_VM
                   "s_sub_u32 s40, s40, 1\n\ts_cselect_b32 s42, 0, s42\n\ts_cselect_b32 s40, 0, s40\n\t"
                   "s_cmp_le_i32 s42, s46\n\ts_cbranch_scc1 42f\n"
                   "41:\n\ts_sub_i32 s47, s47, 1\n\ts_cmp_lt_i32 s47, 0\n\ts_waitcnt lgkmcnt(0)\n\ts_cbranch_scc1 10b\n\t"
                   "v_cmp_gt_u32 vcc, s51, %[pre]\n\ts_cbranch_vccnz 40b\n\ts_branch 10b\n"
                   LD_PUB0(4, 0, 15, 30)
                   "6:\n\ts_waitcnt vmcnt(45)\n\tv_add_u32 %[pub], 2, %[pub]\n\tds_write_b32 %[af], %[pub]\n\ts_sub_u32 s46, s46, 15\n\ts_branch 4b\n"
                   "7:\n\ts_waitcnt lgkmcnt(0)\n\ts_add_u32 s51, s51, 8\n\tv_subrev_u32 %[afr], %[frb], %[afr]\n\ts_branch 3b\n"
                   "8:\n\ts_barrier\n\ts_add_u32 s46, s46, 64\n\ts_branch 10b\n"
                   "9:\n\ts_cmp_lt_i32 s46, 30\n\ts_cbranch_scc1 91f\n\ts_waitcnt vmcnt(30)\n\tv_add_u32 %[pub], 2, %[pub]\n\tds_write_b32 %[af], %[pub]\n"
                   "91:\n\ts_cmp_lt_i32 s46, 15\n\ts_cbranch_scc1 92f\n\tv_add_u32 %[tmp], 1, %[pub]\n\ts_waitcnt vmcnt(27)\n\tds_write_b32 %[af], %[tmp]\n"
                   "92:\n\ts_waitcnt lgkmcnt(0)"
                   : [pub] "+v"(pub), [pre] "+v"(pre), [afr] "+v"(afr), [ts] "+v"(ts), [tmp] "=&v"(tmp),
                     [e0] "=&v"(e0), [e1] "=&v"(e1), [e2] "=&v"(e2), [g0] "=&v"(g0), [g1] "=&v"(g1), [g2] "=&v"(g2)
                   : "{s[64:79]}"(ka), "{s[56:57]}"(f0 + 0x800), "{s[58:59]}"(f0 + 0x30000), "{s[60:61]}"(f0 + 0x31000), [t0] "s"(t0), [af] "v"(afull),
                     [d0] "v"(doff[0]), [d1] "v"(doff[1]), [d2] "v"(doff[2]), [oa] "v"(yy * 393216 + xx * 6144), [ob] "v"(yy * 98304 + xx * 3072),
                     [due0] "s"(helper ? HDUE * (PACE16 / 3) : PACES / 3), [y0] "s"(helper ? -15 : -74), [i0] "s"(helper ? TPW - 1 : 0),
                     [s0] "s"(helper ? ((TPW - 1) % NSLOT) * SLOT : 0),
                     [cap] "i"(SPIN_CAP), [win] "i"(PWIN), [ahi] "i"(AHI), [adl] "i"(ADL), [amg] "i"(AMG), [p3] "i"(PACE16 / 3), [p3s] "i"(PACES / 3), [pk] "i"(PK1), [slot] "i"(SLOT),
                     [ring] "i"(NSLOT * SLOT), [frb] "i"(NSLOT * 4), [lasts] "i"((NSLOT - 1) * SLOT), [tpw] "i"(TPW)
                   : "memory", "scc", "vcc", "s39", "s40", "s41", "s42", "s43", "s44", "s45", "s46", "s47", "s48", "s49", "s51", "s52", "s53", "s62", "s63");
    }
    lds_st32(ct + C_ISS, 1u);
    asm volatile("s_waitcnt vmcnt(15)\n\tds_write_b32 %0, %1\n\ts_waitcnt vmcnt(12)\n\tds_write_b32 %0, %2\n\ts_waitcnt vmcnt(0)\n\tds_write_b32 %0, %3"
                 : : "v"(afull), "v"(2 * TPW - 2), "v"(2 * TPW - 1), "v"(~0u) : "memory");
    TNOW(tl);
    {
      unsigned d;
      asm volatile("global_load_dword %0, %1, %2 sc1\n\ts_setprio 0\n\ts_waitcnt vmcnt(0)" : "=v"(d) : "v"(blockIdx.x * 128), "s"(box) : "memory");
    }
    TNOW(tlx);
    rslot = 7;
    dq = t0 + T0BACK - tent;
  } else {
    if (wave == 1) {
      LANE_VARS
    {
      const unsigned o = lane * 8;
      unsigned z = (o >= C_MREF && o < C_MREF + 64) ? 0xff800000u : 0u;
      if (helper && o >= C_CONS && o < C_MREF) z = NE * ((TPW - 1) / NPK);
      u32x2 zz = {z, z};
      if (o >= C_PFL && o < C_CONS) zz = u32x2{(o - C_PFL) / 2 + 2, (o - C_PFL) / 2 + 4} + (helper ? 2u * NPK * ((TPW - 1) / NPK) : 0u);
      if (o == C_EP) zz[1] = QKW0 + QKW1 * min(t0 + T0BACK - tent, 45u);
      *(LDS_AS(u32x2)*)(L0 + CT_OFF + o) = zz;
      *(LDS_AS(u32x2)*)(L0 + ONES_OFF + (lane < 48 ? o : 0)) = u32x2{0x3f803f80u, 0x3f803f80u};
    }
    }
    if (helper || wave == 1) BARRIER();
    if (wave <= NE) {
      LANE_VARS
      const int ej = wave - 1;
      const int qq = (lane >> 2) & 3, p4 = lane & 3;
      const unsigned apk = lbase + IMG_A + lane * 16;
      const unsigned epoch = epoch_load(box);
      __builtin_amdgcn_s_setprio(2);
      unsigned long long tpv, tp7, tx1;
      {
        static_assert((TPW & 1) == 1 && C_CONS == C_PFL + 64 && C_FREE == C_FULL + 64, "");
        const int k0 = helper ? TPW - 1 : 0;
        unsigned need = 2 * k0 + 2;
        asm volatile("s_mov_b32 s46, 0\n\ts_mov_b64 exec, 0\n\ts_nop 3\n\ts_branch 99f\n"
                     "2:\n\ts_mov_b64 exec, -1\n\ts_nop 7\n\t" PV_Z4(72, 73, 74, 75) PV_Z4(76, 77, 78, 79) PV_Z4(80, 81, 82, 83) PV_Z4(84, 85, 86, 87)
                     "v_mov_b32 v88, 0xff800000\n\t"
                     "s_mov_b32 s46, %[k0]\n\ts_mov_b32 s45, %[s0]\n\ts_mov_b32 s44, %[e0]\n\ts_mov_b32 s47, %[cap]\n\t"
                     "s_cmp_lg_u32 s46, 0\n\ts_cbranch_scc1 32f\n"
                     "3:\n\tv_mov_b32 v148, %[ca]\n\t" PV_WAITF(4, 148) PV_FETCH_A
                     "s_cmp_lg_u32 s46, 0\n\ts_cbranch_scc1 99f\n\t"
                     "s_mov_b64 exec, 2\n\tds_add_u32 v148, %[one] offset:64\n\ts_mov_b64 exec, -2\n\t"
                     "v_add_u32 %[need], 2, %[need]\n\tv_add_u32 v148, 4, v148\n\ts_mov_b64 exec, -1\n\t"
                     PV_WAITF(5, 148)
                     "v_mov_b32 v149, v148\n\ts_mov_b64 exec, -2\n\tv_add_u32 %[need], 2, %[need]\n\tv_add_u32 v148, 4, v148\n\t"
                     "s_mov_b64 exec, 1\n\tv_add_u32 %[need], %[npk2], %[need]\n\ts_mov_b64 exec, -1\n\t"
                     PV_REQ("126:129", "130:133", "134:135", 136, 148) PV_FETCH_B
                     "s_mov_b64 exec, 2\n\tds_add_u32 v149, %[one] offset:64\n\ts_mov_b64 exec, -1\n"
                     "6:\n\t"
                     PV_STEP(1, "126:129", "130:133", "134:135", 134, 135, 136, 148, "138:141", "142:145", "146:147", 137, 149,
                             "92:95", "98:101", "104:107", "90:93", "96:99", "102:105",
                             PV_MOV(92, 90) PV_MOV(93, 91), PV_MOV(98, 96) PV_MOV(99, 97), PV_MOV(104, 102) PV_MOV(105, 103), "")
                     "s_cbranch_vccnz 7f\n"
                     "16:\n\t" PV_FETCH_A PV_REL(148)
                     "s_lshl1_add_u32 s54, s46, -2\n\ts_lshr_b64 s[54:55], %[sch], s54\n\ts_and_b32 s54, s54, 15\n\ts_cbranch_scc1 31f\n"
                     "30:\n\t"
                     PV_STEP(2, "138:141", "142:145", "146:147", 146, 147, 137, 149, "126:129", "130:133", "134:135", 136, 148,
                             "110:113", "116:119", "122:125", "108:111", "114:117", "120:123",
                             PV_MOV(110, 108) PV_MOV(111, 109), PV_MOV(116, 114) PV_MOV(117, 115), PV_MOV(122, 120) PV_MOV(123, 121),
                             "s_cmp_eq_u32 s46, %[last] - 1\n\ts_cbranch_scc1 98f\n\t")
                     "s_cbranch_vccnz 8f\n"
                     "26:\n\t" PV_FETCH_B PV_REL(149)
                     "s_branch 6b\n"
                     PV_WAITF(7, 148) "s_branch 16b\n"
                     PV_WAITF(8, 149) "s_branch 26b\n"
                     PV_SLOW(1, "126:129", "130:133", "134:135", 134, 135, 136, 148, 126, 127, 128, 129)
                     PV_SLOW(2, "138:141", "142:145", "146:147", 146, 147, 137, 149, 138, 139, 140, 141)
                     "31:\n\ts_cmp_lg_u32 s54, 8\n\ts_cbranch_scc1 34f\n"
                     "32:\n\tv_mbcnt_lo_u32_b32 v152, 31, 0\n\tv_lshlrev_b32 v152, 7, v152\n\ts_mov_b32 s55, %[gcap]\n"
                     "33:\n\tglobal_load_dwordx2 v[150:151], v152, %[fl] sc1\n\ts_sub_u32 s55, s55, 1\n\ts_cselect_b32 s56, 0, -1\n\ts_waitcnt vmcnt(0) lgkmcnt(0)\n\t"
                     "v_cmp_ne_u64 s[48:49], %[tag], v[150:151]\n\ts_sleep 2\n\ts_and_b32 s48, s48, s56\n\ts_cbranch_scc1 33b\n\t"
                     "s_mov_b32 s57, 0\n\tv_mov_b32 v150, %[apk]\n\ts_cmp_lg_u32 s46, %[last]\n\ts_cbranch_scc1 30b\n\ts_mov_b32 s54, 7\n\ts_branch 38f\n"
                     "34:\n\ts_cmp_lg_u32 s46, %[last] - 3\n\ts_cbranch_scc1 36f\n\tv_mov_b32 v151, %[iss]\n"
                     "35:\n\tds_read_b32 v150, v151\n\ts_sub_i32 s47, s47, 1\n\ts_cmp_lt_i32 s47, 0\n\ts_cselect_b32 s55, 0, 1\n\ts_waitcnt lgkmcnt(0)\n\t"
                     "v_readfirstlane_b32 s56, v150\n\ts_cmp_lt_u32 s56, s55\n\ts_cbranch_scc1 35b\n"
                     "36:\n\ts_mul_i32 s55, s57, 3072\n\tv_add_u32 v150, s55, %[apk]\n\ts_getpc_b64 s[48:49]\n"
                     "37:\n\ts_mul_i32 s55, s57, 40\n\ts_add_u32 s55, s55, 38f-37b\n\ts_add_u32 s48, s48, s55\n\ts_addc_u32 s49, s49, 0\n\t"
                     "s_add_u32 s57, s57, s54\n\ts_sub_u32 s54, s54, 1\n\ts_setpc_b64 s[48:49]\n"
                     "38:\n\t" PV_W2(0, 3, 4, 7, 8, 11) PV_W2(12, 15, 16, 19, 20, 23) PV_W2(24, 27, 28, 31, 32, 35) PV_W2(36, 39, 40, 43, 44, 47)
                     PV_W2(48, 51, 52, 55, 56, 59) PV_W2(60, 63, 64, 67, 68, 71)
                     "s_waitcnt lgkmcnt(0)\n\ts_branch 3b\n"
                     "96:\n\t" PV_RESC(134, 135, 126, 127, 128, 129) "s_nop 0\n\ts_branch 95f\n"
                     "98:\n\t" PV_MF("76:79", "114:117", "138:141") PV_MF("80:83", "120:123", "138:141")
                     "99:\n\ts_cmp_eq_u32 s46, 0\n\ts_cselect_b32 s54, 0, %[nl]\n\ts_cselect_b32 s55, 0, %[x1n]\n\ts_waitcnt lgkmcnt(0)\n\t"
                     "v_mov_b64 v[108:109], v[90:91]\n\tv_mov_b64 v[110:111], v[90:91]\n\tv_mov_b64 v[112:113], v[96:97]\n\tv_mov_b64 v[114:115], v[96:97]\n\t"
                     "v_mov_b64 v[116:117], v[102:103]\n\tv_mov_b64 v[118:119], v[102:103]\n\tv_add_u32 v120, %[aw], %[apk]\n\tv_add_u32 v121, %[ar], %[apk]\n\t"
                     "v_mov_b32 v122, %[x1]\n\tv_lshrrev_b32 v123, 1, %[apm]\n\tv_add_u32 v123, %[lwo], v123\n\t"
                     "v_mbcnt_lo_u32_b32 v124, -1, 0\n\tv_mbcnt_hi_u32_b32 v124, -1, v124\n\tv_and_b32 v125, 1, v124\n\tv_and_b32 v124, 48, v124\n\t"
                     "v_lshl_add_u32 v124, v125, 2, v124\n\tv_add_u32 v124, %[lw], v124\n"
                     "94:\n\tds_read_b32 v136, v122 offset:%[pfd]\n\tds_read_b64 v[134:135], %[apm] offset:%[el]\n\tds_read_b128 v[126:129], %[apk] offset:8 * %[el]\n\t"
                     "ds_read_b128 v[130:133], %[apu] offset:2 * %[el]\n\ts_sub_i32 s47, s47, 1\n\ts_cmp_lt_i32 s47, 0\n\ts_cselect_b32 s54, 0, s54\n\ts_waitcnt lgkmcnt(3)\n\t"
                     "v_readfirstlane_b32 s56, v136\n\ts_cmp_lt_u32 s56, s54\n\ts_cbranch_scc1 94b\n\ts_waitcnt lgkmcnt(2)\n\t"
                     "v_cmp_neq_f32 vcc, v134, v88\n\tv_cmp_neq_f32 s[50:51], v135, v88\n\ts_or_b64 vcc, vcc, s[50:51]\n\ts_waitcnt lgkmcnt(0)\n\ts_cbranch_scc1 96b\n"
                     "95:\n\t" PV_MF("84:87", "130:133", "126:129") "s_nop 1\n\t" PV_MF("72:75", "92:95", "126:129") "s_nop 1\n\t" PV_MF("72:75", "108:111", "126:129") PV_T(58)
                     "s_nop 1\n\tv_add_f32 v153, v86, v87\n\tv_add_f32 v151, v84, v85\n\t" PV_MF("76:79", "98:101", "126:129")
                     "v_rcp_f32 v150, v153\n\tv_mul_f32 v151, %[nic], v151\n\t" PV_MF("76:79", "112:115", "126:129")
                     "v_mul_f32 v151, v151, v150\n\t" PV_PK(72, 73) PV_MF("80:83", "104:107", "126:129") PV_PK(74, 75)
                     "v_cvt_pk_bf16_f32 v72, v72, v73\n\t" PV_MF("80:83", "116:119", "126:129") "v_cvt_pk_bf16_f32 v73, v74, v75\n\tds_write_b64 v120, v[72:73]\n\t"
                     PV_NRM(76, 77, 78, 79, 2048) "v_log_f32 v153, v153\n\t" PV_NRM(80, 81, 82, 83, 4096)
                     "s_and_b64 exec, exec, 1\n\tds_add_u32 v122, %[one]\n\ts_cselect_b64 exec, -1, 0\n\t" PV_T(60)
                     "v_subrev_f32 v88, %[toff], v88\n\tv_add_f32 v88, v88, v153\n\tds_write_b32 v123, v88\n\tds_read2_b32 v[114:115], v124 offset1:2\n"
                     "97:\n\tds_read_b32 v150, v122\n\tds_read_b128 v[90:93], v121\n\tds_read_b128 v[94:97], v121 offset:1024\n\tds_read_b128 v[98:101], v121 offset:2048\n\t"
                     "ds_read_b128 v[102:105], v121 offset:3072\n\tds_read_b128 v[106:109], v121 offset:4096\n\tds_read_b128 v[110:113], v121 offset:5120\n\t"
                     "s_sub_i32 s47, s47, 1\n\ts_cmp_lt_i32 s47, 0\n\ts_cselect_b32 s55, 0, s55\n\ts_waitcnt vmcnt(0) lgkmcnt(6)\n\t"
                     "v_readfirstlane_b32 s56, v150\n\ts_cmp_lt_u32 s56, s55\n\ts_cbranch_scc1 97b\n\ts_cmp_eq_u32 s46, 0\n\t"
                     PV_G(5, "90:93", "0:3", "4:7", "8:11", "0", "0", "0")
                     PV_G(4, "94:97", "12:15", "16:19", "20:23", "v[72:75]", "v[76:79]", "v[80:83]")
                     PV_G(3, "98:101", "24:27", "28:31", "32:35", "v[72:75]", "v[76:79]", "v[80:83]")
                     PV_G(2, "102:105", "36:39", "40:43", "44:47", "v[72:75]", "v[76:79]", "v[80:83]")
                     PV_G(1, "106:109", "48:51", "52:55", "56:59", "v[72:75]", "v[76:79]", "v[80:83]")
                     PV_G(0, "110:113", "60:63", "64:67", "68:71", "v[72:75]", "v[76:79]", "v[80:83]")
                     "v_xor_b32 v114, v114, %[ltag]\n\tv_xor_b32 v115, v115, %[ltag]\n\ts_nop 3\n\t"
                     "v_cvt_pk_bf16_f32 v116, v72, v73\n\tv_cvt_pk_bf16_f32 v120, v74, v75\n\tv_cvt_pk_bf16_f32 v117, v76, v77\n\tv_cvt_pk_bf16_f32 v121, v78, v79\n\t"
                     "v_cvt_pk_bf16_f32 v118, v80, v81\n\tv_bitop3_b32 v119, v116, v117, v114 bitop3:0x96\n\tv_cvt_pk_bf16_f32 v122, v82, v83\n\t"
                     "v_bitop3_b32 v123, v120, v121, v115 bitop3:0x96\n\tv_xor_b32 v119, v119, v118\n\tv_xor_b32 v123, v123, v122\n\t"
                     "s_cbranch_scc1 2b\n\t"
                     "global_store_dwordx4 %[so], v[116:119], %[bx] sc1\n\tglobal_store_dwordx4 %[so], v[120:123], %[bx2] sc1\n\t"
                     PV_T(62) "s_waitcnt lgkmcnt(0)\n\ts_nop 0"
                     : [need] "+v"(need), "={s[58:59]}"(tpv), "={s[60:61]}"(tp7), "={s[62:63]}"(tx1)
                     : [so] "v"(((ej * 16 + (lane >> 3)) * RSTR + blockIdx.x) * 128 + (lane & 7) * 16), [ltag] "v"(line_tag(epoch, lane & 7)),
                       [bx] "s"(box), [bx2] "s"(box + 8 * RSTR * 128), [aw] "s"(A_IMG - IMG_A + (ej >> 1) * 1024 + (ej & 1) * 8), [ar] "i"(A_IMG - IMG_A),
                       [nic] "i"(__builtin_bit_cast(unsigned, -1.f / CC)), [toff] "i"(__builtin_bit_cast(unsigned, TOFF)),
                       [lwo] "i"(CT_OFF + C_LW - PKM / 2), [lw] "i"(CT_OFF + C_LW), [x1] "i"(CT_OFF + C_X1), [x1n] "i"(NE),
                       [nl] "i"(2 * TPW + 2 * NPK), [el] "i"(((TPW - 1) % NPK) * 128), [pfd] "i"(C_PFL + ((TPW - 1) % NPK) * 4 - C_X1),
                       [apk] "v"(apk), [apm] "v"(lbase + PKM + r * 8), [apu] "v"(lbase + PKU + g * 64 + (r & 3) * 16),
                       [ox] "v"(lbase + tr_off(4 * g + qq, 4 * ej + p4)), [oy] "v"(lbase + 32 * ROWB + tr_off(2 * g + (qq >> 1), 4 * ej + p4)),
                       [ca] "v"(ct + (lane == 0 ? C_PFL + (k0 % NPK) * 4 : C_FULL + (k0 % NSLOT) * 4)),
                       [lim] "v"(ct + (lane == 0 ? C_PFL + NPK * 4 : C_FULL + NSLOT * 4)), [base] "v"(ct + (lane == 0 ? C_PFL : C_FULL)), [one] "v"(1u),
                       [k0] "s"(k0), [s0] "s"(helper ? ((TPW - 1) % NSLOT) * SLOT : 0), [e0] "s"(helper ? ((TPW - 1) % NPK) * 128 : 0),
                       [w2] "s"(box + W2_OFF + ej * 18432 - (lbase + IMG_A)), [fl] "s"(box + W2F_OFF),
                       [tag] "s"(u32x2{line_tag(epoch, 10), line_tag(epoch, 11)}), [gcap] "i"(GSPIN_CAP), [iss] "i"(CT_OFF + C_ISS), [cap] "i"(SPIN_CAP), [slot] "i"(SLOT), [ring] "i"(NSLOT * SLOT), [pring] "i"(NPK * 128),
                       [npk2] "i"(2 * NPK), [last] "i"(TPW - 1), [tight] "i"(TPW - 5),  [sch] "s"(W2SCH)
                     : "memory", "scc", "vcc", "s44", "s45", "s46", "s47", "s48", "s49", "s50", "s51", "s52", "s53", "s54", "s55", "s56", "s57",
                       "v0", "v1", "v2", "v3", "v4", "v5", "v6", "v7", "v8", "v9", "v10", "v11", "v12", "v13", "v14", "v15", "v16", "v17", "v18", "v19", "v20", "v21", "v22", "v23", "v24", "v25", "v26", "v27", "v28", "v29", "v30", "v31", "v32", "v33", "v34", "v35", "v36", "v37", "v38", "v39", "v40", "v41", "v42", "v43", "v44", "v45", "v46", "v47", "v48", "v49", "v50", "v51", "v52", "v53", "v54", "v55", "v56", "v57", "v58", "v59", "v60", "v61", "v62", "v63", "v64", "v65", "v66", "v67", "v68", "v69", "v70", "v71", "v72", "v73", "v74", "v75", "v76", "v77", "v78", "v79", "v80", "v81", "v82", "v83", "v84", "v85", "v86", "v87", "v88",
                       "v90", "v91", "v92", "v93", "v94", "v95", "v96", "v97", "v98", "v99", "v100", "v101", "v102", "v103", "v104", "v105", "v106", "v107",
                       "v108", "v109", "v110", "v111", "v112", "v113", "v114", "v115", "v116", "v117", "v118", "v119", "v120", "v121", "v122", "v123",
                       "v124", "v125", "v126", "v127", "v128", "v129", "v130", "v131", "v132", "v133", "v134", "v135", "v136", "v137", "v138", "v139",
                       "v140", "v141", "v142", "v143", "v144", "v145", "v146", "v147", "v148", "v149", "v150", "v151", "v152", "v153", "v154", "v155");
      }
    } else {
      LANE_VARS
      const int c = wave - 1 - NE;
      SCtx sc;
      sc.L0 = L0; sc.ct = ct; sc.apk = lbase + IMG_A + lane * 16; sc.apm = lbase + PKM + r * 8;
      sc.api = lbase + PKU + g * 64 + (r & 3) * 2 + ((r >> 2) == g ? 0 : 8); sc.amr = ct + C_MREF + r * 4;
      sc.o_rb = r * ROWB + ((g ^ swz(r)) << 4);
      sc.ev = (r & 1) ^ 1;
      sc.o_yb = (r & 1) ? ONES_OFF + (g << 4) : (32 + (r >> 1)) * ROWB + ((g ^ swz(r >> 1)) << 4);
      TNOW(tq0);
      if (c >= NEARLY) {
        static_assert(NS - NEARLY == 3, "");
        const int u = c - NEARLY;
        const unsigned epoch = epoch_load(box);
        const unsigned ta = line_tag(epoch, 8), tb = line_tag(epoch, 9);
        const unsigned qw = lds_ld32(ct + C_EP + 4);
        while ((unsigned)__builtin_amdgcn_s_memrealtime() - tent < qw) __builtin_amdgcn_s_sleep(4);
        u32x4 v[4];
#pragma unroll 1
        for (int sp = 0; sp < GSPIN_CAP; ++sp) {
          asm volatile("global_load_dwordx4 %0, %4, %5 sc1\n\tglobal_load_dwordx4 %1, %4, %5 offset:1024 sc1\n\tglobal_load_dwordx4 %2, %4, %5 offset:2048 sc1\n\t"
                       "global_load_dwordx4 %3, %4, %5 offset:3072 sc1\n\ts_waitcnt vmcnt(0)"
                       : "=&v"(v[0]), "=&v"(v[1]), "=&v"(v[2]), "=&v"(v[3]) : "v"(lane * 16), "s"(box + QK_OFF + u * 4096) : "memory");
          unsigned bad = 0;
#pragma unroll
          for (int t = 0; t < 4; ++t) bad |= (v[t][0] ^ v[t][1] ^ v[t][2] ^ ta) | (v[t][3] ^ tb);
          if (!__any(bad != 0u)) break;
        }
        TSET(tqa);
#pragma unroll
        for (int t = 0; t < 4; ++t)
          *(LDS_AS(u32x2)*)(L0 + IMG_A + (4 * u + t) * 512 + ((g >> 1) * 16 + r) * 16 + (g & 1) * 8) = u32x2{v[t][0], v[t][1]};
        asm volatile("" ::: "memory");
        lds_add(ct + C_QI, 1u);
        lds_st32(ct + C_EP, epoch);
      }
      static_assert(NE == 4 && NPK == 6 && NSLOT == 10, "");
      s_items(sc, helper ? TPW : c, helper || c >= NEARLY);
      static_assert(NH1 == 3 && NS == 7 && TPW == 33, "");
      const int hi = c == 0 ? 5 : c - 2;
      if (hi >= (helper ? 4 : 0)) s_half(sc, TPW - NH1 + (hi >> 1), hi & 1);
      TNOW(tse);
      rslot = c;
    }
  }

  if (rslot >= 0 && ((blockIdx.x >> 3) & 3) == 1) {
    LANE_VARS
    __builtin_amdgcn_s_setprio(0);
    const int rid = ((blockIdx.x >> 5) << 3) | (blockIdx.x & 7);
    const int qa = ((rid >> 1) & 3) * 4 + ((rid >> 3) & 1) * 2;
    const char* src = box + (size_t)(rid * RSTR + rslot * 32) * 128;
    const int e = lane & 7;
    const int oidx = (qa + (e & 1)) * CC + 48 * (rid >> 4) + 16 * (e < 6 ? e >> 1 : 0) + 8 * (rid & 1) + (lane >> 3);
    const unsigned short qv = ((const gus*)q)[oidx];
    u32x4 am;
#pragma unroll
    for (int d = 0; d < 4; ++d) am[d] = (e >> 1) == d ? ((e & 1) ? 0xffff0000u : 0x0000ffffu) : 0u;
    const unsigned epoch = *(LDS_AS(unsigned)*)(L0 + CT_OFF + C_EP);
    const unsigned arf = lbase + RED_OFF + 4096 + (lane & 1) * 4;
    poll_ge_sleep(ct + C_FREE + ((TPW - NPK) % NSLOT) * 4, 8u * ((TPW - NPK) / NSLOT + 1));
    lds_st32(arf + rslot * 8, 0u);
    unsigned long long tr;
#define R_LD(d, m, o) "s_mov_b64 exec, s[" m "]\n\tglobal_load_dwordx4 v[" d "], %[vo], %[src] offset:" #o " sc1\n\t"
#define R_W(l, a0, a1, a2, a3) "v_cvt_pk_bf16_f32 v" #a3 ", v" #a3 ", v" #a3 "\n\tv_and_b32 v" #a0 ", v60, v" #a3 "\n\tv_and_b32 v" #a1 ", v61, v" #a3 "\n\t" \
                               "v_and_b32 v" #a2 ", v62, v" #a3 "\n\tv_and_b32 v" #a3 ", v63, v" #a3 "\n\t"
#define R_G(n, w, m, l, d0, d1, d2, d3, a0, a1, a2, a3, c) \
  "s_mov_b64 exec, s[" m "]\n\ts_cbranch_execz " #n "f\n\ts_waitcnt vmcnt(" #w ")\n\tv_bitop3_b32 v" #l ", v" #d0 ", v" #d1 ", v" #d2 " bitop3:0x96\n\t" \
  "v_bitop3_b32 v" #l ", v" #l ", v" #d3 ", %[tag] bitop3:0x96\n\tv_mov_b32 v" #d3 ", s49\n\ts_mov_b64 exec, -1\n\tv_exp_f32 v" #a3 ", v" #l "\n\t" \
  "v_xor_b32_dpp v59, v" #l ", v" #l " quad_perm:[2,3,0,1] row_mask:0xf bank_mask:0x5\n\tv_xor_b32_dpp v59, v" #l ", v" #l " row_shr:4 row_mask:0xf bank_mask:0xa\n\t" \
  R_W(l, a0, a1, a2, a3) "v_cmp_ne_u32 s[" m "], 0, v59\n\ts_nop 0\n" \
  "1" #n ":\n\tv_mfma_f32_16x16x32_bf16 v[36:39], v[" #a0 ":" #a3 "], v[" #d0 ":" #d3 "], " c "\n\t"
#define R_X(n) #n ":\n\ts_mov_b64 exec, -1\n\ts_nop 2\n\ts_branch 1" #n "b\n"
#define R_F(l, d0, d3, a0, a1, a2, a3, c) "v_sub_f32 v" #a3 ", v" #l ", v56\n\tv_exp_f32 v" #a3 ", v" #a3 "\n\ts_nop 0\n\t" R_W(l, a0, a1, a2, a3) \
  "s_nop 1\n\tv_mfma_f32_16x16x32_bf16 v[36:39], v[" #a0 ":" #a3 "], v[" #d0 ":" #d3 "], " c "\n\t"
#define R_RD(d, a, b) "ds_read2st64_b32 v[" d "], %[ard] offset0:" #a " offset1:" #b "\n\t"
#define R_A(d, a) "v_pk_add_f32 v[" d "], v[" d "], v[" a "]\n\t"
#define R_S(x, f) "v_sub_f32 v" #f ", v" #f ", v56\n\tv_exp_f32 v" #f ", v" #f "\n\ts_nop 0\n\tv_mul_f32 v[" #x "], v[" #x "], v" #f "\n\tv_mul_f32 v[" #x " + 1], v[" #x " + 1], v" #f "\n\t"
    asm volatile("s_mov_b64 s[52:53], 0\n\ts_mov_b64 s[40:41], -1\n\ts_mov_b64 s[42:43], -1\n\ts_mov_b64 s[44:45], -1\n\ts_mov_b64 s[46:47], -1\n\ts_mov_b32 s48, %[cap]\n\ts_mov_b32 s49, 0x3f803f80\n\t"
                 "v_mov_b32 v57, 1\n\tv_mov_b32 v58, %[x1]\n"
                 "20:\n\ts_sleep 1\n\tds_read_b32 v56, v58\n\ts_sub_u32 s48, s48, 1\n\ts_cselect_b32 s50, 0, %[x1n]\n\ts_waitcnt lgkmcnt(0)\n\t"
                 "v_readfirstlane_b32 s51, v56\n\ts_cmp_lt_u32 s51, s50\n\ts_cbranch_scc1 20b\n\tv_mov_b32 v58, %[red]\n\ts_mov_b32 s48, %[gcap]\n\ts_sleep %[rs]\n"
                 "21:\n\t" R_LD("0:3", "40:41", 0) R_LD("4:7", "42:43", 1024) R_LD("8:11", "44:45", 2048) R_LD("12:15", "46:47", 3072)
                 "s_cmp_eq_u32 s48, %[gcap]\n\ts_cbranch_scc1 24f\n\ts_waitcnt vmcnt(0)\n24:\n\t"
                 R_G(1, 3, "40:41", 32, 0, 1, 2, 3, 16, 17, 18, 19, "0") R_G(2, 2, "42:43", 33, 4, 5, 6, 7, 20, 21, 22, 23, "v[36:39]")
                 R_G(3, 1, "44:45", 34, 8, 9, 10, 11, 24, 25, 26, 27, "v[36:39]") R_G(4, 0, "46:47", 35, 12, 13, 14, 15, 28, 29, 30, 31, "v[36:39]")
                 "v_max3_f32 v56, v32, v33, v34\n\ts_sub_u32 s48, s48, 1\n\ts_cbranch_scc1 22f\n\ts_or_b64 s[50:51], s[40:41], s[42:43]\n\tv_max_f32 v56, v56, v35\n\t"
                 "s_or_b64 s[50:51], s[50:51], s[44:45]\n\ts_or_b64 s[50:51], s[50:51], s[46:47]\n\tv_cmp_nlt_f32 vcc, |v56|, %[lim]\n\ts_cbranch_scc1 21b\n\t"
                 "s_cbranch_vccnz 25f\n"
                 "22:\n\ts_mov_b64 exec, %[wm]\n\tds_write_b128 %[awr], v[36:39]\n\ts_mov_b64 exec, 1\n\tds_add_rtn_u32 v59, v58, v57\n\ts_mov_b64 exec, -1\n\t"
                 R_RD("40:41", 0, 1) R_RD("42:43", 2, 3) R_RD("44:45", 4, 5) R_RD("46:47", 6, 7) R_RD("48:49", 8, 9) R_RD("50:51", 10, 11) R_RD("52:53", 12, 13) R_RD("54:55", 14, 15)
                 "s_waitcnt lgkmcnt(8)\n\tv_add_u32 v59, v59, v57\n\ts_nop 0\n\tv_readfirstlane_b32 s50, v59\n\ts_cmp_lg_u32 s50, 8\n\ts_cbranch_scc1 26f\n"
                 "23:\n\ts_waitcnt lgkmcnt(6)\n\t" R_A("40:41", "42:43") "s_waitcnt lgkmcnt(4)\n\t" R_A("44:45", "46:47") "s_waitcnt lgkmcnt(2)\n\t" R_A("48:49", "50:51")
                 R_A("40:41", "44:45") "s_waitcnt lgkmcnt(0)\n\t" R_A("52:53", "54:55") R_A("48:49", "52:53") R_A("40:41", "48:49")
                 "v_add_f32 v40, v40, v41\n\ts_nop 1\n\tv_mov_b32_dpp v41, v40 quad_perm:[2,3,2,3] row_mask:0xf bank_mask:0xf\n\ts_nop 1\n\t"
                 "v_mov_b32_dpp v41, v41 row_shl:4 row_mask:0xf bank_mask:0x5\n\tv_rcp_f32 v41, v41\n\ts_mov_b64 exec, %[om]\n\tv_mul_f32 v40, v40, v41\n\t"
                 "v_cvt_pk_bf16_f32 v40, v40, v40\n\tv_lshlrev_b32 v40, 16, v40\n\tv_add_f32 v40, v40, %[qf]\n\tv_cvt_pk_bf16_f32 v40, v40, v40\n\t"
                 "global_store_short %[oo], v40, %[out]\n\t"
                 "s_cmp_lg_u32 %[rid], 0\n\ts_cbranch_scc1 29f\n\ts_mov_b64 exec, 1\n\tv_mov_b32 v41, %[eo]\n\tglobal_store_dword v41, %[ep], %[bx] sc1\n\ts_branch 29f\n"
                 R_X(1) R_X(2) R_X(3) R_X(4)
                 "25:\n\tv_max_f32_dpp v56, v56, v56 row_ror:8 row_mask:0xf bank_mask:0xf\n\tv_mov_b32 v59, v56\n\ts_nop 1\n\tv_permlane16_swap_b32 v56, v59\n\tv_max_f32 v56, v56, v59\n\t"
                 "v_mov_b32 v59, v56\n\ts_nop 1\n\tv_permlane32_swap_b32 v56, v59\n\tv_max_f32 v56, v56, v59\n\tv_mov_b32 v57, 0x101\n\tv_add_u32 v59, %[rs8], %[arf]\n\tds_write_b32 v59, v56\n\t"
                 R_F(32, 0, 3, 16, 17, 18, 19, "0") R_F(33, 4, 7, 20, 21, 22, 23, "v[36:39]") R_F(34, 8, 11, 24, 25, 26, 27, "v[36:39]") R_F(35, 12, 15, 28, 29, 30, 31, "v[36:39]")
                 "s_nop 7\n\ts_branch 22b\n"
                 "26:\n\ts_waitcnt vmcnt(0)\n\ts_and_b32 s50, s50, 0xff\n\ts_cmp_lg_u32 s50, 8\n\ts_cbranch_scc1 29f\n\t"
                 "ds_read2_b32 v[0:1], %[arf] offset1:2\n\tds_read2_b32 v[2:3], %[arf] offset0:4 offset1:6\n\tds_read2_b32 v[4:5], %[arf] offset0:8 offset1:10\n\t"
                 "ds_read2_b32 v[6:7], %[arf] offset0:12 offset1:14\n\ts_waitcnt lgkmcnt(0)\n\tv_max3_f32 v56, v0, v1, v2\n\tv_max3_f32 v56, v56, v3, v4\n\t"
                 "v_max3_f32 v56, v56, v5, v6\n\tv_max_f32 v56, v56, v7\n\t"
                 R_S(40, 0) R_S(42, 1) R_S(44, 2) R_S(46, 3) R_S(48, 4) R_S(50, 5) R_S(52, 6) R_S(54, 7) "s_branch 23b\n"
                 "29:\n\ts_mov_b64 exec, -1\n\ts_waitcnt lgkmcnt(0)"
                 : "=&{s[52:53]}"(tr)
                 : "{v[60:63]}"(am), [vo] "v"(lane * 16), [src] "s"(src), [tag] "v"(line_tag(epoch, e)),
                   [awr] "v"(lbase + RED_OFF + (rslot * 2 + (r >> 3)) * 256 + (r & 7) * 32 + (g & 1) * 16), [ard] "v"(lbase + RED_OFF + lane * 4), [arf] "v"(arf),
                   [qf] "v"((unsigned)qv << 16), [oo] "v"(oidx * 2), [out] "s"(KARG(kb, 0)), [ep] "v"(epoch + 1u), [bx] "s"(box), [rid] "s"(rid), [rs8] "s"(rslot * 8),
                   [wm] "s"(0xff00ff0000ff00ffull), [om] "s"(0x3f3f3f3f3f3f3f3full), [lim] "s"(64.f),
                   [cap] "i"(SPIN_CAP), [gcap] "i"(GSPIN_CAP), [x1] "i"(CT_OFF + C_X1), [x1n] "i"(NE), [red] "i"(CT_OFF + C_RED), [rs] "i"(RSLEEP), [eo] "i"(EPOCH_OFF)
                 : "memory", "scc", "vcc", "s40", "s41", "s42", "s43", "s44", "s45", "s46", "s47", "s48", "s49", "s50", "s51", "v0", "v1", "v2", "v3", "v4", "v5", "v6", "v7", "v8", "v9", "v10", "v11", "v12", "v13", "v14", "v15", "v16", "v17", "v18", "v19", "v20", "v21", "v22", "v23", "v24", "v25", "v26", "v27", "v28", "v29", "v30", "v31", "v32", "v33", "v34", "v35", "v36", "v37", "v38", "v39", "v40", "v41", "v42", "v43", "v44", "v45", "v46", "v47", "v48", "v49", "v50", "v51", "v52", "v53", "v54", "v55", "v56", "v57", "v58", "v59");
    wait_vm<0>();
    if (blockIdx.x == 8) while ((unsigned)__builtin_amdgcn_s_memrealtime() - tent < 2700 + 4 * dq) __builtin_amdgcn_s_sleep(1);
  }
}

#undef q
#undef f0
#undef f1
#undef wq
#undef wk
#undef wv
#undef wo
#undef box
extern "C" __global__ void dino_end() {}

extern "C" void kernel_launch(void* const* d_in, const int* in_sizes, int n_in,
                              void* d_out, int out_size, void* d_ws, size_t ws_size,
                              hipStream_t stream) {
  dino_main<<<NWG, 768, 0, stream>>>((const unsigned short*)d_in[0], (const char*)d_in[1], (const char*)d_in[2], (const char*)d_in[3],
                                     (const char*)d_in[4], (const char*)d_in[5], (const char*)d_in[6], (char*)d_ws + ((ws_size - (4u << 20)) & ~(size_t)0x1fffff),
                                     (unsigned short*)d_out);
}
```

```cpp
#include <hip/hip_runtime.h>
#include <hip/hip_bf16.h>
#include <stdint.h>

#define PROFOFF (380ll << 20)
#define TNOW(v) do {} while (0)
#define TPUT(id, v) do {} while (0)

using bf16x8  = __attribute__((ext_vector_type(8))) short;
using f32x4   = __attribute__((ext_vector_type(4))) float;
using f32x2   = __attribute__((ext_vector_type(2))) float;
using u32x4   = __attribute__((ext_vector_type(4))) unsigned;
using u32x2   = __attribute__((ext_vector_type(2))) unsigned;
using v4i16_t = __attribute__((ext_vector_type(4))) short;
typedef __attribute__((address_space(3))) char* lds_ptr;
#define LDS_AS(T) __attribute__((address_space(3))) T

constexpr int CC = 192;
constexpr int NWG = 256, NS = 7, NE = 4;
constexpr int TPW = 33;
constexpr int ROWB = 384;
constexpr int SLOT = 40 * ROWB;
constexpr int NSLOT = 10;
constexpr int RSTR = 322;
constexpr int EPOCH_OFF = 64 * RSTR * 128, QK_OFF = EPOCH_OFF + 4096, W2F_OFF = QK_OFF + 12288, W2_OFF = W2F_OFF + 1024;
static_assert(W2_OFF + 72 * 1024 <= (4 << 20), "");
constexpr int GSPIN_CAP = 1 << 16;
#define PACE16 777
#define PACES 810
#define PK1 14
#define PWIN 35
#define AHI 22
#define ADL 60
#define AMG 200
#define T0BACK 7
#define HDUE 93
#define QKW0 200
#define QKW1 3
#define RSLEEP 25
#define W2SCH 0x0311180000000000ull
constexpr int NEARLY = 4;
constexpr int NH1 = 3;
constexpr int NPK = 6;
constexpr float EPS = 1e-5f;
constexpr float SQRT_C = 13.856406460551018f;
constexpr float LOG2E = 1.4426950408889634f;
constexpr float THR = 20.0f;
constexpr float TOFF = 192.0f;
constexpr int SPIN_CAP = 1 << 22;

constexpr int IMG_A = NSLOT * SLOT;
constexpr int CT_OFF = IMG_A + 6144;
constexpr int PKM = CT_OFF + 512, PKU = PKM + NPK * 128;
constexpr int ONES_OFF = PKU + NPK * 256;
constexpr int JUNK = ONES_OFF + ROWB;
constexpr int LDS_BYTES = JUNK + 256;
constexpr int H_R = 192 * ROWB, H_Q = H_R + 32 * ROWB, H_C = H_Q + 6144;
static_assert(LDS_BYTES <= 163840 && (PKU & 15) == 0 && (ONES_OFF & 127) == 0, "lds");
constexpr int C_FULL = 0, C_FREE = 64, C_EP = 128, C_RED = 144, C_ISS = 152, C_X1 = 156, C_QI = 160, C_PFL = 192, C_CONS = 256,
              C_MREF = 320, C_LW = 384;

typedef __bf16 bf16x2_t __attribute__((ext_vector_type(2)));
__device__ __forceinline__ unsigned cvtpk(float lo, float hi) {
  return __builtin_bit_cast(unsigned, __builtin_convertvector(f32x2{lo, hi}, bf16x2_t));
}
__device__ __forceinline__ float bflo(unsigned p) { return __uint_as_float(p << 16); }
__device__ __forceinline__ float bfhi(unsigned p) { return __uint_as_float(p & 0xffff0000u); }
__device__ __forceinline__ bf16x8 pack8(const f32x4& a, const f32x4& b) {
  return __builtin_bit_cast(bf16x8, u32x4{cvtpk(a[0], a[1]), cvtpk(a[2], a[3]), cvtpk(b[0], b[1]), cvtpk(b[2], b[3])});
}
__device__ __forceinline__ float xrow16_max(float x) {
  auto s = __builtin_amdgcn_permlane16_swap(__float_as_uint(x), __float_as_uint(x), false, false);
  x = fmaxf(__uint_as_float(s[0]), __uint_as_float(s[1]));
  auto t = __builtin_amdgcn_permlane32_swap(__float_as_uint(x), __float_as_uint(x), false, false);
  return fmaxf(__uint_as_float(t[0]), __uint_as_float(t[1]));
}
__device__ __forceinline__ float xrow16_sum(float x) {
  auto s = __builtin_amdgcn_permlane16_swap(__float_as_uint(x), __float_as_uint(x), false, false);
  x = __uint_as_float(s[0]) + __uint_as_float(s[1]);
  auto t = __builtin_amdgcn_permlane32_swap(__float_as_uint(x), __float_as_uint(x), false, false);
  return __uint_as_float(t[0]) + __uint_as_float(t[1]);
}
__device__ __forceinline__ v4i16_t tr_read(lds_ptr p) {
  return __builtin_amdgcn_ds_read_tr16_b64_v4i16((LDS_AS(v4i16_t)*)p);
}
__device__ __forceinline__ f32x4 mfma(bf16x8 a, bf16x8 b, f32x4 c) {
  return __builtin_amdgcn_mfma_f32_16x16x32_bf16(a, b, c, 0, 0, 0);
}
__device__ __forceinline__ int swz(int p) { return (p & 6) | ((p >> 2) & 1); }
constexpr unsigned swz_tbl(int i) {
  unsigned t = 0;
  for (int j = 0; j < 8; ++j) { const int p = (8 * i + j) / 3; t |= (unsigned)((p & 6) | ((p >> 2) & 1)) << (4 * j); }
  return t;
}
__device__ __forceinline__ bf16x8 mk8(v4i16_t a, v4i16_t b) { return bf16x8{a[0], a[1], a[2], a[3], b[0], b[1], b[2], b[3]}; }

__device__ __forceinline__ void lds_st32(unsigned addr, unsigned v) {
  asm volatile("ds_write_b32 %0, %1" : : "v"(addr), "v"(v) : "memory");
}
__device__ __forceinline__ unsigned lds_ld32(unsigned addr) {
  unsigned v;
  asm volatile("ds_read_b32 %0, %1\n\ts_waitcnt lgkmcnt(0)" : "=v"(v) : "v"(addr) : "memory");
  return __builtin_amdgcn_readfirstlane(v);
}
__device__ __forceinline__ void lds_add(unsigned addr, unsigned n) {
  asm volatile("s_mov_b64 exec, 1\n\tds_add_u32 %0, %1\n\ts_mov_b64 exec, -1\n\ts_nop 3" : : "v"(addr), "v"(n) : "memory");
}
__device__ __forceinline__ void poll_ge_sleep(unsigned addr, unsigned need) {
  need = __builtin_amdgcn_readfirstlane(need);
  unsigned v, s, n2, cnt = SPIN_CAP;
  asm volatile("s_branch 2f\n1:\n\ts_sleep 2\n2:\n\tds_read_b32 %0, %4\n\ts_sub_u32 %3, %3, 1\n\ts_cselect_b32 %2, 0, %5\n\ts_waitcnt lgkmcnt(0)\n\t"
               "v_readfirstlane_b32 %1, %0\n\ts_cmp_lt_u32 %1, %2\n\ts_cbranch_scc1 1b"
               : "=&v"(v), "=&s"(s), "=&s"(n2), "+s"(cnt) : "v"(addr), "s"(need) : "memory", "scc");
}
__device__ __forceinline__ bf16x8 img_ld(lds_ptr L0, int off, int kk, int lane) { return *(LDS_AS(bf16x8)*)(L0 + off + kk * 1024 + lane * 16); }
__device__ __forceinline__ void img_st(lds_ptr L0, int off, int kk, int lane, bf16x8 v) { *(LDS_AS(bf16x8)*)(L0 + off + kk * 1024 + lane * 16) = v; }

template <int N> __device__ __forceinline__ void wait_vm() { asm volatile("s_waitcnt vmcnt(%0)" :: "i"(N) : "memory"); }
constexpr int A_IMG = ((TPW + 1 - NPK) % NSLOT) * SLOT, RED_OFF = ((TPW - NPK) % NSLOT) * SLOT;
__device__ __forceinline__ unsigned line_tag(unsigned epoch, unsigned i) {
  unsigned x = (epoch + 1u + i * 0x9E3779B9u) * 0x85EBCA6Bu;
  x ^= x >> 15; x *= 0xC2B2AE35u;
  return x ^ (x >> 13);
}
__device__ __forceinline__ unsigned epoch_load(const char* box) {
  unsigned e;
  asm volatile("global_load_dword %0, %1, %2 sc1\n\ts_waitcnt vmcnt(0)" : "=v"(e) : "v"(EPOCH_OFF), "s"(box) : "memory");
  return __builtin_amdgcn_readfirstlane(e);
}
__device__ __forceinline__ int tr_off(int row, int col4) {
  const int ch = col4 >> 1;
  return row * ROWB + (((ch & ~7) | ((ch & 7) ^ swz(row & 15))) << 4) + 8 * (col4 & 1);
}
#define PIN3(a, i) "+v"(a[i]), "+v"(a[i + 1]), "+v"(a[i + 2])

#define DMA1(d, b, off, pol) "global_load_lds_dwordx4 %[" #d "], %[" #b "] offset:" #off pol "\n\t"
__device__ __forceinline__ void issue_rows16(const char* a0, unsigned dst, const unsigned (&d)[3]) {
  unsigned keep;
  asm volatile("s_mov_b32 %[keep], m0\n\ts_mov_b32 m0, %[dst]\n\ts_nop 0\n\t"
               DMA1(d0, a, 0, "") DMA1(d1, a, 1024, "") DMA1(d2, a, 2048, "") DMA1(d0, a, 3072, "")
               "s_add_u32 m0, %[dst], 0x1000\n\ts_nop 0\n\t" DMA1(d1, b, 0, "") DMA1(d2, b, 1024, "") "s_mov_b32 m0, %[keep]"
               : [keep] "=&s"(keep) : [dst] "s"(dst), [a] "s"(a0), [b] "s"(a0 + 4096), [d0] "v"(d[0]), [d1] "v"(d[1]), [d2] "v"(d[2]) : "memory", "scc");
}
#define LD_VM "s_getreg_b32 s43, hwreg(HW_REG_IB_STS, 22, 2)\n\ts_getreg_b32 s42, hwreg(HW_REG_IB_STS, 0, 4)\n\ts_lshl4_add_u32 s42, s43, s42\n\t"
#define LD_GATE(n, X) \
  #n "0:\n\t" LD_VM "s_cmp_le_i32 s42, s46\n\ts_cbranch_scc1 " #n "2f\n" \
  #n "1:\n\t" X "s_cmp_gt_u32 s42, %[win]\n\ts_cbranch_scc1 " #n "5f\n\ts_cmp_ge_u32 s45, s44\n\ts_cbranch_scc1 " #n "7f\n" \
  #n "6:\n\ts_memrealtime s[40:41]\n\ts_sleep 1\n\ts_waitcnt lgkmcnt(0)\n\ts_sub_u32 s45, s40, %[t0]\n\ts_lshl_b32 s45, s45, 4\n\ts_cmp_lt_u32 s45, s44\n\ts_cbranch_scc1 " #n "6b\n" \
  #n "7:\n\t" LD_VM
#define LD_NEXT \
  "s_cmp_gt_u32 s42, %[ahi]\n\ts_cselect_b32 s42, %[amg], 0\n\ts_sub_u32 s43, s45, s44\n\ts_cmp_lt_u32 s43, s42\n\ts_cselect_b32 s42, %[p3] + %[adl], s39\n\t" \
  "s_add_u32 s44, s44, s42\n\ts_add_u32 s46, s46, 5\n\t"
#define LD_PUB0(n, a, b, c) \
  #n "2:\n\tv_add_u32 %[pub], 2, %[pub]\n\ts_cmp_ge_u32 s46, 30\n\ts_cbranch_scc1 " #n "4f\n\ts_cmp_ge_u32 s46, 15\n\ts_cbranch_scc1 " #n "3f\n\t" \
  "s_waitcnt vmcnt(" #a ")\n" #n "3:\n\ts_waitcnt vmcnt(" #b ")\n" #n "4:\n\ts_waitcnt vmcnt(" #c ")\n\tds_write_b32 %[af], %[pub] ; NOTRACK\n\t" \
  "s_sub_u32 s46, s46, 15\n\ts_branch " #n "1b\n"
#define LD_PUB(n, a, b, c) LD_PUB0(n, a, b, c) \
  #n "5:\n\ts_sleep 1\n\ts_sub_i32 s47, s47, 1\n\ts_cmp_gt_i32 s47, 0\n\ts_cbranch_scc1 " #n "0b\n\ts_waitcnt vmcnt(%[win])\n\ts_branch " #n "6b\n"
#define LD1(d, b, off) "global_load_lds_dwordx4 %[" #d "], s[" #b "] offset:" #off " nt\n\t"

#define PV_REQ(P, U, M, C, CA) \
  "v_lshl_add_u32 v150, s44, 3, %[apk]\n\tv_lshl_add_u32 v151, s44, 1, %[apu]\n\tv_add_u32 v152, s44, %[apm]\n\t" \
  "ds_read_b32 v" #C ", v" #CA "\n\tds_read_b128 v[" P "], v150\n\tds_read_b64 v[" M "], v152\n\tds_read_b128 v[" U "], v151\n\t"
#define PV_TR(d0, d1, a, o) "ds_read_b64_tr_b16 v[" #d0 ":" #d1 "], v" #a " offset:" #o "\n\t"
#define PV_FETCH(y0, y0b, a0, a0b, b0, b0b, y1, y1b, a1, a1b, b1, b1b, y2, y2b, a2, a2b, b2, b2b) \
  "v_add_u32 v153, s45, %[ox]\n\tv_add_u32 v154, s45, %[oy]\n\t" \
  PV_TR(a0, a0b, 153, 0) PV_TR(b0, b0b, 153, 6144) PV_TR(a1, a1b, 153, 128) PV_TR(b1, b1b, 153, 6272) PV_TR(a2, a2b, 153, 256) PV_TR(b2, b2b, 153, 6400) \
  PV_TR(y0, y0b, 154, 0) PV_TR(y1, y1b, 154, 128) PV_TR(y2, y2b, 154, 256) \
  "s_add_u32 s45, s45, %[slot]\n\ts_cmp_eq_u32 s45, %[ring]\n\ts_cselect_b32 s45, 0, s45\n\t"
#define PV_FETCH_A PV_FETCH(90, 91, 92, 93, 94, 95, 96, 97, 98, 99, 100, 101, 102, 103, 104, 105, 106, 107)
#define PV_FETCH_B PV_FETCH(108, 109, 110, 111, 112, 113, 114, 115, 116, 117, 118, 119, 120, 121, 122, 123, 124, 125)
#define PV_MF(d, a, p) "v_mfma_f32_16x16x32_bf16 v[" d "], v[" a "], v[" p "], v[" d "]\n\t"
#define PV_MOV(d, s) "v_mov_b32 v" #d ", v" #s "\n\t"
#define PV_WAITF(n, CA)   \
  #n ":\n\tds_read_b32 v155, v" #CA "\n\ts_sub_i32 s47, s47, 1\n\ts_cmp_lt_i32 s47, 0\n\ts_cbranch_scc1 " #n "9f\n\ts_waitcnt lgkmcnt(0)\n\t" \
  "v_cmp_lt_u32 vcc, v155, %[need]\n\ts_and_b64 vcc, vcc, 2\n\ts_cbranch_scc0 " #n "9f\n\ts_sleep 2\n\ts_branch " #n "b\n" #n "9:\n\ts_waitcnt lgkmcnt(0)\n\t"
#define PV_STEP(n, P, U, M, m0, m1, C, CA, P2, U2, M2, C2, CA2, X0, X1, X2, Y0, Y1, Y2, MV0, MV1, MV2, HOOK) \
  "s_waitcnt lgkmcnt(10)\n\t" \
  "v_cmp_lt_u32 vcc, v" #C ", %[need]\n\tv_cmp_neq_f32 s[48:49], v" #m0 ", v88\n\tv_cmp_neq_f32 s[50:51], v" #m1 ", v88\n\t" \
  "v_add_u32 v" #CA2 ", 4, v" #CA "\n\ts_or_b64 s[48:49], s[48:49], vcc\n\tv_cmp_eq_u32 s[52:53], v" #CA2 ", %[lim]\n\t" \
  "s_or_b64 s[48:49], s[48:49], s[50:51]\n\ts_cbranch_scc1 " #n "1f\n" \
  #n "0:\n\ts_add_u32 s44, s44, 128\n\ts_cmp_eq_u32 s44, %[pring]\n\ts_cselect_b32 s44, 0, s44\n\t" \
  "v_cndmask_b32 v" #CA2 ", v" #CA2 ", %[base], s[52:53]\n\t" \
  PV_MF("84:87", U, P) PV_MF("72:75", X0, P) PV_MF("76:79", X1, P) MV0 PV_MF("80:83", X2, P) MV1 \
  PV_MF("72:75", Y0, P) MV2 HOOK PV_REQ(P2, U2, M2, C2, CA2) PV_MF("76:79", Y1, P) PV_MF("80:83", Y2, P)
#define PV_REL(CA) \
  "s_mov_b64 exec, 3\n\tds_add_u32 v" #CA ", %[one] offset:64\n\ts_mov_b64 exec, -1\n\tv_add_u32 %[need], 2, %[need]\n\ts_add_u32 s46, s46, 1\n\t"
#define PV_SC1(p) "v_lshlrev_b32 v151, 16, v" #p "\n\tv_and_b32 v152, 0xffff0000, v" #p "\n\tv_mul_f32 v151, v151, v150\n\tv_mul_f32 v152, v152, v150\n\t" \
                  "v_cvt_pk_bf16_f32 v" #p ", v151, v152\n\t"
#define PV_MUL4(a, b, c, d) "v_mul_f32 v" #a ", v" #a ", v150\n\tv_mul_f32 v" #b ", v" #b ", v150\n\tv_mul_f32 v" #c ", v" #c ", v150\n\tv_mul_f32 v" #d ", v" #d ", v150\n\t"
#define PV_RESC(m0, m1, p0, p1, p2, p3) \
  "v_mov_b32 v150, v88\n\tv_max3_f32 v88, v88, v" #m0 ", v" #m1 "\n\tv_sub_f32 v150, v150, v88\n\tv_exp_f32 v150, v150\n\ts_nop 1\n\t" \
  PV_MUL4(72, 73, 74, 75) PV_MUL4(76, 77, 78, 79) PV_MUL4(80, 81, 82, 83) PV_MUL4(84, 85, 86, 87) \
  "v_sub_f32 v150, v" #m0 ", v88\n\tv_exp_f32 v150, v150\n\ts_nop 1\n\t" PV_SC1(p0) PV_SC1(p1) \
  "v_sub_f32 v150, v" #m1 ", v88\n\tv_exp_f32 v150, v150\n\ts_nop 1\n\t" PV_SC1(p2) PV_SC1(p3)
#define PV_SLOW(n, P, U, M, m0, m1, C, CA, p0, p1, p2, p3) \
  #n "1:\n\ts_bitcmp0_b32 vcc_lo, 0\n\ts_cbranch_scc1 " #n "4f\n\ts_cmp_gt_u32 s46, %[tight]\n\ts_cbranch_scc1 " #n "3f\n" \
  #n "2:\n\ts_sleep 2\n\tds_read_b32 v" #C ", v" #CA "\n\ts_sub_i32 s47, s47, 1\n\ts_cmp_lt_i32 s47, 0\n\ts_cbranch_scc1 " #n "3f\n\ts_waitcnt lgkmcnt(0)\n\t" \
  "v_cmp_lt_u32 vcc, v" #C ", %[need]\n\ts_and_b64 vcc, vcc, 1\n\ts_cbranch_scc1 " #n "2b\n" \
  #n "3:\n\t" PV_REQ(P, U, M, C, CA) "s_sub_i32 s47, s47, 1\n\ts_cmp_lt_i32 s47, 0\n\ts_cselect_b32 s54, 0, 1\n\ts_waitcnt lgkmcnt(0)\n\t" \
  "v_cmp_lt_u32 vcc, v" #C ", %[need]\n\tv_cmp_neq_f32 s[48:49], v" #m0 ", v88\n\tv_cmp_neq_f32 s[50:51], v" #m1 ", v88\n\t" \
  "s_and_b32 s54, s54, vcc_lo\n\ts_cbranch_scc1 " #n "3b\n\ts_or_b64 s[48:49], s[48:49], s[50:51]\n\ts_cbranch_scc0 " #n "0b\n\ts_branch " #n "5f\n" \
  #n "4:\n\tv_cmp_neq_f32 s[48:49], v" #m0 ", v88\n\ts_or_b64 s[48:49], s[48:49], s[50:51]\n\ts_cbranch_scc0 " #n "0b\n" \
  #n "5:\n\t" PV_RESC(m0, m1, p0, p1, p2, p3) \
  "s_branch " #n "0b\n"
#define PV_W2(a0, a1, b0, b1, c0, c1) \
  "global_load_dwordx4 v[" #a0 ":" #a1 "], v150, %[w2] sc1\n\tglobal_load_dwordx4 v[" #b0 ":" #b1 "], v150, %[w2] offset:1024 sc1\n\t" \
  "global_load_dwordx4 v[" #c0 ":" #c1 "], v150, %[w2] offset:2048 sc1\n\tv_add_u32 v150, 0xc00, v150\n\ts_sub_u32 s54, s54, 1\n\ts_cbranch_scc1 30b\n\t"

#define PV_PK(a, b) "v_pk_fma_f32 v[" #a ":" #b "], v[" #a ":" #b "], v[150:151], v[150:151] op_sel:[0,0,1] op_sel_hi:[1,0,1]\n\t"
#define PV_NRM(a, b, c, d, o)   \
  "v_pk_fma_f32 v[" #a ":" #b "], v[" #a ":" #b "], v[150:151], v[150:151] op_sel:[0,0,1] op_sel_hi:[1,0,1]\n\t" \
  "v_pk_fma_f32 v[" #c ":" #d "], v[" #c ":" #d "], v[150:151], v[150:151] op_sel:[0,0,1] op_sel_hi:[1,0,1]\n\t" \
  "v_cvt_pk_bf16_f32 v" #a ", v" #a ", v" #b "\n\tv_cvt_pk_bf16_f32 v" #b ", v" #c ", v" #d "\n\tds_write_b64 v120, v[" #a ":" #b "] offset:" #o "\n\t"
#define PV_G(n, a, w0, w1, w2, c0, c1, c2) "s_waitcnt lgkmcnt(" #n ")\n\tv_mfma_f32_16x16x32_bf16 v[72:75], v[" a "], v[" w0 "], " c0 "\n\t" \
  "v_mfma_f32_16x16x32_bf16 v[76:79], v[" a "], v[" w1 "], " c1 "\n\tv_mfma_f32_16x16x32_bf16 v[80:83], v[" a "], v[" w2 "], " c2 "\n\t"
#define PV_Z4(a, b, c, d) "v_mov_b32 v" #a ", 0\n\tv_mov_b32 v" #b ", 0\n\tv_mov_b32 v" #c ", 0\n\tv_mov_b32 v" #d ", 0\n\t"
#define PV_T(n)

using u32x8 = __attribute__((ext_vector_type(8))) unsigned;
struct SCtx {
  lds_ptr L0;
  unsigned ct, apk, apm, api, amr;
  int o_rb, o_yb, ev;
  u32x8 q[3];
  unsigned tq;
};
__device__ __forceinline__ bf16x8 s_qf(const SCtx& c, int j) {
  const u32x8& v = c.q[j >> 1];
  const int o = 4 * (j & 1);
  return __builtin_bit_cast(bf16x8, u32x4{v[o], v[o + 1], v[o + 2], v[o + 3]});
}
#define S_M(d, a, b) "v_mfma_f32_16x16x32_bf16 v[" d "], v[" a "], v[" b "], v[" d "]\n\t"
#define S_M0(d, a, b) "v_mfma_f32_16x16x32_bf16 v[" d "], v[" a "], v[" b "], 0\n\t"
#define S_R(d, a, o) "ds_read_b128 v[" d "], v" #a " offset:" #o "\n\t"
#define S_W(n) "s_waitcnt lgkmcnt(" #n ")\n\t"
#define S_D " row_mask:0xf bank_mask:0xf\n\t"
#define S_3(x, y) S_M("100:103", x, y) S_M("104:107", x, x) S_M("96:99", y, y)
#define S_SQ(g, h) "v_fmac_f32 v" #g ", 2.0, v" #h "\n\t"
#define S_SQD(g, h) "v_fmac_f32_dpp v" #g ", v" #h ", %[two] quad_perm:[0,0,2,2]" S_D
#define S_U(u, h, y) "v_add_f32_dpp v" #u ", v" #h ", v" #y " quad_perm:[1,1,3,3]" S_D
#define S_C(d, a, b, m) "v_cndmask_b32 v" #d ", v" #a ", v" #b ", %[" #m "]\n\t"
#define S_B(d, n) "v_mov_b32_dpp v" #d ", v157 row_newbcast:" #n S_D
#define S_BASE(d, u, t) "v_pk_fma_f32 v[" d "], v[" u "], v[116:117], v[" t "] op_sel:[0,1,0] op_sel_hi:[1,1,0]\n\t"
#define S_LG(d, r) "v_pk_fma_f32 v[" d "], v[" d "], v[" r "], %[toff] op_sel_hi:[1,1,0]\n\t"
#define S_SUB(p, d) "v_pk_add_f32 v[" p "], v[" d "], v[150:151] op_sel_hi:[1,0] neg_lo:[0,1] neg_hi:[0,1]\n\t"
#define S_E(p) "v_exp_f32 v" #p ", v" #p "\n\t"
#define S_MUL(p, r) "v_pk_mul_f32 v[" p "], v[" p "], v[" r "]\n\t"
#define S_POS   \
  "s_mul_i32 s50, s42, 26\n\ts_lshr_b32 s50, s50, 8\n\ts_mul_i32 s50, s50, -10\n\ts_add_u32 s50, s50, s42\n\ts_mul_i32 s45, s50, %[slot]\n\ts_mov_b32 s43, s45\n\t"
#define S_POLL(n, a, o, need, back, far) \
  #n ":\n\tds_read_b32 v152, v" #a " offset:" #o "\n\ts_sub_i32 s47, s47, 1\n\ts_cmp_lt_i32 s47, 0\n\ts_waitcnt lgkmcnt(0)\n\ts_cbranch_scc1 " back "\n\t" \
  "v_readfirstlane_b32 s40, v152\n\ts_cmp_ge_u32 s40, " need "\n\ts_cbranch_scc1 " back "\n\ts_sleep 2\n\t" \
  "s_add_u32 s40, s40, " far "\n\ts_cmp_ge_u32 s40, " need "\n\ts_cbranch_scc1 " #n "b\n\ts_sleep 12\n\ts_branch " #n "b\n"
__device__ __forceinline__ void s_items(SCtx& c, int k0, int late) {
  unsigned long long tq;
  asm volatile("s_mov_b32 s47, %[cap]\n\ts_mov_b32 s49, 0\n\ts_mov_b32 s42, %[k0]\n\ts_cmp_lg_u32 %[late], 0\n\ts_cbranch_scc1 7f\n"
               "1:\n\ts_cmp_ge_u32 s42, %[end]\n\ts_cbranch_scc1 9f\n\t"
               S_POS
               "s_mul_i32 s48, s42, 43\n\ts_lshr_b32 s48, s48, 8\n\ts_mul_i32 s46, s48, -6\n\ts_add_u32 s46, s46, s42\n\ts_lshl2_add_u32 s48, s48, %[ns]\n\t"
               "v_add_u32 v144, s45, %[orb]\n\tv_mad_u32_u24 v145, s43, %[ev], %[oyb]\n\ts_lshl2_add_u32 s40, s46, %[pfl]\n\ts_lshl2_add_u32 s41, s50, %[full]\n\t"
               "v_xor_b32 v146, 64, v144\n\tv_xor_b32 v147, 64, v145\n\tv_mov_b32 v148, s40\n\tv_mov_b32 v149, s41\n\t"
               "s_lshl1_add_u32 s44, s42, 2\n\ts_lshl_b32 s46, s46, 7\n\ts_mov_b64 vcc, %[dg]\n\ts_cmp_lt_u32 s49, s44\n\ts_cbranch_scc1 5f\n"
               "2:\n\tds_read_b32 v150, %[amr]\n\tds_read_b32 v151, v148 offset:64\n\t"
               S_R("24:27", 145, 0) S_R("48:51", 144, 0) S_R("28:31", 147, 0) S_R("52:55", 146, 0) S_R("32:35", 145, 128) S_R("56:59", 144, 128)
               S_R("36:39", 147, 128) S_R("60:63", 146, 128) S_R("40:43", 145, 256) S_R("64:67", 144, 256) S_R("44:47", 147, 256) S_R("68:71", 146, 256)
               S_W(10) S_M0("100:103", "48:51", "24:27") S_M0("104:107", "48:51", "48:51") S_R("72:75", 144, 6144) S_R("76:79", 146, 6144) S_M0("96:99", "24:27", "24:27")
               S_W(10) S_M("100:103", "52:55", "28:31") S_M("104:107", "52:55", "52:55") S_R("80:83", 144, 6272) S_R("84:87", 146, 6272) S_M("96:99", "28:31", "28:31")
               S_W(10) S_M("100:103", "56:59", "32:35") S_M("104:107", "56:59", "56:59") S_R("88:91", 144, 6400) S_R("92:95", 146, 6400) S_M("96:99", "32:35", "32:35")
               "s_mov_b64 exec, 1\n\tds_add_u32 v149, %[four] offset:64\n\ts_mov_b64 exec, -1\n\tv_readfirstlane_b32 s51, v151\n\ts_setprio 1\n\ts_nop 0\n\t"
               S_W(11) S_3("60:63", "36:39") S_W(9) S_3("64:67", "40:43") S_W(7) S_3("68:71", "44:47")
               S_W(6) S_M0("108:111", "72:75", "24:27") S_M0("112:115", "72:75", "72:75")
               S_W(5) S_M("108:111", "76:79", "28:31") S_M("112:115", "76:79", "76:79")
               S_W(4) S_M("108:111", "80:83", "32:35")
               "v_mov_b32_dpp v152, v96 quad_perm:[1,1,3,3]" S_D "v_mov_b32_dpp v153, v98 quad_perm:[1,1,3,3]" S_D
               S_M("112:115", "80:83", "80:83") S_C(154, 96, 98, r2) S_SQ(104, 100)
               S_W(3) S_M("108:111", "84:87", "36:39") S_SQD(105, 101) S_SQ(106, 102)
               S_M("112:115", "84:87", "84:87") S_SQD(107, 103) "v_add_f32 v154, %[eps], v154\n\t"
               S_W(2) S_M("108:111", "88:91", "40:43") S_U(128, 100, 152) S_U(129, 101, 152)
               S_M("112:115", "88:91", "88:91") S_U(130, 102, 153) S_U(131, 103, 153)
               S_W(1) S_M("108:111", "92:95", "44:47") S_C(104, 104, 105, r1) S_C(106, 106, 107, r1)
               S_M("112:115", "92:95", "92:95") S_C(155, 128, 129, r1) S_C(156, 130, 131, r1)
               "s_cmp_lt_u32 s42, %[early]\n\ts_cbranch_scc1 7f\n"
               "3:\n\t" S_M0("116:119", "24:27", "0:3") S_C(104, 104, 106, r2) S_C(155, 155, 156, r2)
               S_M("116:119", "28:31", "4:7") "v_add_f32_dpp v104, v154, v104 quad_perm:[0,0,2,2]" S_D S_U(132, 108, 152)
               S_M("116:119", "32:35", "8:11") S_U(133, 109, 152) S_U(134, 110, 153)
               S_M("116:119", "36:39", "12:15") S_U(135, 111, 153) S_SQ(112, 108)
               S_M("116:119", "40:43", "16:19") S_SQD(113, 109) S_SQ(114, 110)
               S_M("116:119", "44:47", "20:23") S_SQD(115, 111) S_C(156, 132, 133, r1) S_C(157, 134, 135, r1)
               S_C(112, 112, 113, r1) S_C(114, 114, 115, r1) S_C(156, 156, 157, r2) S_C(112, 112, 114, r2)
               "v_add_f32_dpp v112, v154, v112 quad_perm:[0,0,2,2]" S_D
               "v_mul_f32 v117, %[nic], v117\n\tv_cndmask_b32_dpp v155, v156, v155, vcc row_ror:4" S_D
               S_BASE("120:121", "128:129", "116:117") S_BASE("122:123", "130:131", "118:119")
               "v_cndmask_b32_dpp v104, v112, v104, vcc row_ror:4" S_D "v_mul_f32 v156, %[nic], v155\n\t"
               S_M("120:123", "48:51", "0:3") S_BASE("124:125", "132:133", "116:117") S_BASE("126:127", "134:135", "118:119")
               S_M("120:123", "52:55", "4:7") "v_fmac_f32 v104, v156, v155\n\tv_max_f32 v104, %[eps], v104\n\t"
               S_M("120:123", "56:59", "8:11") "v_rsq_f32 v157, v104\n\t"
               S_M("120:123", "60:63", "12:15") "v_mul_f32 v104, v104, v157\n\tv_mov_b32_dpp v157, v157 row_ror:12 row_mask:0x2 bank_mask:0xf\n\t"
               S_M("120:123", "64:67", "16:19") "v_cvt_pk_bf16_f32 v105, v155, v104\n\tv_mov_b32_dpp v157, v157 row_ror:8 row_mask:0x4 bank_mask:0xf\n\t"
               S_M("120:123", "68:71", "20:23") "v_lshlrev_b32 v106, 16, v105\n\tv_mov_b32_dpp v157, v157 row_ror:4 row_mask:0x8 bank_mask:0xf\n\t"
               S_M("124:127", "72:75", "0:3") "v_and_b32 v107, 0xffff0000, v105\n\t" S_B(136, 0)
               S_M("124:127", "76:79", "4:7") S_B(137, 1) S_B(138, 2)
               S_M("124:127", "80:83", "8:11") S_B(139, 3) S_LG("120:121", "136:137")
               S_M("124:127", "84:87", "12:15") S_LG("122:123", "138:139") S_SUB("128:129", "120:121")
               S_M("124:127", "88:91", "16:19") S_SUB("130:131", "122:123") S_E(128)
               S_M("124:127", "92:95", "20:23") S_E(129) S_E(130) S_E(131)
               S_B(140, 4) S_B(141, 5) S_B(142, 6) S_B(143, 7) "v_sub_f32 v106, v155, v106\n\t"
               S_LG("124:125", "140:141") S_LG("126:127", "142:143") "v_sub_f32 v107, v104, v107\n\tv_max3_f32 v152, v120, v121, v122\n\t"
               S_SUB("132:133", "124:125") S_SUB("134:135", "126:127") "v_max3_f32 v153, v123, v124, v125\n\t"
               S_E(132) S_E(133) S_E(134) S_E(135)
               "v_max3_f32 v152, v152, v126, v127\n\tv_add_f32 v154, %[thr], v150\n\tv_max_f32 v152, v152, v153\n\tv_cvt_pk_bf16_f32 v106, v106, v107\n\t"
               "v_lshl_add_u32 v144, s46, 3, %[apk]\n\tv_cmp_gt_f32 vcc, v152, v154\n\tv_add_u32 v145, s46, %[apm]\n\tv_lshl_add_u32 v146, s46, 1, %[api]\n\t"
               "s_cbranch_vccnz 8f\n"
               "4:\n\t" S_MUL("128:129", "136:137") S_MUL("130:131", "138:139") S_MUL("132:133", "140:141") S_MUL("134:135", "142:143")
               "v_cvt_pk_bf16_f32 v128, v128, v129\n\tv_cvt_pk_bf16_f32 v129, v130, v131\n\tv_cvt_pk_bf16_f32 v130, v132, v133\n\tv_cvt_pk_bf16_f32 v131, v134, v135\n\t"
               "s_cmp_lt_u32 s51, s48\n\ts_cbranch_scc1 6f\n"
               "10:\n\tds_read_b32 v151, v149\n\tds_write_b128 v144, v[128:131]\n\tds_write2_b32 v145, v150, v150 offset0:0 offset1:1\n\ts_mov_b64 exec, %[dg2]\n\t"
               "ds_write_b16 v146, v105\n\tds_write_b16 v146, v106 offset:16\n\tds_write_b16_d16_hi v146, v105 offset:32\n\tds_write_b16_d16_hi v146, v106 offset:48\n\t"
               "s_mov_b64 exec, 1\n\tds_add_u32 v148, %[inc]\n\ts_mov_b64 exec, -1\n\ts_setprio 0\n\ts_add_u32 s42, s42, %[ns]\n\t"
               S_W(7) "v_readfirstlane_b32 s49, v151\n\ts_branch 1b\n"
               S_POLL(5, 149, 0, "s44", "2b", "2")
               S_POLL(6, 148, 64, "s48", "10b", "64")
               "7:\n\tv_mov_b32 v144, %[qi]\n\tv_mov_b32 v145, v152\n\tv_mov_b32 v146, 1\n"
               S_POLL(70, 144, 0, "%[nq]", "71f", "64")
               "71:\n\tv_mov_b32 v152, v145\n\tds_read_b128 v[0:3], %[apk]\n\tds_read_b128 v[4:7], %[apk] offset:1024\n\tds_read_b128 v[8:11], %[apk] offset:2048\n\t"
               "ds_read_b128 v[12:15], %[apk] offset:3072\n\tds_read_b128 v[16:19], %[apk] offset:4096\n\tds_read_b128 v[20:23], %[apk] offset:5120\n\t"
               "v_subrev_u32 v144, %[mc], %[amr]\n\tds_read_b32 v150, %[amr]\n\ts_mov_b64 exec, 63\n\tds_add_u32 v144, v146\n\ts_mov_b64 exec, -1\n\t"
               "s_cmp_lg_u32 %[late], 0\n\ts_waitcnt lgkmcnt(0)\n\ts_nop 1\n\ts_cbranch_scc1 1b\n\ts_branch 3b\n"
               "8:\n\tv_mov_b32 v153, v152\n\ts_nop 1\n\tv_permlane16_swap_b32 v152, v153\n\tv_max_f32 v152, v152, v153\n\tv_mov_b32 v153, v152\n\ts_nop 1\n\t"
               "v_permlane32_swap_b32 v152, v153\n\tv_max_f32 v152, v152, v153\n\tv_mul_f32 v152, 0x3d000000, v152\n\tv_rndne_f32 v152, v152\n\t"
               "v_mul_f32 v152, 0x42000000, v152\n\tv_max_f32 v150, v150, v152\n\tds_max_f32 %[amr], v150\n\t"
               S_SUB("128:129", "120:121") S_SUB("130:131", "122:123") S_SUB("132:133", "124:125") S_SUB("134:135", "126:127")
               S_E(128) S_E(129) S_E(130) S_E(131) S_E(132) S_E(133) S_E(134) S_E(135) "s_branch 4b\n"
               "9:"
               : "=&{v[0:7]}"(c.q[0]), "=&{v[8:15]}"(c.q[1]), "=&{v[16:23]}"(c.q[2]), "=&{s[52:53]}"(tq)
               : [orb] "v"(c.o_rb), [oyb] "v"(c.o_yb), [ev] "v"(c.ev), [apk] "v"(c.apk), [apm] "v"(c.apm), [api] "v"(c.api), [amr] "v"(c.amr),
                 [two] "v"(2.f), [four] "v"(4u), [inc] "v"(2u * NPK), [k0] "s"(k0), [late] "s"(__builtin_amdgcn_readfirstlane(late)), [toff] "s"(f32x2{TOFF, TOFF}),
                 [r1] "s"(0xAAAAAAAAAAAAAAAAull), [r2] "s"(0xCCCCCCCCCCCCCCCCull), [dg] "s"(0xF0000F0000F0000Full), [dg2] "s"(0xF00FFF000FF000FFull),
                 [cap] "i"(SPIN_CAP), [end] "i"(TPW - NH1), [ns] "i"(NS), [pfl] "i"(CT_OFF + C_PFL), [full] "i"(CT_OFF + C_FULL), [early] "i"(NEARLY),
                 [qi] "i"(CT_OFF + C_QI), [nq] "i"(NS - NEARLY), [mc] "i"(C_MREF - C_CONS), [slot] "i"(SLOT),
                 [thr] "i"(__builtin_bit_cast(unsigned, THR)), [eps] "i"(__builtin_bit_cast(unsigned, EPS * CC)), [nic] "i"(__builtin_bit_cast(unsigned, -1.f / CC))
               : "memory", "scc", "vcc", "s40", "s41", "s42", "s43", "s44", "s45", "s46", "s47", "s48", "s49", "s50", "s51",
                 "v24", "v25", "v26", "v27", "v28", "v29", "v30", "v31", "v32", "v33", "v34", "v35", "v36", "v37", "v38", "v39", "v40", "v41", "v42", "v43", "v44", "v45", "v46", "v47", "v48", "v49", "v50", "v51", "v52", "v53", "v54", "v55", "v56", "v57", "v58", "v59", "v60", "v61", "v62", "v63", "v64", "v65", "v66", "v67", "v68", "v69", "v70", "v71", "v72", "v73", "v74", "v75", "v76", "v77", "v78", "v79", "v80", "v81", "v82", "v83", "v84", "v85", "v86", "v87", "v88", "v89", "v90", "v91", "v92", "v93", "v94", "v95", "v96", "v97", "v98", "v99", "v100", "v101", "v102", "v103", "v104", "v105", "v106", "v107", "v108", "v109", "v110", "v111", "v112", "v113", "v114", "v115", "v116", "v117", "v118", "v119", "v120", "v121", "v122", "v123", "v124", "v125", "v126", "v127", "v128", "v129", "v130", "v131", "v132", "v133", "v134", "v135", "v136", "v137", "v138", "v139", "v140", "v141", "v142", "v143", "v144", "v145", "v146", "v147", "v148", "v149", "v150", "v151", "v152", "v153", "v154", "v155", "v156", "v157");
  c.tq = (unsigned)tq;
}

__device__ __forceinline__ void s_half(SCtx& c, int k, int h) {
  const int s = k % NSLOT, e = k % NPK;
  const int sb_ = s * SLOT;
  int yba = c.ev * sb_ + c.o_yb;
  float hc = 1.f - 0.5f * c.ev;
  unsigned xa = (unsigned)(size_t)c.L0 + sb_ + h * 6144 + c.o_rb, xb = xa ^ 64, ai = c.api + e * 256 + h * 8;
  unsigned ap = c.apk + e * 1024 + h * 8, am = c.apm + e * 128 + h * 4, af = c.ct + C_PFL + e * 4, inc = NPK;
  float two = 2.f;
  f32x4 G = {EPS * CC, EPS * CC, EPS * CC, EPS * CC};
  poll_ge_sleep(c.ct + C_CONS + e * 4, (unsigned)(NE * (k / NPK) + NS));
  asm volatile("" : "+v"(yba), "+v"(hc), "+v"(xa), "+v"(xb), "+v"(ai), "+v"(ap), "+v"(am), "+v"(af), "+v"(inc), "+v"(two), "+{v[104:107]}"(G));
  poll_ge_sleep(c.ct + C_FULL, (unsigned)(2 * k + 1));
  __builtin_amdgcn_s_setprio(1);
  bf16x8 yb[6];
#pragma unroll
  for (int j = 0; j < 6; ++j) yb[j] = *(LDS_AS(bf16x8)*)(c.L0 + ((j & 1) ? (yba ^ 64) : yba) + (j >> 1) * 128);
  f32x4 Hx = {0.f, 0.f, 0.f, 0.f}, S = Hx;
#pragma unroll
  for (int j = 0; j < 6; ++j) { Hx = mfma(yb[j], yb[j], Hx); S = mfma(yb[j], s_qf(c, j), S); }
  const float nsq = S[1] * (-1.f / CC);
  Hx = f32x4{Hx[0] * hc, Hx[0] * hc, Hx[2] * hc, Hx[2] * hc};
  S = f32x4{S[0], S[0], S[2], S[2]};
  float mref, mt;
  f32x4 rs;
  unsigned long long raise;
#define SH_M(d, a, b) "v_mfma_f32_16x16x32_bf16 v[" d "], v[" a "], " b ", v[" d "]\n\t"
#define SH_X(n, a, y) "s_waitcnt lgkmcnt(" #n ")\n\t" SH_M("100:103", a, "%[" #y "]") SH_M("104:107", a, "v[" a "]")
#define SH_D " row_mask:0xf bank_mask:0xf\n\t"
  __builtin_amdgcn_s_setprio(0);
  if (k < TPW - 2) poll_ge_sleep(c.ct + C_FULL, (unsigned)(2 * k + 2));
  asm volatile("s_mov_b32 s42, %[cap]\n"
               "1:\n\tds_read_b32 v136, %[afl]\n\tds_read_b128 v[112:115], %[xa]\n\tds_read_b128 v[116:119], %[xb]\n\tds_read_b128 v[120:123], %[xa] offset:128\n\t"
               "ds_read_b128 v[124:127], %[xb] offset:128\n\tds_read_b128 v[128:131], %[xa] offset:256\n\tds_read_b128 v[132:135], %[xb] offset:256\n\t"
               "ds_read_b32 %[mr], %[amr]\n\ts_sub_u32 s42, s42, 1\n\ts_cselect_b32 s41, 0, %[need]\n\ts_waitcnt lgkmcnt(7)\n\t"
               "v_readfirstlane_b32 s40, v136\n\ts_cmp_lt_u32 s40, s41\n\ts_cbranch_scc1 1b\n\ts_setprio 3\n\t"
               SH_X(6, "112:115", y0) SH_X(5, "116:119", y1) SH_X(4, "120:123", y2) SH_X(3, "124:127", y3) SH_X(2, "128:131", y4) SH_X(1, "132:135", y5)
               SH_M("108:111", "112:115", "%[q0]") SH_M("108:111", "116:119", "%[q1]")
               "s_waitcnt lgkmcnt(0)\n\tv_add_f32 v137, %[thr], %[mr]\n\tds_write_b32 %[am], %[mr]\n\ts_nop 1\n\t"
               "v_mov_b32_dpp v140, v100 quad_perm:[1,1,3,3]" SH_D "v_mov_b32_dpp v141, v101 quad_perm:[1,1,3,3]" SH_D
               "v_mov_b32_dpp v142, v102 quad_perm:[1,1,3,3]" SH_D "v_mov_b32_dpp v143, v103 quad_perm:[1,1,3,3]" SH_D
               SH_M("108:111", "120:123", "%[q2]")
               "v_fmac_f32 v104, 2.0, v100\n\tv_fmac_f32_dpp v105, v101, %[two] quad_perm:[0,0,2,2]" SH_D
               "v_fmac_f32 v106, 2.0, v102\n\tv_fmac_f32_dpp v107, v103, %[two] quad_perm:[0,0,2,2]" SH_D
               "v_cndmask_b32 v138, v140, v141, %[r1]\n\tv_cndmask_b32 v139, v142, v143, %[r1]\n\t"
               SH_M("108:111", "124:127", "%[q3]")
               "v_cndmask_b32 v104, v104, v105, %[r1]\n\tv_cndmask_b32 v106, v106, v107, %[r1]\n\tv_cndmask_b32 v138, v138, v139, %[r2]\n\t"
               "v_cndmask_b32 v104, v104, v106, %[r2]\n\tv_mul_f32 v139, %[nic], v138\n\t"
               SH_M("108:111", "128:131", "%[q4]")
               "v_fmac_f32 v104, v139, v138\n\tv_max_f32 v104, %[eps], v104\n\tv_rsq_f32 v105, v104\n\t"
               SH_M("108:111", "132:135", "%[q5]")
               "v_mul_f32 v104, v104, v105\n\t"
               "v_mov_b32_dpp v105, v105 row_ror:12 row_mask:0x2 bank_mask:0xf\n\tv_cvt_pk_bf16_f32 v106, v138, v104\n\ts_nop 0\n\t"
               "v_mov_b32_dpp v105, v105 row_ror:8 row_mask:0x4 bank_mask:0xf\n\tv_lshlrev_b32 v107, 16, v106\n\tv_and_b32 v139, 0xffff0000, v106\n\t"
               "v_mov_b32_dpp v105, v105 row_ror:4 row_mask:0x8 bank_mask:0xf\n\t"
               "v_pk_fma_f32 v[108:109], v[140:141], %[nsq], v[108:109]\n\tv_pk_fma_f32 v[110:111], v[142:143], %[nsq], v[110:111]\n\t"
               "v_mov_b32_dpp v100, v105 row_newbcast:0" SH_D "v_mov_b32_dpp v101, v105 row_newbcast:1" SH_D
               "v_mov_b32_dpp v102, v105 row_newbcast:2" SH_D "v_mov_b32_dpp v103, v105 row_newbcast:3" SH_D
               S_LG("108:109", "100:101") S_LG("110:111", "102:103") S_SUB("140:141", "108:109") S_SUB("142:143", "110:111")
               "v_exp_f32 v140, v140\n\tv_exp_f32 v141, v141\n\tv_exp_f32 v142, v142\n\tv_exp_f32 v143, v143\n\t"
               "v_max3_f32 %[mt], v108, v109, v110\n\tv_sub_f32 v138, v138, v107\n\tv_sub_f32 v104, v104, v139\n\tv_max_f32 %[mt], %[mt], v111\n\t"
               "v_pk_mul_f32 v[140:141], v[140:141], v[100:101]\n\tv_pk_mul_f32 v[142:143], v[142:143], v[102:103]\n\t"
               "v_cmp_gt_f32 %[ra], %[mt], v137\n\tv_cvt_pk_bf16_f32 v107, v138, v104\n\t"
               "v_cvt_pk_bf16_f32 v140, v140, v141\n\tv_cvt_pk_bf16_f32 v141, v142, v143\n\t"
               "ds_write_b64 %[ap], v[140:141]\n\ts_mov_b64 exec, %[dg]\n\t"
               "ds_write_b16 %[ai], v106\n\tds_write_b16 %[ai], v107 offset:16\n\tds_write_b16_d16_hi %[ai], v106 offset:32\n\tds_write_b16_d16_hi %[ai], v107 offset:48\n\t"
               "s_cmp_eq_u64 %[ra], 0\n\ts_cselect_b64 exec, 1, 0\n\tds_add_u32 %[af], %[inc]\n\ts_mov_b64 exec, -1\n\ts_setprio 0"
               : "+{v[100:103]}"(Hx), "+{v[104:107]}"(G), "+{v[108:111]}"(S), [mr] "=&{v150}"(mref), [mt] "=&v"(mt), [ra] "=&s"(raise)
               : [y0] "v"(yb[0]), [y1] "v"(yb[1]), [y2] "v"(yb[2]), [y3] "v"(yb[3]), [y4] "v"(yb[4]), [y5] "v"(yb[5]),
                 [q0] "v"(s_qf(c, 0)), [q1] "v"(s_qf(c, 1)), [q2] "v"(s_qf(c, 2)), [q3] "v"(s_qf(c, 3)), [q4] "v"(s_qf(c, 4)), [q5] "v"(s_qf(c, 5)),
                 [afl] "v"(c.ct + C_FULL), [xa] "v"(xa), [xb] "v"(xb), [amr] "v"(c.amr), [am] "v"(am), [ap] "v"(ap), [ai] "v"(ai),
                 [af] "v"(af), [inc] "v"(inc), [nsq] "v"(f32x2{nsq, nsq}), [two] "v"(two),
                 [need] "s"(__builtin_amdgcn_readfirstlane(2 * k + 2)), [r1] "s"(0xAAAAAAAAAAAAAAAAull), [r2] "s"(0xCCCCCCCCCCCCCCCCull),
                 [dg] "s"(0xF0000F0000F0000Full), [cap] "i"(SPIN_CAP), [thr] "i"(__builtin_bit_cast(unsigned, THR)), [toff] "s"(f32x2{TOFF, TOFF}),
                 [eps] "i"(__builtin_bit_cast(unsigned, EPS * CC)), [nic] "i"(__builtin_bit_cast(unsigned, -1.f / CC))
               : "memory", "scc", "s40", "s41", "s42", "v112", "v113", "v114", "v115", "v116", "v117", "v118", "v119", "v120", "v121", "v122", "v123", "v124", "v125",
                 "v126", "v127", "v128", "v129", "v130", "v131", "v132", "v133", "v134", "v135", "v136", "v137", "v138", "v139", "v140", "v141", "v142", "v143");
  if (__builtin_expect(raise != 0ull, 0)) {
    rs = Hx;
    mref = fmaxf(mref, 32.f * __builtin_rintf(xrow16_max(mt) * (1.f / 32.f)));
    float pp[4];
#pragma unroll
    for (int i = 0; i < 4; ++i) pp[i] = __builtin_amdgcn_exp2f(S[i] - mref) * rs[i];
    asm volatile("ds_max_f32 %6, %3\n\tds_write_b32 %2, %3\n\tds_write_b64 %0, %1\n\ts_mov_b64 exec, 1\n\tds_add_u32 %4, %5\n\ts_mov_b64 exec, -1"
                 : : "v"(ap), "v"(u32x2{cvtpk(pp[0], pp[1]), cvtpk(pp[2], pp[3])}), "v"(am), "v"(mref), "v"(af), "v"((unsigned)NPK), "v"(c.amr) : "memory");
  }
}

using u32x16 = __attribute__((ext_vector_type(16))) unsigned;
typedef __attribute__((address_space(1))) unsigned short gus;
extern "C" __global__ __launch_bounds__(768, 3) void dino_main(const void*, const void*, const void*, const void*, const void*,
                                                               const void*, const void*, void*, void*) {
  u32x16 ka; u32x2 kb; unsigned long long tent64, tx64;
  __shared__ __attribute__((aligned(16))) char lds[LDS_BYTES];
  const int wave = __builtin_amdgcn_readfirstlane((int)threadIdx.x >> 6);
  const bool helper = blockIdx.x < 8;
  lds_ptr L0 = (lds_ptr)lds;
  const unsigned lbase = (unsigned)(size_t)L0;
  const unsigned ct = lbase + CT_OFF;
#define KARG(v, i) ((char*)(((unsigned long long)v[2 * (i) + 1] << 32) | v[2 * (i)]))
#define q KARG(ka, 0)
#define f0 KARG(ka, 1)
#define f1 KARG(ka, 2)
#define wq KARG(ka, 3)
#define wk KARG(ka, 4)
#define wv KARG(ka, 5)
#define wo KARG(ka, 6)
#define box KARG(ka, 7)
#define tent ((unsigned)tent64)
#define LANE_VARS int lane; asm volatile("v_mbcnt_lo_u32_b32 %0, -1, 0\n\tv_mbcnt_hi_u32_b32 %0, -1, %0" : "=v"(lane)); \
  const int r = lane & 15, g = lane >> 4; (void)r; (void)g;
#define DOFF_VARS unsigned doff[3];   \
  _Pragma("unroll") for (int i = 0; i < 3; ++i) doff[i] = (unsigned)(lane ^ ((swz_tbl(i) >> ((lane >> 3) * 4)) & 7)) << 4;
#define BARRIER() do { asm volatile("s_waitcnt lgkmcnt(0)" ::: "memory"); __builtin_amdgcn_s_barrier(); asm volatile("" ::: "memory"); } while (0)

  {
    unsigned doff[3];
    static_assert(swz_tbl(0) == swz_tbl(3) && swz_tbl(1) == swz_tbl(4) && swz_tbl(2) == swz_tbl(5), "");
    unsigned l16, db;
#define PD(d, b, off, pol) "global_load_lds_dwordx4 %[" #d "], s[" #b "] offset:" #off pol "\n\t"
    asm volatile("s_load_dwordx16 s[64:79], %[kp], 0x0\n\ts_load_dwordx2 s[80:81], %[kp], 0x40\n\ts_memrealtime s[82:83]\n\t"
                 "v_mbcnt_lo_u32_b32 %[l16], -1, 0\n\tv_mbcnt_hi_u32_b32 %[l16], -1, %[l16]\n\t"
                 "s_lshr_b32 s84, %[b], 3\n\ts_and_b32 s85, %[b], 7\n\t"
                 "v_lshrrev_b32 %[db], 1, %[l16]\n\tv_and_b32 %[db], -4, %[db]\n\t"
                 "s_mov_b32 s86, %[t0]\n\ts_mov_b32 s87, %[t1]\n\ts_mov_b32 s88, %[t2]\n\t"
                 "v_bfe_u32 %[d0], s86, %[db], 3\n\tv_bfe_u32 %[d1], s87, %[db], 3\n\tv_bfe_u32 %[d2], s88, %[db], 3\n\t"
                 "v_xor_b32 %[d0], %[d0], %[l16]\n\tv_xor_b32 %[d1], %[d1], %[l16]\n\tv_xor_b32 %[d2], %[d2], %[l16]\n\t"
                 "v_lshlrev_b32 %[d0], 4, %[d0]\n\tv_lshlrev_b32 %[d1], 4, %[d1]\n\tv_lshlrev_b32 %[d2], 4, %[d2]\n\t"
                 "v_lshlrev_b32 %[l16], 4, %[l16]\n\t"
                 "s_cmp_eq_u32 s84, 0\n\ts_cbranch_scc1 4f\n\t"
                 "s_cmp_lg_u32 %[w], 0\n\ts_cbranch_scc1 7f\n\t"
                 "s_setprio 3\n\tv_lshlrev_b32 %[db], 3, %[l16]\n\ts_mov_b32 m0, %[junk]\n\ts_lshl_b32 s89, s84, 13\n\t"
                 "s_cmp_gt_u32 s84, 3\n\ts_cbranch_scc1 2f\n\t"
                 "s_getpc_b64 s[90:91]\n1:\n\ts_sub_u32 s86, s89, 8192\n\ts_min_u32 s86, s86, dino_end-1b-8192\n\t"
                 "s_add_u32 s90, s90, s86\n\ts_addc_u32 s91, s91, 0\n\tglobal_load_lds_dword %[db], s[90:91]\n"
                 "2:\n\ts_mul_i32 s86, s85, 98304\n\ts_mul_i32 s87, s84, 3072\n\ts_sub_u32 s87, s87, 3072\n\t"
                 "s_add_u32 s88, s86, s87\n\ts_lshl1_add_u32 s86, s86, s87\n\ts_lshl_b32 s86, s86, 1\n\t"
                 "s_mul_i32 s87, s85, 12288\n\ts_lshr_b32 s90, s89, 1\n\ts_add_u32 s87, s87, s90\n\ts_sub_u32 s87, s87, 13 * 4096\n\t"
                 "s_sub_u32 s89, s89, 4 * 8192\n\ts_cmp_gt_u32 s85, 5\n\ts_cselect_b32 s89, -1, s89\n\ts_cmp_ge_u32 s89, 11 * 8192\n\t"
                 "s_memrealtime s[98:99]\n\ts_waitcnt lgkmcnt(0)\n\ts_cbranch_scc1 3f\n\t"
                 "s_cmp_lt_u32 s89, 9 * 8192\n\ts_cselect_b64 s[90:91], s[70:71], s[72:73]\n\ts_cselect_b32 s89, s89, s87\n\t"
                 "s_add_u32 s90, s90, s89\n\ts_addc_u32 s91, s91, 0\n\tglobal_load_lds_dword %[db], s[90:91]\n"
                 "3:\n\ts_mov_b32 m0, 0x3000\n\t"
                 "s_add_u32 s90, s68, s88\n\ts_addc_u32 s91, s69, 0\n\t"
                 "s_add_u32 s92, s66, s86\n\ts_addc_u32 s93, s67, 0\n\t"
                 PD(d0, 90:91, 0, " nt") PD(d1, 90:91, 1024, " nt") PD(d2, 90:91, 2048, " nt")
                 "s_mov_b32 m0, 0\n\ts_nop 0\n\t"
                 PD(d0, 92:93, 0, " nt") PD(d1, 92:93, 1024, " nt")
                 "s_sub_u32 s89, s84, 15\n\ts_cmp_gt_u32 s85, 5\n\ts_cselect_b32 s89, -1, s89\n\ts_cmp_ge_u32 s89, 11\n\ts_cbranch_scc1 9f\n\t"
                 "s_mov_b32 m0, %[junk]\n\ts_sub_u32 s87, s87, 11 * 4096\n\ts_lshl_b32 s89, s89, 13\n\ts_cmp_lt_u32 s89, 9 * 8192\n\t"
                 "s_cselect_b64 s[90:91], s[76:77], s[74:75]\n\ts_cselect_b32 s89, s89, s87\n\t"
                 "s_add_u32 s90, s90, s89\n\ts_addc_u32 s91, s91, 0\n\tglobal_load_lds_dword %[db], s[90:91]\n\t"
                 "s_branch 9f\n"
                 "7:\n\ts_cmp_eq_u32 %[w], 1\n\ts_cbranch_scc1 8f\n\ts_barrier\n\ts_branch 8f\n"
                 "4:\n\t"
                 "s_cmp_gt_u32 %[b], 5\n\ts_cbranch_scc1 8f\n\ts_cmp_gt_u32 %[w], 9\n\ts_cbranch_scc1 8f\n\t"
                 "s_mov_b32 m0, %[img]\n\ts_mul_i32 s86, %[w], 9216\n\ts_and_b32 s88, %[w], 1\n\ts_lshl1_add_u32 s89, %[b], s88\n\t"
                 "s_mul_i32 s88, s88, 6144\n\ts_mul_i32 s89, s89, 6144\n\ts_add_u32 s88, s88, %[hr]\n\t"
                 "s_cmp_lg_u32 %[w], 0\n\ts_waitcnt lgkmcnt(0)\n\ts_cbranch_scc1 5f\n\t"
                 PD(d0, 64:65, 0, "") PD(d1, 64:65, 1024, "") PD(d2, 64:65, 2048, "") PD(d0, 64:65, 3072, "")
                 "s_mov_b32 m0, %[img] + 4096\n\ts_add_u32 s90, s64, 0x1000\n\ts_addc_u32 s91, s65, 0\n\t"
                 PD(d1, 90:91, 0, "") PD(d2, 90:91, 1024, "")
                 "\n5:\n\ts_cmp_gt_u32 %[w], 7\n\ts_cselect_b64 s[92:93], s[72:73], s[70:71]\n\ts_cselect_b32 m0, s88, s86\n\ts_cselect_b32 s86, s89, s86\n\t"
                 "s_cselect_b32 s87, 2, 3\n\ts_add_u32 s92, s92, s86\n\ts_addc_u32 s93, s93, 0\n"
                 "6:\n\ts_sub_u32 s87, s87, 1\n\t"
                 PD(d0, 92:93, 0, "") PD(d1, 92:93, 1024, "") PD(d2, 92:93, 2048, "")
                 "s_add_u32 m0, m0, 3072\n\ts_add_u32 s92, s92, 3072\n\ts_addc_u32 s93, s93, 0\n\ts_cmp_lg_u32 s87, 0\n\ts_cbranch_scc1 6b"
                 "\n8:\n\ts_waitcnt lgkmcnt(0)\n9:"
                 : "={s[64:79]}"(ka), "={s[80:81]}"(kb), "={s[82:83]}"(tent64), "={s[98:99]}"(tx64),
                   [d0] "=&v"(doff[0]), [d1] "=&v"(doff[1]), [d2] "=&v"(doff[2]), [l16] "=&v"(l16), [db] "=&v"(db)
                 : [kp] "s"(__builtin_amdgcn_kernarg_segment_ptr()), [b] "s"(blockIdx.x), [w] "s"(wave),
                   [t0] "i"(swz_tbl(0)), [t1] "i"(swz_tbl(1)), [t2] "i"(swz_tbl(2)), [junk] "i"(JUNK), [img] "i"(IMG_A), [hr] "i"(H_R)
                 : "memory", "scc", "vcc", "s84", "s85", "s86", "s87", "s88", "s89", "s90", "s91", "s92", "s93", "s94", "s95", "s96", "s97");
  }
  const unsigned t0 = (unsigned)__builtin_amdgcn_s_memrealtime() - T0BACK;

  if (__builtin_expect(blockIdx.x < 6, 0)) {
    LANE_VARS
    DOFF_VARS
    const int j = blockIdx.x;
    TNOW(ta0);
    TNOW(ta1);
    const int qq = (lane >> 2) & 3, p4 = lane & 3, e4 = wave & 3;
    int ra = H_R + r * ROWB + ((g ^ swz(r)) << 4);
    bf16x8 a[6], b[6], w[18];
    auto rows = [&](int t, bf16x8* d) {
#pragma unroll
      for (int kk = 0; kk < 6; ++kk) d[kk] = *(LDS_AS(bf16x8)*)(L0 + t * 6144 + ((kk & 1) ? (ra ^ 64) : ra) + (kk >> 1) * 128);
    };
#define TSET(v)
    int o[3];
#pragma unroll
    for (int t = 0; t < 3; ++t) o[t] = tr_off(4 * g + qq, 4 * (3 * e4 + t) + p4);
    unsigned qo = lbase + H_Q + 3 * e4 * 512 + ((g >> 1) * 16 + r) * 16 + (g & 1) * 8, qa = lbase + IMG_A + tr_off(r, 8 * wave + g);
    if (wave == 11) {
      unsigned d;
      asm volatile("s_load_dword %0, %1, 0x40\n\ts_barrier\n\ts_barrier\n\ts_barrier\n\ts_waitcnt lgkmcnt(0)" : "=&s"(d) : "s"(box + EPOCH_OFF) : "memory");
    } else
    asm volatile("s_waitcnt vmcnt(9)\n\ts_barrier" : PIN3(o, 0), "+v"(qo), "+v"(qa) :: "memory");
    if (wave < 6 || wave == 10) {
      rows((IMG_A - H_R) / 6144, a);
      const u32x2 m0 = *(LDS_AS(u32x2)*)(size_t)qa, m1 = *(LDS_AS(u32x2)*)(size_t)(qa ^ 32);
      static_assert((IMG_A - H_R) % 6144 == 0, "");
      const bf16x8 ones = __builtin_bit_cast(bf16x8, u32x4{0x3f803f80u, 0x3f803f80u, 0x3f803f80u, 0x3f803f80u});
      f32x4 sm = {0.f, 0.f, 0.f, 0.f};
#pragma unroll
      for (int i = 0; i < 6; ++i) sm = mfma(ones, a[i], sm);
      const float mean = sm[0] * (1.f / CC);
      if (__builtin_expect(wave < 6, 1)) {
        img_st(L0, H_C, wave, lane, __builtin_bit_cast(bf16x8, u32x4{cvtpk(bflo(m0[0]) - mean, bfhi(m0[0]) - mean), cvtpk(bflo(m0[1]) - mean, bfhi(m0[1]) - mean),
                                                                   cvtpk(bflo(m1[0]) - mean, bfhi(m1[0]) - mean), cvtpk(bflo(m1[1]) - mean, bfhi(m1[1]) - mean)}));
      } else {
        asm volatile("s_barrier" : PIN3(a, 0), PIN3(a, 3) :: "memory");
        f32x2 v2 = {0.f, 0.f}, v3 = v2;
#pragma unroll
        for (int i = 0; i < 12; ++i) {
          const u32x4 d = __builtin_bit_cast(u32x4, a[i >> 1]);
          const unsigned d0 = d[2 * (i & 1)], d1 = d[2 * (i & 1) + 1];
          const f32x2 x0 = f32x2{bflo(d0), bfhi(d0)} - mean, x1 = f32x2{bflo(d1), bfhi(d1)} - mean;
          v2 += x0 * x0; v3 += x1 * x1;
        }
        v2 += v3;
        *(LDS_AS(float)*)(L0 + CT_OFF + r * 4) = LOG2E * __builtin_amdgcn_rsqf(xrow16_sum(v2[0] + v2[1]) * (1.f / CC) + EPS);
      }
      TSET(ta2);
    }
    if (wave < 4) {
      wait_vm<0>();
      TSET(ta3);
      BARRIER();
      TSET(th1);
#pragma unroll
      for (int kk = 0; kk < 6; ++kk) {
        b[kk] = img_ld(L0, H_C, kk, lane);
        w[kk] = mk8(tr_read(L0 + kk * 12288 + o[0]), tr_read(L0 + kk * 12288 + 6144 + o[0]));
        __builtin_amdgcn_sched_barrier(0);
      }
#pragma unroll
      for (int i = 6; i < 18; ++i) {
        w[i] = mk8(tr_read(L0 + (i % 6) * 12288 + o[i / 6]), tr_read(L0 + (i % 6) * 12288 + 6144 + o[i / 6]));
        __builtin_amdgcn_sched_barrier(0);
      }
#pragma unroll
      for (int t = 0; t < 3; ++t) {
        f32x4 D = {0.f, 0.f, 0.f, 0.f};
#pragma unroll
        for (int kk = 0; kk < 6; ++kk) D = mfma(w[6 * t + kk], b[kk], D);
        *(LDS_AS(u32x2)*)(size_t)(qo + t * 512) = u32x2{cvtpk(D[0], D[1]), cvtpk(D[2], D[3])};
      }
      TSET(ta4);
      BARRIER();
    } else if (wave == 8 || wave == 9) {
      unsigned so = lane * 16 + (2 * j + (wave & 1)) * 1024;
      unsigned epoch;
      asm volatile("global_load_dword %0, %1, %2 sc1\n\ts_barrier\n\ts_waitcnt vmcnt(0)" : "=v"(epoch) : "v"(EPOCH_OFF), "s"(box) : "memory");
      TSET(ta3);
      rows(wave & 1, a);
      unsigned ta = line_tag(epoch, 8), tb = line_tag(epoch, 9);
      asm volatile("" : PIN3(a, 0), PIN3(a, 3), "+v"(ta), "+v"(tb), "+v"(so));
      TSET(ta4);
      BARRIER();
      TSET(ta5);
#pragma unroll
      for (int kk = 0; kk < 6; ++kk) b[kk] = img_ld(L0, H_Q, kk, lane);
      const float sc = *(LDS_AS(float)*)(L0 + CT_OFF + r * 4);
      __builtin_amdgcn_sched_barrier(0);
      f32x4 D = {0.f, 0.f, 0.f, 0.f};
#pragma unroll
      for (int kk = 0; kk < 6; ++kk) D = mfma(a[kk], b[kk], D);
      D *= sc;
      const unsigned p0 = cvtpk(D[0], D[1]), p1 = cvtpk(D[2], D[3]);
      asm volatile("global_store_dwordx4 %0, %1, %2 sc1\n\ts_nop 1" : : "v"(so), "v"(u32x4{p0, p1, p0 ^ p1 ^ ta, tb}), "s"(box + QK_OFF) : "memory");
      TSET(th2);
    } else if (wave != 11) {
      if (wave != 10) asm volatile("s_waitcnt vmcnt(0) lgkmcnt(0)\n\ts_barrier" ::: "memory");
      BARRIER();
    }
    BARRIER();
    issue_rows16(wo + wave * 6144, lbase + wave * 6144, doff);
    if (wave < 2) issue_rows16(wv + (2 * j + wave) * 6144, lbase + H_R + wave * 6144, doff);
    const unsigned epoch = epoch_load(box);
    BARRIER();
    TSET(ta3);
    {
      const int o0 = tr_off(8 * g + qq, 4 * wave + p4), o1 = tr_off(8 * g + 4 + qq, 4 * wave + p4);
#pragma unroll
      for (int kk = 0; kk < 6; ++kk) b[kk] = mk8(tr_read(L0 + kk * 12288 + o0), tr_read(L0 + kk * 12288 + o1));
      f32x4 D[2];
#pragma unroll
      for (int h = 0; h < 2; ++h) {
        rows(h, a);
        D[h] = f32x4{0.f, 0.f, 0.f, 0.f};
#pragma unroll
        for (int kk = 0; kk < 6; ++kk) D[h] = mfma(a[kk], b[kk], D[h]);
      }
      const int e = wave / 3;
      asm volatile("global_store_dwordx4 %0, %1, %2 sc1\n\ts_waitcnt vmcnt(0)"
                   : : "v"(lane * 16 + (e * 15 + 3 * j + wave) * 1024), "v"(pack8(D[0] * SQRT_C, D[1] * SQRT_C)), "s"(box + W2_OFF) : "memory");
    }
    BARRIER();
    if (wave == 11)
      asm volatile("s_mov_b64 exec, 1\n\tglobal_store_dwordx2 %0, %1, %2 sc1\n\ts_mov_b64 exec, -1\n\ts_nop 1"
                   : : "v"(j * 128), "v"(u32x2{line_tag(epoch, 10), line_tag(epoch, 11)}), "s"(box + W2F_OFF) : "memory");
    TPUT(60 + wave * 5, ta0); TPUT(61 + wave * 5, ta1); TPUT(62 + wave * 5, ta2); TPUT(63 + wave * 5, ta3); TPUT(64 + wave * 5, ta4);
  }

  unsigned dq = 0;
  int rslot = -1;
  if (__builtin_expect(wave == 0, 1)) {
    LANE_VARS
    DOFF_VARS
    if (helper) BARRIER();
    const unsigned afull = ct + C_FULL + r * 4;
    unsigned ts = 0;
    {
      const int ti = helper ? 1023 : (lane * 31 + (int)(blockIdx.x >> 3) - 1) & 1023;
      const int sp = ti >> 5, yy = sp * 8 + ((blockIdx.x + sp) & 7), xx = (ti + 5 * sp) & 31;
      unsigned tmp, pub = helper ? 2 * (TPW - 1) : 0, pre = 0, afr = ct + C_FREE + (helper ? ((TPW - 1) % NSLOT) * 4 : 0), e0, e1, e2, g0, g1, g2;
      asm volatile("s_mov_b32 s44, %[due0]\n\ts_mov_b32 s46, %[y0]\n\ts_mov_b32 s47, %[cap]\n\ts_mov_b32 s48, %[i0]\n\ts_mov_b32 s49, %[s0]\n\t"
                   "s_mov_b32 s51, 0\n\ts_mov_b32 s45, 0\n\ts_movk_i32 s39, %[p3s]\n"
                   "1:\n\tv_readlane_b32 s53, %[ob], s48\n\tv_readlane_b32 s52, %[oa], s48\n\ts_add_u32 m0, s49, 0x3000\n\tv_cmp_gt_u32 vcc, s51, %[pre]\n\t"
                   "v_add_u32 %[g0], s53, %[d0]\n\tv_add_u32 %[g1], s53, %[d1]\n\tv_add_u32 %[g2], s53, %[d2]\n\t"
                   "v_add_u32 %[e0], s52, %[d0]\n\tv_add_u32 %[e1], s52, %[d1]\n\tv_add_u32 %[e2], s52, %[d2]\n\t"
                   "s_cbranch_vccnz 5f\n\t"
                   "s_cmp_eq_u32 s48, 0\n\ts_cbranch_scc1 2f\n\ts_cmp_eq_u32 s48, 2\n\ts_cbranch_scc1 8f\n"
                   LD_GATE(1, "")
                   LD1(g0, 68:69, 0) LD1(g1, 68:69, 1024) LD1(g2, 68:69, 2048) "s_mov_b32 m0, s49\n\ts_nop 0\n\t" LD1(e0, 66:67, 0) LD1(e1, 66:67, 1024)
                   LD_NEXT
                   "\n2:\n\tv_add_u32 %[afr], 4, %[afr]\n\ts_cmp_eq_u32 s49, %[lasts]\n\ts_cbranch_scc1 7f\n"
                   "3:\n\tv_mov_b32 %[pre], 0\n\ts_add_u32 m0, s49, 0x800\n\tds_read_b32 %[pre], %[afr] ; NOTRACK\n"
                   LD_GATE(2, "")
                   LD1(e2, 56:57, 0) LD1(e0, 56:57, 1024) LD1(e1, 56:57, 2048) LD1(e2, 56:57, 3072) "s_add_u32 m0, s49, 0x1800\n\ts_add_u32 s48, s48, 1\n\t" LD1(e0, 58:59, 0)
                   LD_NEXT
                   LD_GATE(3, "s_cmp_gt_u32 s48, %[tpw] - 2\n\ts_cbranch_scc1 38f\n39:\n\t")
                   LD1(e1, 58:59, 1024) LD1(e2, 58:59, 2048) LD1(e0, 58:59, 3072) "s_add_u32 m0, s49, 0x2800\n\ts_add_u32 s49, s49, %[slot]\n\t" LD1(e1, 60:61, 0) LD1(e2, 60:61, 1024)
                   LD_NEXT
                   "s_cmp_eq_u32 s49, %[ring]\n\ts_cselect_b32 s49, 0, s49\n\ts_cmp_gt_i32 s46, 30\n\ts_cbranch_scc1 6f\n"
                   "4:\n\ts_cmp_eq_u32 s48, %[pk]\n\ts_cmovk_i32 s39, %[p3]\n\ts_cmp_lg_u32 s48, %[tpw]\n\ts_cbranch_scc1 1b\n\ts_branch 9f\n"
                   LD_PUB(1, 0, 15, 30) LD_PUB(2, 5, 20, 35) LD_PUB(3, 10, 25, 40)
                   "38:\n\ts_cmp_eq_u32 s46, 10\n\ts_cselect_b32 s43, 23, 0\n\ts_cmp_lt_u32 s42, s43\n\ts_cbranch_scc0 39b\n\tv_add_u32 %[tmp], 1, %[pub]\n\ts_waitcnt vmcnt(22)\n\tds_write_b32 %[af], %[tmp] ; NOTRACK\n\ts_branch 39b\n"
                   "5:\n\ts_movk_i32 s40, 0x100\n"
                   "40:\n\tds_read_b32 %[pre], %[afr]\n\t" LD_VM
                   "s_sub_u32 s40, s40, 1\n\ts_cselect_b32 s42, 0, s42\n\ts_cselect_b32 s40, 0, s40\n\t"
                   "s_cmp_le_i32 s42, s46\n\ts_cbranch_scc1 42f\n"
                   "41:\n\ts_sub_i32 s47, s47, 1\n\ts_cmp_lt_i32 s47, 0\n\ts_waitcnt lgkmcnt(0)\n\ts_cbranch_scc1 10b\n\t"
                   "v_cmp_gt_u32 vcc, s51, %[pre]\n\ts_cbranch_vccnz 40b\n\ts_branch 10b\n"
                   LD_PUB0(4, 0, 15, 30)
                   "6:\n\ts_waitcnt vmcnt(45)\n\tv_add_u32 %[pub], 2, %[pub]\n\tds_write_b32 %[af], %[pub]\n\ts_sub_u32 s46, s46, 15\n\ts_branch 4b\n"
                   "7:\n\ts_waitcnt lgkmcnt(0)\n\ts_add_u32 s51, s51, 8\n\tv_subrev_u32 %[afr], %[frb], %[afr]\n\ts_branch 3b\n"
                   "8:\n\ts_barrier\n\ts_add_u32 s46, s46, 64\n\ts_branch 10b\n"
                   "9:\n\ts_cmp_lt_i32 s46, 30\n\ts_cbranch_scc1 91f\n\ts_waitcnt vmcnt(30)\n\tv_add_u32 %[pub], 2, %[pub]\n\tds_write_b32 %[af], %[pub]\n"
                   "91:\n\ts_cmp_lt_i32 s46, 15\n\ts_cbranch_scc1 92f\n\tv_add_u32 %[tmp], 1, %[pub]\n\ts_waitcnt vmcnt(27)\n\tds_write_b32 %[af], %[tmp]\n"
                   "92:\n\ts_waitcnt lgkmcnt(0)"
                   : [pub] "+v"(pub), [pre] "+v"(pre), [afr] "+v"(afr), [ts] "+v"(ts), [tmp] "=&v"(tmp),
                     [e0] "=&v"(e0), [e1] "=&v"(e1), [e2] "=&v"(e2), [g0] "=&v"(g0), [g1] "=&v"(g1), [g2] "=&v"(g2)
                   : "{s[64:79]}"(ka), "{s[56:57]}"(f0 + 0x800), "{s[58:59]}"(f0 + 0x30000), "{s[60:61]}"(f0 + 0x31000), [t0] "s"(t0), [af] "v"(afull),
                     [d0] "v"(doff[0]), [d1] "v"(doff[1]), [d2] "v"(doff[2]), [oa] "v"(yy * 393216 + xx * 6144), [ob] "v"(yy * 98304 + xx * 3072),
                     [due0] "s"(helper ? HDUE * (PACE16 / 3) : PACES / 3), [y0] "s"(helper ? -15 : -74), [i0] "s"(helper ? TPW - 1 : 0),
                     [s0] "s"(helper ? ((TPW - 1) % NSLOT) * SLOT : 0),
                     [cap] "i"(SPIN_CAP), [win] "i"(PWIN), [ahi] "i"(AHI), [adl] "i"(ADL), [amg] "i"(AMG), [p3] "i"(PACE16 / 3), [p3s] "i"(PACES / 3), [pk] "i"(PK1), [slot] "i"(SLOT),
                     [ring] "i"(NSLOT * SLOT), [frb] "i"(NSLOT * 4), [lasts] "i"((NSLOT - 1) * SLOT), [tpw] "i"(TPW)
                   : "memory", "scc", "vcc", "s39", "s40", "s41", "s42", "s43", "s44", "s45", "s46", "s47", "s48", "s49", "s51", "s52", "s53", "s62", "s63");
    }
    lds_st32(ct + C_ISS, 1u);
    asm volatile("s_waitcnt vmcnt(15)\n\tds_write_b32 %0, %1\n\ts_waitcnt vmcnt(12)\n\tds_write_b32 %0, %2\n\ts_waitcnt vmcnt(0)\n\tds_write_b32 %0, %3"
                 : : "v"(afull), "v"(2 * TPW - 2), "v"(2 * TPW - 1), "v"(~0u) : "memory");
    TNOW(tl);
    {
      unsigned d;
      asm volatile("global_load_dword %0, %1, %2 sc1\n\ts_setprio 0\n\ts_waitcnt vmcnt(0)" : "=v"(d) : "v"(blockIdx.x * 128), "s"(box) : "memory");
    }
    TNOW(tlx);
    rslot = 7;
    dq = (unsigned)tx64 - tent;
  } else {
    if (wave == 1) {
      LANE_VARS
    {
      const unsigned o = lane * 8;
      unsigned z = (o >= C_MREF && o < C_MREF + 64) ? 0xff800000u : 0u;
      if (helper && o >= C_CONS && o < C_MREF) z = NE * ((TPW - 1) / NPK);
      u32x2 zz = {z, z};
      if (o >= C_PFL && o < C_CONS) zz = u32x2{(o - C_PFL) / 2 + 2, (o - C_PFL) / 2 + 4} + (helper ? 2u * NPK * ((TPW - 1) / NPK) : 0u);
      if (o == C_EP) zz[1] = QKW0 + QKW1 * min(t0 + T0BACK - tent, 45u);
      *(LDS_AS(u32x2)*)(L0 + CT_OFF + o) = zz;
      *(LDS_AS(u32x2)*)(L0 + ONES_OFF + (lane < 48 ? o : 0)) = u32x2{0x3f803f80u, 0x3f803f80u};
    }
    }
    if (helper || wave == 1) BARRIER();
    if (wave <= NE) {
      LANE_VARS
      const int ej = wave - 1;
      const int qq = (lane >> 2) & 3, p4 = lane & 3;
      const unsigned apk = lbase + IMG_A + lane * 16;
      const unsigned epoch = epoch_load(box);
      __builtin_amdgcn_s_setprio(2);
      unsigned long long tpv, tp7, tx1;
      {
        static_assert((TPW & 1) == 1 && C_CONS == C_PFL + 64 && C_FREE == C_FULL + 64, "");
        const int k0 = helper ? TPW - 1 : 0;
        unsigned need = 2 * k0 + 2;
        asm volatile("s_mov_b32 s46, 0\n\ts_mov_b64 exec, 0\n\ts_nop 3\n\ts_branch 99f\n"
                     "2:\n\ts_mov_b64 exec, -1\n\ts_nop 7\n\t" PV_Z4(72, 73, 74, 75) PV_Z4(76, 77, 78, 79) PV_Z4(80, 81, 82, 83) PV_Z4(84, 85, 86, 87)
                     "v_mov_b32 v88, 0xff800000\n\t"
                     "s_mov_b32 s46, %[k0]\n\ts_mov_b32 s45, %[s0]\n\ts_mov_b32 s44, %[e0]\n\ts_mov_b32 s47, %[cap]\n\t"
                     "s_cmp_lg_u32 s46, 0\n\ts_cbranch_scc1 32f\n"
                     "3:\n\tv_mov_b32 v148, %[ca]\n\t" PV_WAITF(4, 148) PV_FETCH_A
                     "s_cmp_lg_u32 s46, 0\n\ts_cbranch_scc1 99f\n\t"
                     "s_mov_b64 exec, 2\n\tds_add_u32 v148, %[one] offset:64\n\ts_mov_b64 exec, -2\n\t"
                     "v_add_u32 %[need], 2, %[need]\n\tv_add_u32 v148, 4, v148\n\ts_mov_b64 exec, -1\n\t"
                     PV_WAITF(5, 148)
                     "v_mov_b32 v149, v148\n\ts_mov_b64 exec, -2\n\tv_add_u32 %[need], 2, %[need]\n\tv_add_u32 v148, 4, v148\n\t"
                     "s_mov_b64 exec, 1\n\tv_add_u32 %[need], %[npk2], %[need]\n\ts_mov_b64 exec, -1\n\t"
                     PV_REQ("126:129", "130:133", "134:135", 136, 148) PV_FETCH_B
                     "s_mov_b64 exec, 2\n\tds_add_u32 v149, %[one] offset:64\n\ts_mov_b64 exec, -1\n"
                     "6:\n\t"
                     PV_STEP(1, "126:129", "130:133", "134:135", 134, 135, 136, 148, "138:141", "142:145", "146:147", 137, 149,
                             "92:95", "98:101", "104:107", "90:93", "96:99", "102:105",
                             PV_MOV(92, 90) PV_MOV(93, 91), PV_MOV(98, 96) PV_MOV(99, 97), PV_MOV(104, 102) PV_MOV(105, 103), "")
                     "s_cbranch_vccnz 7f\n"
                     "16:\n\t" PV_FETCH_A PV_REL(148)
                     "s_lshl1_add_u32 s54, s46, -2\n\ts_lshr_b64 s[54:55], %[sch], s54\n\ts_and_b32 s54, s54, 15\n\ts_cbranch_scc1 31f\n"
                     "30:\n\t"
                     PV_STEP(2, "138:141", "142:145", "146:147", 146, 147, 137, 149, "126:129", "130:133", "134:135", 136, 148,
                             "110:113", "116:119", "122:125", "108:111", "114:117", "120:123",
                             PV_MOV(110, 108) PV_MOV(111, 109), PV_MOV(116, 114) PV_MOV(117, 115), PV_MOV(122, 120) PV_MOV(123, 121),
                             "s_cmp_eq_u32 s46, %[last] - 1\n\ts_cbranch_scc1 98f\n\t")
                     "s_cbranch_vccnz 8f\n"
                     "26:\n\t" PV_FETCH_B PV_REL(149)
                     "s_branch 6b\n"
                     PV_WAITF(7, 148) "s_branch 16b\n"
                     PV_WAITF(8, 149) "s_branch 26b\n"
                     PV_SLOW(1, "126:129", "130:133", "134:135", 134, 135, 136, 148, 126, 127, 128, 129)
                     PV_SLOW(2, "138:141", "142:145", "146:147", 146, 147, 137, 149, 138, 139, 140, 141)
                     "31:\n\ts_cmp_lg_u32 s54, 8\n\ts_cbranch_scc1 34f\n"
                     "32:\n\tv_mbcnt_lo_u32_b32 v152, 31, 0\n\tv_lshlrev_b32 v152, 7, v152\n\ts_mov_b32 s55, %[gcap]\n"
                     "33:\n\tglobal_load_dwordx2 v[150:151], v152, %[fl] sc1\n\ts_sub_u32 s55, s55, 1\n\ts_cselect_b32 s56, 0, -1\n\ts_waitcnt vmcnt(0) lgkmcnt(0)\n\t"
                     "v_cmp_ne_u64 s[48:49], %[tag], v[150:151]\n\ts_sleep 2\n\ts_and_b32 s48, s48, s56\n\ts_cbranch_scc1 33b\n\t"
                     "s_mov_b32 s57, 0\n\tv_mov_b32 v150, %[apk]\n\ts_cmp_lg_u32 s46, %[last]\n\ts_cbranch_scc1 30b\n\ts_mov_b32 s54, 7\n\ts_branch 38f\n"
                     "34:\n\ts_cmp_lg_u32 s46, %[last] - 3\n\ts_cbranch_scc1 36f\n\tv_mov_b32 v151, %[iss]\n"
                     "35:\n\tds_read_b32 v150, v151\n\ts_sub_i32 s47, s47, 1\n\ts_cmp_lt_i32 s47, 0\n\ts_cselect_b32 s55, 0, 1\n\ts_waitcnt lgkmcnt(0)\n\t"
                     "v_readfirstlane_b32 s56, v150\n\ts_cmp_lt_u32 s56, s55\n\ts_cbranch_scc1 35b\n"
                     "36:\n\ts_mul_i32 s55, s57, 3072\n\tv_add_u32 v150, s55, %[apk]\n\ts_getpc_b64 s[48:49]\n"
                     "37:\n\ts_mul_i32 s55, s57, 40\n\ts_add_u32 s55, s55, 38f-37b\n\ts_add_u32 s48, s48, s55\n\ts_addc_u32 s49, s49, 0\n\t"
                     "s_add_u32 s57, s57, s54\n\ts_sub_u32 s54, s54, 1\n\ts_setpc_b64 s[48:49]\n"
                     "38:\n\t" PV_W2(0, 3, 4, 7, 8, 11) PV_W2(12, 15, 16, 19, 20, 23) PV_W2(24, 27, 28, 31, 32, 35) PV_W2(36, 39, 40, 43, 44, 47)
                     PV_W2(48, 51, 52, 55, 56, 59) PV_W2(60, 63, 64, 67, 68, 71)
                     "s_waitcnt lgkmcnt(0)\n\ts_branch 3b\n"
                     "96:\n\t" PV_RESC(134, 135, 126, 127, 128, 129) "s_nop 0\n\ts_branch 95f\n"
                     "98:\n\t" PV_MF("76:79", "114:117", "138:141") PV_MF("80:83", "120:123", "138:141")
                     "99:\n\ts_cmp_eq_u32 s46, 0\n\ts_cselect_b32 s54, 0, %[nl]\n\ts_cselect_b32 s55, 0, %[x1n]\n\ts_waitcnt lgkmcnt(0)\n\t"
                     "v_mov_b64 v[108:109], v[90:91]\n\tv_mov_b64 v[110:111], v[90:91]\n\tv_mov_b64 v[112:113], v[96:97]\n\tv_mov_b64 v[114:115], v[96:97]\n\t"
                     "v_mov_b64 v[116:117], v[102:103]\n\tv_mov_b64 v[118:119], v[102:103]\n\tv_add_u32 v120, %[aw], %[apk]\n\tv_add_u32 v121, %[ar], %[apk]\n\t"
                     "v_mov_b32 v122, %[x1]\n\tv_lshrrev_b32 v123, 1, %[apm]\n\tv_add_u32 v123, %[lwo], v123\n\t"
                     "v_mbcnt_lo_u32_b32 v124, -1, 0\n\tv_mbcnt_hi_u32_b32 v124, -1, v124\n\tv_and_b32 v125, 1, v124\n\tv_and_b32 v124, 48, v124\n\t"
                     "v_lshl_add_u32 v124, v125, 2, v124\n\tv_add_u32 v124, %[lw], v124\n"
                     "94:\n\tds_read_b32 v136, v122 offset:%[pfd]\n\tds_read_b64 v[134:135], %[apm] offset:%[el]\n\tds_read_b128 v[126:129], %[apk] offset:8 * %[el]\n\t"
                     "ds_read_b128 v[130:133], %[apu] offset:2 * %[el]\n\ts_sub_i32 s47, s47, 1\n\ts_cmp_lt_i32 s47, 0\n\ts_cselect_b32 s54, 0, s54\n\ts_waitcnt lgkmcnt(3)\n\t"
                     "v_readfirstlane_b32 s56, v136\n\ts_cmp_lt_u32 s56, s54\n\ts_cbranch_scc1 94b\n\ts_waitcnt lgkmcnt(2)\n\t"
                     "v_cmp_neq_f32 vcc, v134, v88\n\tv_cmp_neq_f32 s[50:51], v135, v88\n\ts_or_b64 vcc, vcc, s[50:51]\n\ts_waitcnt lgkmcnt(0)\n\ts_cbranch_scc1 96b\n"
                     "95:\n\t" PV_MF("84:87", "130:133", "126:129") "s_nop 1\n\t" PV_MF("72:75", "92:95", "126:129") "s_nop 1\n\t" PV_MF("72:75", "108:111", "126:129") PV_T(58)
                     "s_nop 1\n\tv_add_f32 v153, v86, v87\n\tv_add_f32 v151, v84, v85\n\t" PV_MF("76:79", "98:101", "126:129")
                     "v_rcp_f32 v150, v153\n\tv_mul_f32 v151, %[nic], v151\n\t" PV_MF("76:79", "112:115", "126:129")
                     "v_mul_f32 v151, v151, v150\n\t" PV_PK(72, 73) PV_MF("80:83", "104:107", "126:129") PV_PK(74, 75)
                     "v_cvt_pk_bf16_f32 v72, v72, v73\n\t" PV_MF("80:83", "116:119", "126:129") "v_cvt_pk_bf16_f32 v73, v74, v75\n\tds_write_b64 v120, v[72:73]\n\t"
                     PV_NRM(76, 77, 78, 79, 2048) "v_log_f32 v153, v153\n\t" PV_NRM(80, 81, 82, 83, 4096)
                     "s_and_b64 exec, exec, 1\n\tds_add_u32 v122, %[one]\n\ts_cselect_b64 exec, -1, 0\n\t" PV_T(60)
                     "v_subrev_f32 v88, %[toff], v88\n\tv_add_f32 v88, v88, v153\n\tds_write_b32 v123, v88\n\tds_read2_b32 v[114:115], v124 offset1:2\n"
                     "97:\n\tds_read_b32 v150, v122\n\tds_read_b128 v[90:93], v121\n\tds_read_b128 v[94:97], v121 offset:1024\n\tds_read_b128 v[98:101], v121 offset:2048\n\t"
                     "ds_read_b128 v[102:105], v121 offset:3072\n\tds_read_b128 v[106:109], v121 offset:4096\n\tds_read_b128 v[110:113], v121 offset:5120\n\t"
                     "s_sub_i32 s47, s47, 1\n\ts_cmp_lt_i32 s47, 0\n\ts_cselect_b32 s55, 0, s55\n\ts_waitcnt vmcnt(0) lgkmcnt(6)\n\t"
                     "v_readfirstlane_b32 s56, v150\n\ts_cmp_lt_u32 s56, s55\n\ts_cbranch_scc1 97b\n\ts_cmp_eq_u32 s46, 0\n\t"
                     PV_G(5, "90:93", "0:3", "4:7", "8:11", "0", "0", "0")
                     PV_G(4, "94:97", "12:15", "16:19", "20:23", "v[72:75]", "v[76:79]", "v[80:83]")
                     PV_G(3, "98:101", "24:27", "28:31", "32:35", "v[72:75]", "v[76:79]", "v[80:83]")
                     PV_G(2, "102:105", "36:39", "40:43", "44:47", "v[72:75]", "v[76:79]", "v[80:83]")
                     PV_G(1, "106:109", "48:51", "52:55", "56:59", "v[72:75]", "v[76:79]", "v[80:83]")
                     PV_G(0, "110:113", "60:63", "64:67", "68:71", "v[72:75]", "v[76:79]", "v[80:83]")
                     "v_xor_b32 v114, v114, %[ltag]\n\tv_xor_b32 v115, v115, %[ltag]\n\ts_nop 3\n\t"
                     "v_cvt_pk_bf16_f32 v116, v72, v73\n\tv_cvt_pk_bf16_f32 v120, v74, v75\n\tv_cvt_pk_bf16_f32 v117, v76, v77\n\tv_cvt_pk_bf16_f32 v121, v78, v79\n\t"
                     "v_cvt_pk_bf16_f32 v118, v80, v81\n\tv_bitop3_b32 v119, v116, v117, v114 bitop3:0x96\n\tv_cvt_pk_bf16_f32 v122, v82, v83\n\t"
                     "v_bitop3_b32 v123, v120, v121, v115 bitop3:0x96\n\tv_xor_b32 v119, v119, v118\n\tv_xor_b32 v123, v123, v122\n\t"
                     "s_cbranch_scc1 2b\n\t"
                     "global_store_dwordx4 %[so], v[116:119], %[bx] sc1\n\tglobal_store_dwordx4 %[so], v[120:123], %[bx2] sc1\n\t"
                     PV_T(62) "s_waitcnt lgkmcnt(0)\n\ts_nop 0"
                     : [need] "+v"(need), "={s[58:59]}"(tpv), "={s[60:61]}"(tp7), "={s[62:63]}"(tx1)
                     : [so] "v"(((ej * 16 + (lane >> 3)) * RSTR + blockIdx.x) * 128 + (lane & 7) * 16), [ltag] "v"(line_tag(epoch, lane & 7)),
                       [bx] "s"(box), [bx2] "s"(box + 8 * RSTR * 128), [aw] "s"(A_IMG - IMG_A + (ej >> 1) * 1024 + (ej & 1) * 8), [ar] "i"(A_IMG - IMG_A),
                       [nic] "i"(__builtin_bit_cast(unsigned, -1.f / CC)), [toff] "i"(__builtin_bit_cast(unsigned, TOFF)),
                       [lwo] "i"(CT_OFF + C_LW - PKM / 2), [lw] "i"(CT_OFF + C_LW), [x1] "i"(CT_OFF + C_X1), [x1n] "i"(NE),
                       [nl] "i"(2 * TPW + 2 * NPK), [el] "i"(((TPW - 1) % NPK) * 128), [pfd] "i"(C_PFL + ((TPW - 1) % NPK) * 4 - C_X1),
                       [apk] "v"(apk), [apm] "v"(lbase + PKM + r * 8), [apu] "v"(lbase + PKU + g * 64 + (r & 3) * 16),
                       [ox] "v"(lbase + tr_off(4 * g + qq, 4 * ej + p4)), [oy] "v"(lbase + 32 * ROWB + tr_off(2 * g + (qq >> 1), 4 * ej + p4)),
                       [ca] "v"(ct + (lane == 0 ? C_PFL + (k0 % NPK) * 4 : C_FULL + (k0 % NSLOT) * 4)),
                       [lim] "v"(ct + (lane == 0 ? C_PFL + NPK * 4 : C_FULL + NSLOT * 4)), [base] "v"(ct + (lane == 0 ? C_PFL : C_FULL)), [one] "v"(1u),
                       [k0] "s"(k0), [s0] "s"(helper ? ((TPW - 1) % NSLOT) * SLOT : 0), [e0] "s"(helper ? ((TPW - 1) % NPK) * 128 : 0),
                       [w2] "s"(box + W2_OFF + ej * 18432 - (lbase + IMG_A)), [fl] "s"(box + W2F_OFF),
                       [tag] "s"(u32x2{line_tag(epoch, 10), line_tag(epoch, 11)}), [gcap] "i"(GSPIN_CAP), [iss] "i"(CT_OFF + C_ISS), [cap] "i"(SPIN_CAP), [slot] "i"(SLOT), [ring] "i"(NSLOT * SLOT), [pring] "i"(NPK * 128),
                       [npk2] "i"(2 * NPK), [last] "i"(TPW - 1), [tight] "i"(TPW - 5),  [sch] "s"(W2SCH)
                     : "memory", "scc", "vcc", "s44", "s45", "s46", "s47", "s48", "s49", "s50", "s51", "s52", "s53", "s54", "s55", "s56", "s57",
                       "v0", "v1", "v2", "v3", "v4", "v5", "v6", "v7", "v8", "v9", "v10", "v11", "v12", "v13", "v14", "v15", "v16", "v17", "v18", "v19", "v20", "v21", "v22", "v23", "v24", "v25", "v26", "v27", "v28", "v29", "v30", "v31", "v32", "v33", "v34", "v35", "v36", "v37", "v38", "v39", "v40", "v41", "v42", "v43", "v44", "v45", "v46", "v47", "v48", "v49", "v50", "v51", "v52", "v53", "v54", "v55", "v56", "v57", "v58", "v59", "v60", "v61", "v62", "v63", "v64", "v65", "v66", "v67", "v68", "v69", "v70", "v71", "v72", "v73", "v74", "v75", "v76", "v77", "v78", "v79", "v80", "v81", "v82", "v83", "v84", "v85", "v86", "v87", "v88",
                       "v90", "v91", "v92", "v93", "v94", "v95", "v96", "v97", "v98", "v99", "v100", "v101", "v102", "v103", "v104", "v105", "v106", "v107",
                       "v108", "v109", "v110", "v111", "v112", "v113", "v114", "v115", "v116", "v117", "v118", "v119", "v120", "v121", "v122", "v123",
                       "v124", "v125", "v126", "v127", "v128", "v129", "v130", "v131", "v132", "v133", "v134", "v135", "v136", "v137", "v138", "v139",
                       "v140", "v141", "v142", "v143", "v144", "v145", "v146", "v147", "v148", "v149", "v150", "v151", "v152", "v153", "v154", "v155");
      }
    } else {
      LANE_VARS
      const int c = wave - 1 - NE;
      SCtx sc;
      sc.L0 = L0; sc.ct = ct; sc.apk = lbase + IMG_A + lane * 16; sc.apm = lbase + PKM + r * 8;
      sc.api = lbase + PKU + g * 64 + (r & 3) * 2 + ((r >> 2) == g ? 0 : 8); sc.amr = ct + C_MREF + r * 4;
      sc.o_rb = r * ROWB + ((g ^ swz(r)) << 4);
      sc.ev = (r & 1) ^ 1;
      sc.o_yb = (r & 1) ? ONES_OFF + (g << 4) : (32 + (r >> 1)) * ROWB + ((g ^ swz(r >> 1)) << 4);
      TNOW(tq0);
      if (c >= NEARLY) {
        static_assert(NS - NEARLY == 3, "");
        const int u = c - NEARLY;
        const unsigned epoch = epoch_load(box);
        const unsigned ta = line_tag(epoch, 8), tb = line_tag(epoch, 9);
        const unsigned qw = lds_ld32(ct + C_EP + 4);
        while ((unsigned)__builtin_amdgcn_s_memrealtime() - tent < qw) __builtin_amdgcn_s_sleep(4);
        u32x4 v[4];
#pragma unroll 1
        for (int sp = 0; sp < GSPIN_CAP; ++sp) {
          asm volatile("global_load_dwordx4 %0, %4, %5 sc1\n\tglobal_load_dwordx4 %1, %4, %5 offset:1024 sc1\n\tglobal_load_dwordx4 %2, %4, %5 offset:2048 sc1\n\t"
                       "global_load_dwordx4 %3, %4, %5 offset:3072 sc1\n\ts_waitcnt vmcnt(0)"
                       : "=&v"(v[0]), "=&v"(v[1]), "=&v"(v[2]), "=&v"(v[3]) : "v"(lane * 16), "s"(box + QK_OFF + u * 4096) : "memory");
          unsigned bad = 0;
#pragma unroll
          for (int t = 0; t < 4; ++t) bad |= (v[t][0] ^ v[t][1] ^ v[t][2] ^ ta) | (v[t][3] ^ tb);
          if (!__any(bad != 0u)) break;
        }
        TSET(tqa);
#pragma unroll
        for (int t = 0; t < 4; ++t)
          *(LDS_AS(u32x2)*)(L0 + IMG_A + (4 * u + t) * 512 + ((g >> 1) * 16 + r) * 16 + (g & 1) * 8) = u32x2{v[t][0], v[t][1]};
        asm volatile("" ::: "memory");
        lds_add(ct + C_QI, 1u);
        lds_st32(ct + C_EP, epoch);
      }
      static_assert(NE == 4 && NPK == 6 && NSLOT == 10, "");
      s_items(sc, helper ? TPW : c, helper || c >= NEARLY);
      static_assert(NH1 == 3 && NS == 7 && TPW == 33, "");
      const int hi = c == 0 ? 5 : c - 2;
      if (hi >= (helper ? 4 : 0)) s_half(sc, TPW - NH1 + (hi >> 1), hi & 1);
      TNOW(tse);
      rslot = c;
    }
  }

  if (rslot >= 0 && ((blockIdx.x >> 3) & 3) == 1) {
    LANE_VARS
    __builtin_amdgcn_s_setprio(0);
    const int rid = ((blockIdx.x >> 5) << 3) | (blockIdx.x & 7);
    const int qa = ((rid >> 1) & 3) * 4 + ((rid >> 3) & 1) * 2;
    const char* src = box + (size_t)(rid * RSTR + rslot * 32) * 128;
    const int e = lane & 7;
    const int oidx = (qa + (e & 1)) * CC + 48 * (rid >> 4) + 16 * (e < 6 ? e >> 1 : 0) + 8 * (rid & 1) + (lane >> 3);
    const unsigned short qv = ((const gus*)q)[oidx];
    u32x4 am;
#pragma unroll
    for (int d = 0; d < 4; ++d) am[d] = (e >> 1) == d ? ((e & 1) ? 0xffff0000u : 0x0000ffffu) : 0u;
    const unsigned epoch = *(LDS_AS(unsigned)*)(L0 + CT_OFF + C_EP);
    const unsigned arf = lbase + RED_OFF + 4096 + (lane & 1) * 4;
    poll_ge_sleep(ct + C_FREE + ((TPW - NPK) % NSLOT) * 4, 8u * ((TPW - NPK) / NSLOT + 1));
    lds_st32(arf + rslot * 8, 0u);
    unsigned long long tr;
#define R_LD(d, m, o) "s_mov_b64 exec, s[" m "]\n\tglobal_load_dwordx4 v[" d "], %[vo], %[src] offset:" #o " sc1\n\t"
#define R_W(l, a0, a1, a2, a3) "v_cvt_pk_bf16_f32 v" #a3 ", v" #a3 ", v" #a3 "\n\tv_and_b32 v" #a0 ", v60, v" #a3 "\n\tv_and_b32 v" #a1 ", v61, v" #a3 "\n\t" \
                               "v_and_b32 v" #a2 ", v62, v" #a3 "\n\tv_and_b32 v" #a3 ", v63, v" #a3 "\n\t"
#define R_G(n, w, m, l, d0, d1, d2, d3, a0, a1, a2, a3, c) \
  "s_mov_b64 exec, s[" m "]\n\ts_cbranch_execz " #n "f\n\ts_waitcnt vmcnt(" #w ")\n\tv_bitop3_b32 v" #l ", v" #d0 ", v" #d1 ", v" #d2 " bitop3:0x96\n\t" \
  "v_bitop3_b32 v" #l ", v" #l ", v" #d3 ", %[tag] bitop3:0x96\n\tv_mov_b32 v" #d3 ", s49\n\ts_mov_b64 exec, -1\n\tv_exp_f32 v" #a3 ", v" #l "\n\t" \
  "v_xor_b32_dpp v59, v" #l ", v" #l " quad_perm:[2,3,0,1] row_mask:0xf bank_mask:0x5\n\tv_xor_b32_dpp v59, v" #l ", v" #l " row_shr:4 row_mask:0xf bank_mask:0xa\n\t" \
  R_W(l, a0, a1, a2, a3) "v_cmp_ne_u32 s[" m "], 0, v59\n\ts_nop 0\n" \
  "1" #n ":\n\tv_mfma_f32_16x16x32_bf16 v[36:39], v[" #a0 ":" #a3 "], v[" #d0 ":" #d3 "], " c "\n\t"
#define R_X(n) #n ":\n\ts_mov_b64 exec, -1\n\ts_nop 2\n\ts_branch 1" #n "b\n"
#define R_F(l, d0, d3, a0, a1, a2, a3, c) "v_sub_f32 v" #a3 ", v" #l ", v56\n\tv_exp_f32 v" #a3 ", v" #a3 "\n\ts_nop 0\n\t" R_W(l, a0, a1, a2, a3) \
  "s_nop 1\n\tv_mfma_f32_16x16x32_bf16 v[36:39], v[" #a0 ":" #a3 "], v[" #d0 ":" #d3 "], " c "\n\t"
#define R_RD(d, a, b) "ds_read2st64_b32 v[" d "], %[ard] offset0:" #a " offset1:" #b "\n\t"
#define R_A(d, a) "v_pk_add_f32 v[" d "], v[" d "], v[" a "]\n\t"
#define R_S(x, f) "v_sub_f32 v" #f ", v" #f ", v56\n\tv_exp_f32 v" #f ", v" #f "\n\ts_nop 0\n\tv_mul_f32 v[" #x "], v[" #x "], v" #f "\n\tv_mul_f32 v[" #x " + 1], v[" #x " + 1], v" #f "\n\t"
    asm volatile("s_mov_b64 s[52:53], 0\n\ts_mov_b64 s[40:41], -1\n\ts_mov_b64 s[42:43], -1\n\ts_mov_b64 s[44:45], -1\n\ts_mov_b64 s[46:47], -1\n\ts_mov_b32 s48, %[cap]\n\ts_mov_b32 s49, 0x3f803f80\n\t"
                 "v_mov_b32 v57, 1\n\tv_mov_b32 v58, %[x1]\n"
                 "20:\n\ts_sleep 1\n\tds_read_b32 v56, v58\n\ts_sub_u32 s48, s48, 1\n\ts_cselect_b32 s50, 0, %[x1n]\n\ts_waitcnt lgkmcnt(0)\n\t"
                 "v_readfirstlane_b32 s51, v56\n\ts_cmp_lt_u32 s51, s50\n\ts_cbranch_scc1 20b\n\tv_mov_b32 v58, %[red]\n\ts_mov_b32 s48, %[gcap]\n\ts_sleep %[rs]\n"
                 "21:\n\t" R_LD("0:3", "40:41", 0) R_LD("4:7", "42:43", 1024) R_LD("8:11", "44:45", 2048) R_LD("12:15", "46:47", 3072)
                 "s_cmp_eq_u32 s48, %[gcap]\n\ts_cbranch_scc1 24f\n\ts_waitcnt vmcnt(0)\n24:\n\t"
                 R_G(1, 3, "40:41", 32, 0, 1, 2, 3, 16, 17, 18, 19, "0") R_G(2, 2, "42:43", 33, 4, 5, 6, 7, 20, 21, 22, 23, "v[36:39]")
                 R_G(3, 1, "44:45", 34, 8, 9, 10, 11, 24, 25, 26, 27, "v[36:39]") R_G(4, 0, "46:47", 35, 12, 13, 14, 15, 28, 29, 30, 31, "v[36:39]")
                 "v_max3_f32 v56, v32, v33, v34\n\ts_sub_u32 s48, s48, 1\n\ts_cbranch_scc1 22f\n\ts_or_b64 s[50:51], s[40:41], s[42:43]\n\tv_max_f32 v56, v56, v35\n\t"
                 "s_or_b64 s[50:51], s[50:51], s[44:45]\n\ts_or_b64 s[50:51], s[50:51], s[46:47]\n\tv_cmp_nlt_f32 vcc, |v56|, %[lim]\n\ts_cbranch_scc1 21b\n\t"
                 "s_cbranch_vccnz 25f\n"
                 "22:\n\ts_mov_b64 exec, %[wm]\n\tds_write_b128 %[awr], v[36:39]\n\ts_mov_b64 exec, 1\n\tds_add_rtn_u32 v59, v58, v57\n\ts_mov_b64 exec, -1\n\t"
                 R_RD("40:41", 0, 1) R_RD("42:43", 2, 3) R_RD("44:45", 4, 5) R_RD("46:47", 6, 7) R_RD("48:49", 8, 9) R_RD("50:51", 10, 11) R_RD("52:53", 12, 13) R_RD("54:55", 14, 15)
                 "s_waitcnt lgkmcnt(8)\n\tv_add_u32 v59, v59, v57\n\ts_nop 0\n\tv_readfirstlane_b32 s50, v59\n\ts_cmp_lg_u32 s50, 8\n\ts_cbranch_scc1 26f\n"
                 "23:\n\ts_waitcnt lgkmcnt(6)\n\t" R_A("40:41", "42:43") "s_waitcnt lgkmcnt(4)\n\t" R_A("44:45", "46:47") "s_waitcnt lgkmcnt(2)\n\t" R_A("48:49", "50:51")
                 R_A("40:41", "44:45") "s_waitcnt lgkmcnt(0)\n\t" R_A("52:53", "54:55") R_A("48:49", "52:53") R_A("40:41", "48:49")
                 "v_add_f32 v40, v40, v41\n\ts_nop 1\n\tv_mov_b32_dpp v41, v40 quad_perm:[2,3,2,3] row_mask:0xf bank_mask:0xf\n\ts_nop 1\n\t"
                 "v_mov_b32_dpp v41, v41 row_shl:4 row_mask:0xf bank_mask:0x5\n\tv_rcp_f32 v41, v41\n\ts_mov_b64 exec, %[om]\n\tv_mul_f32 v40, v40, v41\n\t"
                 "v_cvt_pk_bf16_f32 v40, v40, v40\n\tv_lshlrev_b32 v40, 16, v40\n\tv_add_f32 v40, v40, %[qf]\n\tv_cvt_pk_bf16_f32 v40, v40, v40\n\t"
                 "global_store_short %[oo], v40, %[out]\n\t"
                 "s_cmp_lg_u32 %[rid], 0\n\ts_cbranch_scc1 29f\n\ts_mov_b64 exec, 1\n\tv_mov_b32 v41, %[eo]\n\tglobal_store_dword v41, %[ep], %[bx] sc1\n\ts_branch 29f\n"
                 R_X(1) R_X(2) R_X(3) R_X(4)
                 "25:\n\tv_max_f32_dpp v56, v56, v56 row_ror:8 row_mask:0xf bank_mask:0xf\n\tv_mov_b32 v59, v56\n\ts_nop 1\n\tv_permlane16_swap_b32 v56, v59\n\tv_max_f32 v56, v56, v59\n\t"
                 "v_mov_b32 v59, v56\n\ts_nop 1\n\tv_permlane32_swap_b32 v56, v59\n\tv_max_f32 v56, v56, v59\n\tv_mov_b32 v57, 0x101\n\tv_add_u32 v59, %[rs8], %[arf]\n\tds_write_b32 v59, v56\n\t"
                 R_F(32, 0, 3, 16, 17, 18, 19, "0") R_F(33, 4, 7, 20, 21, 22, 23, "v[36:39]") R_F(34, 8, 11, 24, 25, 26, 27, "v[36:39]") R_F(35, 12, 15, 28, 29, 30, 31, "v[36:39]")
                 "s_nop 7\n\ts_branch 22b\n"
                 "26:\n\ts_waitcnt vmcnt(0)\n\ts_and_b32 s50, s50, 0xff\n\ts_cmp_lg_u32 s50, 8\n\ts_cbranch_scc1 29f\n\t"
                 "ds_read2_b32 v[0:1], %[arf] offset1:2\n\tds_read2_b32 v[2:3], %[arf] offset0:4 offset1:6\n\tds_read2_b32 v[4:5], %[arf] offset0:8 offset1:10\n\t"
                 "ds_read2_b32 v[6:7], %[arf] offset0:12 offset1:14\n\ts_waitcnt lgkmcnt(0)\n\tv_max3_f32 v56, v0, v1, v2\n\tv_max3_f32 v56, v56, v3, v4\n\t"
                 "v_max3_f32 v56, v56, v5, v6\n\tv_max_f32 v56, v56, v7\n\t"
                 R_S(40, 0) R_S(42, 1) R_S(44, 2) R_S(46, 3) R_S(48, 4) R_S(50, 5) R_S(52, 6) R_S(54, 7) "s_branch 23b\n"
                 "29:\n\ts_mov_b64 exec, -1\n\ts_waitcnt lgkmcnt(0)"
                 : "=&{s[52:53]}"(tr)
                 : "{v[60:63]}"(am), [vo] "v"(lane * 16), [src] "s"(src), [tag] "v"(line_tag(epoch, e)),
                   [awr] "v"(lbase + RED_OFF + (rslot * 2 + (r >> 3)) * 256 + (r & 7) * 32 + (g & 1) * 16), [ard] "v"(lbase + RED_OFF + lane * 4), [arf] "v"(arf),
                   [qf] "v"((unsigned)qv << 16), [oo] "v"(oidx * 2), [out] "s"(KARG(kb, 0)), [ep] "v"(epoch + 1u), [bx] "s"(box), [rid] "s"(rid), [rs8] "s"(rslot * 8),
                   [wm] "s"(0xff00ff0000ff00ffull), [om] "s"(0x3f3f3f3f3f3f3f3full), [lim] "s"(64.f),
                   [cap] "i"(SPIN_CAP), [gcap] "i"(GSPIN_CAP), [x1] "i"(CT_OFF + C_X1), [x1n] "i"(NE), [red] "i"(CT_OFF + C_RED), [rs] "i"(RSLEEP), [eo] "i"(EPOCH_OFF)
                 : "memory", "scc", "vcc", "s40", "s41", "s42", "s43", "s44", "s45", "s46", "s47", "s48", "s49", "s50", "s51", "v0", "v1", "v2", "v3", "v4", "v5", "v6", "v7", "v8", "v9", "v10", "v11", "v12", "v13", "v14", "v15", "v16", "v17", "v18", "v19", "v20", "v21", "v22", "v23", "v24", "v25", "v26", "v27", "v28", "v29", "v30", "v31", "v32", "v33", "v34", "v35", "v36", "v37", "v38", "v39", "v40", "v41", "v42", "v43", "v44", "v45", "v46", "v47", "v48", "v49", "v50", "v51", "v52", "v53", "v54", "v55", "v56", "v57", "v58", "v59");
    wait_vm<0>();
    if (blockIdx.x == 8) while ((unsigned)__builtin_amdgcn_s_memrealtime() - tent < 2700 + 4 * dq) __builtin_amdgcn_s_sleep(1);
  }
}

#undef q
#undef f0
#undef f1
#undef wq
#undef wk
#undef wv
#undef wo
#undef box
extern "C" __global__ void dino_end() {}

extern "C" void kernel_launch(void* const* d_in, const int* in_sizes, int n_in,
                              void* d_out, int out_size, void* d_ws, size_t ws_size,
                              hipStream_t stream) {
  dino_main<<<NWG, 768, 0, stream>>>((const unsigned short*)d_in[0], (const char*)d_in[1], (const char*)d_in[2], (const char*)d_in[3],
                                     (const char*)d_in[4], (const char*)d_in[5], (const char*)d_in[6], (char*)d_ws + ((ws_size - (4u << 20)) & ~(size_t)0x1fffff),
                                     (unsigned short*)d_out);
}
```

```cpp
#include <hip/hip_runtime.h>
#include <hip/hip_bf16.h>
#include <stdint.h>


using bf16x8  = __attribute__((ext_vector_type(8))) short;
using f32x4   = __attribute__((ext_vector_type(4))) float;
using f32x2   = __attribute__((ext_vector_type(2))) float;
using u32x4   = __attribute__((ext_vector_type(4))) unsigned;
using u32x2   = __attribute__((ext_vector_type(2))) unsigned;
using v4i16_t = __attribute__((ext_vector_type(4))) short;
typedef __attribute__((address_space(3))) char* lds_ptr;
#define LDS_AS(T) __attribute__((address_space(3))) T

constexpr int CC = 192;
constexpr int NWG = 256, NS = 7, NE = 4;
constexpr int TPW = 33;
constexpr int ROWB = 384;
constexpr int SLOT = 40 * ROWB;
constexpr int NSLOT = 10;
constexpr int RSTR = 322;
constexpr int EPOCH_OFF = 64 * RSTR * 128, QK_OFF = EPOCH_OFF + 4096, W2F_OFF = QK_OFF + 12288, W2_OFF = W2F_OFF + 1024;
static_assert(W2_OFF + 72 * 1024 <= (4 << 20), "");
constexpr int GSPIN_CAP = 1 << 16;
#define PACE16 777
#define PACES 810
#define PK1 14
#define PWIN 35
#define AHI 22
#define ADL 60
#define AMG 200
#define T0BACK 7
#define HDUE 93
#define QKW0 200
#define QKW1 2
#define RSLEEP 25
#define W2SCH 0x0311180000000000ull
constexpr int NEARLY = 4;
constexpr int NH1 = 3;
constexpr int NPK = 6;
constexpr float EPS = 1e-5f;
constexpr float SQRT_C = 13.856406460551018f;
constexpr float LOG2E = 1.4426950408889634f;
constexpr float THR = 20.0f;
constexpr float TOFF = 192.0f;
constexpr int SPIN_CAP = 1 << 22;

constexpr int IMG_A = NSLOT * SLOT;
constexpr int CT_OFF = IMG_A + 6144;
constexpr int PKM = CT_OFF + 512, PKU = PKM + NPK * 128;
constexpr int ONES_OFF = PKU + NPK * 256;
constexpr int JUNK = ONES_OFF + ROWB;
constexpr int LDS_BYTES = JUNK + 256;
constexpr int H_R = 192 * ROWB, H_Q = H_R + 32 * ROWB, H_C = H_Q + 6144;
static_assert(LDS_BYTES <= 163840 && (PKU & 15) == 0 && (ONES_OFF & 127) == 0, "lds");
constexpr int C_FULL = 0, C_FREE = 64, C_EP = 128, C_RED = 144, C_ISS = 152, C_X1 = 156, C_QI = 160, C_PFL = 192, C_CONS = 256,
              C_MREF = 320, C_LW = 384;

typedef __bf16 bf16x2_t __attribute__((ext_vector_type(2)));
__device__ __forceinline__ unsigned cvtpk(float lo, float hi) {
  return __builtin_bit_cast(unsigned, __builtin_convertvector(f32x2{lo, hi}, bf16x2_t));
}
__device__ __forceinline__ float bflo(unsigned p) { return __uint_as_float(p << 16); }
__device__ __forceinline__ float bfhi(unsigned p) { return __uint_as_float(p & 0xffff0000u); }
__device__ __forceinline__ bf16x8 pack8(const f32x4& a, const f32x4& b) {
  return __builtin_bit_cast(bf16x8, u32x4{cvtpk(a[0], a[1]), cvtpk(a[2], a[3]), cvtpk(b[0], b[1]), cvtpk(b[2], b[3])});
}
__device__ __forceinline__ float xrow16_max(float x) {
  auto s = __builtin_amdgcn_permlane16_swap(__float_as_uint(x), __float_as_uint(x), false, false);
  x = fmaxf(__uint_as_float(s[0]), __uint_as_float(s[1]));
  auto t = __builtin_amdgcn_permlane32_swap(__float_as_uint(x), __float_as_uint(x), false, false);
  return fmaxf(__uint_as_float(t[0]), __uint_as_float(t[1]));
}
__device__ __forceinline__ float xrow16_sum(float x) {
  auto s = __builtin_amdgcn_permlane16_swap(__float_as_uint(x), __float_as_uint(x), false, false);
  x = __uint_as_float(s[0]) + __uint_as_float(s[1]);
  auto t = __builtin_amdgcn_permlane32_swap(__float_as_uint(x), __float_as_uint(x), false, false);
  return __uint_as_float(t[0]) + __uint_as_float(t[1]);
}
__device__ __forceinline__ v4i16_t tr_read(lds_ptr p) {
  return __builtin_amdgcn_ds_read_tr16_b64_v4i16((LDS_AS(v4i16_t)*)p);
}
__device__ __forceinline__ f32x4 mfma(bf16x8 a, bf16x8 b, f32x4 c) {
  return __builtin_amdgcn_mfma_f32_16x16x32_bf16(a, b, c, 0, 0, 0);
}
__device__ __forceinline__ int swz(int p) { return (p & 6) | ((p >> 2) & 1); }
constexpr unsigned swz_tbl(int i) {
  unsigned t = 0;
  for (int j = 0; j < 8; ++j) { const int p = (8 * i + j) / 3; t |= (unsigned)((p & 6) | ((p >> 2) & 1)) << (4 * j); }
  return t;
}
__device__ __forceinline__ bf16x8 mk8(v4i16_t a, v4i16_t b) { return bf16x8{a[0], a[1], a[2], a[3], b[0], b[1], b[2], b[3]}; }

__device__ __forceinline__ void lds_st32(unsigned addr, unsigned v) {
  asm volatile("ds_write_b32 %0, %1" : : "v"(addr), "v"(v) : "memory");
}
__device__ __forceinline__ unsigned lds_ld32(unsigned addr) {
  unsigned v;
  asm volatile("ds_read_b32 %0, %1\n\ts_waitcnt lgkmcnt(0)" : "=v"(v) : "v"(addr) : "memory");
  return __builtin_amdgcn_readfirstlane(v);
}
__device__ __forceinline__ void lds_add(unsigned addr, unsigned n) {
  asm volatile("s_mov_b64 exec, 1\n\tds_add_u32 %0, %1\n\ts_mov_b64 exec, -1\n\ts_nop 3" : : "v"(addr), "v"(n) : "memory");
}
__device__ __forceinline__ void poll_ge_sleep(unsigned addr, unsigned need) {
  need = __builtin_amdgcn_readfirstlane(need);
  unsigned v, s, n2, cnt = SPIN_CAP;
  asm volatile("s_branch 2f\n1:\n\ts_sleep 2\n2:\n\tds_read_b32 %0, %4\n\ts_sub_u32 %3, %3, 1\n\ts_cselect_b32 %2, 0, %5\n\ts_waitcnt lgkmcnt(0)\n\t"
               "v_readfirstlane_b32 %1, %0\n\ts_cmp_lt_u32 %1, %2\n\ts_cbranch_scc1 1b"
               : "=&v"(v), "=&s"(s), "=&s"(n2), "+s"(cnt) : "v"(addr), "s"(need) : "memory", "scc");
}
__device__ __forceinline__ bf16x8 img_ld(lds_ptr L0, int off, int kk, int lane) { return *(LDS_AS(bf16x8)*)(L0 + off + kk * 1024 + lane * 16); }
__device__ __forceinline__ void img_st(lds_ptr L0, int off, int kk, int lane, bf16x8 v) { *(LDS_AS(bf16x8)*)(L0 + off + kk * 1024 + lane * 16) = v; }

template <int N> __device__ __forceinline__ void wait_vm() { asm volatile("s_waitcnt vmcnt(%0)" :: "i"(N) : "memory"); }
constexpr int A_IMG = ((TPW + 1 - NPK) % NSLOT) * SLOT, RED_OFF = ((TPW - NPK) % NSLOT) * SLOT;
__device__ __forceinline__ unsigned line_tag(unsigned epoch, unsigned i) {
  unsigned x = (epoch + 1u + i * 0x9E3779B9u) * 0x85EBCA6Bu;
  x ^= x >> 15; x *= 0xC2B2AE35u;
  return x ^ (x >> 13);
}
__device__ __forceinline__ unsigned epoch_load(const char* box) {
  unsigned e;
  asm volatile("global_load_dword %0, %1, %2 sc1\n\ts_waitcnt vmcnt(0)" : "=v"(e) : "v"(EPOCH_OFF), "s"(box) : "memory");
  return __builtin_amdgcn_readfirstlane(e);
}
__device__ __forceinline__ int tr_off(int row, int col4) {
  const int ch = col4 >> 1;
  return row * ROWB + (((ch & ~7) | ((ch & 7) ^ swz(row & 15))) << 4) + 8 * (col4 & 1);
}
#define PIN3(a, i) "+v"(a[i]), "+v"(a[i + 1]), "+v"(a[i + 2])

#define DMA1(d, b, off, pol) "global_load_lds_dwordx4 %[" #d "], %[" #b "] offset:" #off pol "\n\t"
__device__ __forceinline__ void issue_rows16(const char* a0, unsigned dst, const unsigned (&d)[3]) {
  unsigned keep;
  asm volatile("s_mov_b32 %[keep], m0\n\ts_mov_b32 m0, %[dst]\n\ts_nop 0\n\t"
               DMA1(d0, a, 0, "") DMA1(d1, a, 1024, "") DMA1(d2, a, 2048, "") DMA1(d0, a, 3072, "")
               "s_add_u32 m0, %[dst], 0x1000\n\ts_nop 0\n\t" DMA1(d1, b, 0, "") DMA1(d2, b, 1024, "") "s_mov_b32 m0, %[keep]"
               : [keep] "=&s"(keep) : [dst] "s"(dst), [a] "s"(a0), [b] "s"(a0 + 4096), [d0] "v"(d[0]), [d1] "v"(d[1]), [d2] "v"(d[2]) : "memory", "scc");
}
#define LD_VM "s_getreg_b32 s43, hwreg(HW_REG_IB_STS, 22, 2)\n\ts_getreg_b32 s42, hwreg(HW_REG_IB_STS, 0, 4)\n\ts_lshl4_add_u32 s42, s43, s42\n\t"
#define LD_GATE(n, X) \
  #n "0:\n\t" LD_VM "s_cmp_le_i32 s42, s46\n\ts_cbranch_scc1 " #n "2f\n" \
  #n "1:\n\t" X "s_cmp_gt_u32 s42, %[win]\n\ts_cbranch_scc1 " #n "5f\n\ts_cmp_ge_u32 s45, s44\n\ts_cbranch_scc1 " #n "7f\n" \
  #n "6:\n\ts_memrealtime s[40:41]\n\ts_sleep 1\n\ts_waitcnt lgkmcnt(0)\n\ts_sub_u32 s45, s40, %[t0]\n\ts_lshl_b32 s45, s45, 4\n\ts_cmp_lt_u32 s45, s44\n\ts_cbranch_scc1 " #n "6b\n" \
  #n "7:\n\t" LD_VM
#define LD_NEXT \
  "s_cmp_gt_u32 s42, %[ahi]\n\ts_cselect_b32 s42, %[amg], 0\n\ts_sub_u32 s43, s45, s44\n\ts_cmp_lt_u32 s43, s42\n\ts_cselect_b32 s42, %[p3] + %[adl], s39\n\t" \
  "s_add_u32 s44, s44, s42\n\ts_add_u32 s46, s46, 5\n\t"
#define LD_PUB0(n, a, b, c) \
  #n "2:\n\tv_add_u32 %[pub], 2, %[pub]\n\ts_cmp_ge_u32 s46, 30\n\ts_cbranch_scc1 " #n "4f\n\ts_cmp_ge_u32 s46, 15\n\ts_cbranch_scc1 " #n "3f\n\t" \
  "s_waitcnt vmcnt(" #a ")\n" #n "3:\n\ts_waitcnt vmcnt(" #b ")\n" #n "4:\n\ts_waitcnt vmcnt(" #c ")\n\tds_write_b32 %[af], %[pub]\n\t" \
  "s_sub_u32 s46, s46, 15\n\ts_branch " #n "1b\n"
#define LD_PUB(n, a, b, c) LD_PUB0(n, a, b, c) \
  #n "5:\n\ts_sleep 1\n\ts_sub_i32 s47, s47, 1\n\ts_cmp_gt_i32 s47, 0\n\ts_cbranch_scc1 " #n "0b\n\ts_waitcnt vmcnt(%[win])\n\ts_branch " #n "6b\n"
#define LD1(d, b, off) "global_load_lds_dwordx4 %[" #d "], s[" #b "] offset:" #off " nt\n\t"

#define PV_REQ(P, U, M, C, CA) \
  "v_lshl_add_u32 v150, s44, 3, %[apk]\n\tv_lshl_add_u32 v151, s44, 1, %[apu]\n\tv_add_u32 v152, s44, %[apm]\n\t" \
  "ds_read_b32 v" #C ", v" #CA "\n\tds_read_b128 v[" P "], v150\n\tds_read_b64 v[" M "], v152\n\tds_read_b128 v[" U "], v151\n\t"
#define PV_TR(d0, d1, a, o) "ds_read_b64_tr_b16 v[" #d0 ":" #d1 "], v" #a " offset:" #o "\n\t"
#define PV_FETCH(y0, y0b, a0, a0b, b0, b0b, y1, y1b, a1, a1b, b1, b1b, y2, y2b, a2, a2b, b2, b2b) \
  "v_add_u32 v153, s45, %[ox]\n\tv_add_u32 v154, s45, %[oy]\n\t" \
  PV_TR(a0, a0b, 153, 0) PV_TR(b0, b0b, 153, 6144) PV_TR(a1, a1b, 153, 128) PV_TR(b1, b1b, 153, 6272) PV_TR(a2, a2b, 153, 256) PV_TR(b2, b2b, 153, 6400) \
  PV_TR(y0, y0b, 154, 0) PV_TR(y1, y1b, 154, 128) PV_TR(y2, y2b, 154, 256) \
  "s_add_u32 s45, s45, %[slot]\n\ts_cmp_eq_u32 s45, %[ring]\n\ts_cselect_b32 s45, 0, s45\n\t"
#define PV_FETCH_A PV_FETCH(90, 91, 92, 93, 94, 95, 96, 97, 98, 99, 100, 101, 102, 103, 104, 105, 106, 107)
#define PV_FETCH_B PV_FETCH(108, 109, 110, 111, 112, 113, 114, 115, 116, 117, 118, 119, 120, 121, 122, 123, 124, 125)
#define PV_MF(d, a, p) "v_mfma_f32_16x16x32_bf16 v[" d "], v[" a "], v[" p "], v[" d "]\n\t"
#define PV_MOV(d, s) "v_mov_b32 v" #d ", v" #s "\n\t"
#define PV_WAITF(n, CA)   \
  #n ":\n\tds_read_b32 v155, v" #CA "\n\ts_sub_i32 s47, s47, 1\n\ts_cmp_lt_i32 s47, 0\n\ts_cbranch_scc1 " #n "9f\n\ts_waitcnt lgkmcnt(0)\n\t" \
  "v_cmp_lt_u32 vcc, v155, %[need]\n\ts_and_b64 vcc, vcc, 2\n\ts_cbranch_scc0 " #n "9f\n\ts_sleep 2\n\ts_branch " #n "b\n" #n "9:\n\ts_waitcnt lgkmcnt(0)\n\t"
#define PV_STEP(n, P, U, M, m0, m1, C, CA, P2, U2, M2, C2, CA2, X0, X1, X2, Y0, Y1, Y2, MV0, MV1, MV2, HOOK) \
  "s_waitcnt lgkmcnt(10)\n\t" \
  "v_cmp_lt_u32 vcc, v" #C ", %[need]\n\tv_cmp_neq_f32 s[48:49], v" #m0 ", v88\n\tv_cmp_neq_f32 s[50:51], v" #m1 ", v88\n\t" \
  "v_add_u32 v" #CA2 ", 4, v" #CA "\n\ts_or_b64 s[48:49], s[48:49], vcc\n\tv_cmp_eq_u32 s[52:53], v" #CA2 ", %[lim]\n\t" \
  "s_or_b64 s[48:49], s[48:49], s[50:51]\n\ts_cbranch_scc1 " #n "1f\n" \
  #n "0:\n\ts_add_u32 s44, s44, 128\n\ts_cmp_eq_u32 s44, %[pring]\n\ts_cselect_b32 s44, 0, s44\n\t" \
  "v_cndmask_b32 v" #CA2 ", v" #CA2 ", %[base], s[52:53]\n\t" \
  PV_MF("84:87", U, P) PV_MF("72:75", X0, P) PV_MF("76:79", X1, P) MV0 PV_MF("80:83", X2, P) MV1 \
  PV_MF("72:75", Y0, P) MV2 HOOK PV_REQ(P2, U2, M2, C2, CA2) PV_MF("76:79", Y1, P) PV_MF("80:83", Y2, P)
#define PV_REL(CA) \
  "s_mov_b64 exec, 3\n\tds_add_u32 v" #CA ", %[one] offset:64\n\ts_mov_b64 exec, -1\n\tv_add_u32 %[need], 2, %[need]\n\ts_add_u32 s46, s46, 1\n\t"
#define PV_SC1(p) "v_lshlrev_b32 v151, 16, v" #p "\n\tv_and_b32 v152, 0xffff0000, v" #p "\n\tv_mul_f32 v151, v151, v150\n\tv_mul_f32 v152, v152, v150\n\t" \
                  "v_cvt_pk_bf16_f32 v" #p ", v151, v152\n\t"
#define PV_MUL4(a, b, c, d) "v_mul_f32 v" #a ", v" #a ", v150\n\tv_mul_f32 v" #b ", v" #b ", v150\n\tv_mul_f32 v" #c ", v" #c ", v150\n\tv_mul_f32 v" #d ", v" #d ", v150\n\t"
#define PV_RESC(m0, m1, p0, p1, p2, p3) \
  "v_mov_b32 v150, v88\n\tv_max3_f32 v88, v88, v" #m0 ", v" #m1 "\n\tv_sub_f32 v150, v150, v88\n\tv_exp_f32 v150, v150\n\ts_nop 1\n\t" \
  PV_MUL4(72, 73, 74, 75) PV_MUL4(76, 77, 78, 79) PV_MUL4(80, 81, 82, 83) PV_MUL4(84, 85, 86, 87) \
  "v_sub_f32 v150, v" #m0 ", v88\n\tv_exp_f32 v150, v150\n\ts_nop 1\n\t" PV_SC1(p0) PV_SC1(p1) \
  "v_sub_f32 v150, v" #m1 ", v88\n\tv_exp_f32 v150, v150\n\ts_nop 1\n\t" PV_SC1(p2) PV_SC1(p3)
#define PV_SLOW(n, P, U, M, m0, m1, C, CA, p0, p1, p2, p3) \
  #n "1:\n\ts_bitcmp0_b32 vcc_lo, 0\n\ts_cbranch_scc1 " #n "4f\n\ts_cmp_gt_u32 s46, %[tight]\n\ts_cbranch_scc1 " #n "3f\n" \
  #n "2:\n\ts_sleep 2\n\tds_read_b32 v" #C ", v" #CA "\n\ts_sub_i32 s47, s47, 1\n\ts_cmp_lt_i32 s47, 0\n\ts_cbranch_scc1 " #n "3f\n\ts_waitcnt lgkmcnt(0)\n\t" \
  "v_cmp_lt_u32 vcc, v" #C ", %[need]\n\ts_and_b64 vcc, vcc, 1\n\ts_cbranch_scc1 " #n "2b\n" \
  #n "3:\n\t" PV_REQ(P, U, M, C, CA) "s_sub_i32 s47, s47, 1\n\ts_cmp_lt_i32 s47, 0\n\ts_cselect_b32 s54, 0, 1\n\ts_waitcnt lgkmcnt(0)\n\t" \
  "v_cmp_lt_u32 vcc, v" #C ", %[need]\n\tv_cmp_neq_f32 s[48:49], v" #m0 ", v88\n\tv_cmp_neq_f32 s[50:51], v" #m1 ", v88\n\t" \
  "s_and_b32 s54, s54, vcc_lo\n\ts_cbranch_scc1 " #n "3b\n\ts_or_b64 s[48:49], s[48:49], s[50:51]\n\ts_cbranch_scc0 " #n "0b\n\ts_branch " #n "5f\n" \
  #n "4:\n\tv_cmp_neq_f32 s[48:49], v" #m0 ", v88\n\ts_or_b64 s[48:49], s[48:49], s[50:51]\n\ts_cbranch_scc0 " #n "0b\n" \
  #n "5:\n\t" PV_RESC(m0, m1, p0, p1, p2, p3) \
  "s_branch " #n "0b\n"
#define PV_W2(a0, a1, b0, b1, c0, c1) \
  "global_load_dwordx4 v[" #a0 ":" #a1 "], v150, %[w2] sc1\n\tglobal_load_dwordx4 v[" #b0 ":" #b1 "], v150, %[w2] offset:1024 sc1\n\t" \
  "global_load_dwordx4 v[" #c0 ":" #c1 "], v150, %[w2] offset:2048 sc1\n\tv_add_u32 v150, 0xc00, v150\n\ts_sub_u32 s54, s54, 1\n\ts_cbranch_scc1 30b\n\t"

#define PV_PK(a, b) "v_pk_fma_f32 v[" #a ":" #b "], v[" #a ":" #b "], v[150:151], v[150:151] op_sel:[0,0,1] op_sel_hi:[1,0,1]\n\t"
#define PV_NRM(a, b, c, d, o)   \
  "v_pk_fma_f32 v[" #a ":" #b "], v[" #a ":" #b "], v[150:151], v[150:151] op_sel:[0,0,1] op_sel_hi:[1,0,1]\n\t" \
  "v_pk_fma_f32 v[" #c ":" #d "], v[" #c ":" #d "], v[150:151], v[150:151] op_sel:[0,0,1] op_sel_hi:[1,0,1]\n\t" \
  "v_cvt_pk_bf16_f32 v" #a ", v" #a ", v" #b "\n\tv_cvt_pk_bf16_f32 v" #b ", v" #c ", v" #d "\n\tds_write_b64 v120, v[" #a ":" #b "] offset:" #o "\n\t"
#define PV_G(n, a, w0, w1, w2, c0, c1, c2) "s_waitcnt lgkmcnt(" #n ")\n\tv_mfma_f32_16x16x32_bf16 v[72:75], v[" a "], v[" w0 "], " c0 "\n\t" \
  "v_mfma_f32_16x16x32_bf16 v[76:79], v[" a "], v[" w1 "], " c1 "\n\tv_mfma_f32_16x16x32_bf16 v[80:83], v[" a "], v[" w2 "], " c2 "\n\t"
#define PV_Z4(a, b, c, d) "v_mov_b32 v" #a ", 0\n\tv_mov_b32 v" #b ", 0\n\tv_mov_b32 v" #c ", 0\n\tv_mov_b32 v" #d ", 0\n\t"

using u32x8 = __attribute__((ext_vector_type(8))) unsigned;
struct SCtx {
  lds_ptr L0;
  unsigned ct, apk, apm, api, amr;
  int o_rb, o_yb, ev;
  u32x8 q[3];
};
__device__ __forceinline__ bf16x8 s_qf(const SCtx& c, int j) {
  const u32x8& v = c.q[j >> 1];
  const int o = 4 * (j & 1);
  return __builtin_bit_cast(bf16x8, u32x4{v[o], v[o + 1], v[o + 2], v[o + 3]});
}
#define S_M(d, a, b) "v_mfma_f32_16x16x32_bf16 v[" d "], v[" a "], v[" b "], v[" d "]\n\t"
#define S_M0(d, a, b) "v_mfma_f32_16x16x32_bf16 v[" d "], v[" a "], v[" b "], 0\n\t"
#define S_R(d, a, o) "ds_read_b128 v[" d "], v" #a " offset:" #o "\n\t"
#define S_W(n) "s_waitcnt lgkmcnt(" #n ")\n\t"
#define S_D " row_mask:0xf bank_mask:0xf\n\t"
#define S_3(x, y) S_M("100:103", x, y) S_M("104:107", x, x) S_M("96:99", y, y)
#define S_SQ(g, h) "v_fmac_f32 v" #g ", 2.0, v" #h "\n\t"
#define S_SQD(g, h) "v_fmac_f32_dpp v" #g ", v" #h ", %[two] quad_perm:[0,0,2,2]" S_D
#define S_U(u, h, y) "v_add_f32_dpp v" #u ", v" #h ", v" #y " quad_perm:[1,1,3,3]" S_D
#define S_C(d, a, b, m) "v_cndmask_b32 v" #d ", v" #a ", v" #b ", %[" #m "]\n\t"
#define S_B(d, n) "v_mov_b32_dpp v" #d ", v157 row_newbcast:" #n S_D
#define S_BASE(d, u, t) "v_pk_fma_f32 v[" d "], v[" u "], v[116:117], v[" t "] op_sel:[0,1,0] op_sel_hi:[1,1,0]\n\t"
#define S_LG(d, r) "v_pk_fma_f32 v[" d "], v[" d "], v[" r "], %[toff] op_sel_hi:[1,1,0]\n\t"
#define S_SUB(p, d) "v_pk_add_f32 v[" p "], v[" d "], v[150:151] op_sel_hi:[1,0] neg_lo:[0,1] neg_hi:[0,1]\n\t"
#define S_E(p) "v_exp_f32 v" #p ", v" #p "\n\t"
#define S_MUL(p, r) "v_pk_mul_f32 v[" p "], v[" p "], v[" r "]\n\t"
#define S_POS   \
  "s_mul_i32 s50, s42, 26\n\ts_lshr_b32 s50, s50, 8\n\ts_mul_i32 s50, s50, -10\n\ts_add_u32 s50, s50, s42\n\ts_mul_i32 s45, s50, %[slot]\n\ts_mov_b32 s43, s45\n\t"
#define S_POLL(n, a, o, need, back, far) \
  #n ":\n\tds_read_b32 v152, v" #a " offset:" #o "\n\ts_sub_i32 s47, s47, 1\n\ts_cmp_lt_i32 s47, 0\n\ts_waitcnt lgkmcnt(0)\n\ts_cbranch_scc1 " back "\n\t" \
  "v_readfirstlane_b32 s40, v152\n\ts_cmp_ge_u32 s40, " need "\n\ts_cbranch_scc1 " back "\n\ts_sleep 2\n\t" \
  "s_add_u32 s40, s40, " far "\n\ts_cmp_ge_u32 s40, " need "\n\ts_cbranch_scc1 " #n "b\n\ts_sleep 12\n\ts_branch " #n "b\n"
__device__ __forceinline__ void s_items(SCtx& c, int k0, int late) {
  asm volatile("s_mov_b32 s47, %[cap]\n\ts_mov_b32 s49, 0\n\ts_mov_b32 s42, %[k0]\n\ts_cmp_lg_u32 %[late], 0\n\ts_cbranch_scc1 7f\n"
               "1:\n\ts_cmp_ge_u32 s42, %[end]\n\ts_cbranch_scc1 9f\n\t"
               S_POS
               "s_mul_i32 s48, s42, 43\n\ts_lshr_b32 s48, s48, 8\n\ts_mul_i32 s46, s48, -6\n\ts_add_u32 s46, s46, s42\n\ts_lshl2_add_u32 s48, s48, %[ns]\n\t"
               "v_add_u32 v144, s45, %[orb]\n\tv_mad_u32_u24 v145, s43, %[ev], %[oyb]\n\ts_lshl2_add_u32 s40, s46, %[pfl]\n\ts_lshl2_add_u32 s41, s50, %[full]\n\t"
               "v_xor_b32 v146, 64, v144\n\tv_xor_b32 v147, 64, v145\n\tv_mov_b32 v148, s40\n\tv_mov_b32 v149, s41\n\t"
               "s_lshl1_add_u32 s44, s42, 2\n\ts_lshl_b32 s46, s46, 7\n\ts_mov_b64 vcc, %[dg]\n\ts_cmp_lt_u32 s49, s44\n\ts_cbranch_scc1 5f\n"
               "2:\n\tds_read_b32 v150, %[amr]\n\tds_read_b32 v151, v148 offset:64\n\t"
               S_R("24:27", 145, 0) S_R("48:51", 144, 0) S_R("28:31", 147, 0) S_R("52:55", 146, 0) S_R("32:35", 145, 128) S_R("56:59", 144, 128)
               S_R("36:39", 147, 128) S_R("60:63", 146, 128) S_R("40:43", 145, 256) S_R("64:67", 144, 256) S_R("44:47", 147, 256) S_R("68:71", 146, 256)
               S_W(10) S_M0("100:103", "48:51", "24:27") S_M0("104:107", "48:51", "48:51") S_R("72:75", 144, 6144) S_R("76:79", 146, 6144) S_M0("96:99", "24:27", "24:27")
               S_W(10) S_M("100:103", "52:55", "28:31") S_M("104:107", "52:55", "52:55") S_R("80:83", 144, 6272) S_R("84:87", 146, 6272) S_M("96:99", "28:31", "28:31")
               S_W(10) S_M("100:103", "56:59", "32:35") S_M("104:107", "56:59", "56:59") S_R("88:91", 144, 6400) S_R("92:95", 146, 6400) S_M("96:99", "32:35", "32:35")
               "s_mov_b64 exec, 1\n\tds_add_u32 v149, %[four] offset:64\n\ts_mov_b64 exec, -1\n\tv_readfirstlane_b32 s51, v151\n\ts_setprio 1\n\ts_nop 0\n\t"
               S_W(11) S_3("60:63", "36:39") S_W(9) S_3("64:67", "40:43") S_W(7) S_3("68:71", "44:47")
               S_W(6) S_M0("108:111", "72:75", "24:27") S_M0("112:115", "72:75", "72:75")
               S_W(5) S_M("108:111", "76:79", "28:31") S_M("112:115", "76:79", "76:79")
               S_W(4) S_M("108:111", "80:83", "32:35")
               "v_mov_b32_dpp v152, v96 quad_perm:[1,1,3,3]" S_D "v_mov_b32_dpp v153, v98 quad_perm:[1,1,3,3]" S_D
               S_M("112:115", "80:83", "80:83") S_C(154, 96, 98, r2) S_SQ(104, 100)
               S_W(3) S_M("108:111", "84:87", "36:39") S_SQD(105, 101) S_SQ(106, 102)
               S_M("112:115", "84:87", "84:87") S_SQD(107, 103) "v_add_f32 v154, %[eps], v154\n\t"
               S_W(2) S_M("108:111", "88:91", "40:43") S_U(128, 100, 152) S_U(129, 101, 152)
               S_M("112:115", "88:91", "88:91") S_U(130, 102, 153) S_U(131, 103, 153)
               S_W(1) S_M("108:111", "92:95", "44:47") S_C(104, 104, 105, r1) S_C(106, 106, 107, r1)
               S_M("112:115", "92:95", "92:95") S_C(155, 128, 129, r1) S_C(156, 130, 131, r1)
               "s_cmp_lt_u32 s42, %[early]\n\ts_cbranch_scc1 7f\n"
               "3:\n\t" S_M0("116:119", "24:27", "0:3") S_C(104, 104, 106, r2) S_C(155, 155, 156, r2)
               S_M("116:119", "28:31", "4:7") "v_add_f32_dpp v104, v154, v104 quad_perm:[0,0,2,2]" S_D S_U(132, 108, 152)
               S_M("116:119", "32:35", "8:11") S_U(133, 109, 152) S_U(134, 110, 153)
               S_M("116:119", "36:39", "12:15") S_U(135, 111, 153) S_SQ(112, 108)
               S_M("116:119", "40:43", "16:19") S_SQD(113, 109) S_SQ(114, 110)
               S_M("116:119", "44:47", "20:23") S_SQD(115, 111) S_C(156, 132, 133, r1) S_C(157, 134, 135, r1)
               S_C(112, 112, 113, r1) S_C(114, 114, 115, r1) S_C(156, 156, 157, r2) S_C(112, 112, 114, r2)
               "v_add_f32_dpp v112, v154, v112 quad_perm:[0,0,2,2]" S_D
               "v_mul_f32 v117, %[nic], v117\n\tv_cndmask_b32_dpp v155, v156, v155, vcc row_ror:4" S_D
               S_BASE("120:121", "128:129", "116:117") S_BASE("122:123", "130:131", "118:119")
               "v_cndmask_b32_dpp v104, v112, v104, vcc row_ror:4" S_D "v_mul_f32 v156, %[nic], v155\n\t"
               S_M("120:123", "48:51", "0:3") S_BASE("124:125", "132:133", "116:117") S_BASE("126:127", "134:135", "118:119")
               S_M("120:123", "52:55", "4:7") "v_fmac_f32 v104, v156, v155\n\tv_max_f32 v104, %[eps], v104\n\t"
               S_M("120:123", "56:59", "8:11") "v_rsq_f32 v157, v104\n\t"
               S_M("120:123", "60:63", "12:15") "v_mul_f32 v104, v104, v157\n\tv_mov_b32_dpp v157, v157 row_ror:12 row_mask:0x2 bank_mask:0xf\n\t"
               S_M("120:123", "64:67", "16:19") "v_cvt_pk_bf16_f32 v105, v155, v104\n\tv_mov_b32_dpp v157, v157 row_ror:8 row_mask:0x4 bank_mask:0xf\n\t"
               S_M("120:123", "68:71", "20:23") "v_lshlrev_b32 v106, 16, v105\n\tv_mov_b32_dpp v157, v157 row_ror:4 row_mask:0x8 bank_mask:0xf\n\t"
               S_M("124:127", "72:75", "0:3") "v_and_b32 v107, 0xffff0000, v105\n\t" S_B(136, 0)
               S_M("124:127", "76:79", "4:7") S_B(137, 1) S_B(138, 2)
               S_M("124:127", "80:83", "8:11") S_B(139, 3) S_LG("120:121", "136:137")
               S_M("124:127", "84:87", "12:15") S_LG("122:123", "138:139") S_SUB("128:129", "120:121")
               S_M("124:127", "88:91", "16:19") S_SUB("130:131", "122:123") S_E(128)
               S_M("124:127", "92:95", "20:23") S_E(129) S_E(130) S_E(131)
               S_B(140, 4) S_B(141, 5) S_B(142, 6) S_B(143, 7) "v_sub_f32 v106, v155, v106\n\t"
               S_LG("124:125", "140:141") S_LG("126:127", "142:143") "v_sub_f32 v107, v104, v107\n\tv_max3_f32 v152, v120, v121, v122\n\t"
               S_SUB("132:133", "124:125") S_SUB("134:135", "126:127") "v_max3_f32 v153, v123, v124, v125\n\t"
               S_E(132) S_E(133) S_E(134) S_E(135)
               "v_max3_f32 v152, v152, v126, v127\n\tv_add_f32 v154, %[thr], v150\n\tv_max_f32 v152, v152, v153\n\tv_cvt_pk_bf16_f32 v106, v106, v107\n\t"
               "v_lshl_add_u32 v144, s46, 3, %[apk]\n\tv_cmp_gt_f32 vcc, v152, v154\n\tv_add_u32 v145, s46, %[apm]\n\tv_lshl_add_u32 v146, s46, 1, %[api]\n\t"
               "s_cbranch_vccnz 8f\n"
               "4:\n\t" S_MUL("128:129", "136:137") S_MUL("130:131", "138:139") S_MUL("132:133", "140:141") S_MUL("134:135", "142:143")
               "v_cvt_pk_bf16_f32 v128, v128, v129\n\tv_cvt_pk_bf16_f32 v129, v130, v131\n\tv_cvt_pk_bf16_f32 v130, v132, v133\n\tv_cvt_pk_bf16_f32 v131, v134, v135\n\t"
               "s_cmp_lt_u32 s51, s48\n\ts_cbranch_scc1 6f\n"
               "10:\n\tds_read_b32 v151, v149\n\tds_write_b128 v144, v[128:131]\n\tds_write2_b32 v145, v150, v150 offset0:0 offset1:1\n\ts_mov_b64 exec, %[dg2]\n\t"
               "ds_write_b16 v146, v105\n\tds_write_b16 v146, v106 offset:16\n\tds_write_b16_d16_hi v146, v105 offset:32\n\tds_write_b16_d16_hi v146, v106 offset:48\n\t"
               "s_mov_b64 exec, 1\n\tds_add_u32 v148, %[inc]\n\ts_mov_b64 exec, -1\n\ts_setprio 0\n\ts_add_u32 s42, s42, %[ns]\n\t"
               S_W(7) "v_readfirstlane_b32 s49, v151\n\ts_branch 1b\n"
               S_POLL(5, 149, 0, "s44", "2b", "2")
               S_POLL(6, 148, 64, "s48", "10b", "64")
               "7:\n\tv_mov_b32 v144, %[qi]\n\tv_mov_b32 v145, v152\n\tv_mov_b32 v146, 1\n"
               S_POLL(70, 144, 0, "%[nq]", "71f", "64")
               "71:\n\tv_mov_b32 v152, v145\n\tds_read_b128 v[0:3], %[apk]\n\tds_read_b128 v[4:7], %[apk] offset:1024\n\tds_read_b128 v[8:11], %[apk] offset:2048\n\t"
               "ds_read_b128 v[12:15], %[apk] offset:3072\n\tds_read_b128 v[16:19], %[apk] offset:4096\n\tds_read_b128 v[20:23], %[apk] offset:5120\n\t"
               "v_subrev_u32 v144, %[mc], %[amr]\n\tds_read_b32 v150, %[amr]\n\ts_mov_b64 exec, 63\n\tds_add_u32 v144, v146\n\ts_mov_b64 exec, -1\n\t"
               "s_cmp_lg_u32 %[late], 0\n\ts_waitcnt lgkmcnt(0)\n\ts_nop 1\n\ts_cbranch_scc1 1b\n\ts_branch 3b\n"
               "8:\n\tv_mov_b32 v153, v152\n\ts_nop 1\n\tv_permlane16_swap_b32 v152, v153\n\tv_max_f32 v152, v152, v153\n\tv_mov_b32 v153, v152\n\ts_nop 1\n\t"
               "v_permlane32_swap_b32 v152, v153\n\tv_max_f32 v152, v152, v153\n\tv_mul_f32 v152, 0x3d000000, v152\n\tv_rndne_f32 v152, v152\n\t"
               "v_mul_f32 v152, 0x42000000, v152\n\tv_max_f32 v150, v150, v152\n\tds_max_f32 %[amr], v150\n\t"
               S_SUB("128:129", "120:121") S_SUB("130:131", "122:123") S_SUB("132:133", "124:125") S_SUB("134:135", "126:127")
               S_E(128) S_E(129) S_E(130) S_E(131) S_E(132) S_E(133) S_E(134) S_E(135) "s_branch 4b\n"
               "9:"
               : "=&{v[0:7]}"(c.q[0]), "=&{v[8:15]}"(c.q[1]), "=&{v[16:23]}"(c.q[2])
               : [orb] "v"(c.o_rb), [oyb] "v"(c.o_yb), [ev] "v"(c.ev), [apk] "v"(c.apk), [apm] "v"(c.apm), [api] "v"(c.api), [amr] "v"(c.amr),
                 [two] "v"(2.f), [four] "v"(4u), [inc] "v"(2u * NPK), [k0] "s"(k0), [late] "s"(__builtin_amdgcn_readfirstlane(late)), [toff] "s"(f32x2{TOFF, TOFF}),
                 [r1] "s"(0xAAAAAAAAAAAAAAAAull), [r2] "s"(0xCCCCCCCCCCCCCCCCull), [dg] "s"(0xF0000F0000F0000Full), [dg2] "s"(0xF00FFF000FF000FFull),
                 [cap] "i"(SPIN_CAP), [end] "i"(TPW - NH1), [ns] "i"(NS), [pfl] "i"(CT_OFF + C_PFL), [full] "i"(CT_OFF + C_FULL), [early] "i"(NEARLY),
                 [qi] "i"(CT_OFF + C_QI), [nq] "i"(NS - NEARLY), [mc] "i"(C_MREF - C_CONS), [slot] "i"(SLOT),
                 [thr] "i"(__builtin_bit_cast(unsigned, THR)), [eps] "i"(__builtin_bit_cast(unsigned, EPS * CC)), [nic] "i"(__builtin_bit_cast(unsigned, -1.f / CC))
               : "memory", "scc", "vcc", "s40", "s41", "s42", "s43", "s44", "s45", "s46", "s47", "s48", "s49", "s50", "s51",
                 "v24", "v25", "v26", "v27", "v28", "v29", "v30", "v31", "v32", "v33", "v34", "v35", "v36", "v37", "v38", "v39", "v40", "v41", "v42", "v43", "v44", "v45", "v46", "v47", "v48", "v49", "v50", "v51", "v52", "v53", "v54", "v55", "v56", "v57", "v58", "v59", "v60", "v61", "v62", "v63", "v64", "v65", "v66", "v67", "v68", "v69", "v70", "v71", "v72", "v73", "v74", "v75", "v76", "v77", "v78", "v79", "v80", "v81", "v82", "v83", "v84", "v85", "v86", "v87", "v88", "v89", "v90", "v91", "v92", "v93", "v94", "v95", "v96", "v97", "v98", "v99", "v100", "v101", "v102", "v103", "v104", "v105", "v106", "v107", "v108", "v109", "v110", "v111", "v112", "v113", "v114", "v115", "v116", "v117", "v118", "v119", "v120", "v121", "v122", "v123", "v124", "v125", "v126", "v127", "v128", "v129", "v130", "v131", "v132", "v133", "v134", "v135", "v136", "v137", "v138", "v139", "v140", "v141", "v142", "v143", "v144", "v145", "v146", "v147", "v148", "v149", "v150", "v151", "v152", "v153", "v154", "v155", "v156", "v157");
}

__device__ __forceinline__ void s_half(SCtx& c, int k, int h) {
  const int s = k % NSLOT, e = k % NPK;
  const int sb_ = s * SLOT;
  int yba = c.ev * sb_ + c.o_yb;
  float hc = 1.f - 0.5f * c.ev;
  unsigned xa = (unsigned)(size_t)c.L0 + sb_ + h * 6144 + c.o_rb, xb = xa ^ 64, ai = c.api + e * 256 + h * 8;
  unsigned ap = c.apk + e * 1024 + h * 8, am = c.apm + e * 128 + h * 4, af = c.ct + C_PFL + e * 4, inc = NPK;
  float two = 2.f;
  f32x4 G = {EPS * CC, EPS * CC, EPS * CC, EPS * CC};
  poll_ge_sleep(c.ct + C_CONS + e * 4, (unsigned)(NE * (k / NPK) + NS));
  asm volatile("" : "+v"(yba), "+v"(hc), "+v"(xa), "+v"(xb), "+v"(ai), "+v"(ap), "+v"(am), "+v"(af), "+v"(inc), "+v"(two), "+{v[104:107]}"(G));
  poll_ge_sleep(c.ct + C_FULL, (unsigned)(2 * k + 1));
  __builtin_amdgcn_s_setprio(1);
  bf16x8 yb[6];
#pragma unroll
  for (int j = 0; j < 6; ++j) yb[j] = *(LDS_AS(bf16x8)*)(c.L0 + ((j & 1) ? (yba ^ 64) : yba) + (j >> 1) * 128);
  f32x4 Hx = {0.f, 0.f, 0.f, 0.f}, S = Hx;
#pragma unroll
  for (int j = 0; j < 6; ++j) { Hx = mfma(yb[j], yb[j], Hx); S = mfma(yb[j], s_qf(c, j), S); }
  const float nsq = S[1] * (-1.f / CC);
  Hx = f32x4{Hx[0] * hc, Hx[0] * hc, Hx[2] * hc, Hx[2] * hc};
  S = f32x4{S[0], S[0], S[2], S[2]};
  float mref, mt;
  f32x4 rs;
  unsigned long long raise;
#define SH_M(d, a, b) "v_mfma_f32_16x16x32_bf16 v[" d "], v[" a "], " b ", v[" d "]\n\t"
#define SH_X(n, a, y) "s_waitcnt lgkmcnt(" #n ")\n\t" SH_M("100:103", a, "%[" #y "]") SH_M("104:107", a, "v[" a "]")
#define SH_D " row_mask:0xf bank_mask:0xf\n\t"
  __builtin_amdgcn_s_setprio(0);
  if (k < TPW - 2) poll_ge_sleep(c.ct + C_FULL, (unsigned)(2 * k + 2));
  asm volatile("s_mov_b32 s42, %[cap]\n"
               "1:\n\tds_read_b32 v136, %[afl]\n\tds_read_b128 v[112:115], %[xa]\n\tds_read_b128 v[116:119], %[xb]\n\tds_read_b128 v[120:123], %[xa] offset:128\n\t"
               "ds_read_b128 v[124:127], %[xb] offset:128\n\tds_read_b128 v[128:131], %[xa] offset:256\n\tds_read_b128 v[132:135], %[xb] offset:256\n\t"
               "ds_read_b32 %[mr], %[amr]\n\ts_sub_u32 s42, s42, 1\n\ts_cselect_b32 s41, 0, %[need]\n\ts_waitcnt lgkmcnt(7)\n\t"
               "v_readfirstlane_b32 s40, v136\n\ts_cmp_lt_u32 s40, s41\n\ts_cbranch_scc1 1b\n\ts_setprio 3\n\t"
               SH_X(6, "112:115", y0) SH_X(5, "116:119", y1) SH_X(4, "120:123", y2) SH_X(3, "124:127", y3) SH_X(2, "128:131", y4) SH_X(1, "132:135", y5)
               SH_M("108:111", "112:115", "%[q0]") SH_M("108:111", "116:119", "%[q1]")
               "s_waitcnt lgkmcnt(0)\n\tv_add_f32 v137, %[thr], %[mr]\n\tds_write_b32 %[am], %[mr]\n\ts_nop 1\n\t"
               "v_mov_b32_dpp v140, v100 quad_perm:[1,1,3,3]" SH_D "v_mov_b32_dpp v141, v101 quad_perm:[1,1,3,3]" SH_D
               "v_mov_b32_dpp v142, v102 quad_perm:[1,1,3,3]" SH_D "v_mov_b32_dpp v143, v103 quad_perm:[1,1,3,3]" SH_D
               SH_M("108:111", "120:123", "%[q2]")
               "v_fmac_f32 v104, 2.0, v100\n\tv_fmac_f32_dpp v105, v101, %[two] quad_perm:[0,0,2,2]" SH_D
               "v_fmac_f32 v106, 2.0, v102\n\tv_fmac_f32_dpp v107, v103, %[two] quad_perm:[0,0,2,2]" SH_D
               "v_cndmask_b32 v138, v140, v141, %[r1]\n\tv_cndmask_b32 v139, v142, v143, %[r1]\n\t"
               SH_M("108:111", "124:127", "%[q3]")
               "v_cndmask_b32 v104, v104, v105, %[r1]\n\tv_cndmask_b32 v106, v106, v107, %[r1]\n\tv_cndmask_b32 v138, v138, v139, %[r2]\n\t"
               "v_cndmask_b32 v104, v104, v106, %[r2]\n\tv_mul_f32 v139, %[nic], v138\n\t"
               SH_M("108:111", "128:131", "%[q4]")
               "v_fmac_f32 v104, v139, v138\n\tv_max_f32 v104, %[eps], v104\n\tv_rsq_f32 v105, v104\n\t"
               SH_M("108:111", "132:135", "%[q5]")
               "v_mul_f32 v104, v104, v105\n\t"
               "v_mov_b32_dpp v105, v105 row_ror:12 row_mask:0x2 bank_mask:0xf\n\tv_cvt_pk_bf16_f32 v106, v138, v104\n\ts_nop 0\n\t"
               "v_mov_b32_dpp v105, v105 row_ror:8 row_mask:0x4 bank_mask:0xf\n\tv_lshlrev_b32 v107, 16, v106\n\tv_and_b32 v139, 0xffff0000, v106\n\t"
               "v_mov_b32_dpp v105, v105 row_ror:4 row_mask:0x8 bank_mask:0xf\n\t"
               "v_pk_fma_f32 v[108:109], v[140:141], %[nsq], v[108:109]\n\tv_pk_fma_f32 v[110:111], v[142:143], %[nsq], v[110:111]\n\t"
               "v_mov_b32_dpp v100, v105 row_newbcast:0" SH_D "v_mov_b32_dpp v101, v105 row_newbcast:1" SH_D
               "v_mov_b32_dpp v102, v105 row_newbcast:2" SH_D "v_mov_b32_dpp v103, v105 row_newbcast:3" SH_D
               S_LG("108:109", "100:101") S_LG("110:111", "102:103") S_SUB("140:141", "108:109") S_SUB("142:143", "110:111")
               "v_exp_f32 v140, v140\n\tv_exp_f32 v141, v141\n\tv_exp_f32 v142, v142\n\tv_exp_f32 v143, v143\n\t"
               "v_max3_f32 %[mt], v108, v109, v110\n\tv_sub_f32 v138, v138, v107\n\tv_sub_f32 v104, v104, v139\n\tv_max_f32 %[mt], %[mt], v111\n\t"
               "v_pk_mul_f32 v[140:141], v[140:141], v[100:101]\n\tv_pk_mul_f32 v[142:143], v[142:143], v[102:103]\n\t"
               "v_cmp_gt_f32 %[ra], %[mt], v137\n\tv_cvt_pk_bf16_f32 v107, v138, v104\n\t"
               "v_cvt_pk_bf16_f32 v140, v140, v141\n\tv_cvt_pk_bf16_f32 v141, v142, v143\n\t"
               "ds_write_b64 %[ap], v[140:141]\n\ts_mov_b64 exec, %[dg]\n\t"
               "ds_write_b16 %[ai], v106\n\tds_write_b16 %[ai], v107 offset:16\n\tds_write_b16_d16_hi %[ai], v106 offset:32\n\tds_write_b16_d16_hi %[ai], v107 offset:48\n\t"
               "s_cmp_eq_u64 %[ra], 0\n\ts_cselect_b64 exec, 1, 0\n\tds_add_u32 %[af], %[inc]\n\ts_mov_b64 exec, -1\n\ts_setprio 0"
               : "+{v[100:103]}"(Hx), "+{v[104:107]}"(G), "+{v[108:111]}"(S), [mr] "=&{v150}"(mref), [mt] "=&v"(mt), [ra] "=&s"(raise)
               : [y0] "v"(yb[0]), [y1] "v"(yb[1]), [y2] "v"(yb[2]), [y3] "v"(yb[3]), [y4] "v"(yb[4]), [y5] "v"(yb[5]),
                 [q0] "v"(s_qf(c, 0)), [q1] "v"(s_qf(c, 1)), [q2] "v"(s_qf(c, 2)), [q3] "v"(s_qf(c, 3)), [q4] "v"(s_qf(c, 4)), [q5] "v"(s_qf(c, 5)),
                 [afl] "v"(c.ct + C_FULL), [xa] "v"(xa), [xb] "v"(xb), [amr] "v"(c.amr), [am] "v"(am), [ap] "v"(ap), [ai] "v"(ai),
                 [af] "v"(af), [inc] "v"(inc), [nsq] "v"(f32x2{nsq, nsq}), [two] "v"(two),
                 [need] "s"(__builtin_amdgcn_readfirstlane(2 * k + 2)), [r1] "s"(0xAAAAAAAAAAAAAAAAull), [r2] "s"(0xCCCCCCCCCCCCCCCCull),
                 [dg] "s"(0xF0000F0000F0000Full), [cap] "i"(SPIN_CAP), [thr] "i"(__builtin_bit_cast(unsigned, THR)), [toff] "s"(f32x2{TOFF, TOFF}),
                 [eps] "i"(__builtin_bit_cast(unsigned, EPS * CC)), [nic] "i"(__builtin_bit_cast(unsigned, -1.f / CC))
               : "memory", "scc", "s40", "s41", "s42", "v112", "v113", "v114", "v115", "v116", "v117", "v118", "v119", "v120", "v121", "v122", "v123", "v124", "v125",
                 "v126", "v127", "v128", "v129", "v130", "v131", "v132", "v133", "v134", "v135", "v136", "v137", "v138", "v139", "v140", "v141", "v142", "v143");
  if (__builtin_expect(raise != 0ull, 0)) {
    rs = Hx;
    mref = fmaxf(mref, 32.f * __builtin_rintf(xrow16_max(mt) * (1.f / 32.f)));
    float pp[4];
#pragma unroll
    for (int i = 0; i < 4; ++i) pp[i] = __builtin_amdgcn_exp2f(S[i] - mref) * rs[i];
    asm volatile("ds_max_f32 %6, %3\n\tds_write_b32 %2, %3\n\tds_write_b64 %0, %1\n\ts_mov_b64 exec, 1\n\tds_add_u32 %4, %5\n\ts_mov_b64 exec, -1"
                 : : "v"(ap), "v"(u32x2{cvtpk(pp[0], pp[1]), cvtpk(pp[2], pp[3])}), "v"(am), "v"(mref), "v"(af), "v"((unsigned)NPK), "v"(c.amr) : "memory");
  }
}

using u32x16 = __attribute__((ext_vector_type(16))) unsigned;
typedef __attribute__((address_space(1))) unsigned short gus;
extern "C" __global__ __launch_bounds__(768, 3) void dino_main(const void*, const void*, const void*, const void*, const void*,
                                                               const void*, const void*, void*, void*) {
  u32x16 ka; u32x2 kb; unsigned long long tent64;
  __shared__ __attribute__((aligned(16))) char lds[LDS_BYTES];
  const int wave = __builtin_amdgcn_readfirstlane((int)threadIdx.x >> 6);
  const bool helper = blockIdx.x < 8;
  lds_ptr L0 = (lds_ptr)lds;
  const unsigned lbase = (unsigned)(size_t)L0;
  const unsigned ct = lbase + CT_OFF;
#define KARG(v, i) ((char*)(((unsigned long long)v[2 * (i) + 1] << 32) | v[2 * (i)]))
#define q KARG(ka, 0)
#define f0 KARG(ka, 1)
#define f1 KARG(ka, 2)
#define wq KARG(ka, 3)
#define wk KARG(ka, 4)
#define wv KARG(ka, 5)
#define wo KARG(ka, 6)
#define box KARG(ka, 7)
#define tent ((unsigned)tent64)
#define LANE_VARS int lane; asm volatile("v_mbcnt_lo_u32_b32 %0, -1, 0\n\tv_mbcnt_hi_u32_b32 %0, -1, %0" : "=v"(lane)); \
  const int r = lane & 15, g = lane >> 4; (void)r; (void)g;
#define DOFF_VARS unsigned doff[3];   \
  _Pragma("unroll") for (int i = 0; i < 3; ++i) doff[i] = (unsigned)(lane ^ ((swz_tbl(i) >> ((lane >> 3) * 4)) & 7)) << 4;
#define BARRIER() do { asm volatile("s_waitcnt lgkmcnt(0)" ::: "memory"); __builtin_amdgcn_s_barrier(); asm volatile("" ::: "memory"); } while (0)

  {
    unsigned doff[3];
    static_assert(swz_tbl(0) == swz_tbl(3) && swz_tbl(1) == swz_tbl(4) && swz_tbl(2) == swz_tbl(5), "");
    unsigned l16, db;
#define PD(d, b, off, pol) "global_load_lds_dwordx4 %[" #d "], s[" #b "] offset:" #off pol "\n\t"
    asm volatile("s_load_dwordx16 s[64:79], %[kp], 0x0\n\ts_load_dwordx2 s[80:81], %[kp], 0x40\n\ts_memrealtime s[82:83]\n\t"
                 "v_mbcnt_lo_u32_b32 %[l16], -1, 0\n\tv_mbcnt_hi_u32_b32 %[l16], -1, %[l16]\n\t"
                 "s_lshr_b32 s84, %[b], 3\n\ts_and_b32 s85, %[b], 7\n\t"
                 "v_lshrrev_b32 %[db], 1, %[l16]\n\tv_and_b32 %[db], -4, %[db]\n\t"
                 "s_mov_b32 s86, %[t0]\n\ts_mov_b32 s87, %[t1]\n\ts_mov_b32 s88, %[t2]\n\t"
                 "v_bfe_u32 %[d0], s86, %[db], 3\n\tv_bfe_u32 %[d1], s87, %[db], 3\n\tv_bfe_u32 %[d2], s88, %[db], 3\n\t"
                 "v_xor_b32 %[d0], %[d0], %[l16]\n\tv_xor_b32 %[d1], %[d1], %[l16]\n\tv_xor_b32 %[d2], %[d2], %[l16]\n\t"
                 "v_lshlrev_b32 %[d0], 4, %[d0]\n\tv_lshlrev_b32 %[d1], 4, %[d1]\n\tv_lshlrev_b32 %[d2], 4, %[d2]\n\t"
                 "v_lshlrev_b32 %[l16], 4, %[l16]\n\t"
                 "s_cmp_eq_u32 s84, 0\n\ts_cbranch_scc1 4f\n\t"
                 "s_cmp_lg_u32 %[w], 0\n\ts_cbranch_scc1 7f\n\t"
                 "s_setprio 3\n\tv_lshlrev_b32 %[db], 3, %[l16]\n\ts_mov_b32 m0, %[junk]\n\ts_lshl_b32 s89, s84, 13\n\t"
                 "s_cmp_gt_u32 s84, 3\n\ts_cbranch_scc1 2f\n\t"
                 "s_getpc_b64 s[90:91]\n1:\n\ts_sub_u32 s86, s89, 8192\n\ts_min_u32 s86, s86, dino_end-1b-8192\n\t"
                 "s_add_u32 s90, s90, s86\n\ts_addc_u32 s91, s91, 0\n\tglobal_load_lds_dword %[db], s[90:91]\n"
                 "2:\n\ts_mul_i32 s86, s85, 98304\n\ts_mul_i32 s87, s84, 3072\n\ts_sub_u32 s87, s87, 3072\n\t"
                 "s_add_u32 s88, s86, s87\n\ts_lshl1_add_u32 s86, s86, s87\n\ts_lshl_b32 s86, s86, 1\n\t"
                 "s_mul_i32 s87, s85, 12288\n\ts_lshr_b32 s90, s89, 1\n\ts_add_u32 s87, s87, s90\n\ts_sub_u32 s87, s87, 13 * 4096\n\t"
                 "s_sub_u32 s89, s89, 4 * 8192\n\ts_cmp_gt_u32 s85, 5\n\ts_cselect_b32 s89, -1, s89\n\ts_cmp_ge_u32 s89, 11 * 8192\n\t"
                 "s_waitcnt lgkmcnt(0)\n\ts_cbranch_scc1 3f\n\t"
                 "s_cmp_lt_u32 s89, 9 * 8192\n\ts_cselect_b64 s[90:91], s[70:71], s[72:73]\n\ts_cselect_b32 s89, s89, s87\n\t"
                 "s_add_u32 s90, s90, s89\n\ts_addc_u32 s91, s91, 0\n\tglobal_load_lds_dword %[db], s[90:91]\n"
                 "3:\n\ts_mov_b32 m0, 0x3000\n\t"
                 "s_add_u32 s90, s68, s88\n\ts_addc_u32 s91, s69, 0\n\t"
                 "s_add_u32 s92, s66, s86\n\ts_addc_u32 s93, s67, 0\n\t"
                 PD(d0, 90:91, 0, " nt") PD(d1, 90:91, 1024, " nt") PD(d2, 90:91, 2048, " nt")
                 "s_mov_b32 m0, 0\n\ts_nop 0\n\t"
                 PD(d0, 92:93, 0, " nt") PD(d1, 92:93, 1024, " nt")
                 "s_sub_u32 s89, s84, 15\n\ts_cmp_gt_u32 s85, 5\n\ts_cselect_b32 s89, -1, s89\n\ts_cmp_ge_u32 s89, 11\n\ts_cbranch_scc1 9f\n\t"
                 "s_mov_b32 m0, %[junk]\n\ts_sub_u32 s87, s87, 11 * 4096\n\ts_lshl_b32 s89, s89, 13\n\ts_cmp_lt_u32 s89, 9 * 8192\n\t"
                 "s_cselect_b64 s[90:91], s[76:77], s[74:75]\n\ts_cselect_b32 s89, s89, s87\n\t"
                 "s_add_u32 s90, s90, s89\n\ts_addc_u32 s91, s91, 0\n\tglobal_load_lds_dword %[db], s[90:91]\n\t"
                 "s_branch 9f\n"
                 "7:\n\ts_cmp_eq_u32 %[w], 1\n\ts_cbranch_scc1 8f\n\ts_barrier\n\ts_branch 8f\n"
                 "4:\n\t"
                 "s_cmp_gt_u32 %[b], 5\n\ts_cbranch_scc1 8f\n\ts_cmp_gt_u32 %[w], 9\n\ts_cbranch_scc1 8f\n\t"
                 "s_mov_b32 m0, %[img]\n\ts_mul_i32 s86, %[w], 9216\n\ts_and_b32 s88, %[w], 1\n\ts_lshl1_add_u32 s89, %[b], s88\n\t"
                 "s_mul_i32 s88, s88, 6144\n\ts_mul_i32 s89, s89, 6144\n\ts_add_u32 s88, s88, %[hr]\n\t"
                 "s_cmp_lg_u32 %[w], 0\n\ts_waitcnt lgkmcnt(0)\n\ts_cbranch_scc1 5f\n\t"
                 PD(d0, 64:65, 0, "") PD(d1, 64:65, 1024, "") PD(d2, 64:65, 2048, "") PD(d0, 64:65, 3072, "")
                 "s_mov_b32 m0, %[img] + 4096\n\ts_add_u32 s90, s64, 0x1000\n\ts_addc_u32 s91, s65, 0\n\t"
                 PD(d1, 90:91, 0, "") PD(d2, 90:91, 1024, "")
                 "\n5:\n\ts_cmp_gt_u32 %[w], 7\n\ts_cselect_b64 s[92:93], s[72:73], s[70:71]\n\ts_cselect_b32 m0, s88, s86\n\ts_cselect_b32 s86, s89, s86\n\t"
                 "s_cselect_b32 s87, 2, 3\n\ts_add_u32 s92, s92, s86\n\ts_addc_u32 s93, s93, 0\n"
                 "6:\n\ts_sub_u32 s87, s87, 1\n\t"
                 PD(d0, 92:93, 0, "") PD(d1, 92:93, 1024, "") PD(d2, 92:93, 2048, "")
                 "s_add_u32 m0, m0, 3072\n\ts_add_u32 s92, s92, 3072\n\ts_addc_u32 s93, s93, 0\n\ts_cmp_lg_u32 s87, 0\n\ts_cbranch_scc1 6b"
                 "\n8:\n\ts_waitcnt lgkmcnt(0)\n9:"
                 : "={s[64:79]}"(ka), "={s[80:81]}"(kb), "={s[82:83]}"(tent64),
                   [d0] "=&v"(doff[0]), [d1] "=&v"(doff[1]), [d2] "=&v"(doff[2]), [l16] "=&v"(l16), [db] "=&v"(db)
                 : [kp] "s"(__builtin_amdgcn_kernarg_segment_ptr()), [b] "s"(blockIdx.x), [w] "s"(wave),
                   [t0] "i"(swz_tbl(0)), [t1] "i"(swz_tbl(1)), [t2] "i"(swz_tbl(2)), [junk] "i"(JUNK), [img] "i"(IMG_A), [hr] "i"(H_R)
                 : "memory", "scc", "vcc", "s84", "s85", "s86", "s87", "s88", "s89", "s90", "s91", "s92", "s93", "s94", "s95", "s96", "s97", "s98", "s99");
  }
  const unsigned t0 = (unsigned)__builtin_amdgcn_s_memrealtime() - T0BACK;

  if (__builtin_expect(blockIdx.x < 6, 0)) {
    LANE_VARS
    DOFF_VARS
    const int j = blockIdx.x;
    const int qq = (lane >> 2) & 3, p4 = lane & 3, e4 = wave & 3;
    int ra = H_R + r * ROWB + ((g ^ swz(r)) << 4);
    bf16x8 a[6], b[6], w[18];
    auto rows = [&](int t, bf16x8* d) {
#pragma unroll
      for (int kk = 0; kk < 6; ++kk) d[kk] = *(LDS_AS(bf16x8)*)(L0 + t * 6144 + ((kk & 1) ? (ra ^ 64) : ra) + (kk >> 1) * 128);
    };
    int o[3];
#pragma unroll
    for (int t = 0; t < 3; ++t) o[t] = tr_off(4 * g + qq, 4 * (3 * e4 + t) + p4);
    unsigned qo = lbase + H_Q + 3 * e4 * 512 + ((g >> 1) * 16 + r) * 16 + (g & 1) * 8, qa = lbase + IMG_A + tr_off(r, 8 * wave + g);
    if (wave == 11) {
      unsigned d;
      asm volatile("s_load_dword %0, %1, 0x40\n\ts_barrier\n\ts_barrier\n\ts_barrier\n\ts_waitcnt lgkmcnt(0)" : "=&s"(d) : "s"(box + EPOCH_OFF) : "memory");
    } else
    asm volatile("s_waitcnt vmcnt(9)\n\ts_barrier" : PIN3(o, 0), "+v"(qo), "+v"(qa) :: "memory");
    if (wave < 6 || wave == 10) {
      rows((IMG_A - H_R) / 6144, a);
      const u32x2 m0 = *(LDS_AS(u32x2)*)(size_t)qa, m1 = *(LDS_AS(u32x2)*)(size_t)(qa ^ 32);
      static_assert((IMG_A - H_R) % 6144 == 0, "");
      const bf16x8 ones = __builtin_bit_cast(bf16x8, u32x4{0x3f803f80u, 0x3f803f80u, 0x3f803f80u, 0x3f803f80u});
      f32x4 sm = {0.f, 0.f, 0.f, 0.f};
#pragma unroll
      for (int i = 0; i < 6; ++i) sm = mfma(ones, a[i], sm);
      const float mean = sm[0] * (1.f / CC);
      if (__builtin_expect(wave < 6, 1)) {
        img_st(L0, H_C, wave, lane, __builtin_bit_cast(bf16x8, u32x4{cvtpk(bflo(m0[0]) - mean, bfhi(m0[0]) - mean), cvtpk(bflo(m0[1]) - mean, bfhi(m0[1]) - mean),
                                                                   cvtpk(bflo(m1[0]) - mean, bfhi(m1[0]) - mean), cvtpk(bflo(m1[1]) - mean, bfhi(m1[1]) - mean)}));
      } else {
        asm volatile("s_barrier" : PIN3(a, 0), PIN3(a, 3) :: "memory");
        f32x2 v2 = {0.f, 0.f}, v3 = v2;
#pragma unroll
        for (int i = 0; i < 12; ++i) {
          const u32x4 d = __builtin_bit_cast(u32x4, a[i >> 1]);
          const unsigned d0 = d[2 * (i & 1)], d1 = d[2 * (i & 1) + 1];
          const f32x2 x0 = f32x2{bflo(d0), bfhi(d0)} - mean, x1 = f32x2{bflo(d1), bfhi(d1)} - mean;
          v2 += x0 * x0; v3 += x1 * x1;
        }
        v2 += v3;
        *(LDS_AS(float)*)(L0 + CT_OFF + r * 4) = LOG2E * __builtin_amdgcn_rsqf(xrow16_sum(v2[0] + v2[1]) * (1.f / CC) + EPS);
      }
    }
    if (wave < 4) {
      wait_vm<0>();
      BARRIER();
#pragma unroll
      for (int kk = 0; kk < 6; ++kk) {
        b[kk] = img_ld(L0, H_C, kk, lane);
        w[kk] = mk8(tr_read(L0 + kk * 12288 + o[0]), tr_read(L0 + kk * 12288 + 6144 + o[0]));
        __builtin_amdgcn_sched_barrier(0);
      }
#pragma unroll
      for (int i = 6; i < 18; ++i) {
        w[i] = mk8(tr_read(L0 + (i % 6) * 12288 + o[i / 6]), tr_read(L0 + (i % 6) * 12288 + 6144 + o[i / 6]));
        __builtin_amdgcn_sched_barrier(0);
      }
#pragma unroll
      for (int t = 0; t < 3; ++t) {
        f32x4 D = {0.f, 0.f, 0.f, 0.f};
#pragma unroll
        for (int kk = 0; kk < 6; ++kk) D = mfma(w[6 * t + kk], b[kk], D);
        *(LDS_AS(u32x2)*)(size_t)(qo + t * 512) = u32x2{cvtpk(D[0], D[1]), cvtpk(D[2], D[3])};
      }
      BARRIER();
    } else if (wave == 8 || wave == 9) {
      unsigned so = lane * 16 + (2 * j + (wave & 1)) * 1024;
      unsigned epoch;
      asm volatile("global_load_dword %0, %1, %2 sc1\n\ts_barrier\n\ts_waitcnt vmcnt(0)" : "=v"(epoch) : "v"(EPOCH_OFF), "s"(box) : "memory");
      rows(wave & 1, a);
      unsigned ta = line_tag(epoch, 8), tb = line_tag(epoch, 9);
      asm volatile("" : PIN3(a, 0), PIN3(a, 3), "+v"(ta), "+v"(tb), "+v"(so));
      BARRIER();
#pragma unroll
      for (int kk = 0; kk < 6; ++kk) b[kk] = img_ld(L0, H_Q, kk, lane);
      const float sc = *(LDS_AS(float)*)(L0 + CT_OFF + r * 4);
      __builtin_amdgcn_sched_barrier(0);
      f32x4 D = {0.f, 0.f, 0.f, 0.f};
#pragma unroll
      for (int kk = 0; kk < 6; ++kk) D = mfma(a[kk], b[kk], D);
      D *= sc;
      const unsigned p0 = cvtpk(D[0], D[1]), p1 = cvtpk(D[2], D[3]);
      asm volatile("global_store_dwordx4 %0, %1, %2 sc1\n\ts_nop 1" : : "v"(so), "v"(u32x4{p0, p1, p0 ^ p1 ^ ta, tb}), "s"(box + QK_OFF) : "memory");
    } else if (wave != 11) {
      if (wave != 10) asm volatile("s_waitcnt vmcnt(0) lgkmcnt(0)\n\ts_barrier" ::: "memory");
      BARRIER();
    }
    BARRIER();
    issue_rows16(wo + wave * 6144, lbase + wave * 6144, doff);
    if (wave < 2) issue_rows16(wv + (2 * j + wave) * 6144, lbase + H_R + wave * 6144, doff);
    const unsigned epoch = epoch_load(box);
    BARRIER();
    {
      const int o0 = tr_off(8 * g + qq, 4 * wave + p4), o1 = tr_off(8 * g + 4 + qq, 4 * wave + p4);
#pragma unroll
      for (int kk = 0; kk < 6; ++kk) b[kk] = mk8(tr_read(L0 + kk * 12288 + o0), tr_read(L0 + kk * 12288 + o1));
      f32x4 D[2];
#pragma unroll
      for (int h = 0; h < 2; ++h) {
        rows(h, a);
        D[h] = f32x4{0.f, 0.f, 0.f, 0.f};
#pragma unroll
        for (int kk = 0; kk < 6; ++kk) D[h] = mfma(a[kk], b[kk], D[h]);
      }
      const int e = wave / 3;
      asm volatile("global_store_dwordx4 %0, %1, %2 sc1\n\ts_waitcnt vmcnt(0)"
                   : : "v"(lane * 16 + (e * 15 + 3 * j + wave) * 1024), "v"(pack8(D[0] * SQRT_C, D[1] * SQRT_C)), "s"(box + W2_OFF) : "memory");
    }
    BARRIER();
    if (wave == 11)
      asm volatile("s_mov_b64 exec, 1\n\tglobal_store_dwordx2 %0, %1, %2 sc1\n\ts_mov_b64 exec, -1\n\ts_nop 1"
                   : : "v"(j * 128), "v"(u32x2{line_tag(epoch, 10), line_tag(epoch, 11)}), "s"(box + W2F_OFF) : "memory");
  }

  int rslot = -1;
  if (__builtin_expect(wave == 0, 1)) {
    LANE_VARS
    DOFF_VARS
    if (helper) BARRIER();
    const unsigned afull = ct + C_FULL + r * 4;
    {
      const int ti = helper ? 1023 : (lane * 31 + (int)(blockIdx.x >> 3) - 1) & 1023;
      const int sp = ti >> 5, yy = sp * 8 + ((blockIdx.x + sp) & 7), xx = (ti + 5 * sp) & 31;
      unsigned tmp, pub = helper ? 2 * (TPW - 1) : 0, pre = 0, afr = ct + C_FREE + (helper ? ((TPW - 1) % NSLOT) * 4 : 0), e0, e1, e2, g0, g1, g2;
      asm volatile("s_mov_b32 s44, %[due0]\n\ts_mov_b32 s46, %[y0]\n\ts_mov_b32 s47, %[cap]\n\ts_mov_b32 s48, %[i0]\n\ts_mov_b32 s49, %[s0]\n\t"
                   "s_mov_b32 s51, 0\n\ts_mov_b32 s45, 0\n\ts_movk_i32 s39, %[p3s]\n"
                   "1:\n\tv_readlane_b32 s53, %[ob], s48\n\tv_readlane_b32 s52, %[oa], s48\n\ts_add_u32 m0, s49, 0x3000\n\tv_cmp_gt_u32 vcc, s51, %[pre]\n\t"
                   "v_add_u32 %[g0], s53, %[d0]\n\tv_add_u32 %[g1], s53, %[d1]\n\tv_add_u32 %[g2], s53, %[d2]\n\t"
                   "v_add_u32 %[e0], s52, %[d0]\n\tv_add_u32 %[e1], s52, %[d1]\n\tv_add_u32 %[e2], s52, %[d2]\n\t"
                   "s_cbranch_vccnz 5f\n\t"
                   "s_cmp_eq_u32 s48, 0\n\ts_cbranch_scc1 2f\n\ts_cmp_eq_u32 s48, 2\n\ts_cbranch_scc1 8f\n"
                   LD_GATE(1, "")
                   LD1(g0, 68:69, 0) LD1(g1, 68:69, 1024) LD1(g2, 68:69, 2048) "s_mov_b32 m0, s49\n\ts_nop 0\n\t" LD1(e0, 66:67, 0) LD1(e1, 66:67, 1024)
                   LD_NEXT
                   "\n2:\n\tv_add_u32 %[afr], 4, %[afr]\n\ts_cmp_eq_u32 s49, %[lasts]\n\ts_cbranch_scc1 7f\n"
                   "3:\n\tv_mov_b32 %[pre], 0\n\ts_add_u32 m0, s49, 0x800\n\tds_read_b32 %[pre], %[afr]\n"
                   LD_GATE(2, "")
                   LD1(e2, 56:57, 0) LD1(e0, 56:57, 1024) LD1(e1, 56:57, 2048) LD1(e2, 56:57, 3072) "s_add_u32 m0, s49, 0x1800\n\ts_add_u32 s48, s48, 1\n\t" LD1(e0, 58:59, 0)
                   LD_NEXT
                   LD_GATE(3, "s_cmp_gt_u32 s48, %[tpw] - 2\n\ts_cbranch_scc1 38f\n39:\n\t")
                   LD1(e1, 58:59, 1024) LD1(e2, 58:59, 2048) LD1(e0, 58:59, 3072) "s_add_u32 m0, s49, 0x2800\n\ts_add_u32 s49, s49, %[slot]\n\t" LD1(e1, 60:61, 0) LD1(e2, 60:61, 1024)
                   LD_NEXT
                   "s_cmp_eq_u32 s49, %[ring]\n\ts_cselect_b32 s49, 0, s49\n\ts_cmp_gt_i32 s46, 30\n\ts_cbranch_scc1 6f\n"
                   "4:\n\ts_cmp_eq_u32 s48, %[pk]\n\ts_cmovk_i32 s39, %[p3]\n\ts_cmp_lg_u32 s48, %[tpw]\n\ts_cbranch_scc1 1b\n\ts_branch 9f\n"
                   LD_PUB(1, 0, 15, 30) LD_PUB(2, 5, 20, 35) LD_PUB(3, 10, 25, 40)
                   "38:\n\ts_cmp_eq_u32 s46, 10\n\ts_cselect_b32 s43, 23, 0\n\ts_cmp_lt_u32 s42, s43\n\ts_cbranch_scc0 39b\n\tv_add_u32 %[tmp], 1, %[pub]\n\ts_waitcnt vmcnt(22)\n\tds_write_b32 %[af], %[tmp]\n\ts_branch 39b\n"
                   "5:\n\ts_movk_i32 s40, 0x100\n"
                   "40:\n\tds_read_b32 %[pre], %[afr]\n\t" LD_VM
                   "s_sub_u32 s40, s40, 1\n\ts_cselect_b32 s42, 0, s42\n\ts_cselect_b32 s40, 0, s40\n\t"
                   "s_cmp_le_i32 s42, s46\n\ts_cbranch_scc1 42f\n"
                   "41:\n\ts_sub_i32 s47, s47, 1\n\ts_cmp_lt_i32 s47, 0\n\ts_waitcnt lgkmcnt(0)\n\ts_cbranch_scc1 10b\n\t"
                   "v_cmp_gt_u32 vcc, s51, %[pre]\n\ts_cbranch_vccnz 40b\n\ts_branch 10b\n"
                   LD_PUB0(4, 0, 15, 30)
                   "6:\n\ts_waitcnt vmcnt(45)\n\tv_add_u32 %[pub], 2, %[pub]\n\tds_write_b32 %[af], %[pub]\n\ts_sub_u32 s46, s46, 15\n\ts_branch 4b\n"
                   "7:\n\ts_waitcnt lgkmcnt(0)\n\ts_add_u32 s51, s51, 8\n\tv_subrev_u32 %[afr], %[frb], %[afr]\n\ts_branch 3b\n"
                   "8:\n\ts_barrier\n\ts_add_u32 s46, s46, 64\n\ts_branch 10b\n"
                   "9:\n\ts_cmp_lt_i32 s46, 30\n\ts_cbranch_scc1 91f\n\ts_waitcnt vmcnt(30)\n\tv_add_u32 %[pub], 2, %[pub]\n\tds_write_b32 %[af], %[pub]\n"
                   "91:\n\ts_cmp_lt_i32 s46, 15\n\ts_cbranch_scc1 92f\n\tv_add_u32 %[tmp], 1, %[pub]\n\ts_waitcnt vmcnt(27)\n\tds_write_b32 %[af], %[tmp]\n"
                   "92:\n\ts_waitcnt lgkmcnt(0)"
                   : [pub] "+v"(pub), [pre] "+v"(pre), [afr] "+v"(afr), [tmp] "=&v"(tmp),
                     [e0] "=&v"(e0), [e1] "=&v"(e1), [e2] "=&v"(e2), [g0] "=&v"(g0), [g1] "=&v"(g1), [g2] "=&v"(g2)
                   : "{s[64:79]}"(ka), "{s[56:57]}"(f0 + 0x800), "{s[58:59]}"(f0 + 0x30000), "{s[60:61]}"(f0 + 0x31000), [t0] "s"(t0), [af] "v"(afull),
                     [d0] "v"(doff[0]), [d1] "v"(doff[1]), [d2] "v"(doff[2]), [oa] "v"(yy * 393216 + xx * 6144), [ob] "v"(yy * 98304 + xx * 3072),
                     [due0] "s"(helper ? HDUE * (PACE16 / 3) : PACES / 3), [y0] "s"(helper ? -15 : -74), [i0] "s"(helper ? TPW - 1 : 0),
                     [s0] "s"(helper ? ((TPW - 1) % NSLOT) * SLOT : 0),
                     [cap] "i"(SPIN_CAP), [win] "i"(PWIN), [ahi] "i"(AHI), [adl] "i"(ADL), [amg] "i"(AMG), [p3] "i"(PACE16 / 3), [p3s] "i"(PACES / 3), [pk] "i"(PK1), [slot] "i"(SLOT),
                     [ring] "i"(NSLOT * SLOT), [frb] "i"(NSLOT * 4), [lasts] "i"((NSLOT - 1) * SLOT), [tpw] "i"(TPW)
                   : "memory", "scc", "vcc", "s39", "s40", "s41", "s42", "s43", "s44", "s45", "s46", "s47", "s48", "s49", "s51", "s52", "s53");
    }
    lds_st32(ct + C_ISS, 1u);
    asm volatile("s_waitcnt vmcnt(15)\n\tds_write_b32 %0, %1\n\ts_waitcnt vmcnt(12)\n\tds_write_b32 %0, %2\n\ts_waitcnt vmcnt(0)\n\tds_write_b32 %0, %3"
                 : : "v"(afull), "v"(2 * TPW - 2), "v"(2 * TPW - 1), "v"(~0u) : "memory");
    {
      unsigned d;
      asm volatile("global_load_dword %0, %1, %2 sc1\n\ts_setprio 0\n\ts_waitcnt vmcnt(0)" : "=v"(d) : "v"((lane * RSTR + blockIdx.x) * 128), "s"(box) : "memory");
    }
    rslot = 7;
  } else {
    if (wave == 1) {
      LANE_VARS
    {
      const unsigned o = lane * 8;
      unsigned z = (o >= C_MREF && o < C_MREF + 64) ? 0xff800000u : 0u;
      if (helper && o >= C_CONS && o < C_MREF) z = NE * ((TPW - 1) / NPK);
      u32x2 zz = {z, z};
      if (o >= C_PFL && o < C_CONS) zz = u32x2{(o - C_PFL) / 2 + 2, (o - C_PFL) / 2 + 4} + (helper ? 2u * NPK * ((TPW - 1) / NPK) : 0u);
      if (o == C_EP) zz[1] = QKW0 + QKW1 * min(t0 + T0BACK - tent, 36u) + min(t0 + T0BACK - tent, 200u);
      *(LDS_AS(u32x2)*)(L0 + CT_OFF + o) = zz;
      *(LDS_AS(u32x2)*)(L0 + ONES_OFF + (lane < 48 ? o : 0)) = u32x2{0x3f803f80u, 0x3f803f80u};
    }
    }
    if (helper || wave == 1) BARRIER();
    if (wave <= NE) {
      LANE_VARS
      const int ej = wave - 1;
      const int qq = (lane >> 2) & 3, p4 = lane & 3;
      const unsigned apk = lbase + IMG_A + lane * 16;
      const unsigned epoch = epoch_load(box);
      __builtin_amdgcn_s_setprio(2);
      {
        static_assert((TPW & 1) == 1 && C_CONS == C_PFL + 64 && C_FREE == C_FULL + 64, "");
        const int k0 = helper ? TPW - 1 : 0;
        unsigned need = 2 * k0 + 2;
        asm volatile("s_mov_b32 s46, 0\n\ts_mov_b64 exec, 0\n\ts_nop 3\n\ts_branch 99f\n"
                     "2:\n\ts_mov_b64 exec, -1\n\ts_nop 7\n\t" PV_Z4(72, 73, 74, 75) PV_Z4(76, 77, 78, 79) PV_Z4(80, 81, 82, 83) PV_Z4(84, 85, 86, 87)
                     "v_mov_b32 v88, 0xff800000\n\t"
                     "s_mov_b32 s46, %[k0]\n\ts_mov_b32 s45, %[s0]\n\ts_mov_b32 s44, %[e0]\n\ts_mov_b32 s47, %[cap]\n\t"
                     "s_cmp_lg_u32 s46, 0\n\ts_cbranch_scc1 32f\n"
                     "3:\n\tv_mov_b32 v148, %[ca]\n\t" PV_WAITF(4, 148) PV_FETCH_A
                     "s_cmp_lg_u32 s46, 0\n\ts_cbranch_scc1 99f\n\t"
                     "s_mov_b64 exec, 2\n\tds_add_u32 v148, %[one] offset:64\n\ts_mov_b64 exec, -2\n\t"
                     "v_add_u32 %[need], 2, %[need]\n\tv_add_u32 v148, 4, v148\n\ts_mov_b64 exec, -1\n\t"
                     PV_WAITF(5, 148)
                     "v_mov_b32 v149, v148\n\ts_mov_b64 exec, -2\n\tv_add_u32 %[need], 2, %[need]\n\tv_add_u32 v148, 4, v148\n\t"
                     "s_mov_b64 exec, 1\n\tv_add_u32 %[need], %[npk2], %[need]\n\ts_mov_b64 exec, -1\n\t"
                     PV_REQ("126:129", "130:133", "134:135", 136, 148) PV_FETCH_B
                     "s_mov_b64 exec, 2\n\tds_add_u32 v149, %[one] offset:64\n\ts_mov_b64 exec, -1\n"
                     "6:\n\t"
                     PV_STEP(1, "126:129", "130:133", "134:135", 134, 135, 136, 148, "138:141", "142:145", "146:147", 137, 149,
                             "92:95", "98:101", "104:107", "90:93", "96:99", "102:105",
                             PV_MOV(92, 90) PV_MOV(93, 91), PV_MOV(98, 96) PV_MOV(99, 97), PV_MOV(104, 102) PV_MOV(105, 103), "")
                     "s_cbranch_vccnz 7f\n"
                     "16:\n\t" PV_FETCH_A PV_REL(148)
                     "s_lshl1_add_u32 s54, s46, -2\n\ts_lshr_b64 s[54:55], %[sch], s54\n\ts_and_b32 s54, s54, 15\n\ts_cbranch_scc1 31f\n"
                     "30:\n\t"
                     PV_STEP(2, "138:141", "142:145", "146:147", 146, 147, 137, 149, "126:129", "130:133", "134:135", 136, 148,
                             "110:113", "116:119", "122:125", "108:111", "114:117", "120:123",
                             PV_MOV(110, 108) PV_MOV(111, 109), PV_MOV(116, 114) PV_MOV(117, 115), PV_MOV(122, 120) PV_MOV(123, 121),
                             "s_cmp_eq_u32 s46, %[last] - 1\n\ts_cbranch_scc1 98f\n\t")
                     "s_cbranch_vccnz 8f\n"
                     "26:\n\t" PV_FETCH_B PV_REL(149)
                     "s_branch 6b\n"
                     PV_WAITF(7, 148) "s_branch 16b\n"
                     PV_WAITF(8, 149) "s_branch 26b\n"
                     PV_SLOW(1, "126:129", "130:133", "134:135", 134, 135, 136, 148, 126, 127, 128, 129)
                     PV_SLOW(2, "138:141", "142:145", "146:147", 146, 147, 137, 149, 138, 139, 140, 141)
                     "31:\n\ts_cmp_lg_u32 s54, 8\n\ts_cbranch_scc1 34f\n"
                     "32:\n\tv_mbcnt_lo_u32_b32 v152, 31, 0\n\tv_lshlrev_b32 v152, 7, v152\n\ts_mov_b32 s55, %[gcap]\n"
                     "33:\n\tglobal_load_dwordx2 v[150:151], v152, %[fl] sc1\n\ts_sub_u32 s55, s55, 1\n\ts_cselect_b32 s56, 0, -1\n\ts_waitcnt vmcnt(0) lgkmcnt(0)\n\t"
                     "v_cmp_ne_u64 s[48:49], %[tag], v[150:151]\n\ts_sleep 2\n\ts_and_b32 s48, s48, s56\n\ts_cbranch_scc1 33b\n\t"
                     "s_mov_b32 s57, 0\n\tv_mov_b32 v150, %[apk]\n\ts_cmp_lg_u32 s46, %[last]\n\ts_cbranch_scc1 30b\n\ts_mov_b32 s54, 7\n\ts_branch 38f\n"
                     "34:\n\ts_cmp_lg_u32 s46, %[last] - 3\n\ts_cbranch_scc1 36f\n\tv_mov_b32 v151, %[iss]\n"
                     "35:\n\tds_read_b32 v150, v151\n\ts_sub_i32 s47, s47, 1\n\ts_cmp_lt_i32 s47, 0\n\ts_cselect_b32 s55, 0, 1\n\ts_waitcnt lgkmcnt(0)\n\t"
                     "v_readfirstlane_b32 s56, v150\n\ts_cmp_lt_u32 s56, s55\n\ts_cbranch_scc1 35b\n"
                     "36:\n\ts_mul_i32 s55, s57, 3072\n\tv_add_u32 v150, s55, %[apk]\n\ts_getpc_b64 s[48:49]\n"
                     "37:\n\ts_mul_i32 s55, s57, 40\n\ts_add_u32 s55, s55, 38f-37b\n\ts_add_u32 s48, s48, s55\n\ts_addc_u32 s49, s49, 0\n\t"
                     "s_add_u32 s57, s57, s54\n\ts_sub_u32 s54, s54, 1\n\ts_setpc_b64 s[48:49]\n"
                     "38:\n\t" PV_W2(0, 3, 4, 7, 8, 11) PV_W2(12, 15, 16, 19, 20, 23) PV_W2(24, 27, 28, 31, 32, 35) PV_W2(36, 39, 40, 43, 44, 47)
                     PV_W2(48, 51, 52, 55, 56, 59) PV_W2(60, 63, 64, 67, 68, 71)
                     "s_waitcnt lgkmcnt(0)\n\ts_branch 3b\n"
                     "96:\n\t" PV_RESC(134, 135, 126, 127, 128, 129) "s_nop 0\n\ts_branch 95f\n"
                     "98:\n\t" PV_MF("76:79", "114:117", "138:141") PV_MF("80:83", "120:123", "138:141")
                     "99:\n\ts_cmp_eq_u32 s46, 0\n\ts_cselect_b32 s54, 0, %[nl]\n\ts_cselect_b32 s55, 0, %[x1n]\n\ts_waitcnt lgkmcnt(0)\n\t"
                     "v_mov_b64 v[108:109], v[90:91]\n\tv_mov_b64 v[110:111], v[90:91]\n\tv_mov_b64 v[112:113], v[96:97]\n\tv_mov_b64 v[114:115], v[96:97]\n\t"
                     "v_mov_b64 v[116:117], v[102:103]\n\tv_mov_b64 v[118:119], v[102:103]\n\tv_add_u32 v120, %[aw], %[apk]\n\tv_add_u32 v121, %[ar], %[apk]\n\t"
                     "v_mov_b32 v122, %[x1]\n\tv_lshrrev_b32 v123, 1, %[apm]\n\tv_add_u32 v123, %[lwo], v123\n\t"
                     "v_mbcnt_lo_u32_b32 v124, -1, 0\n\tv_mbcnt_hi_u32_b32 v124, -1, v124\n\tv_and_b32 v125, 1, v124\n\tv_and_b32 v124, 48, v124\n\t"
                     "v_lshl_add_u32 v124, v125, 2, v124\n\tv_add_u32 v124, %[lw], v124\n"
                     "94:\n\tds_read_b32 v136, v122 offset:%[pfd]\n\tds_read_b64 v[134:135], %[apm] offset:%[el]\n\tds_read_b128 v[126:129], %[apk] offset:8 * %[el]\n\t"
                     "ds_read_b128 v[130:133], %[apu] offset:2 * %[el]\n\ts_sub_i32 s47, s47, 1\n\ts_cmp_lt_i32 s47, 0\n\ts_cselect_b32 s54, 0, s54\n\ts_waitcnt lgkmcnt(3)\n\t"
                     "v_readfirstlane_b32 s56, v136\n\ts_cmp_lt_u32 s56, s54\n\ts_cbranch_scc1 94b\n\ts_waitcnt lgkmcnt(2)\n\t"
                     "v_cmp_neq_f32 vcc, v134, v88\n\tv_cmp_neq_f32 s[50:51], v135, v88\n\ts_or_b64 vcc, vcc, s[50:51]\n\ts_waitcnt lgkmcnt(0)\n\ts_cbranch_scc1 96b\n"
                     "95:\n\t" PV_MF("84:87", "130:133", "126:129") "s_nop 1\n\t" PV_MF("72:75", "92:95", "126:129") "s_nop 1\n\t" PV_MF("72:75", "108:111", "126:129")
                     "s_nop 1\n\tv_add_f32 v153, v86, v87\n\tv_add_f32 v151, v84, v85\n\t" PV_MF("76:79", "98:101", "126:129")
                     "v_rcp_f32 v150, v153\n\tv_mul_f32 v151, %[nic], v151\n\t" PV_MF("76:79", "112:115", "126:129")
                     "v_mul_f32 v151, v151, v150\n\t" PV_PK(72, 73) PV_MF("80:83", "104:107", "126:129") PV_PK(74, 75)
                     "v_cvt_pk_bf16_f32 v72, v72, v73\n\t" PV_MF("80:83", "116:119", "126:129") "v_cvt_pk_bf16_f32 v73, v74, v75\n\tds_write_b64 v120, v[72:73]\n\t"
                     PV_NRM(76, 77, 78, 79, 2048) "v_log_f32 v153, v153\n\t" PV_NRM(80, 81, 82, 83, 4096)
                     "s_and_b64 exec, exec, 1\n\tds_add_u32 v122, %[one]\n\ts_cselect_b64 exec, -1, 0\n\t"
                     "v_subrev_f32 v88, %[toff], v88\n\tv_add_f32 v88, v88, v153\n\tds_write_b32 v123, v88\n\tds_read2_b32 v[114:115], v124 offset1:2\n"
                     "97:\n\tds_read_b32 v150, v122\n\tds_read_b128 v[90:93], v121\n\tds_read_b128 v[94:97], v121 offset:1024\n\tds_read_b128 v[98:101], v121 offset:2048\n\t"
                     "ds_read_b128 v[102:105], v121 offset:3072\n\tds_read_b128 v[106:109], v121 offset:4096\n\tds_read_b128 v[110:113], v121 offset:5120\n\t"
                     "s_sub_i32 s47, s47, 1\n\ts_cmp_lt_i32 s47, 0\n\ts_cselect_b32 s55, 0, s55\n\ts_waitcnt vmcnt(0) lgkmcnt(6)\n\t"
                     "v_readfirstlane_b32 s56, v150\n\ts_cmp_lt_u32 s56, s55\n\ts_cbranch_scc1 97b\n\ts_cmp_eq_u32 s46, 0\n\t"
                     PV_G(5, "90:93", "0:3", "4:7", "8:11", "0", "0", "0")
                     PV_G(4, "94:97", "12:15", "16:19", "20:23", "v[72:75]", "v[76:79]", "v[80:83]")
                     PV_G(3, "98:101", "24:27", "28:31", "32:35", "v[72:75]", "v[76:79]", "v[80:83]")
                     PV_G(2, "102:105", "36:39", "40:43", "44:47", "v[72:75]", "v[76:79]", "v[80:83]")
                     PV_G(1, "106:109", "48:51", "52:55", "56:59", "v[72:75]", "v[76:79]", "v[80:83]")
                     PV_G(0, "110:113", "60:63", "64:67", "68:71", "v[72:75]", "v[76:79]", "v[80:83]")
                     "v_xor_b32 v114, v114, %[ltag]\n\tv_xor_b32 v115, v115, %[ltag]\n\ts_nop 3\n\t"
                     "v_cvt_pk_bf16_f32 v116, v72, v73\n\tv_cvt_pk_bf16_f32 v120, v74, v75\n\tv_cvt_pk_bf16_f32 v117, v76, v77\n\tv_cvt_pk_bf16_f32 v121, v78, v79\n\t"
                     "v_cvt_pk_bf16_f32 v118, v80, v81\n\tv_bitop3_b32 v119, v116, v117, v114 bitop3:0x96\n\tv_cvt_pk_bf16_f32 v122, v82, v83\n\t"
                     "v_bitop3_b32 v123, v120, v121, v115 bitop3:0x96\n\tv_xor_b32 v119, v119, v118\n\tv_xor_b32 v123, v123, v122\n\t"
                     "s_cbranch_scc1 2b\n\t"
                     "global_store_dwordx4 %[so], v[116:119], %[bx] sc1\n\tglobal_store_dwordx4 %[so], v[120:123], %[bx2] sc1\n\t"
                     "s_waitcnt lgkmcnt(0)\n\ts_nop 0"
                     : [need] "+v"(need)
                     : [so] "v"(((ej * 16 + (lane >> 3)) * RSTR + blockIdx.x) * 128 + (lane & 7) * 16), [ltag] "v"(line_tag(epoch, lane & 7)),
                       [bx] "s"(box), [bx2] "s"(box + 8 * RSTR * 128), [aw] "s"(A_IMG - IMG_A + (ej >> 1) * 1024 + (ej & 1) * 8), [ar] "i"(A_IMG - IMG_A),
                       [nic] "i"(__builtin_bit_cast(unsigned, -1.f / CC)), [toff] "i"(__builtin_bit_cast(unsigned, TOFF)),
                       [lwo] "i"(CT_OFF + C_LW - PKM / 2), [lw] "i"(CT_OFF + C_LW), [x1] "i"(CT_OFF + C_X1), [x1n] "i"(NE),
                       [nl] "i"(2 * TPW + 2 * NPK), [el] "i"(((TPW - 1) % NPK) * 128), [pfd] "i"(C_PFL + ((TPW - 1) % NPK) * 4 - C_X1),
                       [apk] "v"(apk), [apm] "v"(lbase + PKM + r * 8), [apu] "v"(lbase + PKU + g * 64 + (r & 3) * 16),
                       [ox] "v"(lbase + tr_off(4 * g + qq, 4 * ej + p4)), [oy] "v"(lbase + 32 * ROWB + tr_off(2 * g + (qq >> 1), 4 * ej + p4)),
                       [ca] "v"(ct + (lane == 0 ? C_PFL + (k0 % NPK) * 4 : C_FULL + (k0 % NSLOT) * 4)),
                       [lim] "v"(ct + (lane == 0 ? C_PFL + NPK * 4 : C_FULL + NSLOT * 4)), [base] "v"(ct + (lane == 0 ? C_PFL : C_FULL)), [one] "v"(1u),
                       [k0] "s"(k0), [s0] "s"(helper ? ((TPW - 1) % NSLOT) * SLOT : 0), [e0] "s"(helper ? ((TPW - 1) % NPK) * 128 : 0),
                       [w2] "s"(box + W2_OFF + ej * 18432 - (lbase + IMG_A)), [fl] "s"(box + W2F_OFF),
                       [tag] "s"(u32x2{line_tag(epoch, 10), line_tag(epoch, 11)}), [gcap] "i"(GSPIN_CAP), [iss] "i"(CT_OFF + C_ISS), [cap] "i"(SPIN_CAP), [slot] "i"(SLOT), [ring] "i"(NSLOT * SLOT), [pring] "i"(NPK * 128),
                       [npk2] "i"(2 * NPK), [last] "i"(TPW - 1), [tight] "i"(TPW - 5),  [sch] "s"(W2SCH)
                     : "memory", "scc", "vcc", "s44", "s45", "s46", "s47", "s48", "s49", "s50", "s51", "s52", "s53", "s54", "s55", "s56", "s57",
                       "v0", "v1", "v2", "v3", "v4", "v5", "v6", "v7", "v8", "v9", "v10", "v11", "v12", "v13", "v14", "v15", "v16", "v17", "v18", "v19", "v20", "v21", "v22", "v23", "v24", "v25", "v26", "v27", "v28", "v29", "v30", "v31", "v32", "v33", "v34", "v35", "v36", "v37", "v38", "v39", "v40", "v41", "v42", "v43", "v44", "v45", "v46", "v47", "v48", "v49", "v50", "v51", "v52", "v53", "v54", "v55", "v56", "v57", "v58", "v59", "v60", "v61", "v62", "v63", "v64", "v65", "v66", "v67", "v68", "v69", "v70", "v71", "v72", "v73", "v74", "v75", "v76", "v77", "v78", "v79", "v80", "v81", "v82", "v83", "v84", "v85", "v86", "v87", "v88",
                       "v90", "v91", "v92", "v93", "v94", "v95", "v96", "v97", "v98", "v99", "v100", "v101", "v102", "v103", "v104", "v105", "v106", "v107",
                       "v108", "v109", "v110", "v111", "v112", "v113", "v114", "v115", "v116", "v117", "v118", "v119", "v120", "v121", "v122", "v123",
                       "v124", "v125", "v126", "v127", "v128", "v129", "v130", "v131", "v132", "v133", "v134", "v135", "v136", "v137", "v138", "v139",
                       "v140", "v141", "v142", "v143", "v144", "v145", "v146", "v147", "v148", "v149", "v150", "v151", "v152", "v153", "v154", "v155");
      }
    } else {
      LANE_VARS
      const int c = wave - 1 - NE;
      SCtx sc;
      sc.L0 = L0; sc.ct = ct; sc.apk = lbase + IMG_A + lane * 16; sc.apm = lbase + PKM + r * 8;
      sc.api = lbase + PKU + g * 64 + (r & 3) * 2 + ((r >> 2) == g ? 0 : 8); sc.amr = ct + C_MREF + r * 4;
      sc.o_rb = r * ROWB + ((g ^ swz(r)) << 4);
      sc.ev = (r & 1) ^ 1;
      sc.o_yb = (r & 1) ? ONES_OFF + (g << 4) : (32 + (r >> 1)) * ROWB + ((g ^ swz(r >> 1)) << 4);
      if (c >= NEARLY) {
        static_assert(NS - NEARLY == 3, "");
        const int u = c - NEARLY;
        const unsigned epoch = epoch_load(box);
        const unsigned ta = line_tag(epoch, 8), tb = line_tag(epoch, 9);
        const unsigned qw = lds_ld32(ct + C_EP + 4);
        while ((unsigned)__builtin_amdgcn_s_memrealtime() - tent < qw) __builtin_amdgcn_s_sleep(4);
        u32x4 v[4];
#pragma unroll 1
        for (int sp = 0; sp < GSPIN_CAP; ++sp) {
          asm volatile("global_load_dwordx4 %0, %4, %5 sc1\n\tglobal_load_dwordx4 %1, %4, %5 offset:1024 sc1\n\tglobal_load_dwordx4 %2, %4, %5 offset:2048 sc1\n\t"
                       "global_load_dwordx4 %3, %4, %5 offset:3072 sc1\n\ts_waitcnt vmcnt(0)"
                       : "=&v"(v[0]), "=&v"(v[1]), "=&v"(v[2]), "=&v"(v[3]) : "v"(lane * 16), "s"(box + QK_OFF + u * 4096) : "memory");
          unsigned bad = 0;
#pragma unroll
          for (int t = 0; t < 4; ++t) bad |= (v[t][0] ^ v[t][1] ^ v[t][2] ^ ta) | (v[t][3] ^ tb);
          if (!__any(bad != 0u)) break;
        }
#pragma unroll
        for (int t = 0; t < 4; ++t)
          *(LDS_AS(u32x2)*)(L0 + IMG_A + (4 * u + t) * 512 + ((g >> 1) * 16 + r) * 16 + (g & 1) * 8) = u32x2{v[t][0], v[t][1]};
        asm volatile("" ::: "memory");
        lds_add(ct + C_QI, 1u);
        lds_st32(ct + C_EP, epoch);
      }
      static_assert(NE == 4 && NPK == 6 && NSLOT == 10, "");
      s_items(sc, helper ? TPW : c, helper || c >= NEARLY);
      static_assert(NH1 == 3 && NS == 7 && TPW == 33, "");
      const int hi = c == 0 ? 5 : c - 2;
      if (hi >= (helper ? 4 : 0)) s_half(sc, TPW - NH1 + (hi >> 1), hi & 1);
      rslot = c;
    }
  }

  if (rslot >= 0 && ((blockIdx.x >> 3) & 3) == 1) {
    LANE_VARS
    __builtin_amdgcn_s_setprio(0);
    const int rid = ((blockIdx.x >> 5) << 3) | (blockIdx.x & 7);
    const int qa = ((rid >> 1) & 3) * 4 + ((rid >> 3) & 1) * 2;
    const char* src = box + (size_t)(rid * RSTR + rslot * 32) * 128;
    const int e = lane & 7;
    const int oidx = (qa + (e & 1)) * CC + 48 * (rid >> 4) + 16 * (e < 6 ? e >> 1 : 0) + 8 * (rid & 1) + (lane >> 3);
    const unsigned short qv = ((const gus*)q)[oidx];
    u32x4 am;
#pragma unroll
    for (int d = 0; d < 4; ++d) am[d] = (e >> 1) == d ? ((e & 1) ? 0xffff0000u : 0x0000ffffu) : 0u;
    const unsigned epoch = *(LDS_AS(unsigned)*)(L0 + CT_OFF + C_EP);
    const unsigned arf = lbase + RED_OFF + 4096 + (lane & 1) * 4;
    poll_ge_sleep(ct + C_FREE + ((TPW - NPK) % NSLOT) * 4, 8u * ((TPW - NPK) / NSLOT + 1));
    lds_st32(arf + rslot * 8, 0u);
#define R_LD(d, m, o) "s_mov_b64 exec, s[" m "]\n\tglobal_load_dwordx4 v[" d "], %[vo], %[src] offset:" #o " sc1\n\t"
#define R_W(l, a0, a1, a2, a3) "v_cvt_pk_bf16_f32 v" #a3 ", v" #a3 ", v" #a3 "\n\tv_and_b32 v" #a0 ", v60, v" #a3 "\n\tv_and_b32 v" #a1 ", v61, v" #a3 "\n\t" \
                               "v_and_b32 v" #a2 ", v62, v" #a3 "\n\tv_and_b32 v" #a3 ", v63, v" #a3 "\n\t"
#define R_G(n, w, m, l, d0, d1, d2, d3, a0, a1, a2, a3, c) \
  "s_mov_b64 exec, s[" m "]\n\ts_cbranch_execz " #n "f\n\ts_waitcnt vmcnt(" #w ")\n\tv_bitop3_b32 v" #l ", v" #d0 ", v" #d1 ", v" #d2 " bitop3:0x96\n\t" \
  "v_bitop3_b32 v" #l ", v" #l ", v" #d3 ", %[tag] bitop3:0x96\n\tv_mov_b32 v" #d3 ", s49\n\ts_mov_b64 exec, -1\n\tv_exp_f32 v" #a3 ", v" #l "\n\t" \
  "v_xor_b32_dpp v59, v" #l ", v" #l " quad_perm:[2,3,0,1] row_mask:0xf bank_mask:0x5\n\tv_xor_b32_dpp v59, v" #l ", v" #l " row_shr:4 row_mask:0xf bank_mask:0xa\n\t" \
  R_W(l, a0, a1, a2, a3) "v_cmp_ne_u32 s[" m "], 0, v59\n\ts_nop 0\n" \
  "1" #n ":\n\tv_mfma_f32_16x16x32_bf16 v[36:39], v[" #a0 ":" #a3 "], v[" #d0 ":" #d3 "], " c "\n\t"
#define R_X(n) #n ":\n\ts_mov_b64 exec, -1\n\ts_nop 2\n\ts_branch 1" #n "b\n"
#define R_F(l, d0, d3, a0, a1, a2, a3, c) "v_sub_f32 v" #a3 ", v" #l ", v56\n\tv_exp_f32 v" #a3 ", v" #a3 "\n\ts_nop 0\n\t" R_W(l, a0, a1, a2, a3) \
  "s_nop 1\n\tv_mfma_f32_16x16x32_bf16 v[36:39], v[" #a0 ":" #a3 "], v[" #d0 ":" #d3 "], " c "\n\t"
#define R_RD(d, a, b) "ds_read2st64_b32 v[" d "], %[ard] offset0:" #a " offset1:" #b "\n\t"
#define R_A(d, a) "v_pk_add_f32 v[" d "], v[" d "], v[" a "]\n\t"
#define R_S(x, f) "v_sub_f32 v" #f ", v" #f ", v56\n\tv_exp_f32 v" #f ", v" #f "\n\ts_nop 0\n\tv_mul_f32 v[" #x "], v[" #x "], v" #f "\n\tv_mul_f32 v[" #x " + 1], v[" #x " + 1], v" #f "\n\t"
    asm volatile("s_mov_b64 s[40:41], -1\n\ts_mov_b64 s[42:43], -1\n\ts_mov_b64 s[44:45], -1\n\ts_mov_b64 s[46:47], -1\n\ts_mov_b32 s48, %[cap]\n\ts_mov_b32 s49, 0x3f803f80\n\t"
                 "v_mov_b32 v57, 1\n\tv_mov_b32 v58, %[x1]\n"
                 "20:\n\ts_sleep 1\n\tds_read_b32 v56, v58\n\ts_sub_u32 s48, s48, 1\n\ts_cselect_b32 s50, 0, %[x1n]\n\ts_waitcnt lgkmcnt(0)\n\t"
                 "v_readfirstlane_b32 s51, v56\n\ts_cmp_lt_u32 s51, s50\n\ts_cbranch_scc1 20b\n\tv_mov_b32 v58, %[red]\n\ts_mov_b32 s48, %[gcap]\n\ts_sleep %[rs]\n"
                 "21:\n\t" R_LD("0:3", "40:41", 0) R_LD("4:7", "42:43", 1024) R_LD("8:11", "44:45", 2048) R_LD("12:15", "46:47", 3072)
                 "s_cmp_eq_u32 s48, %[gcap]\n\ts_cbranch_scc1 24f\n\ts_waitcnt vmcnt(0)\n24:\n\t"
                 R_G(1, 3, "40:41", 32, 0, 1, 2, 3, 16, 17, 18, 19, "0") R_G(2, 2, "42:43", 33, 4, 5, 6, 7, 20, 21, 22, 23, "v[36:39]")
                 R_G(3, 1, "44:45", 34, 8, 9, 10, 11, 24, 25, 26, 27, "v[36:39]") R_G(4, 0, "46:47", 35, 12, 13, 14, 15, 28, 29, 30, 31, "v[36:39]")
                 "v_max3_f32 v56, v32, v33, v34\n\ts_sub_u32 s48, s48, 1\n\ts_cbranch_scc1 22f\n\ts_or_b64 s[50:51], s[40:41], s[42:43]\n\tv_max_f32 v56, v56, v35\n\t"
                 "s_or_b64 s[50:51], s[50:51], s[44:45]\n\ts_or_b64 s[50:51], s[50:51], s[46:47]\n\tv_cmp_nlt_f32 vcc, |v56|, %[lim]\n\ts_cbranch_scc1 21b\n\t"
                 "s_cbranch_vccnz 25f\n"
                 "22:\n\ts_mov_b64 exec, %[wm]\n\tds_write_b128 %[awr], v[36:39]\n\ts_mov_b64 exec, 1\n\tds_add_rtn_u32 v59, v58, v57\n\ts_mov_b64 exec, -1\n\t"
                 R_RD("40:41", 0, 1) R_RD("42:43", 2, 3) R_RD("44:45", 4, 5) R_RD("46:47", 6, 7) R_RD("48:49", 8, 9) R_RD("50:51", 10, 11) R_RD("52:53", 12, 13) R_RD("54:55", 14, 15)
                 "s_waitcnt lgkmcnt(8)\n\tv_add_u32 v59, v59, v57\n\ts_nop 0\n\tv_readfirstlane_b32 s50, v59\n\ts_cmp_lg_u32 s50, 8\n\ts_cbranch_scc1 26f\n"
                 "23:\n\ts_waitcnt lgkmcnt(6)\n\t" R_A("40:41", "42:43") "s_waitcnt lgkmcnt(4)\n\t" R_A("44:45", "46:47") "s_waitcnt lgkmcnt(2)\n\t" R_A("48:49", "50:51")
                 R_A("40:41", "44:45") "s_waitcnt lgkmcnt(0)\n\t" R_A("52:53", "54:55") R_A("48:49", "52:53") R_A("40:41", "48:49")
                 "v_add_f32 v40, v40, v41\n\ts_nop 1\n\tv_mov_b32_dpp v41, v40 quad_perm:[2,3,2,3] row_mask:0xf bank_mask:0xf\n\ts_nop 1\n\t"
                 "v_mov_b32_dpp v41, v41 row_shl:4 row_mask:0xf bank_mask:0x5\n\tv_rcp_f32 v41, v41\n\ts_mov_b64 exec, %[om]\n\tv_mul_f32 v40, v40, v41\n\t"
                 "v_cvt_pk_bf16_f32 v40, v40, v40\n\tv_lshlrev_b32 v40, 16, v40\n\tv_add_f32 v40, v40, %[qf]\n\tv_cvt_pk_bf16_f32 v40, v40, v40\n\t"
                 "global_store_short %[oo], v40, %[out]\n\t"
                 "s_cmp_lg_u32 %[rid], 0\n\ts_cbranch_scc1 29f\n\ts_mov_b64 exec, 1\n\tv_mov_b32 v41, %[eo]\n\tglobal_store_dword v41, %[ep], %[bx] sc1\n\ts_branch 29f\n"
                 R_X(1) R_X(2) R_X(3) R_X(4)
                 "25:\n\tv_max_f32_dpp v56, v56, v56 row_ror:8 row_mask:0xf bank_mask:0xf\n\tv_mov_b32 v59, v56\n\ts_nop 1\n\tv_permlane16_swap_b32 v56, v59\n\tv_max_f32 v56, v56, v59\n\t"
                 "v_mov_b32 v59, v56\n\ts_nop 1\n\tv_permlane32_swap_b32 v56, v59\n\tv_max_f32 v56, v56, v59\n\tv_mov_b32 v57, 0x101\n\tv_add_u32 v59, %[rs8], %[arf]\n\tds_write_b32 v59, v56\n\t"
                 R_F(32, 0, 3, 16, 17, 18, 19, "0") R_F(33, 4, 7, 20, 21, 22, 23, "v[36:39]") R_F(34, 8, 11, 24, 25, 26, 27, "v[36:39]") R_F(35, 12, 15, 28, 29, 30, 31, "v[36:39]")
                 "s_nop 7\n\ts_branch 22b\n"
                 "26:\n\ts_waitcnt vmcnt(0)\n\ts_and_b32 s50, s50, 0xff\n\ts_cmp_lg_u32 s50, 8\n\ts_cbranch_scc1 29f\n\t"
                 "ds_read2_b32 v[0:1], %[arf] offset1:2\n\tds_read2_b32 v[2:3], %[arf] offset0:4 offset1:6\n\tds_read2_b32 v[4:5], %[arf] offset0:8 offset1:10\n\t"
                 "ds_read2_b32 v[6:7], %[arf] offset0:12 offset1:14\n\ts_waitcnt lgkmcnt(0)\n\tv_max3_f32 v56, v0, v1, v2\n\tv_max3_f32 v56, v56, v3, v4\n\t"
                 "v_max3_f32 v56, v56, v5, v6\n\tv_max_f32 v56, v56, v7\n\t"
                 R_S(40, 0) R_S(42, 1) R_S(44, 2) R_S(46, 3) R_S(48, 4) R_S(50, 5) R_S(52, 6) R_S(54, 7) "s_branch 23b\n"
                 "29:\n\ts_mov_b64 exec, -1\n\ts_waitcnt lgkmcnt(0)"
                 :
                 : "{v[60:63]}"(am), [vo] "v"(lane * 16), [src] "s"(src), [tag] "v"(line_tag(epoch, e)),
                   [awr] "v"(lbase + RED_OFF + (rslot * 2 + (r >> 3)) * 256 + (r & 7) * 32 + (g & 1) * 16), [ard] "v"(lbase + RED_OFF + lane * 4), [arf] "v"(arf),
                   [qf] "v"((unsigned)qv << 16), [oo] "v"(oidx * 2), [out] "s"(KARG(kb, 0)), [ep] "v"(epoch + 1u), [bx] "s"(box), [rid] "s"(rid), [rs8] "s"(rslot * 8),
                   [wm] "s"(0xff00ff0000ff00ffull), [om] "s"(0x3f3f3f3f3f3f3f3full), [lim] "s"(64.f),
                   [cap] "i"(SPIN_CAP), [gcap] "i"(GSPIN_CAP), [x1] "i"(CT_OFF + C_X1), [x1n] "i"(NE), [red] "i"(CT_OFF + C_RED), [rs] "i"(RSLEEP), [eo] "i"(EPOCH_OFF)
                 : "memory", "scc", "vcc", "s40", "s41", "s42", "s43", "s44", "s45", "s46", "s47", "s48", "s49", "s50", "s51", "v0", "v1", "v2", "v3", "v4", "v5", "v6", "v7", "v8", "v9", "v10", "v11", "v12", "v13", "v14", "v15", "v16", "v17", "v18", "v19", "v20", "v21", "v22", "v23", "v24", "v25", "v26", "v27", "v28", "v29", "v30", "v31", "v32", "v33", "v34", "v35", "v36", "v37", "v38", "v39", "v40", "v41", "v42", "v43", "v44", "v45", "v46", "v47", "v48", "v49", "v50", "v51", "v52", "v53", "v54", "v55", "v56", "v57", "v58", "v59");
  }
}

#undef q
#undef f0
#undef f1
#undef wq
#undef wk
#undef wv
#undef wo
#undef box
extern "C" __global__ void dino_end() {}

extern "C" void kernel_launch(void* const* d_in, const int* in_sizes, int n_in,
                              void* d_out, int out_size, void* d_ws, size_t ws_size,
                              hipStream_t stream) {
  dino_main<<<NWG, 768, 0, stream>>>((const unsigned short*)d_in[0], (const char*)d_in[1], (const char*)d_in[2], (const char*)d_in[3],
                                     (const char*)d_in[4], (const char*)d_in[5], (const char*)d_in[6], (char*)d_ws + ((ws_size - (4u << 20)) & ~(size_t)0x1fffff),
                                     (unsigned short*)d_out);
}
```

```cpp
#include <hip/hip_runtime.h>
#include <hip/hip_bf16.h>
#include <stdint.h>


using bf16x8  = __attribute__((ext_vector_type(8))) short;
using f32x4   = __attribute__((ext_vector_type(4))) float;
using f32x2   = __attribute__((ext_vector_type(2))) float;
using u32x4   = __attribute__((ext_vector_type(4))) unsigned;
using u32x2   = __attribute__((ext_vector_type(2))) unsigned;
using v4i16_t = __attribute__((ext_vector_type(4))) short;
typedef __attribute__((address_space(3))) char* lds_ptr;
#define LDS_AS(T) __attribute__((address_space(3))) T

constexpr int CC = 192;
constexpr int NWG = 256, NS = 7, NE = 4;
constexpr int TPW = 33;
constexpr int ROWB = 384;
constexpr int SLOT = 40 * ROWB;
constexpr int NSLOT = 10;
constexpr int RSTR = 322;
constexpr int EPOCH_OFF = 64 * RSTR * 128, QK_OFF = EPOCH_OFF + 4096, W2F_OFF = QK_OFF + 12288, W2_OFF = W2F_OFF + 1024;
static_assert(W2_OFF + 72 * 1024 <= (4 << 20), "");
constexpr int GSPIN_CAP = 1 << 16;
#define PACE16 777
#define PACES 810
#define PK1 14
#define PWIN 35
#define AHI 22
#define ADL 60
#define AMG 200
#define T0BACK 7
#define HDUE 93
#define QKW0 200
#define QKW1 2
#define RSLEEP 25
#define W2SCH 0x0311180000000000ull
constexpr int NEARLY = 4;
constexpr int NH1 = 3;
constexpr int NPK = 6;
constexpr float EPS = 1e-5f;
constexpr float SQRT_C = 13.856406460551018f;
constexpr float LOG2E = 1.4426950408889634f;
constexpr float THR = 20.0f;
constexpr float TOFF = 192.0f;
constexpr int SPIN_CAP = 1 << 22;

constexpr int IMG_A = NSLOT * SLOT;
constexpr int CT_OFF = IMG_A + 6144;
constexpr int PKM = CT_OFF + 512, PKU = PKM + NPK * 128;
constexpr int ONES_OFF = PKU + NPK * 256;
constexpr int JUNK = ONES_OFF + ROWB;
constexpr int LDS_BYTES = JUNK + 256;
constexpr int H_R = 192 * ROWB, H_Q = H_R + 32 * ROWB, H_C = H_Q + 6144;
static_assert(LDS_BYTES <= 163840 && (PKU & 15) == 0 && (ONES_OFF & 127) == 0, "lds");
constexpr int C_FULL = 0, C_FREE = 64, C_EP = 128, C_RED = 144, C_ISS = 152, C_X1 = 156, C_QI = 160, C_PFL = 192, C_CONS = 256,
              C_MREF = 320, C_LW = 384;

typedef __bf16 bf16x2_t __attribute__((ext_vector_type(2)));
__device__ __forceinline__ unsigned cvtpk(float lo, float hi) {
  return __builtin_bit_cast(unsigned, __builtin_convertvector(f32x2{lo, hi}, bf16x2_t));
}
__device__ __forceinline__ float bflo(unsigned p) { return __uint_as_float(p << 16); }
__device__ __forceinline__ float bfhi(unsigned p) { return __uint_as_float(p & 0xffff0000u); }
__device__ __forceinline__ bf16x8 pack8(const f32x4& a, const f32x4& b) {
  return __builtin_bit_cast(bf16x8, u32x4{cvtpk(a[0], a[1]), cvtpk(a[2], a[3]), cvtpk(b[0], b[1]), cvtpk(b[2], b[3])});
}
__device__ __forceinline__ float xrow16_max(float x) {
  auto s = __builtin_amdgcn_permlane16_swap(__float_as_uint(x), __float_as_uint(x), false, false);
  x = fmaxf(__uint_as_float(s[0]), __uint_as_float(s[1]));
  auto t = __builtin_amdgcn_permlane32_swap(__float_as_uint(x), __float_as_uint(x), false, false);
  return fmaxf(__uint_as_float(t[0]), __uint_as_float(t[1]));
}
__device__ __forceinline__ float xrow16_sum(float x) {
  auto s = __builtin_amdgcn_permlane16_swap(__float_as_uint(x), __float_as_uint(x), false, false);
  x = __uint_as_float(s[0]) + __uint_as_float(s[1]);
  auto t = __builtin_amdgcn_permlane32_swap(__float_as_uint(x), __float_as_uint(x), false, false);
  return __uint_as_float(t[0]) + __uint_as_float(t[1]);
}
__device__ __forceinline__ v4i16_t tr_read(lds_ptr p) {
  return __builtin_amdgcn_ds_read_tr16_b64_v4i16((LDS_AS(v4i16_t)*)p);
}
__device__ __forceinline__ f32x4 mfma(bf16x8 a, bf16x8 b, f32x4 c) {
  return __builtin_amdgcn_mfma_f32_16x16x32_bf16(a, b, c, 0, 0, 0);
}
__device__ __forceinline__ int swz(int p) { return (p & 6) | ((p >> 2) & 1); }
constexpr unsigned swz_tbl(int i) {
  unsigned t = 0;
  for (int j = 0; j < 8; ++j) { const int p = (8 * i + j) / 3; t |= (unsigned)((p & 6) | ((p >> 2) & 1)) << (4 * j); }
  return t;
}
__device__ __forceinline__ bf16x8 mk8(v4i16_t a, v4i16_t b) { return bf16x8{a[0], a[1], a[2], a[3], b[0], b[1], b[2], b[3]}; }

__device__ __forceinline__ void lds_st32(unsigned addr, unsigned v) {
  asm volatile("ds_write_b32 %0, %1" : : "v"(addr), "v"(v) : "memory");
}
__device__ __forceinline__ unsigned lds_ld32(unsigned addr) {
  unsigned v;
  asm volatile("ds_read_b32 %0, %1\n\ts_waitcnt lgkmcnt(0)" : "=v"(v) : "v"(addr) : "memory");
  return __builtin_amdgcn_readfirstlane(v);
}
__device__ __forceinline__ void lds_add(unsigned addr, unsigned n) {
  asm volatile("s_mov_b64 exec, 1\n\tds_add_u32 %0, %1\n\ts_mov_b64 exec, -1\n\ts_nop 3" : : "v"(addr), "v"(n) : "memory");
}
__device__ __forceinline__ void poll_ge_sleep(unsigned addr, unsigned need) {
  need = __builtin_amdgcn_readfirstlane(need);
  unsigned v, s, n2, cnt = SPIN_CAP;
  asm volatile("s_branch 2f\n1:\n\ts_sleep 2\n2:\n\tds_read_b32 %0, %4\n\ts_sub_u32 %3, %3, 1\n\ts_cselect_b32 %2, 0, %5\n\ts_waitcnt lgkmcnt(0)\n\t"
               "v_readfirstlane_b32 %1, %0\n\ts_cmp_lt_u32 %1, %2\n\ts_cbranch_scc1 1b"
               : "=&v"(v), "=&s"(s), "=&s"(n2), "+s"(cnt) : "v"(addr), "s"(need) : "memory", "scc");
}
__device__ __forceinline__ bf16x8 img_ld(lds_ptr L0, int off, int kk, int lane) { return *(LDS_AS(bf16x8)*)(L0 + off + kk * 1024 + lane * 16); }
__device__ __forceinline__ void img_st(lds_ptr L0, int off, int kk, int lane, bf16x8 v) { *(LDS_AS(bf16x8)*)(L0 + off + kk * 1024 + lane * 16) = v; }

template <int N> __device__ __forceinline__ void wait_vm() { asm volatile("s_waitcnt vmcnt(%0)" :: "i"(N) : "memory"); }
constexpr int A_IMG = ((TPW + 1 - NPK) % NSLOT) * SLOT, RED_OFF = ((TPW - NPK) % NSLOT) * SLOT;
__device__ __forceinline__ unsigned line_tag(unsigned epoch, unsigned i) {
  unsigned x = (epoch + 1u + i * 0x9E3779B9u) * 0x85EBCA6Bu;
  x ^= x >> 15; x *= 0xC2B2AE35u;
  return x ^ (x >> 13);
}
__device__ __forceinline__ unsigned epoch_load(const char* box) {
  unsigned e;
  asm volatile("global_load_dword %0, %1, %2 sc1\n\ts_waitcnt vmcnt(0)" : "=v"(e) : "v"(EPOCH_OFF), "s"(box) : "memory");
  return __builtin_amdgcn_readfirstlane(e);
}
__device__ __forceinline__ int tr_off(int row, int col4) {
  const int ch = col4 >> 1;
  return row * ROWB + (((ch & ~7) | ((ch & 7) ^ swz(row & 15))) << 4) + 8 * (col4 & 1);
}
#define PIN3(a, i) "+v"(a[i]), "+v"(a[i + 1]), "+v"(a[i + 2])

#define DMA1(d, b, off, pol) "global_load_lds_dwordx4 %[" #d "], %[" #b "] offset:" #off pol "\n\t"
__device__ __forceinline__ void issue_rows16(const char* a0, unsigned dst, const unsigned (&d)[3]) {
  unsigned keep;
  asm volatile("s_mov_b32 %[keep], m0\n\ts_mov_b32 m0, %[dst]\n\ts_nop 0\n\t"
               DMA1(d0, a, 0, "") DMA1(d1, a, 1024, "") DMA1(d2, a, 2048, "") DMA1(d0, a, 3072, "")
               "s_add_u32 m0, %[dst], 0x1000\n\ts_nop 0\n\t" DMA1(d1, b, 0, "") DMA1(d2, b, 1024, "") "s_mov_b32 m0, %[keep]"
               : [keep] "=&s"(keep) : [dst] "s"(dst), [a] "s"(a0), [b] "s"(a0 + 4096), [d0] "v"(d[0]), [d1] "v"(d[1]), [d2] "v"(d[2]) : "memory", "scc");
}
#define LD_VM "s_getreg_b32 s43, hwreg(HW_REG_IB_STS, 22, 2)\n\ts_getreg_b32 s42, hwreg(HW_REG_IB_STS, 0, 4)\n\ts_lshl4_add_u32 s42, s43, s42\n\t"
#define LD_GATE(n, X) \
  #n "0:\n\t" LD_VM "s_cmp_le_i32 s42, s46\n\ts_cbranch_scc1 " #n "2f\n" \
  #n "1:\n\t" X "s_cmp_gt_u32 s42, %[win]\n\ts_cbranch_scc1 " #n "5f\n\ts_cmp_ge_u32 s45, s44\n\ts_cbranch_scc1 " #n "7f\n" \
  #n "6:\n\ts_memrealtime s[40:41]\n\ts_sleep 1\n\ts_waitcnt lgkmcnt(0)\n\ts_sub_u32 s45, s40, %[t0]\n\ts_lshl_b32 s45, s45, 4\n\ts_cmp_lt_u32 s45, s44\n\ts_cbranch_scc1 " #n "6b\n" \
  #n "7:\n\t" LD_VM
#define LD_NEXT \
  "s_cmp_gt_u32 s42, %[ahi]\n\ts_cselect_b32 s42, %[amg], 0\n\ts_sub_u32 s43, s45, s44\n\ts_cmp_lt_u32 s43, s42\n\ts_cselect_b32 s42, %[p3] + %[adl], s39\n\t" \
  "s_add_u32 s44, s44, s42\n\ts_add_u32 s46, s46, 5\n\t"
#define LD_PUB0(n, a, b, c) \
  #n "2:\n\tv_add_u32 %[pub], 2, %[pub]\n\ts_cmp_ge_u32 s46, 30\n\ts_cbranch_scc1 " #n "4f\n\ts_cmp_ge_u32 s46, 15\n\ts_cbranch_scc1 " #n "3f\n\t" \
  "s_waitcnt vmcnt(" #a ")\n" #n "3:\n\ts_waitcnt vmcnt(" #b ")\n" #n "4:\n\ts_waitcnt vmcnt(" #c ")\n\tds_write_b32 %[af], %[pub]\n\t" \
  "s_sub_u32 s46, s46, 15\n\ts_branch " #n "1b\n"
#define LD_PUB(n, a, b, c) LD_PUB0(n, a, b, c) \
  #n "5:\n\ts_sleep 1\n\ts_sub_i32 s47, s47, 1\n\ts_cmp_gt_i32 s47, 0\n\ts_cbranch_scc1 " #n "0b\n\ts_waitcnt vmcnt(%[win])\n\ts_branch " #n "6b\n"
#define LD1(d, b, off) "global_load_lds_dwordx4 %[" #d "], s[" #b "] offset:" #off " nt\n\t"

#define PV_REQ(P, U, M, C, CA) \
  "v_lshl_add_u32 v150, s44, 3, %[apk]\n\tv_lshl_add_u32 v151, s44, 1, %[apu]\n\tv_add_u32 v152, s44, %[apm]\n\t" \
  "ds_read_b32 v" #C ", v" #CA "\n\tds_read_b128 v[" P "], v150\n\tds_read_b64 v[" M "], v152\n\tds_read_b128 v[" U "], v151\n\t"
#define PV_TR(d0, d1, a, o) "ds_read_b64_tr_b16 v[" #d0 ":" #d1 "], v" #a " offset:" #o "\n\t"
#define PV_FETCH(y0, y0b, a0, a0b, b0, b0b, y1, y1b, a1, a1b, b1, b1b, y2, y2b, a2, a2b, b2, b2b) \
  "v_add_u32 v153, s45, %[ox]\n\tv_add_u32 v154, s45, %[oy]\n\t" \
  PV_TR(a0, a0b, 153, 0) PV_TR(b0, b0b, 153, 6144) PV_TR(a1, a1b, 153, 128) PV_TR(b1, b1b, 153, 6272) PV_TR(a2, a2b, 153, 256) PV_TR(b2, b2b, 153, 6400) \
  PV_TR(y0, y0b, 154, 0) PV_TR(y1, y1b, 154, 128) PV_TR(y2, y2b, 154, 256) \
  "s_add_u32 s45, s45, %[slot]\n\ts_cmp_eq_u32 s45, %[ring]\n\ts_cselect_b32 s45, 0, s45\n\t"
#define PV_FETCH_A PV_FETCH(90, 91, 92, 93, 94, 95, 96, 97, 98, 99, 100, 101, 102, 103, 104, 105, 106, 107)
#define PV_FETCH_B PV_FETCH(108, 109, 110, 111, 112, 113, 114, 115, 116, 117, 118, 119, 120, 121, 122, 123, 124, 125)
#define PV_MF(d, a, p) "v_mfma_f32_16x16x32_bf16 v[" d "], v[" a "], v[" p "], v[" d "]\n\t"
#define PV_MOV(d, s) "v_mov_b32 v" #d ", v" #s "\n\t"
#define PV_WAITF(n, CA)   \
  #n ":\n\tds_read_b32 v155, v" #CA "\n\ts_sub_i32 s47, s47, 1\n\ts_cmp_lt_i32 s47, 0\n\ts_cbranch_scc1 " #n "9f\n\ts_waitcnt lgkmcnt(0)\n\t" \
  "v_cmp_lt_u32 vcc, v155, %[need]\n\ts_and_b64 vcc, vcc, 2\n\ts_cbranch_scc0 " #n "9f\n\ts_sleep 2\n\ts_branch " #n "b\n" #n "9:\n\ts_waitcnt lgkmcnt(0)\n\t"
#define PV_STEP(n, P, U, M, m0, m1, C, CA, P2, U2, M2, C2, CA2, X0, X1, X2, Y0, Y1, Y2, MV0, MV1, MV2, HOOK) \
  "s_waitcnt lgkmcnt(10)\n\t" \
  "v_cmp_lt_u32 vcc, v" #C ", %[need]\n\tv_cmp_neq_f32 s[48:49], v" #m0 ", v88\n\tv_cmp_neq_f32 s[50:51], v" #m1 ", v88\n\t" \
  "v_add_u32 v" #CA2 ", 4, v" #CA "\n\ts_or_b64 s[48:49], s[48:49], vcc\n\tv_cmp_eq_u32 s[52:53], v" #CA2 ", %[lim]\n\t" \
  "s_or_b64 s[48:49], s[48:49], s[50:51]\n\ts_cbranch_scc1 " #n "1f\n" \
  #n "0:\n\ts_add_u32 s44, s44, 128\n\ts_cmp_eq_u32 s44, %[pring]\n\ts_cselect_b32 s44, 0, s44\n\t" \
  "v_cndmask_b32 v" #CA2 ", v" #CA2 ", %[base], s[52:53]\n\t" \
  PV_MF("84:87", U, P) PV_MF("72:75", X0, P) PV_MF("76:79", X1, P) MV0 PV_MF("80:83", X2, P) MV1 \
  PV_MF("72:75", Y0, P) MV2 HOOK PV_REQ(P2, U2, M2, C2, CA2) PV_MF("76:79", Y1, P) PV_MF("80:83", Y2, P)
#define PV_REL(CA) \
  "s_mov_b64 exec, 3\n\tds_add_u32 v" #CA ", %[one] offset:64\n\ts_mov_b64 exec, -1\n\tv_add_u32 %[need], 2, %[need]\n\ts_add_u32 s46, s46, 1\n\t"
#define PV_SC1(p) "v_lshlrev_b32 v151, 16, v" #p "\n\tv_and_b32 v152, 0xffff0000, v" #p "\n\tv_mul_f32 v151, v151, v150\n\tv_mul_f32 v152, v152, v150\n\t" \
                  "v_cvt_pk_bf16_f32 v" #p ", v151, v152\n\t"
#define PV_MUL4(a, b, c, d) "v_mul_f32 v" #a ", v" #a ", v150\n\tv_mul_f32 v" #b ", v" #b ", v150\n\tv_mul_f32 v" #c ", v" #c ", v150\n\tv_mul_f32 v" #d ", v" #d ", v150\n\t"
#define PV_RESC(m0, m1, p0, p1, p2, p3) \
  "v_mov_b32 v150, v88\n\tv_max3_f32 v88, v88, v" #m0 ", v" #m1 "\n\tv_sub_f32 v150, v150, v88\n\tv_exp_f32 v150, v150\n\ts_nop 1\n\t" \
  PV_MUL4(72, 73, 74, 75) PV_MUL4(76, 77, 78, 79) PV_MUL4(80, 81, 82, 83) PV_MUL4(84, 85, 86, 87) \
  "v_sub_f32 v150, v" #m0 ", v88\n\tv_exp_f32 v150, v150\n\ts_nop 1\n\t" PV_SC1(p0) PV_SC1(p1) \
  "v_sub_f32 v150, v" #m1 ", v88\n\tv_exp_f32 v150, v150\n\ts_nop 1\n\t" PV_SC1(p2) PV_SC1(p3)
#define PV_SLOW(n, P, U, M, m0, m1, C, CA, p0, p1, p2, p3) \
  #n "1:\n\ts_bitcmp0_b32 vcc_lo, 0\n\ts_cbranch_scc1 " #n "4f\n\ts_cmp_gt_u32 s46, %[tight]\n\ts_cbranch_scc1 " #n "3f\n" \
  #n "2:\n\ts_sleep 2\n\tds_read_b32 v" #C ", v" #CA "\n\ts_sub_i32 s47, s47, 1\n\ts_cmp_lt_i32 s47, 0\n\ts_cbranch_scc1 " #n "3f\n\ts_waitcnt lgkmcnt(0)\n\t" \
  "v_cmp_lt_u32 vcc, v" #C ", %[need]\n\ts_and_b64 vcc, vcc, 1\n\ts_cbranch_scc1 " #n "2b\n" \
  #n "3:\n\t" PV_REQ(P, U, M, C, CA) "s_sub_i32 s47, s47, 1\n\ts_cmp_lt_i32 s47, 0\n\ts_cselect_b32 s54, 0, 1\n\ts_waitcnt lgkmcnt(0)\n\t" \
  "v_cmp_lt_u32 vcc, v" #C ", %[need]\n\tv_cmp_neq_f32 s[48:49], v" #m0 ", v88\n\tv_cmp_neq_f32 s[50:51], v" #m1 ", v88\n\t" \
  "s_and_b32 s54, s54, vcc_lo\n\ts_cbranch_scc1 " #n "3b\n\ts_or_b64 s[48:49], s[48:49], s[50:51]\n\ts_cbranch_scc0 " #n "0b\n\ts_branch " #n "5f\n" \
  #n "4:\n\tv_cmp_neq_f32 s[48:49], v" #m0 ", v88\n\ts_or_b64 s[48:49], s[48:49], s[50:51]\n\ts_cbranch_scc0 " #n "0b\n" \
  #n "5:\n\t" PV_RESC(m0, m1, p0, p1, p2, p3) \
  "s_branch " #n "0b\n"
#define PV_W2(a0, a1, b0, b1, c0, c1) \
  "global_load_dwordx4 v[" #a0 ":" #a1 "], v150, %[w2] sc1\n\tglobal_load_dwordx4 v[" #b0 ":" #b1 "], v150, %[w2] offset:1024 sc1\n\t" \
  "global_load_dwordx4 v[" #c0 ":" #c1 "], v150, %[w2] offset:2048 sc1\n\tv_add_u32 v150, 0xc00, v150\n\ts_sub_u32 s54, s54, 1\n\ts_cbranch_scc1 30b\n\t"

#define PV_PK(a, b) "v_pk_fma_f32 v[" #a ":" #b "], v[" #a ":" #b "], v[150:151], v[150:151] op_sel:[0,0,1] op_sel_hi:[1,0,1]\n\t"
#define PV_NRM(a, b, c, d, o)   \
  "v_pk_fma_f32 v[" #a ":" #b "], v[" #a ":" #b "], v[150:151], v[150:151] op_sel:[0,0,1] op_sel_hi:[1,0,1]\n\t" \
  "v_pk_fma_f32 v[" #c ":" #d "], v[" #c ":" #d "], v[150:151], v[150:151] op_sel:[0,0,1] op_sel_hi:[1,0,1]\n\t" \
  "v_cvt_pk_bf16_f32 v" #a ", v" #a ", v" #b "\n\tv_cvt_pk_bf16_f32 v" #b ", v" #c ", v" #d "\n\tds_write_b64 v120, v[" #a ":" #b "] offset:" #o "\n\t"
#define PV_G(n, a, w0, w1, w2, c0, c1, c2) "s_waitcnt lgkmcnt(" #n ")\n\tv_mfma_f32_16x16x32_bf16 v[72:75], v[" a "], v[" w0 "], " c0 "\n\t" \
  "v_mfma_f32_16x16x32_bf16 v[76:79], v[" a "], v[" w1 "], " c1 "\n\tv_mfma_f32_16x16x32_bf16 v[80:83], v[" a "], v[" w2 "], " c2 "\n\t"
#define PV_Z4(a, b, c, d) "v_mov_b32 v" #a ", 0\n\tv_mov_b32 v" #b ", 0\n\tv_mov_b32 v" #c ", 0\n\tv_mov_b32 v" #d ", 0\n\t"

using u32x8 = __attribute__((ext_vector_type(8))) unsigned;
struct SCtx {
  lds_ptr L0;
  unsigned ct, apk, apm, api, amr;
  int o_rb, o_yb, ev;
  u32x8 q[3];
};
#define S_M(d, a, b) "v_mfma_f32_16x16x32_bf16 v[" d "], v[" a "], v[" b "], v[" d "]\n\t"
#define S_M0(d, a, b) "v_mfma_f32_16x16x32_bf16 v[" d "], v[" a "], v[" b "], 0\n\t"
#define S_R(d, a, o) "ds_read_b128 v[" d "], v" #a " offset:" #o "\n\t"
#define S_W(n) "s_waitcnt lgkmcnt(" #n ")\n\t"
#define S_D " row_mask:0xf bank_mask:0xf\n\t"
#define S_3(x, y) S_M("100:103", x, y) S_M("104:107", x, x) S_M("96:99", y, y)
#define S_SQ(g, h) "v_fmac_f32 v" #g ", 2.0, v" #h "\n\t"
#define S_SQD(g, h) "v_fmac_f32_dpp v" #g ", v" #h ", %[two] quad_perm:[0,0,2,2]" S_D
#define S_U(u, h, y) "v_add_f32_dpp v" #u ", v" #h ", v" #y " quad_perm:[1,1,3,3]" S_D
#define S_C(d, a, b, m) "v_cndmask_b32 v" #d ", v" #a ", v" #b ", %[" #m "]\n\t"
#define S_B(d, n) "v_mov_b32_dpp v" #d ", v157 row_newbcast:" #n S_D
#define S_BASE(d, u, t) "v_pk_fma_f32 v[" d "], v[" u "], v[116:117], v[" t "] op_sel:[0,1,0] op_sel_hi:[1,1,0]\n\t"
#define S_LG(d, r) "v_pk_fma_f32 v[" d "], v[" d "], v[" r "], %[toff] op_sel_hi:[1,1,0]\n\t"
#define S_SUB(p, d) "v_pk_add_f32 v[" p "], v[" d "], v[150:151] op_sel_hi:[1,0] neg_lo:[0,1] neg_hi:[0,1]\n\t"
#define S_E(p) "v_exp_f32 v" #p ", v" #p "\n\t"
#define S_MUL(p, r) "v_pk_mul_f32 v[" p "], v[" p "], v[" r "]\n\t"
#define S_POS   \
  "s_mul_i32 s50, s42, 26\n\ts_lshr_b32 s50, s50, 8\n\ts_mul_i32 s50, s50, -10\n\ts_add_u32 s50, s50, s42\n\ts_mul_i32 s45, s50, %[slot]\n\ts_mov_b32 s43, s45\n\t"
#define S_POLL(n, a, o, need, back, far) \
  #n ":\n\tds_read_b32 v152, v" #a " offset:" #o "\n\ts_sub_i32 s47, s47, 1\n\ts_cmp_lt_i32 s47, 0\n\ts_waitcnt lgkmcnt(0)\n\ts_cbranch_scc1 " back "\n\t" \
  "v_readfirstlane_b32 s40, v152\n\ts_cmp_ge_u32 s40, " need "\n\ts_cbranch_scc1 " back "\n\ts_sleep 2\n\t" \
  "s_add_u32 s40, s40, " far "\n\ts_cmp_ge_u32 s40, " need "\n\ts_cbranch_scc1 " #n "b\n\ts_sleep 12\n\ts_branch " #n "b\n"
__device__ __forceinline__ void s_items(SCtx& c, int k0, int late) {
  asm volatile("s_mov_b32 s47, %[cap]\n\ts_mov_b32 s49, 0\n\ts_mov_b32 s42, %[k0]\n\ts_cmp_lg_u32 %[late], 0\n\ts_cbranch_scc1 7f\n"
               "1:\n\ts_cmp_ge_u32 s42, %[end]\n\ts_cbranch_scc1 9f\n\t"
               S_POS
               "s_mul_i32 s48, s42, 43\n\ts_lshr_b32 s48, s48, 8\n\ts_mul_i32 s46, s48, -6\n\ts_add_u32 s46, s46, s42\n\ts_lshl2_add_u32 s48, s48, %[ns]\n\t"
               "v_add_u32 v144, s45, %[orb]\n\tv_mad_u32_u24 v145, s43, %[ev], %[oyb]\n\ts_lshl2_add_u32 s40, s46, %[pfl]\n\ts_lshl2_add_u32 s41, s50, %[full]\n\t"
               "v_xor_b32 v146, 64, v144\n\tv_xor_b32 v147, 64, v145\n\tv_mov_b32 v148, s40\n\tv_mov_b32 v149, s41\n\t"
               "s_lshl1_add_u32 s44, s42, 2\n\ts_lshl_b32 s46, s46, 7\n\ts_mov_b64 vcc, %[dg]\n\ts_cmp_lt_u32 s49, s44\n\ts_cbranch_scc1 5f\n"
               "2:\n\tds_read_b32 v150, %[amr]\n\tds_read_b32 v151, v148 offset:64\n\t"
               S_R("24:27", 145, 0) S_R("48:51", 144, 0) S_R("28:31", 147, 0) S_R("52:55", 146, 0) S_R("32:35", 145, 128) S_R("56:59", 144, 128)
               S_R("36:39", 147, 128) S_R("60:63", 146, 128) S_R("40:43", 145, 256) S_R("64:67", 144, 256) S_R("44:47", 147, 256) S_R("68:71", 146, 256)
               S_W(10) S_M0("100:103", "48:51", "24:27") S_M0("104:107", "48:51", "48:51") S_R("72:75", 144, 6144) S_R("76:79", 146, 6144) S_M0("96:99", "24:27", "24:27")
               S_W(10) S_M("100:103", "52:55", "28:31") S_M("104:107", "52:55", "52:55") S_R("80:83", 144, 6272) S_R("84:87", 146, 6272) S_M("96:99", "28:31", "28:31")
               S_W(10) S_M("100:103", "56:59", "32:35") S_M("104:107", "56:59", "56:59") S_R("88:91", 144, 6400) S_R("92:95", 146, 6400) S_M("96:99", "32:35", "32:35")
               "s_mov_b64 exec, 1\n\tds_add_u32 v149, %[four] offset:64\n\ts_mov_b64 exec, -1\n\tv_readfirstlane_b32 s51, v151\n\ts_setprio 1\n\ts_nop 0\n\t"
               S_W(11) S_3("60:63", "36:39") S_W(9) S_3("64:67", "40:43") S_W(7) S_3("68:71", "44:47")
               S_W(6) S_M0("108:111", "72:75", "24:27") S_M0("112:115", "72:75", "72:75")
               S_W(5) S_M("108:111", "76:79", "28:31") S_M("112:115", "76:79", "76:79")
               S_W(4) S_M("108:111", "80:83", "32:35")
               "v_mov_b32_dpp v152, v96 quad_perm:[1,1,3,3]" S_D "v_mov_b32_dpp v153, v98 quad_perm:[1,1,3,3]" S_D
               S_M("112:115", "80:83", "80:83") S_C(154, 96, 98, r2) S_SQ(104, 100)
               S_W(3) S_M("108:111", "84:87", "36:39") S_SQD(105, 101) S_SQ(106, 102)
               S_M("112:115", "84:87", "84:87") S_SQD(107, 103) "v_add_f32 v154, %[eps], v154\n\t"
               S_W(2) S_M("108:111", "88:91", "40:43") S_U(128, 100, 152) S_U(129, 101, 152)
               S_M("112:115", "88:91", "88:91") S_U(130, 102, 153) S_U(131, 103, 153)
               S_W(1) S_M("108:111", "92:95", "44:47") S_C(104, 104, 105, r1) S_C(106, 106, 107, r1)
               S_M("112:115", "92:95", "92:95") S_C(155, 128, 129, r1) S_C(156, 130, 131, r1)
               "s_cmp_lt_u32 s42, %[early]\n\ts_cbranch_scc1 7f\n"
               "3:\n\t" S_M0("116:119", "24:27", "0:3") S_C(104, 104, 106, r2) S_C(155, 155, 156, r2)
               S_M("116:119", "28:31", "4:7") "v_add_f32_dpp v104, v154, v104 quad_perm:[0,0,2,2]" S_D S_U(132, 108, 152)
               S_M("116:119", "32:35", "8:11") S_U(133, 109, 152) S_U(134, 110, 153)
               S_M("116:119", "36:39", "12:15") S_U(135, 111, 153) S_SQ(112, 108)
               S_M("116:119", "40:43", "16:19") S_SQD(113, 109) S_SQ(114, 110)
               S_M("116:119", "44:47", "20:23") S_SQD(115, 111) S_C(156, 132, 133, r1) S_C(157, 134, 135, r1)
               S_C(112, 112, 113, r1) S_C(114, 114, 115, r1) S_C(156, 156, 157, r2) S_C(112, 112, 114, r2)
               "v_add_f32_dpp v112, v154, v112 quad_perm:[0,0,2,2]" S_D
               "v_mul_f32 v117, %[nic], v117\n\tv_cndmask_b32_dpp v155, v156, v155, vcc row_ror:4" S_D
               S_BASE("120:121", "128:129", "116:117") S_BASE("122:123", "130:131", "118:119")
               "v_cndmask_b32_dpp v104, v112, v104, vcc row_ror:4" S_D "v_mul_f32 v156, %[nic], v155\n\t"
               S_M("120:123", "48:51", "0:3") S_BASE("124:125", "132:133", "116:117") S_BASE("126:127", "134:135", "118:119")
               S_M("120:123", "52:55", "4:7") "v_fmac_f32 v104, v156, v155\n\tv_max_f32 v104, %[eps], v104\n\t"
               S_M("120:123", "56:59", "8:11") "v_rsq_f32 v157, v104\n\t"
               S_M("120:123", "60:63", "12:15") "v_mul_f32 v104, v104, v157\n\tv_mov_b32_dpp v157, v157 row_ror:12 row_mask:0x2 bank_mask:0xf\n\t"
               S_M("120:123", "64:67", "16:19") "v_cvt_pk_bf16_f32 v105, v155, v104\n\tv_mov_b32_dpp v157, v157 row_ror:8 row_mask:0x4 bank_mask:0xf\n\t"
               S_M("120:123", "68:71", "20:23") "v_lshlrev_b32 v106, 16, v105\n\tv_mov_b32_dpp v157, v157 row_ror:4 row_mask:0x8 bank_mask:0xf\n\t"
               S_M("124:127", "72:75", "0:3") "v_and_b32 v107, 0xffff0000, v105\n\t" S_B(136, 0)
               S_M("124:127", "76:79", "4:7") S_B(137, 1) S_B(138, 2)
               S_M("124:127", "80:83", "8:11") S_B(139, 3) S_LG("120:121", "136:137")
               S_M("124:127", "84:87", "12:15") S_LG("122:123", "138:139") S_SUB("128:129", "120:121")
               S_M("124:127", "88:91", "16:19") S_SUB("130:131", "122:123") S_E(128)
               S_M("124:127", "92:95", "20:23") S_E(129) S_E(130) S_E(131)
               S_B(140, 4) S_B(141, 5) S_B(142, 6) S_B(143, 7) "v_sub_f32 v106, v155, v106\n\t"
               S_LG("124:125", "140:141") S_LG("126:127", "142:143") "v_sub_f32 v107, v104, v107\n\tv_max3_f32 v152, v120, v121, v122\n\t"
               S_SUB("132:133", "124:125") S_SUB("134:135", "126:127") "v_max3_f32 v153, v123, v124, v125\n\t"
               S_E(132) S_E(133) S_E(134) S_E(135)
               "v_max3_f32 v152, v152, v126, v127\n\tv_add_f32 v154, %[thr], v150\n\tv_max_f32 v152, v152, v153\n\tv_cvt_pk_bf16_f32 v106, v106, v107\n\t"
               "v_lshl_add_u32 v144, s46, 3, %[apk]\n\tv_cmp_gt_f32 vcc, v152, v154\n\tv_add_u32 v145, s46, %[apm]\n\tv_lshl_add_u32 v146, s46, 1, %[api]\n\t"
               "s_cbranch_vccnz 8f\n"
               "4:\n\t" S_MUL("128:129", "136:137") S_MUL("130:131", "138:139") S_MUL("132:133", "140:141") S_MUL("134:135", "142:143")
               "v_cvt_pk_bf16_f32 v128, v128, v129\n\tv_cvt_pk_bf16_f32 v129, v130, v131\n\tv_cvt_pk_bf16_f32 v130, v132, v133\n\tv_cvt_pk_bf16_f32 v131, v134, v135\n\t"
               "s_cmp_lt_u32 s51, s48\n\ts_cbranch_scc1 6f\n"
               "10:\n\tds_read_b32 v151, v149\n\tds_write_b128 v144, v[128:131]\n\tds_write2_b32 v145, v150, v150 offset0:0 offset1:1\n\ts_mov_b64 exec, %[dg2]\n\t"
               "ds_write_b16 v146, v105\n\tds_write_b16 v146, v106 offset:16\n\tds_write_b16_d16_hi v146, v105 offset:32\n\tds_write_b16_d16_hi v146, v106 offset:48\n\t"
               "s_mov_b64 exec, 1\n\tds_add_u32 v148, %[inc]\n\ts_mov_b64 exec, -1\n\ts_setprio 0\n\ts_add_u32 s42, s42, %[ns]\n\t"
               S_W(7) "v_readfirstlane_b32 s49, v151\n\ts_branch 1b\n"
               S_POLL(5, 149, 0, "s44", "2b", "2")
               S_POLL(6, 148, 64, "s48", "10b", "64")
               "7:\n\tv_mov_b32 v144, %[qi]\n\tv_mov_b32 v145, v152\n\tv_mov_b32 v146, 1\n"
               S_POLL(70, 144, 0, "%[nq]", "71f", "64")
               "71:\n\tv_mov_b32 v152, v145\n\tds_read_b128 v[0:3], %[apk]\n\tds_read_b128 v[4:7], %[apk] offset:1024\n\tds_read_b128 v[8:11], %[apk] offset:2048\n\t"
               "ds_read_b128 v[12:15], %[apk] offset:3072\n\tds_read_b128 v[16:19], %[apk] offset:4096\n\tds_read_b128 v[20:23], %[apk] offset:5120\n\t"
               "v_subrev_u32 v144, %[mc], %[amr]\n\tds_read_b32 v150, %[amr]\n\ts_mov_b64 exec, 63\n\tds_add_u32 v144, v146\n\ts_mov_b64 exec, -1\n\t"
               "s_cmp_lg_u32 %[late], 0\n\ts_waitcnt lgkmcnt(0)\n\ts_nop 1\n\ts_cbranch_scc1 1b\n\ts_branch 3b\n"
               "8:\n\tv_mov_b32 v153, v152\n\ts_nop 1\n\tv_permlane16_swap_b32 v152, v153\n\tv_max_f32 v152, v152, v153\n\tv_mov_b32 v153, v152\n\ts_nop 1\n\t"
               "v_permlane32_swap_b32 v152, v153\n\tv_max_f32 v152, v152, v153\n\tv_mul_f32 v152, 0x3d000000, v152\n\tv_rndne_f32 v152, v152\n\t"
               "v_mul_f32 v152, 0x42000000, v152\n\tv_max_f32 v150, v150, v152\n\tds_max_f32 %[amr], v150\n\t"
               S_SUB("128:129", "120:121") S_SUB("130:131", "122:123") S_SUB("132:133", "124:125") S_SUB("134:135", "126:127")
               S_E(128) S_E(129) S_E(130) S_E(131) S_E(132) S_E(133) S_E(134) S_E(135) "s_branch 4b\n"
               "9:"
               : "=&{v[0:7]}"(c.q[0]), "=&{v[8:15]}"(c.q[1]), "=&{v[16:23]}"(c.q[2])
               : [orb] "v"(c.o_rb), [oyb] "v"(c.o_yb), [ev] "v"(c.ev), [apk] "v"(c.apk), [apm] "v"(c.apm), [api] "v"(c.api), [amr] "v"(c.amr),
                 [two] "v"(2.f), [four] "v"(4u), [inc] "v"(2u * NPK), [k0] "s"(k0), [late] "s"(__builtin_amdgcn_readfirstlane(late)), [toff] "s"(f32x2{TOFF, TOFF}),
                 [r1] "s"(0xAAAAAAAAAAAAAAAAull), [r2] "s"(0xCCCCCCCCCCCCCCCCull), [dg] "s"(0xF0000F0000F0000Full), [dg2] "s"(0xF00FFF000FF000FFull),
                 [cap] "i"(SPIN_CAP), [end] "i"(TPW - NH1), [ns] "i"(NS), [pfl] "i"(CT_OFF + C_PFL), [full] "i"(CT_OFF + C_FULL), [early] "i"(NEARLY),
                 [qi] "i"(CT_OFF + C_QI), [nq] "i"(NS - NEARLY), [mc] "i"(C_MREF - C_CONS), [slot] "i"(SLOT),
                 [thr] "i"(__builtin_bit_cast(unsigned, THR)), [eps] "i"(__builtin_bit_cast(unsigned, EPS * CC)), [nic] "i"(__builtin_bit_cast(unsigned, -1.f / CC))
               : "memory", "scc", "vcc", "s40", "s41", "s42", "s43", "s44", "s45", "s46", "s47", "s48", "s49", "s50", "s51",
                 "v24", "v25", "v26", "v27", "v28", "v29", "v30", "v31", "v32", "v33", "v34", "v35", "v36", "v37", "v38", "v39", "v40", "v41", "v42", "v43", "v44", "v45", "v46", "v47", "v48", "v49", "v50", "v51", "v52", "v53", "v54", "v55", "v56", "v57", "v58", "v59", "v60", "v61", "v62", "v63", "v64", "v65", "v66", "v67", "v68", "v69", "v70", "v71", "v72", "v73", "v74", "v75", "v76", "v77", "v78", "v79", "v80", "v81", "v82", "v83", "v84", "v85", "v86", "v87", "v88", "v89", "v90", "v91", "v92", "v93", "v94", "v95", "v96", "v97", "v98", "v99", "v100", "v101", "v102", "v103", "v104", "v105", "v106", "v107", "v108", "v109", "v110", "v111", "v112", "v113", "v114", "v115", "v116", "v117", "v118", "v119", "v120", "v121", "v122", "v123", "v124", "v125", "v126", "v127", "v128", "v129", "v130", "v131", "v132", "v133", "v134", "v135", "v136", "v137", "v138", "v139", "v140", "v141", "v142", "v143", "v144", "v145", "v146", "v147", "v148", "v149", "v150", "v151", "v152", "v153", "v154", "v155", "v156", "v157");
}

__device__ __forceinline__ void s_half(SCtx& c, int k, int h) {
  const int sb_ = (k % NSLOT) * SLOT, e = k % NPK;
  const unsigned ya = c.ev * sb_ + c.o_yb, xa = (unsigned)(size_t)c.L0 + sb_ + h * 6144 + c.o_rb;
  const unsigned ap = c.apk + e * 1024 + h * 8, am = c.apm + e * 128 + h * 4, af = c.ct + C_PFL + e * 4;
  poll_ge_sleep(c.ct + C_CONS + e * 4, (unsigned)(NE * (k / NPK) + NS));
  float mref, mt;
  f32x4 rs, S;
  unsigned long long raise;
#define SH_Y(n, y, q) S_W(n) S_M("100:103", y, y) S_M("108:111", y, q)
#define SH_X(n, a, y) S_W(n) S_M("100:103", a, y) S_M("104:107", a, a)
#define SH_P(n, need)   \
  #n ":\n\tds_read_b32 v136, %[afl]\n\ts_sub_u32 s42, s42, 1\n\ts_cselect_b32 s41, 0, " need "\n\ts_waitcnt lgkmcnt(0)\n\tv_readfirstlane_b32 s40, v136\n\t" \
  "s_cmp_ge_u32 s40, s41\n\ts_cbranch_scc1 " #n "0f\n\ts_sleep 2\n\ts_branch " #n "b\n" #n "0:\n\t"
  asm volatile("s_mov_b32 s42, %[cap]\n\ts_sub_u32 s43, %[need], 1\n\tv_mov_b32 v104, %[eps]\n\tv_mov_b32 v105, v104\n\tv_mov_b32 v106, v104\n\tv_mov_b32 v107, v104\n"
               SH_P(2, "s43")
               "ds_read_b128 v[24:27], %[ya]\n\tds_read_b128 v[28:31], %[yb]\n\tds_read_b128 v[32:35], %[ya] offset:128\n\tds_read_b128 v[36:39], %[yb] offset:128\n\t"
               "ds_read_b128 v[40:43], %[ya] offset:256\n\tds_read_b128 v[44:47], %[yb] offset:256\n\ts_setprio 1\n\t"
               S_W(5) S_M0("100:103", "24:27", "24:27") S_M0("108:111", "24:27", "0:3")
               SH_Y(4, "28:31", "4:7") SH_Y(3, "32:35", "8:11") SH_Y(2, "36:39", "12:15") SH_Y(1, "40:43", "16:19") SH_Y(0, "44:47", "20:23")
               "s_mov_b32 s42, %[cap]\n\ts_cmp_lt_u32 %[need], %[lt]\n\ts_cselect_b32 s43, %[need], 0\n\ts_nop 3\n\tv_mul_f32 v100, v100, %[hc]\n\tv_mul_f32 v102, v102, %[hc]\n\t"
               "v_mul_f32 v144, %[nic], v109\n\tv_mov_b32 v101, v100\n\tv_mov_b32 v103, v102\n\tv_mov_b32 v109, v108\n\tv_mov_b32 v111, v110\n\ts_setprio 0\n\ts_cbranch_scc0 30f\n"
               SH_P(3, "s43")
               "s_mov_b32 s42, %[cap]\n"
               "1:\n\tds_read_b32 v136, %[afl]\n\tds_read_b128 v[112:115], %[xa]\n\tds_read_b128 v[116:119], %[xb]\n\tds_read_b128 v[120:123], %[xa] offset:128\n\t"
               "ds_read_b128 v[124:127], %[xb] offset:128\n\tds_read_b128 v[128:131], %[xa] offset:256\n\tds_read_b128 v[132:135], %[xb] offset:256\n\t"
               "ds_read_b32 %[mr], %[amr]\n\ts_sub_u32 s42, s42, 1\n\ts_cselect_b32 s41, 0, %[need]\n\ts_waitcnt lgkmcnt(7)\n\t"
               "v_readfirstlane_b32 s40, v136\n\ts_cmp_lt_u32 s40, s41\n\ts_cbranch_scc1 1b\n\ts_setprio 3\n\t"
               SH_X(6, "112:115", "24:27") SH_X(5, "116:119", "28:31") SH_X(4, "120:123", "32:35") SH_X(3, "124:127", "36:39") SH_X(2, "128:131", "40:43") SH_X(1, "132:135", "44:47")
               S_M("108:111", "112:115", "0:3") S_M("108:111", "116:119", "4:7")
               "s_waitcnt lgkmcnt(0)\n\tv_add_f32 v137, %[thr], %[mr]\n\tds_write_b32 %[am], %[mr]\n\ts_nop 1\n\t"
               "v_mov_b32_dpp v140, v100 quad_perm:[1,1,3,3]" S_D "v_mov_b32_dpp v141, v101 quad_perm:[1,1,3,3]" S_D
               "v_mov_b32_dpp v142, v102 quad_perm:[1,1,3,3]" S_D "v_mov_b32_dpp v143, v103 quad_perm:[1,1,3,3]" S_D
               S_M("108:111", "120:123", "8:11")
               "v_fmac_f32 v104, 2.0, v100\n\tv_fmac_f32_dpp v105, v101, %[two] quad_perm:[0,0,2,2]" S_D
               "v_fmac_f32 v106, 2.0, v102\n\tv_fmac_f32_dpp v107, v103, %[two] quad_perm:[0,0,2,2]" S_D
               "v_cndmask_b32 v138, v140, v141, %[r1]\n\tv_cndmask_b32 v139, v142, v143, %[r1]\n\t"
               S_M("108:111", "124:127", "12:15")
               "v_cndmask_b32 v104, v104, v105, %[r1]\n\tv_cndmask_b32 v106, v106, v107, %[r1]\n\tv_cndmask_b32 v138, v138, v139, %[r2]\n\t"
               "v_cndmask_b32 v104, v104, v106, %[r2]\n\tv_mul_f32 v139, %[nic], v138\n\t"
               S_M("108:111", "128:131", "16:19")
               "v_fmac_f32 v104, v139, v138\n\tv_max_f32 v104, %[eps], v104\n\tv_rsq_f32 v105, v104\n\t"
               S_M("108:111", "132:135", "20:23")
               "v_mul_f32 v104, v104, v105\n\t"
               "v_mov_b32_dpp v105, v105 row_ror:12 row_mask:0x2 bank_mask:0xf\n\tv_cvt_pk_bf16_f32 v106, v138, v104\n\ts_nop 0\n\t"
               "v_mov_b32_dpp v105, v105 row_ror:8 row_mask:0x4 bank_mask:0xf\n\tv_lshlrev_b32 v107, 16, v106\n\tv_and_b32 v139, 0xffff0000, v106\n\t"
               "v_mov_b32_dpp v105, v105 row_ror:4 row_mask:0x8 bank_mask:0xf\n\t"
               "v_pk_fma_f32 v[108:109], v[140:141], v[144:145], v[108:109] op_sel_hi:[1,0,1]\n\tv_pk_fma_f32 v[110:111], v[142:143], v[144:145], v[110:111] op_sel_hi:[1,0,1]\n\t"
               "v_mov_b32_dpp v100, v105 row_newbcast:0" S_D "v_mov_b32_dpp v101, v105 row_newbcast:1" S_D
               "v_mov_b32_dpp v102, v105 row_newbcast:2" S_D "v_mov_b32_dpp v103, v105 row_newbcast:3" S_D
               S_LG("108:109", "100:101") S_LG("110:111", "102:103") S_SUB("140:141", "108:109") S_SUB("142:143", "110:111")
               "v_exp_f32 v140, v140\n\tv_exp_f32 v141, v141\n\tv_exp_f32 v142, v142\n\tv_exp_f32 v143, v143\n\t"
               "v_max3_f32 %[mt], v108, v109, v110\n\tv_sub_f32 v138, v138, v107\n\tv_sub_f32 v104, v104, v139\n\tv_max_f32 %[mt], %[mt], v111\n\t"
               "v_pk_mul_f32 v[140:141], v[140:141], v[100:101]\n\tv_pk_mul_f32 v[142:143], v[142:143], v[102:103]\n\t"
               "v_cmp_gt_f32 %[ra], %[mt], v137\n\tv_cvt_pk_bf16_f32 v107, v138, v104\n\t"
               "v_cvt_pk_bf16_f32 v140, v140, v141\n\tv_cvt_pk_bf16_f32 v141, v142, v143\n\t"
               "ds_write_b64 %[ap], v[140:141]\n\ts_mov_b64 exec, %[dg]\n\t"
               "ds_write_b16 %[ai], v106\n\tds_write_b16 %[ai], v107 offset:16\n\tds_write_b16_d16_hi %[ai], v106 offset:32\n\tds_write_b16_d16_hi %[ai], v107 offset:48\n\t"
               "s_cmp_eq_u64 %[ra], 0\n\ts_cselect_b64 exec, 1, 0\n\tds_add_u32 %[af], %[inc]\n\ts_mov_b64 exec, -1\n\ts_setprio 0"
               : "=&{v[100:103]}"(rs), "=&{v[108:111]}"(S), [mr] "=&{v150}"(mref), [mt] "=&v"(mt), [ra] "=&s"(raise)
               : "{v[0:7]}"(c.q[0]), "{v[8:15]}"(c.q[1]), "{v[16:23]}"(c.q[2]), [ya] "v"(ya), [yb] "v"(ya ^ 64), [hc] "v"(1.f - 0.5f * c.ev),
                 [afl] "v"(c.ct + C_FULL), [xa] "v"(xa), [xb] "v"(xa ^ 64), [amr] "v"(c.amr), [am] "v"(am), [ap] "v"(ap), [ai] "v"(c.api + e * 256 + h * 8),
                 [af] "v"(af), [inc] "v"((unsigned)NPK), [two] "v"(2.f), [lt] "i"(2 * TPW - 2),
                 [need] "s"(__builtin_amdgcn_readfirstlane(2 * k + 2)), [r1] "s"(0xAAAAAAAAAAAAAAAAull), [r2] "s"(0xCCCCCCCCCCCCCCCCull),
                 [dg] "s"(0xF0000F0000F0000Full), [cap] "i"(SPIN_CAP), [thr] "i"(__builtin_bit_cast(unsigned, THR)), [toff] "s"(f32x2{TOFF, TOFF}),
                 [eps] "i"(__builtin_bit_cast(unsigned, EPS * CC)), [nic] "i"(__builtin_bit_cast(unsigned, -1.f / CC))
               : "memory", "scc", "s40", "s41", "s42", "s43", "v24", "v25", "v26", "v27", "v28", "v29", "v30", "v31", "v32", "v33", "v34", "v35", "v36", "v37", "v38", "v39", "v40", "v41", "v42", "v43", "v44", "v45", "v46", "v47",
                 "v104", "v105", "v106", "v107", "v144", "v112", "v113", "v114", "v115", "v116", "v117", "v118", "v119", "v120", "v121", "v122", "v123", "v124", "v125",
                 "v126", "v127", "v128", "v129", "v130", "v131", "v132", "v133", "v134", "v135", "v136", "v137", "v138", "v139", "v140", "v141", "v142", "v143");
  if (__builtin_expect(raise != 0ull, 0)) {
    mref = fmaxf(mref, 32.f * __builtin_rintf(xrow16_max(mt) * (1.f / 32.f)));
    float pp[4];
#pragma unroll
    for (int i = 0; i < 4; ++i) pp[i] = __builtin_amdgcn_exp2f(S[i] - mref) * rs[i];
    asm volatile("ds_max_f32 %6, %3\n\tds_write_b32 %2, %3\n\tds_write_b64 %0, %1\n\ts_mov_b64 exec, 1\n\tds_add_u32 %4, %5\n\ts_mov_b64 exec, -1"
                 : : "v"(ap), "v"(u32x2{cvtpk(pp[0], pp[1]), cvtpk(pp[2], pp[3])}), "v"(am), "v"(mref), "v"(af), "v"((unsigned)NPK), "v"(c.amr) : "memory");
  }
}

using u32x16 = __attribute__((ext_vector_type(16))) unsigned;
typedef __attribute__((address_space(1))) unsigned short gus;
extern "C" __global__ __launch_bounds__(768, 3) void dino_main(const void*, const void*, const void*, const void*, const void*,
                                                               const void*, const void*, void*, void*) {
  u32x16 ka; u32x2 kb; unsigned long long tent64;
  __shared__ __attribute__((aligned(16))) char lds[LDS_BYTES];
  const int wave = __builtin_amdgcn_readfirstlane((int)threadIdx.x >> 6);
  const bool helper = blockIdx.x < 8;
  lds_ptr L0 = (lds_ptr)lds;
  const unsigned lbase = (unsigned)(size_t)L0;
  const unsigned ct = lbase + CT_OFF;
#define KARG(v, i) ((char*)(((unsigned long long)v[2 * (i) + 1] << 32) | v[2 * (i)]))
#define q KARG(ka, 0)
#define f0 KARG(ka, 1)
#define f1 KARG(ka, 2)
#define wq KARG(ka, 3)
#define wk KARG(ka, 4)
#define wv KARG(ka, 5)
#define wo KARG(ka, 6)
#define box KARG(ka, 7)
#define tent ((unsigned)tent64)
#define LANE_VARS int lane; asm volatile("v_mbcnt_lo_u32_b32 %0, -1, 0\n\tv_mbcnt_hi_u32_b32 %0, -1, %0" : "=v"(lane)); \
  const int r = lane & 15, g = lane >> 4; (void)r; (void)g;
#define DOFF_VARS unsigned doff[3];   \
  _Pragma("unroll") for (int i = 0; i < 3; ++i) doff[i] = (unsigned)(lane ^ ((swz_tbl(i) >> ((lane >> 3) * 4)) & 7)) << 4;
#define BARRIER() do { asm volatile("s_waitcnt lgkmcnt(0)" ::: "memory"); __builtin_amdgcn_s_barrier(); asm volatile("" ::: "memory"); } while (0)

  {
    unsigned doff[3];
    static_assert(swz_tbl(0) == swz_tbl(3) && swz_tbl(1) == swz_tbl(4) && swz_tbl(2) == swz_tbl(5), "");
    unsigned l16, db;
#define PD(d, b, off, pol) "global_load_lds_dwordx4 %[" #d "], s[" #b "] offset:" #off pol "\n\t"
    asm volatile("s_load_dwordx16 s[64:79], %[kp], 0x0\n\ts_load_dwordx2 s[80:81], %[kp], 0x40\n\ts_memrealtime s[82:83]\n\t"
                 "v_mbcnt_lo_u32_b32 %[l16], -1, 0\n\tv_mbcnt_hi_u32_b32 %[l16], -1, %[l16]\n\t"
                 "s_lshr_b32 s84, %[b], 3\n\ts_and_b32 s85, %[b], 7\n\t"
                 "v_lshrrev_b32 %[db], 1, %[l16]\n\tv_and_b32 %[db], -4, %[db]\n\t"
                 "s_mov_b32 s86, %[t0]\n\ts_mov_b32 s87, %[t1]\n\ts_mov_b32 s88, %[t2]\n\t"
                 "v_bfe_u32 %[d0], s86, %[db], 3\n\tv_bfe_u32 %[d1], s87, %[db], 3\n\tv_bfe_u32 %[d2], s88, %[db], 3\n\t"
                 "v_xor_b32 %[d0], %[d0], %[l16]\n\tv_xor_b32 %[d1], %[d1], %[l16]\n\tv_xor_b32 %[d2], %[d2], %[l16]\n\t"
                 "v_lshlrev_b32 %[d0], 4, %[d0]\n\tv_lshlrev_b32 %[d1], 4, %[d1]\n\tv_lshlrev_b32 %[d2], 4, %[d2]\n\t"
                 "v_lshlrev_b32 %[l16], 4, %[l16]\n\t"
                 "s_cmp_eq_u32 s84, 0\n\ts_cbranch_scc1 4f\n\t"
                 "s_cmp_lg_u32 %[w], 0\n\ts_cbranch_scc1 7f\n\t"
                 "s_setprio 3\n\tv_lshlrev_b32 %[db], 3, %[l16]\n\ts_mov_b32 m0, %[junk]\n\ts_lshl_b32 s89, s84, 13\n\t"
                 "s_cmp_gt_u32 s84, 3\n\ts_cbranch_scc1 2f\n\t"
                 "s_getpc_b64 s[90:91]\n1:\n\ts_sub_u32 s86, s89, 8192\n\ts_min_u32 s86, s86, dino_end-1b-8192\n\t"
                 "s_add_u32 s90, s90, s86\n\ts_addc_u32 s91, s91, 0\n\tglobal_load_lds_dword %[db], s[90:91]\n"
                 "2:\n\ts_mul_i32 s86, s85, 98304\n\ts_mul_i32 s87, s84, 3072\n\ts_sub_u32 s87, s87, 3072\n\t"
                 "s_add_u32 s88, s86, s87\n\ts_lshl1_add_u32 s86, s86, s87\n\ts_lshl_b32 s86, s86, 1\n\t"
                 "s_mul_i32 s87, s85, 12288\n\ts_lshr_b32 s90, s89, 1\n\ts_add_u32 s87, s87, s90\n\ts_sub_u32 s87, s87, 13 * 4096\n\t"
                 "s_sub_u32 s89, s89, 4 * 8192\n\ts_cmp_gt_u32 s85, 5\n\ts_cselect_b32 s89, -1, s89\n\ts_cmp_ge_u32 s89, 11 * 8192\n\t"
                 "s_waitcnt lgkmcnt(0)\n\ts_cbranch_scc1 3f\n\t"
                 "s_cmp_lt_u32 s89, 9 * 8192\n\ts_cselect_b64 s[90:91], s[70:71], s[72:73]\n\ts_cselect_b32 s89, s89, s87\n\t"
                 "s_add_u32 s90, s90, s89\n\ts_addc_u32 s91, s91, 0\n\tglobal_load_lds_dword %[db], s[90:91]\n"
                 "3:\n\ts_mov_b32 m0, 0x3000\n\t"
                 "s_add_u32 s90, s68, s88\n\ts_addc_u32 s91, s69, 0\n\t"
                 "s_add_u32 s92, s66, s86\n\ts_addc_u32 s93, s67, 0\n\t"
                 PD(d0, 90:91, 0, " nt") PD(d1, 90:91, 1024, " nt") PD(d2, 90:91, 2048, " nt")
                 "s_mov_b32 m0, 0\n\ts_nop 0\n\t"
                 PD(d0, 92:93, 0, " nt") PD(d1, 92:93, 1024, " nt")
                 "s_sub_u32 s89, s84, 15\n\ts_cmp_gt_u32 s85, 5\n\ts_cselect_b32 s89, -1, s89\n\ts_cmp_ge_u32 s89, 11\n\ts_cbranch_scc1 9f\n\t"
                 "s_mov_b32 m0, %[junk]\n\ts_sub_u32 s87, s87, 11 * 4096\n\ts_lshl_b32 s89, s89, 13\n\ts_cmp_lt_u32 s89, 9 * 8192\n\t"
                 "s_cselect_b64 s[90:91], s[76:77], s[74:75]\n\ts_cselect_b32 s89, s89, s87\n\t"
                 "s_add_u32 s90, s90, s89\n\ts_addc_u32 s91, s91, 0\n\tglobal_load_lds_dword %[db], s[90:91]\n\t"
                 "s_branch 9f\n"
                 "7:\n\ts_cmp_eq_u32 %[w], 1\n\ts_cbranch_scc1 8f\n\ts_barrier\n\ts_branch 8f\n"
                 "4:\n\t"
                 "s_cmp_gt_u32 %[b], 5\n\ts_cbranch_scc1 8f\n\ts_cmp_gt_u32 %[w], 9\n\ts_cbranch_scc1 8f\n\t"
                 "s_mov_b32 m0, %[img]\n\ts_mul_i32 s86, %[w], 9216\n\ts_and_b32 s88, %[w], 1\n\ts_lshl1_add_u32 s89, %[b], s88\n\t"
                 "s_mul_i32 s88, s88, 6144\n\ts_mul_i32 s89, s89, 6144\n\ts_add_u32 s88, s88, %[hr]\n\t"
                 "s_cmp_lg_u32 %[w], 0\n\ts_waitcnt lgkmcnt(0)\n\ts_cbranch_scc1 5f\n\t"
                 PD(d0, 64:65, 0, "") PD(d1, 64:65, 1024, "") PD(d2, 64:65, 2048, "") PD(d0, 64:65, 3072, "")
                 "s_mov_b32 m0, %[img] + 4096\n\ts_add_u32 s90, s64, 0x1000\n\ts_addc_u32 s91, s65, 0\n\t"
                 PD(d1, 90:91, 0, "") PD(d2, 90:91, 1024, "")
                 "\n5:\n\ts_cmp_gt_u32 %[w], 7\n\ts_cselect_b64 s[92:93], s[72:73], s[70:71]\n\ts_cselect_b32 m0, s88, s86\n\ts_cselect_b32 s86, s89, s86\n\t"
                 "s_cselect_b32 s87, 2, 3\n\ts_add_u32 s92, s92, s86\n\ts_addc_u32 s93, s93, 0\n"
                 "6:\n\ts_sub_u32 s87, s87, 1\n\t"
                 PD(d0, 92:93, 0, "") PD(d1, 92:93, 1024, "") PD(d2, 92:93, 2048, "")
                 "s_add_u32 m0, m0, 3072\n\ts_add_u32 s92, s92, 3072\n\ts_addc_u32 s93, s93, 0\n\ts_cmp_lg_u32 s87, 0\n\ts_cbranch_scc1 6b"
                 "\n8:\n\ts_waitcnt lgkmcnt(0)\n9:"
                 : "={s[64:79]}"(ka), "={s[80:81]}"(kb), "={s[82:83]}"(tent64),
                   [d0] "=&v"(doff[0]), [d1] "=&v"(doff[1]), [d2] "=&v"(doff[2]), [l16] "=&v"(l16), [db] "=&v"(db)
                 : [kp] "s"(__builtin_amdgcn_kernarg_segment_ptr()), [b] "s"(blockIdx.x), [w] "s"(wave),
                   [t0] "i"(swz_tbl(0)), [t1] "i"(swz_tbl(1)), [t2] "i"(swz_tbl(2)), [junk] "i"(JUNK), [img] "i"(IMG_A), [hr] "i"(H_R)
                 : "memory", "scc", "vcc", "s84", "s85", "s86", "s87", "s88", "s89", "s90", "s91", "s92", "s93", "s94", "s95", "s96", "s97", "s98", "s99");
  }
  const unsigned t0 = (unsigned)__builtin_amdgcn_s_memrealtime() - T0BACK;

  if (__builtin_expect(blockIdx.x < 6, 0)) {
    LANE_VARS
    DOFF_VARS
    const int j = blockIdx.x;
    const int qq = (lane >> 2) & 3, p4 = lane & 3, e4 = wave & 3;
    int ra = H_R + r * ROWB + ((g ^ swz(r)) << 4);
    bf16x8 a[6], b[6], w[18];
    auto rows = [&](int t, bf16x8* d) {
#pragma unroll
      for (int kk = 0; kk < 6; ++kk) d[kk] = *(LDS_AS(bf16x8)*)(L0 + t * 6144 + ((kk & 1) ? (ra ^ 64) : ra) + (kk >> 1) * 128);
    };
    int o[3];
#pragma unroll
    for (int t = 0; t < 3; ++t) o[t] = tr_off(4 * g + qq, 4 * (3 * e4 + t) + p4);
    unsigned qo = lbase + H_Q + 3 * e4 * 512 + ((g >> 1) * 16 + r) * 16 + (g & 1) * 8, qa = lbase + IMG_A + tr_off(r, 8 * wave + g);
    if (wave == 11) {
      unsigned d;
      asm volatile("s_load_dword %0, %1, 0x40\n\ts_barrier\n\ts_barrier\n\ts_barrier\n\ts_waitcnt lgkmcnt(0)" : "=&s"(d) : "s"(box + EPOCH_OFF) : "memory");
    } else
    asm volatile("s_waitcnt vmcnt(9)\n\ts_barrier" : PIN3(o, 0), "+v"(qo), "+v"(qa) :: "memory");
    if (wave < 6 || wave == 10) {
      rows((IMG_A - H_R) / 6144, a);
      const u32x2 m0 = *(LDS_AS(u32x2)*)(size_t)qa, m1 = *(LDS_AS(u32x2)*)(size_t)(qa ^ 32);
      static_assert((IMG_A - H_R) % 6144 == 0, "");
      const bf16x8 ones = __builtin_bit_cast(bf16x8, u32x4{0x3f803f80u, 0x3f803f80u, 0x3f803f80u, 0x3f803f80u});
      f32x4 sm = {0.f, 0.f, 0.f, 0.f};
#pragma unroll
      for (int i = 0; i < 6; ++i) sm = mfma(ones, a[i], sm);
      const float mean = sm[0] * (1.f / CC);
      if (__builtin_expect(wave < 6, 1)) {
        img_st(L0, H_C, wave, lane, __builtin_bit_cast(bf16x8, u32x4{cvtpk(bflo(m0[0]) - mean, bfhi(m0[0]) - mean), cvtpk(bflo(m0[1]) - mean, bfhi(m0[1]) - mean),
                                                                   cvtpk(bflo(m1[0]) - mean, bfhi(m1[0]) - mean), cvtpk(bflo(m1[1]) - mean, bfhi(m1[1]) - mean)}));
      } else {
        asm volatile("s_barrier" : PIN3(a, 0), PIN3(a, 3) :: "memory");
        f32x2 v2 = {0.f, 0.f}, v3 = v2;
#pragma unroll
        for (int i = 0; i < 12; ++i) {
          const u32x4 d = __builtin_bit_cast(u32x4, a[i >> 1]);
          const unsigned d0 = d[2 * (i & 1)], d1 = d[2 * (i & 1) + 1];
          const f32x2 x0 = f32x2{bflo(d0), bfhi(d0)} - mean, x1 = f32x2{bflo(d1), bfhi(d1)} - mean;
          v2 += x0 * x0; v3 += x1 * x1;
        }
        v2 += v3;
        *(LDS_AS(float)*)(L0 + CT_OFF + r * 4) = LOG2E * __builtin_amdgcn_rsqf(xrow16_sum(v2[0] + v2[1]) * (1.f / CC) + EPS);
      }
    }
    if (wave < 4) {
      wait_vm<0>();
      BARRIER();
#pragma unroll
      for (int kk = 0; kk < 6; ++kk) {
        b[kk] = img_ld(L0, H_C, kk, lane);
        w[kk] = mk8(tr_read(L0 + kk * 12288 + o[0]), tr_read(L0 + kk * 12288 + 6144 + o[0]));
        __builtin_amdgcn_sched_barrier(0);
      }
#pragma unroll
      for (int i = 6; i < 18; ++i) {
        w[i] = mk8(tr_read(L0 + (i % 6) * 12288 + o[i / 6]), tr_read(L0 + (i % 6) * 12288 + 6144 + o[i / 6]));
        __builtin_amdgcn_sched_barrier(0);
      }
#pragma unroll
      for (int t = 0; t < 3; ++t) {
        f32x4 D = {0.f, 0.f, 0.f, 0.f};
#pragma unroll
        for (int kk = 0; kk < 6; ++kk) D = mfma(w[6 * t + kk], b[kk], D);
        *(LDS_AS(u32x2)*)(size_t)(qo + t * 512) = u32x2{cvtpk(D[0], D[1]), cvtpk(D[2], D[3])};
      }
      BARRIER();
    } else if (wave == 8 || wave == 9) {
      unsigned so = lane * 16 + (2 * j + (wave & 1)) * 1024;
      unsigned epoch;
      asm volatile("global_load_dword %0, %1, %2 sc1\n\ts_barrier\n\ts_waitcnt vmcnt(0)" : "=v"(epoch) : "v"(EPOCH_OFF), "s"(box) : "memory");
      rows(wave & 1, a);
      unsigned ta = line_tag(epoch, 8), tb = line_tag(epoch, 9);
      asm volatile("" : PIN3(a, 0), PIN3(a, 3), "+v"(ta), "+v"(tb), "+v"(so));
      BARRIER();
#pragma unroll
      for (int kk = 0; kk < 6; ++kk) b[kk] = img_ld(L0, H_Q, kk, lane);
      const float sc = *(LDS_AS(float)*)(L0 + CT_OFF + r * 4);
      __builtin_amdgcn_sched_barrier(0);
      f32x4 D = {0.f, 0.f, 0.f, 0.f};
#pragma unroll
      for (int kk = 0; kk < 6; ++kk) D = mfma(a[kk], b[kk], D);
      D *= sc;
      const unsigned p0 = cvtpk(D[0], D[1]), p1 = cvtpk(D[2], D[3]);
      asm volatile("global_store_dwordx4 %0, %1, %2 sc1\n\ts_nop 1" : : "v"(so), "v"(u32x4{p0, p1, p0 ^ p1 ^ ta, tb}), "s"(box + QK_OFF) : "memory");
    } else if (wave != 11) {
      if (wave != 10) asm volatile("s_waitcnt vmcnt(0) lgkmcnt(0)\n\ts_barrier" ::: "memory");
      BARRIER();
    }
    BARRIER();
    issue_rows16(wo + wave * 6144, lbase + wave * 6144, doff);
    if (wave < 2) issue_rows16(wv + (2 * j + wave) * 6144, lbase + H_R + wave * 6144, doff);
    const unsigned epoch = epoch_load(box);
    BARRIER();
    {
      const int o0 = tr_off(8 * g + qq, 4 * wave + p4), o1 = tr_off(8 * g + 4 + qq, 4 * wave + p4);
#pragma unroll
      for (int kk = 0; kk < 6; ++kk) b[kk] = mk8(tr_read(L0 + kk * 12288 + o0), tr_read(L0 + kk * 12288 + o1));
      f32x4 D[2];
#pragma unroll
      for (int h = 0; h < 2; ++h) {
        rows(h, a);
        D[h] = f32x4{0.f, 0.f, 0.f, 0.f};
#pragma unroll
        for (int kk = 0; kk < 6; ++kk) D[h] = mfma(a[kk], b[kk], D[h]);
      }
      const int e = wave / 3;
      asm volatile("global_store_dwordx4 %0, %1, %2 sc1\n\ts_waitcnt vmcnt(0)"
                   : : "v"(lane * 16 + (e * 15 + 3 * j + wave) * 1024), "v"(pack8(D[0] * SQRT_C, D[1] * SQRT_C)), "s"(box + W2_OFF) : "memory");
    }
    BARRIER();
    if (wave == 11)
      asm volatile("s_mov_b64 exec, 1\n\tglobal_store_dwordx2 %0, %1, %2 sc1\n\ts_mov_b64 exec, -1\n\ts_nop 1"
                   : : "v"(j * 128), "v"(u32x2{line_tag(epoch, 10), line_tag(epoch, 11)}), "s"(box + W2F_OFF) : "memory");
  }

  int rslot = -1;
  if (__builtin_expect(wave == 0, 1)) {
    LANE_VARS
    DOFF_VARS
    if (helper) BARRIER();
    const unsigned afull = ct + C_FULL + r * 4;
    {
      const int ti = helper ? 1023 : (lane * 31 + (int)(blockIdx.x >> 3) - 1) & 1023;
      const int sp = ti >> 5, yy = sp * 8 + ((blockIdx.x + sp) & 7), xx = (ti + 5 * sp) & 31;
      unsigned tmp, pub = helper ? 2 * (TPW - 1) : 0, pre = 0, afr = ct + C_FREE + (helper ? ((TPW - 1) % NSLOT) * 4 : 0), e0, e1, e2, g0, g1, g2;
      asm volatile("s_mov_b32 s44, %[due0]\n\ts_mov_b32 s46, %[y0]\n\ts_mov_b32 s47, %[cap]\n\ts_mov_b32 s48, %[i0]\n\ts_mov_b32 s49, %[s0]\n\t"
                   "s_mov_b32 s51, 0\n\ts_mov_b32 s45, 0\n\ts_movk_i32 s39, %[p3s]\n"
                   "1:\n\tv_readlane_b32 s53, %[ob], s48\n\tv_readlane_b32 s52, %[oa], s48\n\ts_add_u32 m0, s49, 0x3000\n\tv_cmp_gt_u32 vcc, s51, %[pre]\n\t"
                   "v_add_u32 %[g0], s53, %[d0]\n\tv_add_u32 %[g1], s53, %[d1]\n\tv_add_u32 %[g2], s53, %[d2]\n\t"
                   "v_add_u32 %[e0], s52, %[d0]\n\tv_add_u32 %[e1], s52, %[d1]\n\tv_add_u32 %[e2], s52, %[d2]\n\t"
                   "s_cbranch_vccnz 5f\n\t"
                   "s_cmp_eq_u32 s48, 0\n\ts_cbranch_scc1 2f\n\ts_cmp_eq_u32 s48, 2\n\ts_cbranch_scc1 8f\n"
                   LD_GATE(1, "")
                   LD1(g0, 68:69, 0) LD1(g1, 68:69, 1024) LD1(g2, 68:69, 2048) "s_mov_b32 m0, s49\n\ts_nop 0\n\t" LD1(e0, 66:67, 0) LD1(e1, 66:67, 1024)
                   LD_NEXT
                   "\n2:\n\tv_add_u32 %[afr], 4, %[afr]\n\ts_cmp_eq_u32 s49, %[lasts]\n\ts_cbranch_scc1 7f\n"
                   "3:\n\tv_mov_b32 %[pre], 0\n\ts_add_u32 m0, s49, 0x800\n\tds_read_b32 %[pre], %[afr]\n"
                   LD_GATE(2, "")
                   LD1(e2, 56:57, 0) LD1(e0, 56:57, 1024) LD1(e1, 56:57, 2048) LD1(e2, 56:57, 3072) "s_add_u32 m0, s49, 0x1800\n\ts_add_u32 s48, s48, 1\n\t" LD1(e0, 58:59, 0)
                   LD_NEXT
                   LD_GATE(3, "s_cmp_gt_u32 s48, %[tpw] - 2\n\ts_cbranch_scc1 38f\n39:\n\t")
                   LD1(e1, 58:59, 1024) LD1(e2, 58:59, 2048) LD1(e0, 58:59, 3072) "s_add_u32 m0, s49, 0x2800\n\ts_add_u32 s49, s49, %[slot]\n\t" LD1(e1, 60:61, 0) LD1(e2, 60:61, 1024)
                   LD_NEXT
                   "s_cmp_eq_u32 s49, %[ring]\n\ts_cselect_b32 s49, 0, s49\n\ts_cmp_gt_i32 s46, 30\n\ts_cbranch_scc1 6f\n"
                   "4:\n\ts_cmp_eq_u32 s48, %[pk]\n\ts_cmovk_i32 s39, %[p3]\n\ts_cmp_lg_u32 s48, %[tpw]\n\ts_cbranch_scc1 1b\n\ts_branch 9f\n"
                   LD_PUB(1, 0, 15, 30) LD_PUB(2, 5, 20, 35) LD_PUB(3, 10, 25, 40)
                   "38:\n\ts_cmp_eq_u32 s46, 10\n\ts_cselect_b32 s43, 23, 0\n\ts_cmp_lt_u32 s42, s43\n\ts_cbranch_scc0 39b\n\tv_add_u32 %[tmp], 1, %[pub]\n\ts_waitcnt vmcnt(22)\n\tds_write_b32 %[af], %[tmp]\n\ts_branch 39b\n"
                   "5:\n\ts_movk_i32 s40, 0x100\n"
                   "40:\n\tds_read_b32 %[pre], %[afr]\n\t" LD_VM
                   "s_sub_u32 s40, s40, 1\n\ts_cselect_b32 s42, 0, s42\n\ts_cselect_b32 s40, 0, s40\n\t"
                   "s_cmp_le_i32 s42, s46\n\ts_cbranch_scc1 42f\n"
                   "41:\n\ts_sub_i32 s47, s47, 1\n\ts_cmp_lt_i32 s47, 0\n\ts_waitcnt lgkmcnt(0)\n\ts_cbranch_scc1 10b\n\t"
                   "v_cmp_gt_u32 vcc, s51, %[pre]\n\ts_cbranch_vccnz 40b\n\ts_branch 10b\n"
                   LD_PUB0(4, 0, 15, 30)
                   "6:\n\ts_waitcnt vmcnt(45)\n\tv_add_u32 %[pub], 2, %[pub]\n\tds_write_b32 %[af], %[pub]\n\ts_sub_u32 s46, s46, 15\n\ts_branch 4b\n"
                   "7:\n\ts_waitcnt lgkmcnt(0)\n\ts_add_u32 s51, s51, 8\n\tv_subrev_u32 %[afr], %[frb], %[afr]\n\ts_branch 3b\n"
                   "8:\n\ts_barrier\n\ts_add_u32 s46, s46, 64\n\ts_branch 10b\n"
                   "9:\n\ts_cmp_lt_i32 s46, 30\n\ts_cbranch_scc1 91f\n\ts_waitcnt vmcnt(30)\n\tv_add_u32 %[pub], 2, %[pub]\n\tds_write_b32 %[af], %[pub]\n"
                   "91:\n\ts_cmp_lt_i32 s46, 15\n\ts_cbranch_scc1 92f\n\tv_add_u32 %[tmp], 1, %[pub]\n\ts_waitcnt vmcnt(27)\n\tds_write_b32 %[af], %[tmp]\n"
                   "92:\n\ts_waitcnt lgkmcnt(0)"
                   : [pub] "+v"(pub), [pre] "+v"(pre), [afr] "+v"(afr), [tmp] "=&v"(tmp),
                     [e0] "=&v"(e0), [e1] "=&v"(e1), [e2] "=&v"(e2), [g0] "=&v"(g0), [g1] "=&v"(g1), [g2] "=&v"(g2)
                   : "{s[64:79]}"(ka), "{s[56:57]}"(f0 + 0x800), "{s[58:59]}"(f0 + 0x30000), "{s[60:61]}"(f0 + 0x31000), [t0] "s"(t0), [af] "v"(afull),
                     [d0] "v"(doff[0]), [d1] "v"(doff[1]), [d2] "v"(doff[2]), [oa] "v"(yy * 393216 + xx * 6144), [ob] "v"(yy * 98304 + xx * 3072),
                     [due0] "s"(helper ? HDUE * (PACE16 / 3) : PACES / 3), [y0] "s"(helper ? -15 : -74), [i0] "s"(helper ? TPW - 1 : 0),
                     [s0] "s"(helper ? ((TPW - 1) % NSLOT) * SLOT : 0),
                     [cap] "i"(SPIN_CAP), [win] "i"(PWIN), [ahi] "i"(AHI), [adl] "i"(ADL), [amg] "i"(AMG), [p3] "i"(PACE16 / 3), [p3s] "i"(PACES / 3), [pk] "i"(PK1), [slot] "i"(SLOT),
                     [ring] "i"(NSLOT * SLOT), [frb] "i"(NSLOT * 4), [lasts] "i"((NSLOT - 1) * SLOT), [tpw] "i"(TPW)
                   : "memory", "scc", "vcc", "s39", "s40", "s41", "s42", "s43", "s44", "s45", "s46", "s47", "s48", "s49", "s51", "s52", "s53");
    }
    lds_st32(ct + C_ISS, 1u);
    asm volatile("s_waitcnt vmcnt(15)\n\tds_write_b32 %0, %1\n\ts_waitcnt vmcnt(12)\n\tds_write_b32 %0, %2\n\ts_waitcnt vmcnt(0)\n\tds_write_b32 %0, %3"
                 : : "v"(afull), "v"(2 * TPW - 2), "v"(2 * TPW - 1), "v"(~0u) : "memory");
    {
      unsigned d;
      asm volatile("global_load_dword %0, %1, %2 sc1\n\ts_setprio 0\n\ts_waitcnt vmcnt(0)" : "=v"(d) : "v"((lane * RSTR + blockIdx.x) * 128), "s"(box) : "memory");
    }
    rslot = 7;
  } else {
    if (wave == 1) {
      LANE_VARS
    {
      const unsigned o = lane * 8;
      unsigned z = (o >= C_MREF && o < C_MREF + 64) ? 0xff800000u : 0u;
      if (helper && o >= C_CONS && o < C_MREF) z = NE * ((TPW - 1) / NPK);
      u32x2 zz = {z, z};
      if (o >= C_PFL && o < C_CONS) zz = u32x2{(o - C_PFL) / 2 + 2, (o - C_PFL) / 2 + 4} + (helper ? 2u * NPK * ((TPW - 1) / NPK) : 0u);
      if (o == C_EP) zz[1] = QKW0 + QKW1 * min(t0 + T0BACK - tent, 36u) + min(t0 + T0BACK - tent, 200u);
      *(LDS_AS(u32x2)*)(L0 + CT_OFF + o) = zz;
      *(LDS_AS(u32x2)*)(L0 + ONES_OFF + (lane < 48 ? o : 0)) = u32x2{0x3f803f80u, 0x3f803f80u};
    }
    }
    if (helper || wave == 1) BARRIER();
    if (wave <= NE) {
      LANE_VARS
      const int ej = wave - 1;
      const int qq = (lane >> 2) & 3, p4 = lane & 3;
      const unsigned apk = lbase + IMG_A + lane * 16;
      const unsigned epoch = epoch_load(box);
      __builtin_amdgcn_s_setprio(2);
      {
        static_assert((TPW & 1) == 1 && C_CONS == C_PFL + 64 && C_FREE == C_FULL + 64, "");
        const int k0 = helper ? TPW - 1 : 0;
        unsigned need = 2 * k0 + 2;
        asm volatile("s_mov_b32 s46, 0\n\ts_mov_b64 exec, 0\n\ts_nop 3\n\ts_branch 99f\n"
                     "2:\n\ts_mov_b64 exec, -1\n\ts_nop 7\n\t" PV_Z4(72, 73, 74, 75) PV_Z4(76, 77, 78, 79) PV_Z4(80, 81, 82, 83) PV_Z4(84, 85, 86, 87)
                     "v_mov_b32 v88, 0xff800000\n\t"
                     "s_mov_b32 s46, %[k0]\n\ts_mov_b32 s45, %[s0]\n\ts_mov_b32 s44, %[e0]\n\ts_mov_b32 s47, %[cap]\n\t"
                     "s_cmp_lg_u32 s46, 0\n\ts_cbranch_scc1 32f\n"
                     "3:\n\tv_mov_b32 v148, %[ca]\n\t" PV_WAITF(4, 148) PV_FETCH_A
                     "s_cmp_lg_u32 s46, 0\n\ts_cbranch_scc1 99f\n\t"
                     "s_mov_b64 exec, 2\n\tds_add_u32 v148, %[one] offset:64\n\ts_mov_b64 exec, -2\n\t"
                     "v_add_u32 %[need], 2, %[need]\n\tv_add_u32 v148, 4, v148\n\ts_mov_b64 exec, -1\n\t"
                     PV_WAITF(5, 148)
                     "v_mov_b32 v149, v148\n\ts_mov_b64 exec, -2\n\tv_add_u32 %[need], 2, %[need]\n\tv_add_u32 v148, 4, v148\n\t"
                     "s_mov_b64 exec, 1\n\tv_add_u32 %[need], %[npk2], %[need]\n\ts_mov_b64 exec, -1\n\t"
                     PV_REQ("126:129", "130:133", "134:135", 136, 148) PV_FETCH_B
                     "s_mov_b64 exec, 2\n\tds_add_u32 v149, %[one] offset:64\n\ts_mov_b64 exec, -1\n"
                     "6:\n\t"
                     PV_STEP(1, "126:129", "130:133", "134:135", 134, 135, 136, 148, "138:141", "142:145", "146:147", 137, 149,
                             "92:95", "98:101", "104:107", "90:93", "96:99", "102:105",
                             PV_MOV(92, 90) PV_MOV(93, 91), PV_MOV(98, 96) PV_MOV(99, 97), PV_MOV(104, 102) PV_MOV(105, 103), "")
                     "s_cbranch_vccnz 7f\n"
                     "16:\n\t" PV_FETCH_A PV_REL(148)
                     "s_lshl1_add_u32 s54, s46, -2\n\ts_lshr_b64 s[54:55], %[sch], s54\n\ts_and_b32 s54, s54, 15\n\ts_cbranch_scc1 31f\n"
                     "30:\n\t"
                     PV_STEP(2, "138:141", "142:145", "146:147", 146, 147, 137, 149, "126:129", "130:133", "134:135", 136, 148,
                             "110:113", "116:119", "122:125", "108:111", "114:117", "120:123",
                             PV_MOV(110, 108) PV_MOV(111, 109), PV_MOV(116, 114) PV_MOV(117, 115), PV_MOV(122, 120) PV_MOV(123, 121),
                             "s_cmp_eq_u32 s46, %[last] - 1\n\ts_cbranch_scc1 98f\n\t")
                     "s_cbranch_vccnz 8f\n"
                     "26:\n\t" PV_FETCH_B PV_REL(149)
                     "s_branch 6b\n"
                     PV_WAITF(7, 148) "s_branch 16b\n"
                     PV_WAITF(8, 149) "s_branch 26b\n"
                     PV_SLOW(1, "126:129", "130:133", "134:135", 134, 135, 136, 148, 126, 127, 128, 129)
                     PV_SLOW(2, "138:141", "142:145", "146:147", 146, 147, 137, 149, 138, 139, 140, 141)
                     "31:\n\ts_cmp_lg_u32 s54, 8\n\ts_cbranch_scc1 34f\n"
                     "32:\n\tv_mbcnt_lo_u32_b32 v152, 31, 0\n\tv_lshlrev_b32 v152, 7, v152\n\ts_mov_b32 s55, %[gcap]\n"
                     "33:\n\tglobal_load_dwordx2 v[150:151], v152, %[fl] sc1\n\ts_sub_u32 s55, s55, 1\n\ts_cselect_b32 s56, 0, -1\n\ts_waitcnt vmcnt(0) lgkmcnt(0)\n\t"
                     "v_cmp_ne_u64 s[48:49], %[tag], v[150:151]\n\ts_sleep 2\n\ts_and_b32 s48, s48, s56\n\ts_cbranch_scc1 33b\n\t"
                     "s_mov_b32 s57, 0\n\tv_mov_b32 v150, %[apk]\n\ts_cmp_lg_u32 s46, %[last]\n\ts_cbranch_scc1 30b\n\ts_mov_b32 s54, 7\n\ts_branch 38f\n"
                     "34:\n\ts_cmp_lg_u32 s46, %[last] - 3\n\ts_cbranch_scc1 36f\n\tv_mov_b32 v151, %[iss]\n"
                     "35:\n\tds_read_b32 v150, v151\n\ts_sub_i32 s47, s47, 1\n\ts_cmp_lt_i32 s47, 0\n\ts_cselect_b32 s55, 0, 1\n\ts_waitcnt lgkmcnt(0)\n\t"
                     "v_readfirstlane_b32 s56, v150\n\ts_cmp_lt_u32 s56, s55\n\ts_cbranch_scc1 35b\n"
                     "36:\n\ts_mul_i32 s55, s57, 3072\n\tv_add_u32 v150, s55, %[apk]\n\ts_getpc_b64 s[48:49]\n"
                     "37:\n\ts_mul_i32 s55, s57, 40\n\ts_add_u32 s55, s55, 38f-37b\n\ts_add_u32 s48, s48, s55\n\ts_addc_u32 s49, s49, 0\n\t"
                     "s_add_u32 s57, s57, s54\n\ts_sub_u32 s54, s54, 1\n\ts_setpc_b64 s[48:49]\n"
                     "38:\n\t" PV_W2(0, 3, 4, 7, 8, 11) PV_W2(12, 15, 16, 19, 20, 23) PV_W2(24, 27, 28, 31, 32, 35) PV_W2(36, 39, 40, 43, 44, 47)
                     PV_W2(48, 51, 52, 55, 56, 59) PV_W2(60, 63, 64, 67, 68, 71)
                     "s_waitcnt lgkmcnt(0)\n\ts_branch 3b\n"
                     "96:\n\t" PV_RESC(134, 135, 126, 127, 128, 129) "s_nop 0\n\ts_branch 95f\n"
                     "98:\n\t" PV_MF("76:79", "114:117", "138:141") PV_MF("80:83", "120:123", "138:141")
                     "99:\n\ts_cmp_eq_u32 s46, 0\n\ts_cselect_b32 s54, 0, %[nl]\n\ts_cselect_b32 s55, 0, %[x1n]\n\ts_waitcnt lgkmcnt(0)\n\t"
                     "v_mov_b64 v[108:109], v[90:91]\n\tv_mov_b64 v[110:111], v[90:91]\n\tv_mov_b64 v[112:113], v[96:97]\n\tv_mov_b64 v[114:115], v[96:97]\n\t"
                     "v_mov_b64 v[116:117], v[102:103]\n\tv_mov_b64 v[118:119], v[102:103]\n\tv_add_u32 v120, %[aw], %[apk]\n\tv_add_u32 v121, %[ar], %[apk]\n\t"
                     "v_mov_b32 v122, %[x1]\n\tv_lshrrev_b32 v123, 1, %[apm]\n\tv_add_u32 v123, %[lwo], v123\n\t"
                     "v_mbcnt_lo_u32_b32 v124, -1, 0\n\tv_mbcnt_hi_u32_b32 v124, -1, v124\n\tv_and_b32 v125, 1, v124\n\tv_and_b32 v124, 48, v124\n\t"
                     "v_lshl_add_u32 v124, v125, 2, v124\n\tv_add_u32 v124, %[lw], v124\n"
                     "94:\n\tds_read_b32 v136, v122 offset:%[pfd]\n\tds_read_b64 v[134:135], %[apm] offset:%[el]\n\tds_read_b128 v[126:129], %[apk] offset:8 * %[el]\n\t"
                     "ds_read_b128 v[130:133], %[apu] offset:2 * %[el]\n\ts_sub_i32 s47, s47, 1\n\ts_cmp_lt_i32 s47, 0\n\ts_cselect_b32 s54, 0, s54\n\ts_waitcnt lgkmcnt(3)\n\t"
                     "v_readfirstlane_b32 s56, v136\n\ts_cmp_lt_u32 s56, s54\n\ts_cbranch_scc1 94b\n\ts_waitcnt lgkmcnt(2)\n\t"
                     "v_cmp_neq_f32 vcc, v134, v88\n\tv_cmp_neq_f32 s[50:51], v135, v88\n\ts_or_b64 vcc, vcc, s[50:51]\n\ts_waitcnt lgkmcnt(0)\n\ts_cbranch_scc1 96b\n"
                     "95:\n\t" PV_MF("84:87", "130:133", "126:129") "s_nop 1\n\t" PV_MF("72:75", "92:95", "126:129") "s_nop 1\n\t" PV_MF("72:75", "108:111", "126:129")
                     "s_nop 1\n\tv_add_f32 v153, v86, v87\n\tv_add_f32 v151, v84, v85\n\t" PV_MF("76:79", "98:101", "126:129")
                     "v_rcp_f32 v150, v153\n\tv_mul_f32 v151, %[nic], v151\n\t" PV_MF("76:79", "112:115", "126:129")
                     "v_mul_f32 v151, v151, v150\n\t" PV_PK(72, 73) PV_MF("80:83", "104:107", "126:129") PV_PK(74, 75)
                     "v_cvt_pk_bf16_f32 v72, v72, v73\n\t" PV_MF("80:83", "116:119", "126:129") "v_cvt_pk_bf16_f32 v73, v74, v75\n\tds_write_b64 v120, v[72:73]\n\t"
                     PV_NRM(76, 77, 78, 79, 2048) "v_log_f32 v153, v153\n\t" PV_NRM(80, 81, 82, 83, 4096)
                     "s_and_b64 exec, exec, 1\n\tds_add_u32 v122, %[one]\n\ts_cselect_b64 exec, -1, 0\n\t"
                     "v_subrev_f32 v88, %[toff], v88\n\tv_add_f32 v88, v88, v153\n\tds_write_b32 v123, v88\n\tds_read2_b32 v[114:115], v124 offset1:2\n"
                     "97:\n\tds_read_b32 v150, v122\n\tds_read_b128 v[90:93], v121\n\tds_read_b128 v[94:97], v121 offset:1024\n\tds_read_b128 v[98:101], v121 offset:2048\n\t"
                     "ds_read_b128 v[102:105], v121 offset:3072\n\tds_read_b128 v[106:109], v121 offset:4096\n\tds_read_b128 v[110:113], v121 offset:5120\n\t"
                     "s_sub_i32 s47, s47, 1\n\ts_cmp_lt_i32 s47, 0\n\ts_cselect_b32 s55, 0, s55\n\ts_waitcnt vmcnt(0) lgkmcnt(6)\n\t"
                     "v_readfirstlane_b32 s56, v150\n\ts_cmp_lt_u32 s56, s55\n\ts_cbranch_scc1 97b\n\ts_cmp_eq_u32 s46, 0\n\t"
                     PV_G(5, "90:93", "0:3", "4:7", "8:11", "0", "0", "0")
                     PV_G(4, "94:97", "12:15", "16:19", "20:23", "v[72:75]", "v[76:79]", "v[80:83]")
                     PV_G(3, "98:101", "24:27", "28:31", "32:35", "v[72:75]", "v[76:79]", "v[80:83]")
                     PV_G(2, "102:105", "36:39", "40:43", "44:47", "v[72:75]", "v[76:79]", "v[80:83]")
                     PV_G(1, "106:109", "48:51", "52:55", "56:59", "v[72:75]", "v[76:79]", "v[80:83]")
                     PV_G(0, "110:113", "60:63", "64:67", "68:71", "v[72:75]", "v[76:79]", "v[80:83]")
                     "v_xor_b32 v114, v114, %[ltag]\n\tv_xor_b32 v115, v115, %[ltag]\n\ts_nop 3\n\t"
                     "v_cvt_pk_bf16_f32 v116, v72, v73\n\tv_cvt_pk_bf16_f32 v120, v74, v75\n\tv_cvt_pk_bf16_f32 v117, v76, v77\n\tv_cvt_pk_bf16_f32 v121, v78, v79\n\t"
                     "v_cvt_pk_bf16_f32 v118, v80, v81\n\tv_bitop3_b32 v119, v116, v117, v114 bitop3:0x96\n\tv_cvt_pk_bf16_f32 v122, v82, v83\n\t"
                     "v_bitop3_b32 v123, v120, v121, v115 bitop3:0x96\n\tv_xor_b32 v119, v119, v118\n\tv_xor_b32 v123, v123, v122\n\t"
                     "s_cbranch_scc1 2b\n\t"
                     "global_store_dwordx4 %[so], v[116:119], %[bx] sc1\n\tglobal_store_dwordx4 %[so], v[120:123], %[bx2] sc1\n\t"
                     "s_waitcnt lgkmcnt(0)\n\ts_nop 0"
                     : [need] "+v"(need)
                     : [so] "v"(((ej * 16 + (lane >> 3)) * RSTR + blockIdx.x) * 128 + (lane & 7) * 16), [ltag] "v"(line_tag(epoch, lane & 7)),
                       [bx] "s"(box), [bx2] "s"(box + 8 * RSTR * 128), [aw] "s"(A_IMG - IMG_A + (ej >> 1) * 1024 + (ej & 1) * 8), [ar] "i"(A_IMG - IMG_A),
                       [nic] "i"(__builtin_bit_cast(unsigned, -1.f / CC)), [toff] "i"(__builtin_bit_cast(unsigned, TOFF)),
                       [lwo] "i"(CT_OFF + C_LW - PKM / 2), [lw] "i"(CT_OFF + C_LW), [x1] "i"(CT_OFF + C_X1), [x1n] "i"(NE),
                       [nl] "i"(2 * TPW + 2 * NPK), [el] "i"(((TPW - 1) % NPK) * 128), [pfd] "i"(C_PFL + ((TPW - 1) % NPK) * 4 - C_X1),
                       [apk] "v"(apk), [apm] "v"(lbase + PKM + r * 8), [apu] "v"(lbase + PKU + g * 64 + (r & 3) * 16),
                       [ox] "v"(lbase + tr_off(4 * g + qq, 4 * ej + p4)), [oy] "v"(lbase + 32 * ROWB + tr_off(2 * g + (qq >> 1), 4 * ej + p4)),
                       [ca] "v"(ct + (lane == 0 ? C_PFL + (k0 % NPK) * 4 : C_FULL + (k0 % NSLOT) * 4)),
                       [lim] "v"(ct + (lane == 0 ? C_PFL + NPK * 4 : C_FULL + NSLOT * 4)), [base] "v"(ct + (lane == 0 ? C_PFL : C_FULL)), [one] "v"(1u),
                       [k0] "s"(k0), [s0] "s"(helper ? ((TPW - 1) % NSLOT) * SLOT : 0), [e0] "s"(helper ? ((TPW - 1) % NPK) * 128 : 0),
                       [w2] "s"(box + W2_OFF + ej * 18432 - (lbase + IMG_A)), [fl] "s"(box + W2F_OFF),
                       [tag] "s"(u32x2{line_tag(epoch, 10), line_tag(epoch, 11)}), [gcap] "i"(GSPIN_CAP), [iss] "i"(CT_OFF + C_ISS), [cap] "i"(SPIN_CAP), [slot] "i"(SLOT), [ring] "i"(NSLOT * SLOT), [pring] "i"(NPK * 128),
                       [npk2] "i"(2 * NPK), [last] "i"(TPW - 1), [tight] "i"(TPW - 5),  [sch] "s"(W2SCH)
                     : "memory", "scc", "vcc", "s44", "s45", "s46", "s47", "s48", "s49", "s50", "s51", "s52", "s53", "s54", "s55", "s56", "s57",
                       "v0", "v1", "v2", "v3", "v4", "v5", "v6", "v7", "v8", "v9", "v10", "v11", "v12", "v13", "v14", "v15", "v16", "v17", "v18", "v19", "v20", "v21", "v22", "v23", "v24", "v25", "v26", "v27", "v28", "v29", "v30", "v31", "v32", "v33", "v34", "v35", "v36", "v37", "v38", "v39", "v40", "v41", "v42", "v43", "v44", "v45", "v46", "v47", "v48", "v49", "v50", "v51", "v52", "v53", "v54", "v55", "v56", "v57", "v58", "v59", "v60", "v61", "v62", "v63", "v64", "v65", "v66", "v67", "v68", "v69", "v70", "v71", "v72", "v73", "v74", "v75", "v76", "v77", "v78", "v79", "v80", "v81", "v82", "v83", "v84", "v85", "v86", "v87", "v88",
                       "v90", "v91", "v92", "v93", "v94", "v95", "v96", "v97", "v98", "v99", "v100", "v101", "v102", "v103", "v104", "v105", "v106", "v107",
                       "v108", "v109", "v110", "v111", "v112", "v113", "v114", "v115", "v116", "v117", "v118", "v119", "v120", "v121", "v122", "v123",
                       "v124", "v125", "v126", "v127", "v128", "v129", "v130", "v131", "v132", "v133", "v134", "v135", "v136", "v137", "v138", "v139",
                       "v140", "v141", "v142", "v143", "v144", "v145", "v146", "v147", "v148", "v149", "v150", "v151", "v152", "v153", "v154", "v155");
      }
    } else {
      LANE_VARS
      const int c = wave - 1 - NE;
      SCtx sc;
      sc.L0 = L0; sc.ct = ct; sc.apk = lbase + IMG_A + lane * 16; sc.apm = lbase + PKM + r * 8;
      sc.api = lbase + PKU + g * 64 + (r & 3) * 2 + ((r >> 2) == g ? 0 : 8); sc.amr = ct + C_MREF + r * 4;
      sc.o_rb = r * ROWB + ((g ^ swz(r)) << 4);
      sc.ev = (r & 1) ^ 1;
      sc.o_yb = (r & 1) ? ONES_OFF + (g << 4) : (32 + (r >> 1)) * ROWB + ((g ^ swz(r >> 1)) << 4);
      if (c >= NEARLY) {
        static_assert(NS - NEARLY == 3, "");
        const int u = c - NEARLY;
        const unsigned epoch = epoch_load(box);
        const unsigned ta = line_tag(epoch, 8), tb = line_tag(epoch, 9);
        const unsigned qw = lds_ld32(ct + C_EP + 4);
        while ((unsigned)__builtin_amdgcn_s_memrealtime() - tent < qw) __builtin_amdgcn_s_sleep(4);
        u32x4 v[4];
#pragma unroll 1
        for (int sp = 0; sp < GSPIN_CAP; ++sp) {
          asm volatile("global_load_dwordx4 %0, %4, %5 sc1\n\tglobal_load_dwordx4 %1, %4, %5 offset:1024 sc1\n\tglobal_load_dwordx4 %2, %4, %5 offset:2048 sc1\n\t"
                       "global_load_dwordx4 %3, %4, %5 offset:3072 sc1\n\ts_waitcnt vmcnt(0)"
                       : "=&v"(v[0]), "=&v"(v[1]), "=&v"(v[2]), "=&v"(v[3]) : "v"(lane * 16), "s"(box + QK_OFF + u * 4096) : "memory");
          unsigned bad = 0;
#pragma unroll
          for (int t = 0; t < 4; ++t) bad |= (v[t][0] ^ v[t][1] ^ v[t][2] ^ ta) | (v[t][3] ^ tb);
          if (!__any(bad != 0u)) break;
        }
#pragma unroll
        for (int t = 0; t < 4; ++t)
          *(LDS_AS(u32x2)*)(L0 + IMG_A + (4 * u + t) * 512 + ((g >> 1) * 16 + r) * 16 + (g & 1) * 8) = u32x2{v[t][0], v[t][1]};
        asm volatile("" ::: "memory");
        lds_add(ct + C_QI, 1u);
        lds_st32(ct + C_EP, epoch);
      }
      static_assert(NE == 4 && NPK == 6 && NSLOT == 10, "");
      s_items(sc, helper ? TPW : c, helper || c >= NEARLY);
      static_assert(NH1 == 3 && NS == 7 && TPW == 33, "");
      const int hi = c == 0 ? 5 : c - 2;
      if (hi >= (helper ? 4 : 0)) s_half(sc, TPW - NH1 + (hi >> 1), hi & 1);
      rslot = c;
    }
  }

  if (rslot >= 0 && ((blockIdx.x >> 3) & 3) == 1) {
    LANE_VARS
    __builtin_amdgcn_s_setprio(0);
    const int rid = ((blockIdx.x >> 5) << 3) | (blockIdx.x & 7);
    const int qa = ((rid >> 1) & 3) * 4 + ((rid >> 3) & 1) * 2;
    const char* src = box + (size_t)(rid * RSTR + rslot * 32) * 128;
    const int e = lane & 7;
    const int oidx = (qa + (e & 1)) * CC + 48 * (rid >> 4) + 16 * (e < 6 ? e >> 1 : 0) + 8 * (rid & 1) + (lane >> 3);
    const unsigned short qv = ((const gus*)q)[oidx];
    u32x4 am;
#pragma unroll
    for (int d = 0; d < 4; ++d) am[d] = (e >> 1) == d ? ((e & 1) ? 0xffff0000u : 0x0000ffffu) : 0u;
    const unsigned epoch = *(LDS_AS(unsigned)*)(L0 + CT_OFF + C_EP);
    const unsigned arf = lbase + RED_OFF + 4096 + (lane & 1) * 4;
    poll_ge_sleep(ct + C_FREE + ((TPW - NPK) % NSLOT) * 4, 8u * ((TPW - NPK) / NSLOT + 1));
    lds_st32(arf + rslot * 8, 0u);
#define R_LD(d, m, o) "s_mov_b64 exec, s[" m "]\n\tglobal_load_dwordx4 v[" d "], %[vo], %[src] offset:" #o " sc1\n\t"
#define R_W(l, a0, a1, a2, a3) "v_cvt_pk_bf16_f32 v" #a3 ", v" #a3 ", v" #a3 "\n\tv_and_b32 v" #a0 ", v60, v" #a3 "\n\tv_and_b32 v" #a1 ", v61, v" #a3 "\n\t" \
                               "v_and_b32 v" #a2 ", v62, v" #a3 "\n\tv_and_b32 v" #a3 ", v63, v" #a3 "\n\t"
#define R_G(n, w, m, l, d0, d1, d2, d3, a0, a1, a2, a3, c) \
  "s_mov_b64 exec, s[" m "]\n\ts_cbranch_execz " #n "f\n\ts_waitcnt vmcnt(" #w ")\n\tv_bitop3_b32 v" #l ", v" #d0 ", v" #d1 ", v" #d2 " bitop3:0x96\n\t" \
  "v_bitop3_b32 v" #l ", v" #l ", v" #d3 ", %[tag] bitop3:0x96\n\tv_mov_b32 v" #d3 ", s49\n\ts_mov_b64 exec, -1\n\tv_exp_f32 v" #a3 ", v" #l "\n\t" \
  "v_xor_b32_dpp v59, v" #l ", v" #l " quad_perm:[2,3,0,1] row_mask:0xf bank_mask:0x5\n\tv_xor_b32_dpp v59, v" #l ", v" #l " row_shr:4 row_mask:0xf bank_mask:0xa\n\t" \
  R_W(l, a0, a1, a2, a3) "v_cmp_ne_u32 s[" m "], 0, v59\n\ts_nop 0\n" \
  "1" #n ":\n\tv_mfma_f32_16x16x32_bf16 v[36:39], v[" #a0 ":" #a3 "], v[" #d0 ":" #d3 "], " c "\n\t"
#define R_X(n) #n ":\n\ts_mov_b64 exec, -1\n\ts_nop 2\n\ts_branch 1" #n "b\n"
#define R_F(l, d0, d3, a0, a1, a2, a3, c) "v_sub_f32 v" #a3 ", v" #l ", v56\n\tv_exp_f32 v" #a3 ", v" #a3 "\n\ts_nop 0\n\t" R_W(l, a0, a1, a2, a3) \
  "s_nop 1\n\tv_mfma_f32_16x16x32_bf16 v[36:39], v[" #a0 ":" #a3 "], v[" #d0 ":" #d3 "], " c "\n\t"
#define R_RD(d, a, b) "ds_read2st64_b32 v[" d "], %[ard] offset0:" #a " offset1:" #b "\n\t"
#define R_A(d, a) "v_pk_add_f32 v[" d "], v[" d "], v[" a "]\n\t"
#define R_S(x, f) "v_sub_f32 v" #f ", v" #f ", v56\n\tv_exp_f32 v" #f ", v" #f "\n\ts_nop 0\n\tv_mul_f32 v[" #x "], v[" #x "], v" #f "\n\tv_mul_f32 v[" #x " + 1], v[" #x " + 1], v" #f "\n\t"
    asm volatile("s_mov_b64 s[40:41], -1\n\ts_mov_b64 s[42:43], -1\n\ts_mov_b64 s[44:45], -1\n\ts_mov_b64 s[46:47], -1\n\ts_mov_b32 s48, %[cap]\n\ts_mov_b32 s49, 0x3f803f80\n\t"
                 "v_mov_b32 v57, 1\n\tv_mov_b32 v58, %[x1]\n"
                 "20:\n\ts_sleep 1\n\tds_read_b32 v56, v58\n\ts_sub_u32 s48, s48, 1\n\ts_cselect_b32 s50, 0, %[x1n]\n\ts_waitcnt lgkmcnt(0)\n\t"
                 "v_readfirstlane_b32 s51, v56\n\ts_cmp_lt_u32 s51, s50\n\ts_cbranch_scc1 20b\n\tv_mov_b32 v58, %[red]\n\ts_mov_b32 s48, %[gcap]\n\ts_sleep %[rs]\n"
                 "21:\n\t" R_LD("0:3", "40:41", 0) R_LD("4:7", "42:43", 1024) R_LD("8:11", "44:45", 2048) R_LD("12:15", "46:47", 3072)
                 "s_cmp_eq_u32 s48, %[gcap]\n\ts_cbranch_scc1 24f\n\ts_waitcnt vmcnt(0)\n24:\n\t"
                 R_G(1, 3, "40:41", 32, 0, 1, 2, 3, 16, 17, 18, 19, "0") R_G(2, 2, "42:43", 33, 4, 5, 6, 7, 20, 21, 22, 23, "v[36:39]")
                 R_G(3, 1, "44:45", 34, 8, 9, 10, 11, 24, 25, 26, 27, "v[36:39]") R_G(4, 0, "46:47", 35, 12, 13, 14, 15, 28, 29, 30, 31, "v[36:39]")
                 "v_max3_f32 v56, v32, v33, v34\n\ts_sub_u32 s48, s48, 1\n\ts_cbranch_scc1 25f\n\ts_or_b64 s[50:51], s[40:41], s[42:43]\n\tv_max_f32 v56, v56, v35\n\t"
                 "s_or_b64 s[50:51], s[50:51], s[44:45]\n\ts_or_b64 s[50:51], s[50:51], s[46:47]\n\tv_cmp_nlt_f32 vcc, |v56|, %[lim]\n\ts_cbranch_scc1 21b\n\t"
                 "s_cbranch_vccnz 25f\n"
                 "22:\n\ts_mov_b64 exec, %[wm]\n\tds_write_b128 %[awr], v[36:39]\n\ts_mov_b64 exec, 1\n\tds_add_rtn_u32 v59, v58, v57\n\ts_mov_b64 exec, -1\n\t"
                 R_RD("40:41", 0, 1) R_RD("42:43", 2, 3) R_RD("44:45", 4, 5) R_RD("46:47", 6, 7) R_RD("48:49", 8, 9) R_RD("50:51", 10, 11) R_RD("52:53", 12, 13) R_RD("54:55", 14, 15)
                 "s_waitcnt lgkmcnt(8)\n\tv_add_u32 v59, v59, v57\n\ts_nop 0\n\tv_readfirstlane_b32 s50, v59\n\ts_cmp_lg_u32 s50, 8\n\ts_cbranch_scc1 26f\n"
                 "23:\n\ts_waitcnt lgkmcnt(6)\n\t" R_A("40:41", "42:43") "s_waitcnt lgkmcnt(4)\n\t" R_A("44:45", "46:47") "s_waitcnt lgkmcnt(2)\n\t" R_A("48:49", "50:51")
                 R_A("40:41", "44:45") "s_waitcnt lgkmcnt(0)\n\t" R_A("52:53", "54:55") R_A("48:49", "52:53") R_A("40:41", "48:49")
                 "v_add_f32 v40, v40, v41\n\ts_nop 1\n\tv_mov_b32_dpp v41, v40 quad_perm:[2,3,2,3] row_mask:0xf bank_mask:0xf\n\ts_nop 1\n\t"
                 "v_mov_b32_dpp v41, v41 row_shl:4 row_mask:0xf bank_mask:0x5\n\tv_rcp_f32 v41, v41\n\ts_mov_b64 exec, %[om]\n\tv_mul_f32 v40, v40, v41\n\t"
                 "v_cvt_pk_bf16_f32 v40, v40, v40\n\tv_lshlrev_b32 v40, 16, v40\n\tv_add_f32 v40, v40, %[qf]\n\tv_cvt_pk_bf16_f32 v40, v40, v40\n\t"
                 "global_store_short %[oo], v40, %[out]\n\t"
                 "s_cmp_lg_u32 %[rid], 0\n\ts_cbranch_scc1 29f\n\ts_mov_b64 exec, 1\n\tv_mov_b32 v41, %[eo]\n\tglobal_store_dword v41, %[ep], %[bx] sc1\n\ts_branch 29f\n"
                 R_X(1) R_X(2) R_X(3) R_X(4)
                 "25:\n\tv_max_f32_dpp v56, v56, v56 row_ror:8 row_mask:0xf bank_mask:0xf\n\tv_mov_b32 v59, v56\n\ts_nop 1\n\tv_permlane16_swap_b32 v56, v59\n\tv_max_f32 v56, v56, v59\n\t"
                 "v_mov_b32 v59, v56\n\ts_nop 1\n\tv_permlane32_swap_b32 v56, v59\n\tv_max_f32 v56, v56, v59\n\tv_mov_b32 v57, 0x101\n\tv_add_u32 v59, %[rs8], %[arf]\n\tds_write_b32 v59, v56\n\t"
                 R_F(32, 0, 3, 16, 17, 18, 19, "0") R_F(33, 4, 7, 20, 21, 22, 23, "v[36:39]") R_F(34, 8, 11, 24, 25, 26, 27, "v[36:39]") R_F(35, 12, 15, 28, 29, 30, 31, "v[36:39]")
                 "s_nop 7\n\ts_branch 22b\n"
                 "26:\n\ts_waitcnt vmcnt(0)\n\ts_and_b32 s50, s50, 0xff\n\ts_cmp_lg_u32 s50, 8\n\ts_cbranch_scc1 29f\n\t"
                 "ds_read2_b32 v[0:1], %[arf] offset1:2\n\tds_read2_b32 v[2:3], %[arf] offset0:4 offset1:6\n\tds_read2_b32 v[4:5], %[arf] offset0:8 offset1:10\n\t"
                 "ds_read2_b32 v[6:7], %[arf] offset0:12 offset1:14\n\ts_waitcnt lgkmcnt(0)\n\tv_max3_f32 v56, v0, v1, v2\n\tv_max3_f32 v56, v56, v3, v4\n\t"
                 "v_max3_f32 v56, v56, v5, v6\n\tv_max_f32 v56, v56, v7\n\t"
                 R_S(40, 0) R_S(42, 1) R_S(44, 2) R_S(46, 3) R_S(48, 4) R_S(50, 5) R_S(52, 6) R_S(54, 7) "s_branch 23b\n"
                 "29:\n\ts_mov_b64 exec, -1\n\ts_waitcnt lgkmcnt(0)"
                 :
                 : "{v[60:63]}"(am), [vo] "v"(lane * 16), [src] "s"(src), [tag] "v"(line_tag(epoch, e)),
                   [awr] "v"(lbase + RED_OFF + (rslot * 2 + (r >> 3)) * 256 + (r & 7) * 32 + (g & 1) * 16), [ard] "v"(lbase + RED_OFF + lane * 4), [arf] "v"(arf),
                   [qf] "v"((unsigned)qv << 16), [oo] "v"(oidx * 2), [out] "s"(KARG(kb, 0)), [ep] "v"(epoch + 1u), [bx] "s"(box), [rid] "s"(rid), [rs8] "s"(rslot * 8),
                   [wm] "s"(0xff00ff0000ff00ffull), [om] "s"(0x3f3f3f3f3f3f3f3full), [lim] "s"(64.f),
                   [cap] "i"(SPIN_CAP), [gcap] "i"(GSPIN_CAP), [x1] "i"(CT_OFF + C_X1), [x1n] "i"(NE), [red] "i"(CT_OFF + C_RED), [rs] "i"(RSLEEP), [eo] "i"(EPOCH_OFF)
                 : "memory", "scc", "vcc", "s40", "s41", "s42", "s43", "s44", "s45", "s46", "s47", "s48", "s49", "s50", "s51", "v0", "v1", "v2", "v3", "v4", "v5", "v6", "v7", "v8", "v9", "v10", "v11", "v12", "v13", "v14", "v15", "v16", "v17", "v18", "v19", "v20", "v21", "v22", "v23", "v24", "v25", "v26", "v27", "v28", "v29", "v30", "v31", "v32", "v33", "v34", "v35", "v36", "v37", "v38", "v39", "v40", "v41", "v42", "v43", "v44", "v45", "v46", "v47", "v48", "v49", "v50", "v51", "v52", "v53", "v54", "v55", "v56", "v57", "v58", "v59");
  }
}

#undef q
#undef f0
#undef f1
#undef wq
#undef wk
#undef wv
#undef wo
#undef box
extern "C" __global__ void dino_end() {}

extern "C" void kernel_launch(void* const* d_in, const int* in_sizes, int n_in,
                              void* d_out, int out_size, void* d_ws, size_t ws_size,
                              hipStream_t stream) {
  dino_main<<<NWG, 768, 0, stream>>>((const unsigned short*)d_in[0], (const char*)d_in[1], (const char*)d_in[2], (const char*)d_in[3],
                                     (const char*)d_in[4], (const char*)d_in[5], (const char*)d_in[6], (char*)d_ws + ((ws_size - (4u << 20)) & ~(size_t)0x1fffff),
                                     (unsigned short*)d_out);
}
```
